```python
import torch

def _softmax(x, axis=-1):
    return torch.softmax(x, dim=axis)
def _gelu(x):
    return torch.nn.functional.gelu(x, approximate="tanh")
def _silu(x):
    return torch.nn.functional.silu(x)
def _sigmoid(x):
    return torch.sigmoid(x)
def _layernorm(x, axis=-1, eps=1e-5):
    m = x.mean(dim=axis, keepdim=True)
    v = x.var(dim=axis, keepdim=True, unbiased=False)
    return (x - m) / torch.sqrt(v + eps)
def _scatter_add(tgt, idx, src, axis=0):
    return tgt.clone().index_add_(axis, idx, src)
def _segment_sum(src, seg, n):
    out = torch.zeros((n,) + src.shape[1:], dtype=src.dtype)
    return out.index_add_(0, seg, src)
def _pad(x, pw):
    flat = tuple(v for pair in reversed(pw) for v in pair)
    return torch.nn.functional.pad(x, flat)

kernel_name = 'concat_heads__ssm_conv_s4__linear_kernel_hedgehog__prompt'

def setup_inputs():

    _w = (lambda a: (lambda t: t.to(torch.bfloat16) if t.is_floating_point() else t)(torch.as_tensor(a)))
    def _A_setup():
        B, L, D, N = 1, 2048, 640, 128
        A_re = -(lambda _x: torch.exp(torch.as_tensor(_x)))((lambda *s: torch.randn(*s, dtype=torch.bfloat16) * 3.2)(D, N))
        A_im = torch.pi * (lambda *s: torch.randn(*s, dtype=torch.bfloat16) * 0.2)(D, N)
        B_re = (lambda *s: torch.randn(*s, dtype=torch.bfloat16) * 0.4)(D, N)
        B_im = (lambda *s: torch.randn(*s, dtype=torch.bfloat16) * 0.4)(D, N)
        C_re = (lambda *s: torch.randn(*s, dtype=torch.bfloat16) * 0.4)(D, N)
        C_im = (lambda *s: torch.randn(*s, dtype=torch.bfloat16) * 0.4)(D, N)
        log_dt = (lambda *s: torch.randn(*s, dtype=torch.bfloat16) * 0.2)(D) * 0.5 - 4.0
        Dskip = (lambda *s: torch.randn(*s, dtype=torch.bfloat16) * 0.4)(D)
        return {
            "u": _w((lambda *s: torch.randn(*s, dtype=torch.bfloat16) * 0.05)(B, L, D)),
            "A_re": _w(A_re),
            "A_im": _w(A_im),
            "B_re": _w(B_re),
            "B_im": _w(B_im),
            "C_re": _w(C_re),
            "C_im": _w(C_im),
            "log_dt": _w(log_dt),
            "Dskip": _w(Dskip),
        }
    _A_in = _A_setup()

    def _B_setup():
        B, S, D, H = 1, 2048, 640, 32
        Dh = D // H
        return {
            "x": _w((lambda *s: torch.randn(*s, dtype=torch.bfloat16) * 0.4)(B, S, D)),
            "wqkv": _w((lambda *s: torch.randn(*s, dtype=torch.bfloat16) * 0.4)(D, 3 * D)),
            "w_phi": _w((lambda *s: torch.randn(*s, dtype=torch.bfloat16) * 0.4)(H, Dh, Dh) * (Dh ** -0.5)),
            "wo": _w((lambda *s: torch.randn(*s, dtype=torch.bfloat16) * 0.05)(D, D)),
        }
    _B_in = _B_setup()

    return {
        "A_u": _A_in["u"],
        "A_A_re": _A_in["A_re"],
        "A_A_im": _A_in["A_im"],
        "A_B_re": _A_in["B_re"],
        "A_B_im": _A_in["B_im"],
        "A_C_re": _A_in["C_re"],
        "A_C_im": _A_in["C_im"],
        "A_log_dt": _A_in["log_dt"],
        "A_Dskip": _A_in["Dskip"],
        "B_x": _B_in["x"],
        "B_wqkv": _B_in["wqkv"],
        "B_w_phi": _B_in["w_phi"],
        "B_wo": _B_in["wo"],
    }

def reference(A_u, A_A_re, A_A_im, A_B_re, A_B_im, A_C_re, A_C_im, A_log_dt, A_Dskip, B_x, B_wqkv, B_w_phi, B_wo):
    def _A_ref(u, A_re, A_im, B_re, B_im, C_re, C_im, log_dt, Dskip):
        Bsz, L, D = u.shape
        dt = torch.exp(log_dt)[:, None]
        A = A_re + 1j * A_im
        Bc = B_re + 1j * B_im
        Cc = C_re + 1j * C_im
        logdA = dt * A
        dB = dt * Bc
        l = (lambda x, dt: x.to(dt))(torch.arange(L), A_re.dtype)
        P = torch.exp(logdA[:, :, None] * l[None, None, :])
        K = torch.einsum("dn,dnl,dn->dl", Cc, P, dB).real
        L2 = 2 * L
        Kp = _pad(K, ((0, 0), (0, L)))
        up = (lambda x, *d: x.permute(*d))(u, 0, 2, 1)
        up = _pad(up, ((0, 0), (0, 0), (0, L)))
        Kf = (lambda x, axes=(-2,-1): torch.fft.rfft2(x.float(), dim=axes))(Kp[:, None, :], axes=(-2, -1))
        Uf = (lambda x, axes=(-2,-1): torch.fft.rfft2(x.float(), dim=axes))(up[:, :, None, :], axes=(-2, -1))
        Yf = Uf * Kf[None, :, :, :]
        y = (lambda x, s, axes=(-2,-1): torch.fft.irfft2(x, s=s, dim=axes).to(torch.bfloat16))(Yf, (1, L2), axes=(-2, -1))[:, :, 0, :L]
        y = (lambda x, *d: x.permute(*d))(y, 0, 2, 1) + u * Dskip[None, None, :]
        return y

    def _B_ref(x, wqkv, w_phi, wo):
        B, S, D = x.shape
        H = 32; Dh = D // H
        qkv = (lambda x, *d: x.permute(*d))((x @ wqkv).reshape(B, S, 3, H, Dh), 2, 0, 3, 1, 4)
        q, k, v = qkv[0], qkv[1], qkv[2]
        pq = torch.einsum("bhsd,hdf->bhsf", q, w_phi)
        pk = torch.einsum("bhsd,hdf->bhsf", k, w_phi)
        phi_q = torch.concatenate([_softmax(pq, axis=-1), _softmax(-pq, axis=-1)], axis=-1)
        phi_k = torch.concatenate([_softmax(pk, axis=-1), _softmax(-pk, axis=-1)], axis=-1)
        kv = torch.einsum("bhsf,bhsd->bhfd", phi_k, v)
        z  = torch.einsum("bhsf->bhf", phi_k)
        num = torch.einsum("bhqf,bhfd->bhqd", phi_q, kv)
        den = torch.einsum("bhqf,bhf->bhq", phi_q, z)[..., None] + 1e-6
        o = num / den
        return (lambda x, *d: x.permute(*d))(o, 0, 2, 1, 3).reshape(B, S, D) @ wo

    a = _A_ref(A_u, A_A_re, A_A_im, A_B_re, A_B_im, A_C_re, A_C_im, A_log_dt, A_Dskip)
    b = _B_ref(B_x, B_wqkv, B_w_phi, B_wo)
    return torch.concatenate([a, b], axis=-1)
```

```cpp
#include <hip/hip_runtime.h>
#include <stdint.h>

typedef unsigned short u16; typedef unsigned u32; typedef unsigned long long u64;
typedef __attribute__((ext_vector_type(4))) short s16x4;
typedef __attribute__((ext_vector_type(8))) short s16x8;
typedef __attribute__((ext_vector_type(8))) _Float16 f16x8;
typedef __attribute__((ext_vector_type(8))) __bf16 bf16x8;
typedef __attribute__((ext_vector_type(2))) __bf16 bf16x2;
typedef __attribute__((ext_vector_type(2))) _Float16 f16x2;
typedef __attribute__((ext_vector_type(2))) float f32x2;
typedef __attribute__((ext_vector_type(4))) float f32x4;
typedef __attribute__((ext_vector_type(16))) float f32x16;
typedef __attribute__((ext_vector_type(2))) unsigned u32x2;
typedef __attribute__((ext_vector_type(4))) unsigned u32x4;
#define LAS __attribute__((address_space(3)))
#define GAS __attribute__((address_space(1)))
#define DI __device__ __forceinline__
typedef LAS char* lptr;

#define ST(i) do{}while(0)

DI u32 pk_bf16(float a, float b){ f32x2 v={a,b}; return __builtin_bit_cast(u32,__builtin_convertvector(v,bf16x2)); }
DI u32 pk_f16(f32x2 v){ return __builtin_bit_cast(u32,__builtin_convertvector(v,f16x2)); }
DI float bflo(u32 x){ return __builtin_bit_cast(float,x<<16); }
DI float bfhi(u32 x){ return __builtin_bit_cast(float,x&0xffff0000u); }
DI float rbf(float a){ return bflo(pk_bf16(a,a)); }
template<class T> DI T lds_ld(lptr p){ return *(LAS T*)p; }
template<class T> DI void lds_st(lptr p, T v){ *(LAS T*)p=v; }
DI u32x2 lds_tr(lptr p){ return __builtin_bit_cast(u32x2,__builtin_amdgcn_ds_read_tr16_b64_v4i16((LAS s16x4*)p)); }
DI f32x4 mfma16(u32x4 a,u32x4 b,f32x4 c){ return __builtin_amdgcn_mfma_f32_16x16x32_bf16(__builtin_bit_cast(bf16x8,a),__builtin_bit_cast(bf16x8,b),c,0,0,0); }
DI u32x4 cat(u32x2 a,u32x2 b){ u32x4 r={a.x,a.y,b.x,b.y}; return r; }
DI u32 p_ll(u32 a,u32 b){ return __builtin_amdgcn_perm(b,a,0x05040100u); }
DI u32 p_lh(u32 a,u32 b){ return __builtin_amdgcn_perm(b,a,0x07060100u); }
DI u32 p_hl(u32 a,u32 b){ return __builtin_amdgcn_perm(b,a,0x05040302u); }
DI u32 p_hh(u32 a,u32 b){ return __builtin_amdgcn_perm(b,a,0x07060302u); }
DI void bar(){ asm volatile("s_waitcnt lgkmcnt(0)":::"memory"); __builtin_amdgcn_s_barrier(); asm volatile("":::"memory"); }

constexpr int A_UB=4576, A_KA=9120, A_KB=4224  , A_SLOT=17824;
constexpr int A_KS=8*A_SLOT, A_KSZ=1296, A_XP=A_KS+8*A_KSZ, A_XPZ=2688;
static_assert(A_XP+4*A_XPZ<=163840,"lds");

DI f32x2 cmul(f32x2 a, f32x2 b){ f32x2 sw={-b.y,b.x}; return __builtin_elementwise_fma(a.yy,sw,a.xx*b); }
DI f32x2 cexp_e(float re2, float rev, float e){
  float m=__builtin_amdgcn_exp2f(re2*e); float a=__builtin_amdgcn_fractf(rev*e);
  f32x2 r={m*__builtin_amdgcn_cosf(a), m*__builtin_amdgcn_sinf(a)}; return r; }

struct SsmP { u32 are,aim,bre,bim,cre,cim; float dt; };
DI SsmP load_ssm(const u16* const* pp, const u16* logdt, int ch, int lane){
  SsmP p; int o=ch*64+lane;
  p.are=((const u32*)pp[0])[o]; p.aim=((const u32*)pp[1])[o]; p.bre=((const u32*)pp[2])[o];
  p.bim=((const u32*)pp[3])[o]; p.cre=((const u32*)pp[4])[o]; p.cim=((const u32*)pp[5])[o];
  p.dt=__builtin_bit_cast(float,(u32)logdt[ch]<<16);
  return p; }
DI f32x2 ssm_w(const SsmP& p, int half){
  f32x2 b={half?bfhi(p.bre):bflo(p.bre), half?bfhi(p.bim):bflo(p.bim)};
  f32x2 c={half?bfhi(p.cre):bflo(p.cre), half?bfhi(p.cim):bflo(p.cim)};
  return cmul(c,b*p.dt); }
DI void ssm_scale(const SsmP& p, float& s, float& inv){
  f32x2 w0=ssm_w(p,0), w1=ssm_w(p,1);
  float m=fmaxf(fmaxf(fabsf(w0.x),fabsf(w0.y)),fmaxf(fabsf(w1.x),fabsf(w1.y)));
  #pragma unroll
  for(int o=1;o<64;o<<=1) m=fmaxf(m,__shfl_xor(m,o));
  int e=__builtin_amdgcn_frexp_expf(m); e=max(-100,min(100,e));
  s=__builtin_amdgcn_ldexpf(1.0f,-e); inv=__builtin_amdgcn_ldexpf(1.0f,e); }

DI void kgen_half(const SsmP& p, int half, float s, lptr tab, int lane, f32x16& acc){
  const float are=(half?bfhi(p.are):bflo(p.are))*p.dt, aim=(half?bfhi(p.aim):bflo(p.aim))*p.dt;
  const float re2=are*1.4426950408889634f, rev=aim*0.15915494309189535f;
  lptr wr=tab+lane*4; lptr rd=tab+(lane&31)*272+(lane>>5)*16;
  {
    f32x2 e8=cexp_e(re2,rev,8.f), e16=cmul(e8,e8);
    f32x2 a={1.f,0.f}, b=e8;
    #pragma unroll
    for(int j=0;j<32;j+=2){ lds_st<u32>(wr+j*272,pk_f16(a)); lds_st<u32>(wr+(j+1)*272,pk_f16(b)); if(j<30){ a=cmul(a,e16); b=cmul(b,e16);} }
  }
  u32x4 bf[8];
  #pragma unroll
  for(int k=0;k<8;++k) bf[k]=lds_ld<u32x4>(rd+k*32);
  {
    f32x2 w=ssm_w(p,half)*s; w.y=-w.y;
    f32x2 e1=cexp_e(re2,-rev,1.f), g=cexp_e(re2,-rev,256.f);
    f32x2 g2=cmul(g,g), g3=cmul(g2,g), g4=cmul(g2,g2);
    f32x2 x[8]; x[0]=w;
    #pragma unroll
    for(int i=1;i<8;++i) x[i]=cmul(x[i-1],e1);
    #pragma unroll
    for(int i=0;i<8;++i) lds_st<u32>(wr+i*272,pk_f16(x[i]));
    #pragma unroll
    for(int i=0;i<4;++i) lds_st<u32>(wr+(8+i)*272,pk_f16(cmul(x[2*i],g)));
    lds_st<u32>(wr+12*272,pk_f16(cmul(x[0],g2))); lds_st<u32>(wr+13*272,pk_f16(cmul(x[4],g2)));
    lds_st<u32>(wr+14*272,pk_f16(cmul(x[0],g3))); lds_st<u32>(wr+15*272,pk_f16(cmul(x[4],g3)));
    f32x2 y=cmul(w,g4);
    #pragma unroll
    for(int i=0;i<5;++i){ lds_st<u32>(wr+(16+i)*272,pk_f16(y)); if(i<4) y=cmul(y,g); }
  }
  #pragma unroll
  for(int k=0;k<8;++k){ u32x4 af=lds_ld<u32x4>(rd+k*32);
    acc=__builtin_amdgcn_mfma_f32_32x32x16_f16(__builtin_bit_cast(f16x8,af),__builtin_bit_cast(f16x8,bf[k]),acc,0,0,0); }
}
DI void ks_write(const float* v, lptr ks, int lane){
  const int j=lane&31, h=lane>>5;
  u32x2 a={pk_bf16(v[0],v[1]),pk_bf16(v[2],v[3])};
  lds_st<u32x2>(ks+(8*j+4*h)*2,a);
  u32x2 b={pk_bf16(v[4],v[5]),pk_bf16(v[6],v[7])};
  u32 c0=pk_bf16(v[8],v[9]), c1=pk_bf16(v[10],v[11]);
  if(h==0){ lds_st<u32x2>(ks+(256+4*j)*2,b);
    lds_st<u16>(ks+(512+j)*2,(u16)c0); lds_st<u16>(ks+(544+j)*2,(u16)(c0>>16));
    lds_st<u16>(ks+(576+j)*2,(u16)c1); lds_st<u16>(ks+(608+j)*2,(u16)(c1>>16));
  } else { lds_st<u32>(ks+(384+2*j)*2,b.x); lds_st<u32>(ks+(448+2*j)*2,b.y); if(j==0) lds_st<u16>(ks+640*2,(u16)c0); }
}
DI void k_expand(lptr ks, lptr ka, int lane){
  lptr kb=ka+A_KB;
  { int M=lane; u32x4 A,B;
    if(M<32){ u32x4 v=lds_ld<u32x4>(ks+M*16); u32 pv=(M>0)?lds_ld<u16>(ks+M*16-2):0u;
      B.x=p_hl(v.w,v.w); B.y=p_hl(v.z,v.z); B.z=p_hl(v.y,v.y); B.w=p_hl(v.x,v.x);
      A.x=p_lh(v.w,v.z); A.y=p_lh(v.z,v.y); A.z=p_lh(v.y,v.x); A.w=p_ll(v.x,pv);
    } else { u32x2 v=lds_ld<u32x2>(ks+(128+4*M)*2); u32 v4=lds_ld<u16>(ks+(132+4*M)*2); u32 pv=lds_ld<u16>(ks+((M==32)?255:(128+4*M))*2);
      B.x=p_lh(v4,v.y); B.y=p_ll(v.y,v.y); B.z=p_lh(v.y,v.x); B.w=p_ll(v.x,v.x);
      A.x=p_hl(v.y,v.y); A.y=B.y; A.z=p_hl(v.x,v.x); A.w=p_ll(v.x,pv);
    }
    lds_st<u32x4>(ka+(255-M)*16,A); lds_st<u32x4>(kb+(255-M)*16,B); }
  { int M=lane+64; u32 ab=lds_ld<u32>(ks+(256+2*M)*2), c=lds_ld<u16>(ks+(258+2*M)*2); u32x4 A,B;
    B.x=p_ll(c,c); B.y=p_hh(ab,ab); B.z=p_hl(ab,ab); B.w=p_ll(ab,ab);
    A.x=p_lh(c,ab); A.y=B.y; A.z=B.w; A.w=B.w;
    lds_st<u32x4>(ka+(255-M)*16,A); lds_st<u32x4>(kb+(255-M)*16,B); }
  #pragma unroll
  for(int i=2;i<4;++i){ int M=lane+64*i; u32 a=lds_ld<u16>(ks+(384+M)*2), b=lds_ld<u16>(ks+(385+M)*2); u32 c=(M&1)?b:a; u32x4 A,B;
    B.x=p_ll(b,b); B.y=p_ll(b,c); B.z=p_ll(a,a); B.w=B.z;
    A.x=B.x; A.y=p_ll(c,a); A.z=B.z; A.w=B.z;
    lds_st<u32x4>(ka+(255-M)*16,A); lds_st<u32x4>(kb+(255-M)*16,B); }
  if(lane<8){ u32x4 z={0,0,0,0}; lds_st<u32x4>(((lane&4)?kb:ka)+(256+(lane&3))*16,z); }
}
template<int NS> DI void conv_steps(lptr ua, lptr ka, int s0, int lane, f32x4* acc){
  const int a=lane&15, g=lane>>4;
  lptr up=ua+((a&1)?0:A_UB)+(32*(a>>1)+8*g+32*s0)*2;
  lptr kp=ka+((a&1)?A_KB:0)+(28-4*(a>>1)+8*g+32*s0)*2;
  #pragma unroll
  for(int s=0;s<NS;++s){
    u32x4 A[8],B[8];
    #pragma unroll
    for(int q=0;q<8;++q){ B[q]=lds_ld<u32x4>(up+(256*q+32*s)*2);
      A[q]=cat(lds_ld<u32x2>(kp+(1792-256*q+32*s)*2),lds_ld<u32x2>(kp+(1792-256*q+32*s)*2+8)); }
    #pragma unroll
    for(int q=0;q<8;++q)
      #pragma unroll
      for(int c=q;c<8;++c) acc[c]=mfma16(A[q],B[c-q],acc[c]);
  }
}
DI u32 fin2(float y0,float y1,u32 u,float dsk){
  u32 y=pk_bf16(y0,y1), s=pk_bf16(bflo(u)*dsk,bfhi(u)*dsk);
  return pk_bf16(bflo(y)+bflo(s),bfhi(y)+bfhi(s)); }

DI void role_a(int j, lptr L, const u16* u, const u16* const* pp, const u16* logdt, const u16* dskip, u16* out, void* ws){
  const int tid=threadIdx.x, lane=tid&63, w=__builtin_amdgcn_readfirstlane(tid>>6);
  const int ch0=10*j;
  SsmP p=load_ssm(pp,logdt,ch0+w,lane);
  SsmP px; if(w<4) px=load_ssm(pp,logdt,ch0+8+(w>>1),lane);
  const float dsk=__builtin_bit_cast(float,(u32)dskip[ch0+w]<<16);
  const u32 dx=*(const u32*)(dskip+ch0+8);
  u32x4 um[4]; u32 ux[4];
  { const char* up=(const char*)u+(size_t)tid*4*1280+20*j;
    #pragma unroll
    for(int r=0;r<4;++r){ typedef u32x4 __attribute__((aligned(4))) u32x4u; um[r]=*(const u32x4u*)(up+r*1280); ux[r]=*(const u32*)(up+r*1280+16); } }
  lptr slot=L+w*A_SLOT;
  if(lane<57){ u32x4 z={0,0,0,0}; if(lane<28) lds_st<u32x4>(slot+lane*16,z); else lds_st<u32x4>(slot+A_UB+(lane-28)*16,z); }
  p.dt=rbf(expf(p.dt));
  ST(1);
  if(w<4){ px.dt=rbf(expf(px.dt)); float s,inv; ssm_scale(px,s,inv); f32x16 acc={};
    kgen_half(px,w&1,s,slot+A_KA,lane,acc);
    lptr xp=L+A_XP+w*A_XPZ+(lane&31)*4; const int h=lane>>5;
    #pragma unroll
    for(int r=0;r<8;++r) lds_st<float>(xp+((r&3)+8*(r>>2)+4*h)*128,acc[r]*inv);
    if(h==0){
      #pragma unroll
      for(int r=8;r<12;++r) lds_st<float>(xp+(r+8)*128,acc[r]*inv);
    } else lds_st<float>(xp+20*128,acc[8]*inv);
  }
  { float s,inv; ssm_scale(p,s,inv); f32x16 acc={};
    kgen_half(p,0,s,slot+A_KA,lane,acc); kgen_half(p,1,s,slot+A_KA,lane,acc);
    float v[12];
    #pragma unroll
    for(int r=0;r<12;++r) v[r]=acc[r]*inv;
    ks_write(v,L+A_KS+w*A_KSZ,lane);
    k_expand(L+A_KS+w*A_KSZ,slot+A_KA,lane); }
  ST(2);
  bar();
  {
    lptr d=L+(224+4*tid)*2;
    #pragma unroll
    for(int c=0;c<4;++c){
      u32x2 e={p_ll(um[0][c],um[1][c]),p_ll(um[2][c],um[3][c])}, o={p_hh(um[0][c],um[1][c]),p_hh(um[2][c],um[3][c])};
      lds_st<u32x2>(d+(2*c)*A_SLOT,e); lds_st<u32x2>(d+(2*c+1)*A_SLOT,o);
      lds_st<u32>(d+(2*c)*A_SLOT+A_UB+4,e.x); lds_st<u32>(d+(2*c+1)*A_SLOT+A_UB+4,o.x);
      if(tid<511){ lds_st<u32>(d+(2*c)*A_SLOT+A_UB+8,e.y); lds_st<u32>(d+(2*c+1)*A_SLOT+A_UB+8,o.y); }
    } }
  bar();
  ST(3);
  if(w<2){
    lptr xp=L+A_XP+2*w*A_XPZ+(lane&31)*4; const int h=lane>>5; float v[12];
    #pragma unroll
    for(int r=0;r<12;++r){ int row=min((r&3)+8*(r>>2)+4*h,20); v[r]=lds_ld<float>(xp+row*128)+lds_ld<float>(xp+A_XPZ+row*128); }
    ks_write(v,L+A_KS+w*A_KSZ,lane); }
  const int a=lane&15, g=lane>>4;
  const int tl=32*(a>>1)+8*g+2*(a&1);
  { f32x4 acc[8];
    #pragma unroll
    for(int c=0;c<8;++c) acc[c]=f32x4{0,0,0,0};
    conv_steps<8>(slot,slot+A_KA,0,lane,acc);
    ST(4);
    #pragma unroll
    for(int c=0;c<8;++c){
      u32 u0=lds_ld<u32>(slot+(224+256*c+tl)*2), u1=lds_ld<u32>(slot+(228+256*c+tl)*2);
      lds_st<u32>(slot+A_KA+(256*c+tl)*2,fin2(acc[c][0],acc[c][1],u0,dsk));
      lds_st<u32>(slot+A_KA+(256*c+tl+4)*2,fin2(acc[c][2],acc[c][3],u1,dsk)); } }
  bar();
  ST(5);
  {
    lptr d=L+(224+4*tid)*2;
    u32x2 e={p_ll(ux[0],ux[1]),p_ll(ux[2],ux[3])}, o={p_hh(ux[0],ux[1]),p_hh(ux[2],ux[3])};
    lds_st<u32x2>(d,e); lds_st<u32x2>(d+A_SLOT,o);
    lds_st<u32>(d+A_UB+4,e.x); lds_st<u32>(d+A_SLOT+A_UB+4,o.x);
    if(tid<511){ lds_st<u32>(d+A_UB+8,e.y); lds_st<u32>(d+A_SLOT+A_UB+8,o.y); }
    if(w<2) k_expand(L+A_KS+w*A_KSZ,L+(2+w)*A_SLOT,lane);
    lptr yp=L+((lane&15)>>2)*A_SLOT+A_KA+(256*w+16*g+4*(lane&3))*2;
    char* op=(char*)out+(size_t)(256*w+lane)*2560+20*j;
    #pragma unroll
    for(int i=0;i<4;++i){ typedef u32x4 __attribute__((aligned(4))) u32x4u;
      *(u32x4u*)(op+(size_t)i*64*2560)=cat(lds_tr(yp+i*128),lds_tr(yp+4*A_SLOT+i*128)); } }
  bar();
  ST(6);
  { f32x4 acc[8];
    #pragma unroll
    for(int c=0;c<8;++c) acc[c]=f32x4{0,0,0,0};
    conv_steps<2>(L+(w>>2)*A_SLOT,L+(2+(w>>2))*A_SLOT,2*(w&3),lane,acc);
    #pragma unroll
    for(int c=0;c<8;++c) lds_st<f32x4>(slot+A_KA+c*1024+lane*16,acc[c]); }
  bar();
  { f32x4 y[2]; u32 r[2][2];
    #pragma unroll
    for(int e=0;e<2;++e){ lptr q=L+4*e*A_SLOT+A_KA+tid*16;
      y[e]=(lds_ld<f32x4>(q)+lds_ld<f32x4>(q+A_SLOT))+(lds_ld<f32x4>(q+2*A_SLOT)+lds_ld<f32x4>(q+3*A_SLOT));
      u32 u0=lds_ld<u32>(L+e*A_SLOT+(224+256*w+tl)*2), u1=lds_ld<u32>(L+e*A_SLOT+(228+256*w+tl)*2);
      float d=e?bfhi(dx):bflo(dx);
      r[e][0]=fin2(y[e][0],y[e][1],u0,d); r[e][1]=fin2(y[e][2],y[e][3],u1,d); }
    char* op=(char*)out+(size_t)(256*w+tl)*2560+20*j+16;
    *(u32*)(op)=p_ll(r[0][0],r[1][0]); *(u32*)(op+2560)=p_hh(r[0][0],r[1][0]);
    *(u32*)(op+4*2560)=p_ll(r[0][1],r[1][1]); *(u32*)(op+5*2560)=p_hh(r[0][1],r[1][1]); }
  ST(7);
}

constexpr int B_ST=36864, B_WP=4*B_ST, B_Z=B_WP+6400;
constexpr u32 WS_FLAG=256, WS_P1=4096, WS_R=WS_P1+8*14*16*960, WS_O=WS_R+57344, WS_END=WS_O+128*20*1024;
constexpr u32 SENT=0xFFFFFFFFu;
constexpr int SPIN_MAX=1<<20;
typedef __amdgpu_buffer_rsrc_t rsrc_t;
template<int N> DI void waitv(){ asm volatile("s_waitcnt vmcnt(%0)"::"i"(N):"memory"); }
DI void dma16(const void* sb, u32 voff, lptr l){ u32 keep;
  asm volatile("s_mov_b32 %0, m0\n\ts_mov_b32 m0, %3\n\ts_nop 0\n\tglobal_load_lds_dwordx4 %1, %2\n\ts_mov_b32 m0, %0":"=&s"(keep):"v"(voff),"s"(sb),"s"((u32)(uintptr_t)l):"memory"); }
DI u32x4 ld_sc1(rsrc_t rs,u32 off){ return __builtin_amdgcn_raw_buffer_load_b128(rs,off,0,16); }
DI void st_sc1(rsrc_t rs,u32 off,u32x4 v){ __builtin_amdgcn_raw_buffer_store_b128(v,rs,off,0,16); }
DI u32 umax4(u32x4 v){ return max(max(v.x,v.y),max(v.z,v.w)); }
DI u32x4 canon(u32x4 v){ u32x4 r={min(v.x,SENT-1),min(v.y,SENT-1),min(v.z,SENT-1),min(v.w,SENT-1)}; return r; }

DI void softmax20(const f32x4* z, u32* o){
  float m=fmaxf(fmaxf(z[0][0],z[0][1]),fmaxf(z[0][2],z[0][3]));
  #pragma unroll
  for(int i=1;i<5;++i){ m=fmaxf(fmaxf(m,z[i][0]),z[i][1]); m=fmaxf(fmaxf(m,z[i][2]),z[i][3]); }
  const float c=1.4426950408889634f, mc=-m*c;
  f32x2 e[10]; f32x2 s={0.f,0.f};
  #pragma unroll
  for(int i=0;i<10;++i){ f32x2 t={z[i>>1][(i&1)*2],z[i>>1][(i&1)*2+1]}; t=__builtin_elementwise_fma(t,f32x2{c,c},f32x2{mc,mc});
    e[i]=f32x2{__builtin_amdgcn_exp2f(t.x),__builtin_amdgcn_exp2f(t.y)}; s+=e[i]; }
  const float r=__builtin_amdgcn_rcpf(s.x+s.y);
  #pragma unroll
  for(int i=0;i<10;++i){ f32x2 t=e[i]*r; o[i]=pk_bf16(t.x,t.y); }
}
DI u32x4 pack2(f32x4 a,f32x4 b){ u32x4 r={pk_bf16(a[0],a[1]),pk_bf16(a[2],a[3]),pk_bf16(b[0],b[1]),pk_bf16(b[2],b[3])}; return r; }

template<int C0> DI void features(lptr L,int hl,int lane,const u32x4 (*b)[2],u32 (*ph)[10]){
  const int q=(lane&15)>>2, p=lane&3, gk=lane>>4;
  const u32 sg=(lane&8)?0x80008000u:0u;
  constexpr int IA=(C0==0)?0:2;
  f32x4 z[2][5];
  #pragma unroll
  for(int i=0;i<5;++i){ z[0][i]=f32x4{0,0,0,0}; z[1][i]=f32x4{0,0,0,0}; }
  #pragma unroll
  for(int s=0;s<2;++s){
    lptr ad[2];
    #pragma unroll
    for(int t=0;t<2;++t){ int d=16*(IA+2*s+t)+4*gk-C0-20*(p&1); bool ok=(d>=0)&&(d<20)&&(IA+2*s+t<5);
      ad[t]=ok?(L+B_WP+(hl+(p&1))*800+(d+q)*40):(L+B_Z); }
    #pragma unroll
    for(int i=0;i<5;++i){ u32x4 a=cat(lds_tr(ad[0]+8*i),lds_tr(ad[1]+8*i)); a^=sg;
      z[0][i]=mfma16(a,b[0][s],z[0][i]); z[1][i]=mfma16(a,b[1][s],z[1][i]); }
  }
  softmax20(z[0],ph[0]); softmax20(z[1],ph[1]);
}
DI bool flags_ready(const void* ws,u64 E,int lane){
  const GAS u64* f=(const GAS u64*)((const char*)ws+WS_FLAG);
  for(int it=0;it<SPIN_MAX;++it){
    u64 a=__hip_atomic_load(f+lane,__ATOMIC_RELAXED,__HIP_MEMORY_SCOPE_AGENT), b=__hip_atomic_load(f+64+lane,__ATOMIC_RELAXED,__HIP_MEMORY_SCOPE_AGENT), c=__hip_atomic_load(f+128+lane,__ATOMIC_RELAXED,__HIP_MEMORY_SCOPE_AGENT);
    if(__all(a==E&&b==E&&c==E)) return true;
    __builtin_amdgcn_s_sleep(2); }
  return false; }

DI void wo_stage(lptr L,int off,const u16* wo,int ct,int k0,int nk,int w,int lane){
  const int r=lane>>4, f=(r|(((w>>1)&1)<<2))<<1;
  const char* g=(const char*)wo+(size_t)(k0+4*w)*1280+ct*256; const u32 vo=r*1280+(((lane&15)^f)<<4);
  #pragma unroll
  for(int i=0;i<nk/32;++i) dma16(g+(size_t)i*32*1280,vo,L+off+(w+8*i)*1024);
}
DI void wo_frags(lptr L,int off,int nks,int w,int lane,u32x4* wf){
  const int g=lane>>4,q=(lane&15)>>2,p=lane&3, f=(q|((g&1)<<2))<<1;
  lptr a=L+off+(8*g+q)*256+(((2*w+(p>>1))^f)<<4)+8*(p&1);
  #pragma unroll
  for(int s=0;s<8;++s) if(s<nks) wf[s]=cat(lds_tr(a+s*8192),lds_tr(a+s*8192+1024));
}

template<bool ISQ> DI void role_b(int X,int li,lptr L,const u16* x,const u16* wqkv,const u16* wphi,const u16* wo,u16* out,void* ws){
  const int tid=threadIdx.x, lane=tid&63, w=__builtin_amdgcn_readfirstlane(tid>>6);
  const int rt=4*(X>>1)+(ISQ?(li>>1):(li>>2));
  const int grp=ISQ?(2*(X&1)+(li&1)):(4*(X&1)+(li&3));
  const int me=ISQ?(128+X*8+li):(X*16+li);
#define ROT 5
  const int s0=ISQ?((li*ROT+1)%10):(((li>>2)+(li&3))*ROT)%10;
  const rsrc_t rs=__builtin_amdgcn_make_buffer_rsrc(ws,0,WS_END,0x00020000);
  u32 xo[2], wo_[3];
  #pragma unroll
  for(int i=0;i<2;++i){ int r=(2*w+i)*8+(lane>>3); xo[i]=(u32)(rt*128+r)*1280u+(u32)(((lane&7)^((r>>1)&7))<<4); }
  #pragma unroll
  for(int i=0;i<3;++i){ int c=(w+8*i)*64+lane, r=c/20, pos=c-r*20, sc=pos^(((r>>3)&1)<<1);
    wo_[i]=(u32)r*3840u+(ISQ?(u32)(320*grp+16*sc):(u32)((sc<10?1280:2400)+160*grp+16*sc)); }
  auto issue=[&](int s){ int ss=s+s0; if(ss>=10) ss-=10; lptr d=L+(s&3)*B_ST;
    const char* xb=(const char*)x+ss*128; const char* wb=(const char*)wqkv+(size_t)ss*64*3840;
    dma16(xb,xo[0],d+(2*w)*1024); dma16(xb,xo[1],d+(2*w+1)*1024);
    dma16(wb,wo_[0],d+16384+w*1024); dma16(wb,wo_[1],d+16384+(w+8)*1024);
    if(w<4) dma16(wb,wo_[2],d+16384+(w+16)*1024); };
  {
    constexpr int NB=ISQ?6400:3200; const int h0=ISQ?8*grp:4*grp;
    if(tid*16<NB) dma16((const char*)wphi+h0*800,tid*16,L+B_WP+w*1024);
    if(tid<4) lds_st<u32x4>(L+B_Z+tid*16,u32x4{0,0,0,0}); }
  issue(0); issue(1); issue(2);
  {
    const u32x4 sv={SENT,SENT,SENT,SENT};
    if(ISQ){
      #pragma unroll
      for(int i=0;i<5;++i) st_sc1(rs,WS_O+((8*rt+w)*20+5*grp+i)*1024+lane*16,sv);
    } else {
      #pragma unroll
      for(int i=0;i<2;++i){ int c=tid+512*i; int sl=c/60; u32 o=WS_P1+((grp*14+sl)*16+rt)*960+(c-sl*60)*16;
        if(c>=840) o=(c<870&&rt<14)?(WS_R+(grp*84+6*rt)*80+(c-840)*16):0x80000000u;
        st_sc1(rs,o,sv); } } }
  u64 E; asm volatile("s_load_dwordx2 %0, %1, 0x0\n\ts_waitcnt lgkmcnt(0)":"=s"(E):"s"(ws):"memory"); E+=1;
  constexpr int NC=ISQ?5:2;
  const int g=lane>>4, tq=w>>1, hh=w&1;
  int ab[5], bb;
  { const int q=(lane&15)>>2,p=lane&3;
    #pragma unroll
    for(int i=0;i<5;++i){ int n=2*i+(p>>1); int cid=ISQ?(10*hh+n):(5*hh+n+(n>=5?5:0)); ab[i]=16384+(8*g+q)*320+((cid^((g&1)<<1))<<4)+8*(p&1); }
    bb=(32*tq+(lane&15))*128+((g^((lane&15)>>1))<<4); }
  f32x4 acc[5][2];
  #pragma unroll
  for(int i=0;i<5;++i){ acc[i][0]=f32x4{0,0,0,0}; acc[i][1]=f32x4{0,0,0,0}; }
  ST(1);
  #pragma unroll
  for(int s=0;s<10;++s){
    if(s<3){ if(w<4) waitv<10+NC>(); else waitv<8+NC>(); }
    else if(s<8){ if(w<4) waitv<10>(); else waitv<8>(); }
    else if(s==8){ if(w<4) waitv<5>(); else waitv<4>(); }
    else waitv<0>();
    bar();
    if(s==0) ST(2);
    if(s+3<10) issue(s+3);
    if(s==3 && tid==0) __hip_atomic_store((GAS u64*)((char*)ws+WS_FLAG)+me,E,__ATOMIC_RELAXED,__HIP_MEMORY_SCOPE_AGENT);
    lptr d=L+(s&3)*B_ST;
    #pragma unroll
    for(int ks=0;ks<2;++ks){
      u32x4 b0=lds_ld<u32x4>(d+(bb^(ks*64))), b1=lds_ld<u32x4>(d+(bb^(ks*64))+2048);
      #pragma unroll
      for(int i=0;i<5;++i){ u32x4 a=cat(lds_tr(d+ab[i]+ks*10240),lds_tr(d+ab[i]+ks*10240+1280));
        acc[i][0]=mfma16(a,b0,acc[i][0]); acc[i][1]=mfma16(a,b1,acc[i][1]); } }
  }
  ST(3);
  const int T=ISQ?16+li:li; const bool doout=T<20; const int tt64=4*X+T/5, ct=T%5;
  u32x4 wf[20];
  if(!ISQ){
    u32 ph[2][10];
    { u32x4 b[2][2];
      #pragma unroll
      for(int t=0;t<2;++t){ b[t][0]=pack2(acc[0][t],acc[1][t]); b[t][1]=pack2(acc[2][t],acc[3][t]); }
      features<0>(L,2*hh,lane,b,ph); }
    bar();
    constexpr int VI=128*352;
    #pragma unroll
    for(int t=0;t<2;++t){ const int tok=32*tq+16*t+(lane&15);
      lptr pp=L+tok*352+(2*hh+(g&1))*80+(g>>1)*40;
      #pragma unroll
      for(int i=0;i<5;++i) lds_st<u32x2>(pp+8*i,u32x2{ph[t][2*i],ph[t][2*i+1]});
      lptr vp=L+VI+tok*288+2*hh*64;
      #pragma unroll
      for(int i=2;i<5;++i){ int dv=16*i+4*g-40; u32x2 v={pk_bf16(acc[i][t][0],acc[i][t][1]),pk_bf16(acc[i][t][2],acc[i][t][3])};
        int o=(dv>=20)?(64+2*(dv-20)):2*dv;
        if(i==2&&g<2){ v=u32x2{0x3F80u,0u}; o=64*g+40; }
        lds_st<u32x2>(vp+o,v); } }
    bar();
    ST(4);
    { const int hd=w>>1, dt=w&1, q=(lane&15)>>2, p=lane&3; const int tk=16*(g>>1)+4*(g&1)+q;
      lptr pa=L+tk*352+hd*80+8*p, vb=L+VI+tk*288+hd*64+dt*32+8*p;
      f32x4 c[3]={f32x4{0,0,0,0},f32x4{0,0,0,0},f32x4{0,0,0,0}};
      #pragma unroll
      for(int ks=0;ks<4;++ks){ u32x4 b=cat(lds_tr(vb+ks*32*288),lds_tr(vb+(ks*32+8)*288));
        #pragma unroll
        for(int i=0;i<3;++i){ u32x4 a=cat(lds_tr(pa+ks*32*352+32*i),lds_tr(pa+(ks*32+8)*352+32*i)); c[i]=mfma16(a,b,c[i]); } }
      const int d=16*dt+(lane&15), R=hd*21+d, sl=R/6;
      const u32 o=WS_P1+((grp*14+sl)*16+rt)*960+(R-6*sl)*160+16*g;
      #pragma unroll
      for(int i=0;i<3;++i) st_sc1(rs,(d<21&&(i<2||g<2))?o+64*i:0x80000000u,canon(__builtin_bit_cast(u32x4,c[i]))); }
    ST(5);
    bool ok=true;
    if(w==0){ ok=flags_ready(ws,E,lane);
      if(me==0&&lane==0) __hip_atomic_store((GAS u64*)ws,E,__ATOMIC_RELAXED,__HIP_MEMORY_SCOPE_AGENT);
      if(rt<14){ const u32 o=(lane<60)?(WS_P1+(grp*14+rt)*16*960+lane*16):0x80000000u; u32x4 v[16];
        for(int it=0;it<SPIN_MAX;++it){ asm volatile("":::"memory"); u32 m=0;
          #pragma unroll
          for(int i=0;i<16;++i) v[i]=ld_sc1(rs,o+i*960);
          #pragma unroll
          for(int i=0;i<16;++i) m=max(m,umax4(v[i]));
          if(__all(m!=SENT)) break; }
        f32x4 sum=__builtin_bit_cast(f32x4,v[0]);
        #pragma unroll
        for(int i=1;i<16;++i) sum+=__builtin_bit_cast(f32x4,v[i]);
        u32x2 r={min(pk_bf16(sum[0],sum[1]),SENT-1),min(pk_bf16(sum[2],sum[3]),SENT-1)};
        __builtin_amdgcn_raw_buffer_store_b64(r,rs,(lane<60)?(WS_R+(grp*84+6*rt)*80+lane*8):0x80000000u,0,16); } }
    ST(6);
    bar();
  } else {
    u32 ph[2][2][10];
    { u32x4 b[2][2];
      #pragma unroll
      for(int t=0;t<2;++t){ b[t][0]=pack2(acc[0][t],acc[1][t]); b[t][1]=pack2(acc[2][t],acc[3][t]); }
      features<0>(L,4*hh,lane,b,ph[0]);
      #pragma unroll
      for(int t=0;t<2;++t){ b[t][0]=b[t][1]; b[t][1]=pack2(acc[4][t],acc[4][t]); }
      features<40>(L,4*hh+2,lane,b,ph[1]); }
    ST(4);
    if(w==0) flags_ready(ws,E,lane);
    bar();
    if(doout){
      wo_stage(L,0,wo,ct,0,512,w,lane); waitv<0>(); bar(); wo_frags(L,0,8,w,lane,wf); wo_frags(L,65536,8,w,lane,wf+8); bar();
      wo_stage(L,0,wo,ct,512,128,w,lane); waitv<0>(); bar(); wo_frags(L,0,4,w,lane,wf+16); bar(); }
    ST(5);
    { u32x4 v0,v1; const u32 o=WS_R+grp*13440+tid*16;
      for(int it=0;it<SPIN_MAX;++it){ asm volatile("":::"memory");
        v0=ld_sc1(rs,o); v1=ld_sc1(rs,(tid<328)?o+8192:0x80000000u);
        if(__all(max(umax4(v0),umax4(v1))!=SENT)) break; }
      lds_st<u32x4>(L+tid*16,v0); if(tid<328) lds_st<u32x4>(L+8192+tid*16,v1); }
    bar();
    ST(6);
    constexpr int OI=16384;
    #pragma unroll
    for(int ps=0;ps<2;++ps){
      const int gp=(lane&15)>>2, e=lane&3; const bool mt=((gp>>1)==(g&1));
      f32x4 n[2][3];
      #pragma unroll
      for(int i=0;i<3;++i){ int sg=4*i+e; int d=(sg<10)?(10*(gp&1)+sg):20;
        lptr a=mt?(L+((4*hh+2*ps+(gp>>1))*21+d)*80+(g>>1)*40):(L+B_Z);
        u32x4 a0=cat(lds_ld<u32x2>(a),lds_ld<u32x2>(a+8)), a1=cat(lds_ld<u32x2>(a+16),lds_ld<u32x2>(a+24)), a2=cat(lds_ld<u32x2>(a+32),u32x2{0,0});
        #pragma unroll
        for(int t=0;t<2;++t){ const u32* f=ph[ps][t];
          f32x4 c=mfma16(a0,u32x4{f[0],f[1],f[2],f[3]},f32x4{0,0,0,0}); c=mfma16(a1,u32x4{f[4],f[5],f[6],f[7]},c); n[t][i]=mfma16(a2,u32x4{f[8],f[9],0,0},c); } }
      #pragma unroll
      for(int t=0;t<2;++t){ const float den=rbf(rbf(n[t][2][2])+1e-6f), r=__builtin_amdgcn_rcpf(den);
        lptr op=L+OI+(32*tq+16*t+(lane&15))*336+((4*hh+2*ps+(g>>1))*20+10*(g&1))*2;
        #pragma unroll
        for(int i=0;i<5;++i){ u32 nb=pk_bf16(n[t][i>>1][(i&1)*2],n[t][i>>1][(i&1)*2+1]); lds_st<u32>(op+4*i,pk_bf16(bflo(nb)*r,bfhi(nb)*r)); } } }
    bar();
    #pragma unroll
    for(int i=0;i<5;++i) st_sc1(rs,WS_O+((8*rt+w)*20+5*grp+i)*1024+lane*16,canon(lds_ld<u32x4>(L+OI+(16*w+(lane&15))*336+64*i+16*g)));
    ST(7);
    bar();
  }
  if(!doout) return;
  if(!ISQ){
    wo_stage(L,0,wo,ct,0,512,w,lane); waitv<0>(); bar(); wo_frags(L,0,8,w,lane,wf); wo_frags(L,65536,8,w,lane,wf+8); bar();
    wo_stage(L,0,wo,ct,512,128,w,lane); waitv<0>(); bar(); wo_frags(L,0,4,w,lane,wf+16); bar(); }
  ST(8);
  { u32x4 v[10]; const u32 o=WS_O+tt64*81920+w*10240+lane*16;
    for(int it=0;it<SPIN_MAX;++it){ asm volatile("":::"memory"); u32 m=0;
      #pragma unroll
      for(int i=0;i<10;++i) v[i]=ld_sc1(rs,o+i*1024);
      #pragma unroll
      for(int i=0;i<10;++i) m=max(m,umax4(v[i]));
      if(__all(m!=SENT)) break; }
    #pragma unroll
    for(int i=0;i<10;++i) lds_st<u32x4>(L+w*10240+i*1024+lane*16,v[i]); }
  bar();
  ST(9);
  { f32x4 c[4]={f32x4{0,0,0,0},f32x4{0,0,0,0},f32x4{0,0,0,0},f32x4{0,0,0,0}};
    #pragma unroll
    for(int ks=0;ks<20;++ks)
      #pragma unroll
      for(int t=0;t<4;++t) c[t]=mfma16(wf[ks],lds_ld<u32x4>(L+(t*20+ks)*1024+lane*16),c[t]);
    constexpr int CI=81920;
    #pragma unroll
    for(int t=0;t<4;++t) lds_st<u32x2>(L+CI+(16*t+(lane&15))*272+w*32+8*g,u32x2{pk_bf16(c[t][0],c[t][1]),pk_bf16(c[t][2],c[t][3])});
    bar();
    #pragma unroll
    for(int i=0;i<2;++i){ const int r=32*i+(tid>>4);
      *(u32x4*)((char*)out+(size_t)(64*tt64+r)*2560+1280+ct*256+(tid&15)*16)=lds_ld<u32x4>(L+CI+r*272+(tid&15)*16); } }
  ST(10);
}

__global__ __launch_bounds__(512,2) void fused(const u16* u,const u16* are,const u16* aim,const u16* bre,const u16* bim,const u16* cre,const u16* cim,
    const u16* logdt,const u16* dskip,const u16* x,const u16* wqkv,const u16* wphi,const u16* wo,u16* out,void* ws){
  extern __shared__ __attribute__((aligned(16))) char smem[];
  lptr L=(lptr)smem;
  const int b=blockIdx.x, X=b&7, idx=b>>3;
  ST(0);
  if(idx<16) role_b<false>(X,idx,L,x,wqkv,wphi,wo,out,ws);
  else if(idx<24){
    const u16* pp[6]={are,aim,bre,bim,cre,cim}; role_a(X*8+(idx-16),L,u,pp,logdt,dskip,out,ws);
  }
  else role_b<true>(X,idx-24,L,x,wqkv,wphi,wo,out,ws);
}

extern "C" void kernel_launch(void* const* d_in, const int* in_sizes, int n_in, void* d_out, int out_size, void* d_ws, size_t ws_size, hipStream_t stream){
  (void)hipFuncSetAttribute((const void*)fused,hipFuncAttributeMaxDynamicSharedMemorySize,163840);
  fused<<<256,512,163840,stream>>>((const u16*)d_in[0],(const u16*)d_in[1],(const u16*)d_in[2],(const u16*)d_in[3],(const u16*)d_in[4],(const u16*)d_in[5],(const u16*)d_in[6],
    (const u16*)d_in[7],(const u16*)d_in[8],(const u16*)d_in[9],(const u16*)d_in[10],(const u16*)d_in[11],(const u16*)d_in[12],(u16*)d_out,d_ws);
}
```

```cpp
#include <hip/hip_runtime.h>
#include <stdint.h>

typedef unsigned short u16; typedef unsigned u32; typedef unsigned long long u64;
typedef __attribute__((ext_vector_type(4))) short s16x4;
typedef __attribute__((ext_vector_type(8))) short s16x8;
typedef __attribute__((ext_vector_type(8))) _Float16 f16x8;
typedef __attribute__((ext_vector_type(8))) __bf16 bf16x8;
typedef __attribute__((ext_vector_type(2))) __bf16 bf16x2;
typedef __attribute__((ext_vector_type(2))) _Float16 f16x2;
typedef __attribute__((ext_vector_type(2))) float f32x2;
typedef __attribute__((ext_vector_type(4))) float f32x4;
typedef __attribute__((ext_vector_type(16))) float f32x16;
typedef __attribute__((ext_vector_type(2))) unsigned u32x2;
typedef __attribute__((ext_vector_type(4))) unsigned u32x4;
#define LAS __attribute__((address_space(3)))
#define GAS __attribute__((address_space(1)))
#define DI __device__ __forceinline__
typedef LAS char* lptr;

#define ST(i) do{}while(0)

DI u32 pk_bf16(float a, float b){ f32x2 v={a,b}; return __builtin_bit_cast(u32,__builtin_convertvector(v,bf16x2)); }
DI u32 pk_f16(f32x2 v){ return __builtin_bit_cast(u32,__builtin_convertvector(v,f16x2)); }
DI float bflo(u32 x){ return __builtin_bit_cast(float,x<<16); }
DI float bfhi(u32 x){ return __builtin_bit_cast(float,x&0xffff0000u); }
DI float rbf(float a){ return bflo(pk_bf16(a,a)); }
template<class T> DI T lds_ld(lptr p){ return *(LAS T*)p; }
template<class T> DI void lds_st(lptr p, T v){ *(LAS T*)p=v; }
DI u32x2 lds_tr(lptr p){ return __builtin_bit_cast(u32x2,__builtin_amdgcn_ds_read_tr16_b64_v4i16((LAS s16x4*)p)); }
DI f32x4 mfma16(u32x4 a,u32x4 b,f32x4 c){ return __builtin_amdgcn_mfma_f32_16x16x32_bf16(__builtin_bit_cast(bf16x8,a),__builtin_bit_cast(bf16x8,b),c,0,0,0); }
DI u32x4 cat(u32x2 a,u32x2 b){ u32x4 r={a.x,a.y,b.x,b.y}; return r; }
DI u32 p_ll(u32 a,u32 b){ return __builtin_amdgcn_perm(b,a,0x05040100u); }
DI u32 p_lh(u32 a,u32 b){ return __builtin_amdgcn_perm(b,a,0x07060100u); }
DI u32 p_hl(u32 a,u32 b){ return __builtin_amdgcn_perm(b,a,0x05040302u); }
DI u32 p_hh(u32 a,u32 b){ return __builtin_amdgcn_perm(b,a,0x07060302u); }
#define SB() __builtin_amdgcn_sched_barrier(0)
DI void rbar(){ asm volatile("":::"memory"); __builtin_amdgcn_s_barrier(); asm volatile("":::"memory"); }
DI u32x2 lds_ld64(lptr p){ return *(volatile LAS u32x2*)p; }
DI float wave_max(float m){
  m=fmaxf(m,__builtin_bit_cast(float,__builtin_amdgcn_update_dpp(0,__builtin_bit_cast(int,m),0xB1,0xf,0xf,true)));
  m=fmaxf(m,__builtin_bit_cast(float,__builtin_amdgcn_update_dpp(0,__builtin_bit_cast(int,m),0x4E,0xf,0xf,true)));
  m=fmaxf(m,__builtin_bit_cast(float,__builtin_amdgcn_update_dpp(0,__builtin_bit_cast(int,m),0x141,0xf,0xf,true)));
  m=fmaxf(m,__builtin_bit_cast(float,__builtin_amdgcn_update_dpp(0,__builtin_bit_cast(int,m),0x140,0xf,0xf,true)));
  int x=__builtin_bit_cast(int,m);
  float a=__builtin_bit_cast(float,__builtin_amdgcn_readlane(x,0)), b=__builtin_bit_cast(float,__builtin_amdgcn_readlane(x,16));
  float c=__builtin_bit_cast(float,__builtin_amdgcn_readlane(x,32)), d=__builtin_bit_cast(float,__builtin_amdgcn_readlane(x,48));
  return fmaxf(fmaxf(a,b),fmaxf(c,d)); }
DI void bar(){ asm volatile("s_waitcnt lgkmcnt(0)":::"memory"); __builtin_amdgcn_s_barrier(); asm volatile("":::"memory"); }

constexpr int A_UB=4576, A_KA=9120, A_KB=4224  , A_SLOT=17824;
constexpr int A_KS=8*A_SLOT, A_KSZ=1296, A_XP=A_KS+8*A_KSZ, A_XPZ=2688;
static_assert(A_XP+4*A_XPZ<=163840,"lds");

DI f32x2 cmul(f32x2 a, f32x2 b){ f32x2 sw={-b.y,b.x}; return __builtin_elementwise_fma(a.yy,sw,a.xx*b); }
DI f32x2 cexp_e(float re2, float rev, float e){
  float m=__builtin_amdgcn_exp2f(re2*e); float a=__builtin_amdgcn_fractf(rev*e);
  f32x2 r={m*__builtin_amdgcn_cosf(a), m*__builtin_amdgcn_sinf(a)}; return r; }

struct SsmP { u32 are,aim,bre,bim,cre,cim; float dt; };
DI SsmP load_ssm(const u16* const* pp, const u16* logdt, int ch, int lane){
  SsmP p; int o=ch*64+lane;
  p.are=((const u32*)pp[0])[o]; p.aim=((const u32*)pp[1])[o]; p.bre=((const u32*)pp[2])[o];
  p.bim=((const u32*)pp[3])[o]; p.cre=((const u32*)pp[4])[o]; p.cim=((const u32*)pp[5])[o];
  p.dt=__builtin_bit_cast(float,(u32)logdt[ch]<<16);
  return p; }
DI f32x2 ssm_w(const SsmP& p, int half){
  f32x2 b={half?bfhi(p.bre):bflo(p.bre), half?bfhi(p.bim):bflo(p.bim)};
  f32x2 c={half?bfhi(p.cre):bflo(p.cre), half?bfhi(p.cim):bflo(p.cim)};
  return cmul(c,b*p.dt); }
DI void ssm_scale(const SsmP& p, float& s, float& inv){
  f32x2 w0=ssm_w(p,0), w1=ssm_w(p,1);
  float m=fmaxf(fmaxf(fabsf(w0.x),fabsf(w0.y)),fmaxf(fabsf(w1.x),fabsf(w1.y)));
  m=wave_max(m);
  int e=__builtin_amdgcn_frexp_expf(m); e=max(-100,min(100,e));
  s=__builtin_amdgcn_ldexpf(1.0f,-e); inv=__builtin_amdgcn_ldexpf(1.0f,e); }

DI void kgen_half(const SsmP& p, int half, float s, lptr tab, int lane, f32x16& acc){
  const float are=(half?bfhi(p.are):bflo(p.are))*p.dt, aim=(half?bfhi(p.aim):bflo(p.aim))*p.dt;
  const float re2=are*1.4426950408889634f, rev=aim*0.15915494309189535f;
  lptr wr=tab+lane*4; lptr rd=tab+(lane&31)*272+(lane>>5)*16;
  {
    f32x2 e8=cexp_e(re2,rev,8.f), e16=cmul(e8,e8);
    f32x2 a={1.f,0.f}, b=e8;
    #pragma unroll
    for(int j=0;j<32;j+=2){ lds_st<u32>(wr+j*272,pk_f16(a)); lds_st<u32>(wr+(j+1)*272,pk_f16(b)); if(j<30){ a=cmul(a,e16); b=cmul(b,e16);} }
  }
  u32x4 bf[8];
  #pragma unroll
  for(int k=0;k<8;++k) bf[k]=lds_ld<u32x4>(rd+k*32);
  {
    f32x2 w=ssm_w(p,half)*s; w.y=-w.y;
    f32x2 e1=cexp_e(re2,-rev,1.f), g=cexp_e(re2,-rev,256.f);
    f32x2 g2=cmul(g,g), g3=cmul(g2,g), g4=cmul(g2,g2);
    f32x2 x[8]; x[0]=w;
    #pragma unroll
    for(int i=1;i<8;++i) x[i]=cmul(x[i-1],e1);
    #pragma unroll
    for(int i=0;i<8;++i) lds_st<u32>(wr+i*272,pk_f16(x[i]));
    #pragma unroll
    for(int i=0;i<4;++i) lds_st<u32>(wr+(8+i)*272,pk_f16(cmul(x[2*i],g)));
    lds_st<u32>(wr+12*272,pk_f16(cmul(x[0],g2))); lds_st<u32>(wr+13*272,pk_f16(cmul(x[4],g2)));
    lds_st<u32>(wr+14*272,pk_f16(cmul(x[0],g3))); lds_st<u32>(wr+15*272,pk_f16(cmul(x[4],g3)));
    f32x2 y=cmul(w,g4);
    #pragma unroll
    for(int i=0;i<5;++i){ lds_st<u32>(wr+(16+i)*272,pk_f16(y)); if(i<4) y=cmul(y,g); }
  }
  #pragma unroll
  for(int k=0;k<8;++k){ u32x4 af=lds_ld<u32x4>(rd+k*32);
    acc=__builtin_amdgcn_mfma_f32_32x32x16_f16(__builtin_bit_cast(f16x8,af),__builtin_bit_cast(f16x8,bf[k]),acc,0,0,0); }
}
DI void ks_write(const float* v, lptr ks, int lane){
  const int j=lane&31, h=lane>>5;
  u32x2 a={pk_bf16(v[0],v[1]),pk_bf16(v[2],v[3])};
  lds_st<u32x2>(ks+(8*j+4*h)*2,a);
  u32x2 b={pk_bf16(v[4],v[5]),pk_bf16(v[6],v[7])};
  u32 c0=pk_bf16(v[8],v[9]), c1=pk_bf16(v[10],v[11]);
  if(h==0){ lds_st<u32x2>(ks+(256+4*j)*2,b);
    lds_st<u16>(ks+(512+j)*2,(u16)c0); lds_st<u16>(ks+(544+j)*2,(u16)(c0>>16));
    lds_st<u16>(ks+(576+j)*2,(u16)c1); lds_st<u16>(ks+(608+j)*2,(u16)(c1>>16));
  } else { lds_st<u32>(ks+(384+2*j)*2,b.x); lds_st<u32>(ks+(448+2*j)*2,b.y); if(j==0) lds_st<u16>(ks+640*2,(u16)c0); }
}
DI void k_expand(lptr ks, lptr ka, int lane){
  lptr kb=ka+A_KB;
  { int M=lane; u32x4 A,B;
    if(M<32){ u32x4 v=lds_ld<u32x4>(ks+M*16); u32 pv=(M>0)?lds_ld<u16>(ks+M*16-2):0u;
      B.x=p_hl(v.w,v.w); B.y=p_hl(v.z,v.z); B.z=p_hl(v.y,v.y); B.w=p_hl(v.x,v.x);
      A.x=p_lh(v.w,v.z); A.y=p_lh(v.z,v.y); A.z=p_lh(v.y,v.x); A.w=p_ll(v.x,pv);
    } else { u32x2 v=lds_ld<u32x2>(ks+(128+4*M)*2); u32 v4=lds_ld<u16>(ks+(132+4*M)*2); u32 pv=lds_ld<u16>(ks+((M==32)?255:(128+4*M))*2);
      B.x=p_lh(v4,v.y); B.y=p_ll(v.y,v.y); B.z=p_lh(v.y,v.x); B.w=p_ll(v.x,v.x);
      A.x=p_hl(v.y,v.y); A.y=B.y; A.z=p_hl(v.x,v.x); A.w=p_ll(v.x,pv);
    }
    lds_st<u32x4>(ka+(255-M)*16,A); lds_st<u32x4>(kb+(255-M)*16,B); }
  { int M=lane+64; u32 ab=lds_ld<u32>(ks+(256+2*M)*2), c=lds_ld<u16>(ks+(258+2*M)*2); u32x4 A,B;
    B.x=p_ll(c,c); B.y=p_hh(ab,ab); B.z=p_hl(ab,ab); B.w=p_ll(ab,ab);
    A.x=p_lh(c,ab); A.y=B.y; A.z=B.w; A.w=B.w;
    lds_st<u32x4>(ka+(255-M)*16,A); lds_st<u32x4>(kb+(255-M)*16,B); }
  #pragma unroll
  for(int i=2;i<4;++i){ int M=lane+64*i; u32 a=lds_ld<u16>(ks+(384+M)*2), b=lds_ld<u16>(ks+(385+M)*2); u32 c=(M&1)?b:a; u32x4 A,B;
    B.x=p_ll(b,b); B.y=p_ll(b,c); B.z=p_ll(a,a); B.w=B.z;
    A.x=B.x; A.y=p_ll(c,a); A.z=B.z; A.w=B.z;
    lds_st<u32x4>(ka+(255-M)*16,A); lds_st<u32x4>(kb+(255-M)*16,B); }
  if(lane<8){ u32x4 z={0,0,0,0}; lds_st<u32x4>(((lane&4)?kb:ka)+(256+(lane&3))*16,z); }
}
template<int NS> DI void conv_steps(lptr ua, lptr ka, int s0, int lane, f32x4* acc){
  const int a=lane&15, g=lane>>4;
  lptr up=ua+((a&1)?0:A_UB)+(32*(a>>1)+8*g+32*s0)*2;
  lptr kp=ka+((a&1)?A_KB:0)+(28-4*(a>>1)+8*g+32*s0)*2;
  u32x4 A[2][8],B[2][8];
  auto ldA=[&](int s,int q){ A[s&1][q]=cat(lds_ld64(kp+(1792-256*q+32*s)*2),lds_ld64(kp+(1792-256*q+32*s)*2+8)); };
  auto ldB=[&](int s,int q){ B[s&1][q]=lds_ld<u32x4>(up+(256*q+32*s)*2); };
  #pragma unroll
  for(int q=0;q<8;++q){ ldB(0,q); ldA(0,q); }
  SB();
  #pragma unroll
  for(int s=0;s<NS;++s){
    #pragma unroll
    for(int c=0;c<8;++c){
      #pragma unroll
      for(int q=0;q<=c;++q) acc[c]=mfma16(A[s&1][q],B[s&1][c-q],acc[c]);
      if(s+1<NS){ if(c<2){ ldB(s+1,c); } else if(c==2){ ldA(s+1,0); } else if(c==3){ ldA(s+1,1); ldB(s+1,2);} else if(c==4){ ldA(s+1,2); ldB(s+1,3); ldA(s+1,3);} else if(c==5){ ldB(s+1,4); ldA(s+1,4); ldB(s+1,5);} else if(c==6){ ldA(s+1,5); ldB(s+1,6); ldA(s+1,6);} else { ldB(s+1,7); ldA(s+1,7);} }
      SB();
    }
  }
}
DI u32 fin2(float y0,float y1,u32 u,float dsk){
  u32 y=pk_bf16(y0,y1), s=pk_bf16(bflo(u)*dsk,bfhi(u)*dsk);
  return pk_bf16(bflo(y)+bflo(s),bfhi(y)+bfhi(s)); }

DI void role_a(int j, lptr L, const u16* u, const u16* const* pp, const u16* logdt, const u16* dskip, const u16* wo, u16* out, void* ws){
  u32 pf0,pf1;
  const int tid=threadIdx.x, lane=tid&63, w=__builtin_amdgcn_readfirstlane(tid>>6);
  const int ch0=10*j;
  SsmP p=load_ssm(pp,logdt,ch0+w,lane);
  SsmP px; if(w<4) px=load_ssm(pp,logdt,ch0+8+(w>>1),lane);
  const float dsk=__builtin_bit_cast(float,(u32)dskip[ch0+w]<<16);
  const u32 dx=*(const u32*)(dskip+ch0+8);
  lptr slot=L+w*A_SLOT;
  if(lane<57){ u32x4 z={0,0,0,0}; if(lane<28) lds_st<u32x4>(slot+lane*16,z); else lds_st<u32x4>(slot+A_UB+(lane-28)*16,z); }
  p.dt=rbf(expf(p.dt));
  asm volatile("":"+v"(p.dt),"+v"(p.are),"+v"(p.aim),"+v"(p.bre),"+v"(p.bim),"+v"(p.cre),"+v"(p.cim)::"memory");
  if(w<4) asm volatile("":"+v"(px.dt),"+v"(px.are),"+v"(px.aim),"+v"(px.bre),"+v"(px.bim),"+v"(px.cre),"+v"(px.cim)::"memory");
  bar();
  u32x4 um[4]; u32 ux[4];
  { const char* up=(const char*)u+(size_t)tid*4*1280+20*j;
    #pragma unroll
    for(int r=0;r<4;++r){ typedef u32x4 __attribute__((aligned(4))) u32x4u; um[r]=*(const u32x4u*)(up+r*1280); ux[r]=*(const u32*)(up+r*1280+16); } }
  {
    const char* wp=(const char*)wo+(j&7)*102400+tid*128;
    pf0=*(const u32*)wp; pf1=*(const u32*)(wp+((tid<288)?65536:0)); }
  ST(1);
  if(w<4){ px.dt=rbf(expf(px.dt)); float s,inv; ssm_scale(px,s,inv); f32x16 acc={};
    kgen_half(px,w&1,s,slot+A_KA,lane,acc);
    lptr xp=L+A_XP+w*A_XPZ+(lane&31)*4; const int h=lane>>5;
    #pragma unroll
    for(int r=0;r<8;++r) lds_st<float>(xp+((r&3)+8*(r>>2)+4*h)*128,acc[r]*inv);
    if(h==0){
      #pragma unroll
      for(int r=8;r<12;++r) lds_st<float>(xp+(r+8)*128,acc[r]*inv);
    } else lds_st<float>(xp+20*128,acc[8]*inv);
  }
  { float s,inv; ssm_scale(p,s,inv); f32x16 acc={};
    kgen_half(p,0,s,slot+A_KA,lane,acc); kgen_half(p,1,s,slot+A_KA,lane,acc);
    float v[12];
    #pragma unroll
    for(int r=0;r<12;++r) v[r]=acc[r]*inv;
    ks_write(v,L+A_KS+w*A_KSZ,lane);
    k_expand(L+A_KS+w*A_KSZ,slot+A_KA,lane); }
  ST(2);
  asm volatile(""::"v"(pf0),"v"(pf1):"memory");
  {
    lptr d=L+(224+4*tid)*2;
    #pragma unroll
    for(int c=0;c<4;++c){
      u32x2 e={p_ll(um[0][c],um[1][c]),p_ll(um[2][c],um[3][c])}, o={p_hh(um[0][c],um[1][c]),p_hh(um[2][c],um[3][c])};
      lds_st<u32x2>(d+(2*c)*A_SLOT,e); lds_st<u32x2>(d+(2*c+1)*A_SLOT,o);
      lds_st<u32>(d+(2*c)*A_SLOT+A_UB+4,e.x); lds_st<u32>(d+(2*c+1)*A_SLOT+A_UB+4,o.x);
      if(tid<511){ lds_st<u32>(d+(2*c)*A_SLOT+A_UB+8,e.y); lds_st<u32>(d+(2*c+1)*A_SLOT+A_UB+8,o.y); }
    } }
  bar();
  ST(3);
  if(w<2){
    lptr xp=L+A_XP+2*w*A_XPZ+(lane&31)*4; const int h=lane>>5; float v[12];
    #pragma unroll
    for(int r=0;r<12;++r){ int row=min((r&3)+8*(r>>2)+4*h,20); v[r]=lds_ld<float>(xp+row*128)+lds_ld<float>(xp+A_XPZ+row*128); }
    ks_write(v,L+A_KS+w*A_KSZ,lane); }
  const int a=lane&15, g=lane>>4;
  const int tl=32*(a>>1)+8*g+2*(a&1);
  { f32x4 acc[8];
    #pragma unroll
    for(int c=0;c<8;++c) acc[c]=f32x4{0,0,0,0};
    conv_steps<8>(slot,slot+A_KA,0,lane,acc);
    ST(4);
    #pragma unroll
    for(int c=0;c<8;++c){
      u32 u0=lds_ld<u32>(slot+(224+256*c+tl)*2), u1=lds_ld<u32>(slot+(228+256*c+tl)*2);
      lds_st<u32>(slot+A_KA+(256*c+tl)*2,fin2(acc[c][0],acc[c][1],u0,dsk));
      lds_st<u32>(slot+A_KA+(256*c+tl+4)*2,fin2(acc[c][2],acc[c][3],u1,dsk)); } }
  bar();
  ST(5);
  {
    lptr d=L+(224+4*tid)*2;
    u32x2 e={p_ll(ux[0],ux[1]),p_ll(ux[2],ux[3])}, o={p_hh(ux[0],ux[1]),p_hh(ux[2],ux[3])};
    lds_st<u32x2>(d,e); lds_st<u32x2>(d+A_SLOT,o);
    lds_st<u32>(d+A_UB+4,e.x); lds_st<u32>(d+A_SLOT+A_UB+4,o.x);
    if(tid<511){ lds_st<u32>(d+A_UB+8,e.y); lds_st<u32>(d+A_SLOT+A_UB+8,o.y); }
    if(w<2) k_expand(L+A_KS+w*A_KSZ,L+(2+w)*A_SLOT,lane);
    lptr yp=L+((lane&15)>>2)*A_SLOT+A_KA+(256*w+16*g+4*(lane&3))*2;
    char* op=(char*)out+(size_t)(256*w+lane)*2560+20*j;
    #pragma unroll
    for(int i=0;i<4;++i){ typedef u32x4 __attribute__((aligned(4))) u32x4u;
      *(u32x4u*)(op+(size_t)i*64*2560)=cat(lds_tr(yp+i*128),lds_tr(yp+4*A_SLOT+i*128)); } }
  bar();
  ST(6);
  { f32x4 acc[8];
    #pragma unroll
    for(int c=0;c<8;++c) acc[c]=f32x4{0,0,0,0};
    conv_steps<2>(L+(w>>2)*A_SLOT,L+(2+(w>>2))*A_SLOT,2*(w&3),lane,acc);
    #pragma unroll
    for(int c=0;c<8;++c) lds_st<f32x4>(slot+A_KA+c*1024+lane*16,acc[c]); }
  bar();
  { f32x4 y[2]; u32 r[2][2];
    #pragma unroll
    for(int e=0;e<2;++e){ lptr q=L+4*e*A_SLOT+A_KA+tid*16;
      y[e]=(lds_ld<f32x4>(q)+lds_ld<f32x4>(q+A_SLOT))+(lds_ld<f32x4>(q+2*A_SLOT)+lds_ld<f32x4>(q+3*A_SLOT));
      u32 u0=lds_ld<u32>(L+e*A_SLOT+(224+256*w+tl)*2), u1=lds_ld<u32>(L+e*A_SLOT+(228+256*w+tl)*2);
      float d=e?bfhi(dx):bflo(dx);
      r[e][0]=fin2(y[e][0],y[e][1],u0,d); r[e][1]=fin2(y[e][2],y[e][3],u1,d); }
    char* op=(char*)out+(size_t)(256*w+tl)*2560+20*j+16;
    *(u32*)(op)=p_ll(r[0][0],r[1][0]); *(u32*)(op+2560)=p_hh(r[0][0],r[1][0]);
    *(u32*)(op+4*2560)=p_ll(r[0][1],r[1][1]); *(u32*)(op+5*2560)=p_hh(r[0][1],r[1][1]); }
  ST(7);
}

constexpr int B_ST=36864, B_WP=4*B_ST, B_Z=B_WP+6400;
constexpr u32 WS_FLAG=256, WS_P1=4096, WS_R=WS_P1+8*14*16*960, WS_O=WS_R+57344, WS_END=WS_O+128*20*1024;
constexpr u32 SENT=0xFFFFFFFFu;
constexpr int SPIN_MAX=1<<20;
typedef __amdgpu_buffer_rsrc_t rsrc_t;
template<int N> DI void waitv(){ asm volatile("s_waitcnt vmcnt(%0)"::"i"(N):"memory"); }
DI void dma16(const void* sb, u32 voff, lptr l){ u32 keep;
  asm volatile("s_mov_b32 %0, m0\n\ts_mov_b32 m0, %3\n\ts_nop 0\n\tglobal_load_lds_dwordx4 %1, %2\n\ts_mov_b32 m0, %0":"=&s"(keep):"v"(voff),"s"(sb),"s"((u32)(uintptr_t)l):"memory"); }
DI u32x4 ld_sc1(rsrc_t rs,u32 off){ return __builtin_amdgcn_raw_buffer_load_b128(rs,off,0,16); }
DI void st_sc1(rsrc_t rs,u32 off,u32x4 v){ __builtin_amdgcn_raw_buffer_store_b128(v,rs,off,0,16); }
DI u32 umax4(u32x4 v){ return max(max(v.x,v.y),max(v.z,v.w)); }
DI u32x4 canon(u32x4 v){ u32x4 r={min(v.x,SENT-1),min(v.y,SENT-1),min(v.z,SENT-1),min(v.w,SENT-1)}; return r; }

DI void softmax20(const f32x4* z, u32* o){
  float m=fmaxf(fmaxf(z[0][0],z[0][1]),fmaxf(z[0][2],z[0][3]));
  #pragma unroll
  for(int i=1;i<5;++i){ m=fmaxf(fmaxf(m,z[i][0]),z[i][1]); m=fmaxf(fmaxf(m,z[i][2]),z[i][3]); }
  const float c=1.4426950408889634f, mc=-m*c;
  f32x2 e[10]; f32x2 s={0.f,0.f};
  #pragma unroll
  for(int i=0;i<10;++i){ f32x2 t={z[i>>1][(i&1)*2],z[i>>1][(i&1)*2+1]}; t=__builtin_elementwise_fma(t,f32x2{c,c},f32x2{mc,mc});
    e[i]=f32x2{__builtin_amdgcn_exp2f(t.x),__builtin_amdgcn_exp2f(t.y)}; s+=e[i]; }
  const float r=__builtin_amdgcn_rcpf(s.x+s.y);
  #pragma unroll
  for(int i=0;i<10;++i){ f32x2 t=e[i]*r; o[i]=pk_bf16(t.x,t.y); }
}
DI u32x4 pack2(f32x4 a,f32x4 b){ u32x4 r={pk_bf16(a[0],a[1]),pk_bf16(a[2],a[3]),pk_bf16(b[0],b[1]),pk_bf16(b[2],b[3])}; return r; }

template<int C0> DI void features(lptr L,int hl,int lane,const u32x4 (*b)[2],u32 (*ph)[10]){
  const int q=(lane&15)>>2, p=lane&3, gk=lane>>4;
  const u32 sg=(lane&8)?0x80008000u:0u;
  constexpr int IA=(C0==0)?0:2;
  f32x4 z[2][5];
  #pragma unroll
  for(int i=0;i<5;++i){ z[0][i]=f32x4{0,0,0,0}; z[1][i]=f32x4{0,0,0,0}; }
  #pragma unroll
  for(int s=0;s<2;++s){
    lptr ad[2];
    #pragma unroll
    for(int t=0;t<2;++t){ int d=16*(IA+2*s+t)+4*gk-C0-20*(p&1); bool ok=(d>=0)&&(d<20)&&(IA+2*s+t<5);
      ad[t]=ok?(L+B_WP+(hl+(p&1))*800+(d+q)*40):(L+B_Z); }
    #pragma unroll
    for(int i=0;i<5;++i){ u32x4 a=cat(lds_tr(ad[0]+8*i),lds_tr(ad[1]+8*i)); a^=sg;
      z[0][i]=mfma16(a,b[0][s],z[0][i]); z[1][i]=mfma16(a,b[1][s],z[1][i]); }
  }
  softmax20(z[0],ph[0]); softmax20(z[1],ph[1]);
}
DI bool flags_ready(const void* ws,u64 E,int lane){
  const GAS u64* f=(const GAS u64*)((const char*)ws+WS_FLAG);
  for(int it=0;it<SPIN_MAX;++it){
    u64 a=__hip_atomic_load(f+lane,__ATOMIC_RELAXED,__HIP_MEMORY_SCOPE_AGENT), b=__hip_atomic_load(f+64+lane,__ATOMIC_RELAXED,__HIP_MEMORY_SCOPE_AGENT), c=__hip_atomic_load(f+128+lane,__ATOMIC_RELAXED,__HIP_MEMORY_SCOPE_AGENT);
    if(__all(a==E&&b==E&&c==E)) return true;
    __builtin_amdgcn_s_sleep(2); }
  return false; }

DI void wo_stage(lptr L,int off,const u16* wo,int ct,int k0,int nk,int w,int lane){
  const int r=lane>>4, f=(r|(((w>>1)&1)<<2))<<1;
  const char* g=(const char*)wo+(size_t)(k0+4*w)*1280+ct*256; const u32 vo=r*1280+(((lane&15)^f)<<4);
  #pragma unroll
  for(int i=0;i<nk/32;++i) dma16(g+(size_t)i*32*1280,vo,L+off+(w+8*i)*1024);
}
DI void wo_frags(lptr L,int off,int nks,int w,int lane,u32x4* wf){
  const int g=lane>>4,q=(lane&15)>>2,p=lane&3, f=(q|((g&1)<<2))<<1;
  lptr a=L+off+(8*g+q)*256+(((2*w+(p>>1))^f)<<4)+8*(p&1);
  #pragma unroll
  for(int s=0;s<9;++s) if(s<nks) wf[s]=cat(lds_tr(a+s*8192),lds_tr(a+s*8192+1024));
}

template<bool ISQ> DI void role_b(int X,int li,lptr L,const u16* x,const u16* wqkv,const u16* wphi,const u16* wo,u16* out,void* ws){
  const int tid=threadIdx.x, lane=tid&63, w=__builtin_amdgcn_readfirstlane(tid>>6);
  const int rt=4*(X>>1)+(ISQ?(li>>1):(li>>2));
  const int grp=ISQ?(2*(X&1)+(li&1)):(4*(X&1)+(li&3));
  const int me=ISQ?(128+X*8+li):(X*16+li);
#define ROT 5
  const int s0=ISQ?((li*ROT+1)%10):(((li>>2)+(li&3))*ROT)%10;
  const rsrc_t rs=__builtin_amdgcn_make_buffer_rsrc(ws,0,WS_END,0x00020000);
  u32 xo[2], wo_[3];
  #pragma unroll
  for(int i=0;i<2;++i){ int r=(2*w+i)*8+(lane>>3); xo[i]=(u32)(rt*128+r)*1280u+(u32)(((lane&7)^((r>>1)&7))<<4); }
  #pragma unroll
  for(int i=0;i<3;++i){ int c=(w+8*i)*64+lane, r=c/20, pos=c-r*20, sc=pos^(((r>>3)&1)<<1);
    wo_[i]=(u32)r*3840u+(ISQ?(u32)(320*grp+16*sc):(u32)((sc<10?1280:2400)+160*grp+16*sc)); }
  auto issue=[&](int s){ int ss=s+s0; if(ss>=10) ss-=10; lptr d=L+(s&3)*B_ST;
    const char* xb=(const char*)x+ss*128; const char* wb=(const char*)wqkv+(size_t)ss*64*3840;
    dma16(xb,xo[0],d+(2*w)*1024); dma16(xb,xo[1],d+(2*w+1)*1024);
    dma16(wb,wo_[0],d+16384+w*1024); dma16(wb,wo_[1],d+16384+(w+8)*1024);
    if(w<4) dma16(wb,wo_[2],d+16384+(w+16)*1024); };
  {
    constexpr int NB=ISQ?6400:3200; const int h0=ISQ?8*grp:4*grp;
    if(tid*16<NB) dma16((const char*)wphi+h0*800,tid*16,L+B_WP+w*1024);
    if(tid<4) lds_st<u32x4>(L+B_Z+tid*16,u32x4{0,0,0,0}); }
  issue(0); issue(1); issue(2); issue(3);
  {
    const u32x4 sv={SENT,SENT,SENT,SENT};
    if(ISQ){
      #pragma unroll
      for(int i=0;i<5;++i) st_sc1(rs,WS_O+((8*rt+w)*20+5*grp+i)*1024+lane*16,sv);
    } else {
      #pragma unroll
      for(int i=0;i<2;++i){ int c=tid+512*i; int sl=c/60; u32 o=WS_P1+((grp*14+sl)*16+rt)*960+(c-sl*60)*16;
        if(c>=840) o=(c<870&&rt<14)?(WS_R+(grp*84+6*rt)*80+(c-840)*16):0x80000000u;
        st_sc1(rs,o,sv); } } }
  u64 E; asm volatile("s_load_dwordx2 %0, %1, 0x0\n\ts_waitcnt lgkmcnt(0)":"=s"(E):"s"(ws):"memory"); E+=1;
  constexpr int NC=ISQ?5:2;
  const int g=lane>>4, tq=w>>1, hh=w&1;
  int ab[5], bb;
  { const int q=(lane&15)>>2,p=lane&3;
    #pragma unroll
    for(int i=0;i<5;++i){ int n=2*i+(p>>1); int cid=ISQ?(10*hh+n):(5*hh+n+(n>=5?5:0)); ab[i]=16384+(8*g+q)*320+((cid^((g&1)<<1))<<4)+8*(p&1); }
    bb=(32*tq+(lane&15))*128+((g^((lane&15)>>1))<<4); }
  f32x4 acc[5][2];
  #pragma unroll
  for(int i=0;i<5;++i){ acc[i][0]=f32x4{0,0,0,0}; acc[i][1]=f32x4{0,0,0,0}; }
  ST(1);
  u32x4 FA[2][5], FB[2][2];
  #pragma unroll
  for(int s=0;s<10;++s){
    if(s==0){ if(w<4) waitv<15+NC>(); else waitv<12+NC>(); }
    else if(s==1){ if(w<4) waitv<10+NC>(); else waitv<8+NC>(); }
    else if(s<4){ if(w<4) waitv<5+NC>(); else waitv<4+NC>(); }
    else if(s<9){ if(w<4) waitv<5>(); else waitv<4>(); }
    else waitv<0>();
    rbar();
    if(s==0) ST(2);
    SB();
    lptr d=L+(s&3)*B_ST;
    int ss=s+2+s0; if(ss>=10) ss-=10; lptr dn=L+((s+2)&3)*B_ST;
    const char* xb=(const char*)x+ss*128; const char* wb=(const char*)wqkv+(size_t)ss*64*3840;
    const bool pf=(s>=2&&s<8);
    #pragma unroll
    for(int i=0;i<5;++i){
      if(s>0){ acc[i][0]=mfma16(FA[1][i],FB[1][0],acc[i][0]); acc[i][1]=mfma16(FA[1][i],FB[1][1],acc[i][1]); }
      if(i==0){ FB[0][0]=lds_ld<u32x4>(d+bb); FB[0][1]=lds_ld<u32x4>(d+bb+2048); }
      FA[0][i]=cat(lds_tr(d+ab[i]),lds_tr(d+ab[i]+1280));
      if(pf){ if(i<2) dma16(xb,xo[i],dn+(2*w+i)*1024); else if(i<4) dma16(wb,wo_[i-2],dn+16384+(w+8*(i-2))*1024); else if(w<4) dma16(wb,wo_[2],dn+16384+(w+16)*1024); }
      SB(); }
    if(s==4 && tid==0) __hip_atomic_store((GAS u64*)((char*)ws+WS_FLAG)+me,E,__ATOMIC_RELAXED,__HIP_MEMORY_SCOPE_AGENT);
    #pragma unroll
    for(int i=0;i<5;++i){
      acc[i][0]=mfma16(FA[0][i],FB[0][0],acc[i][0]); acc[i][1]=mfma16(FA[0][i],FB[0][1],acc[i][1]);
      if(i==0){ FB[1][0]=lds_ld<u32x4>(d+(bb^64)); FB[1][1]=lds_ld<u32x4>(d+(bb^64)+2048); }
      FA[1][i]=cat(lds_tr(d+ab[i]+10240),lds_tr(d+ab[i]+10240+1280));
      SB(); }
  }
  #pragma unroll
  for(int i=0;i<5;++i){ acc[i][0]=mfma16(FA[1][i],FB[1][0],acc[i][0]); acc[i][1]=mfma16(FA[1][i],FB[1][1],acc[i][1]); }
  ST(3);
  const int T=ISQ?16+li:li; const bool doout=T<20; const int tt64=4*X+T/5, ct=T%5;
  u32x4 wf[20];
  if(!ISQ){
    u32 ph[2][10];
    { u32x4 b[2][2];
      #pragma unroll
      for(int t=0;t<2;++t){ b[t][0]=pack2(acc[0][t],acc[1][t]); b[t][1]=pack2(acc[2][t],acc[3][t]); }
      features<0>(L,2*hh,lane,b,ph); }
    constexpr int PI=2*B_ST, VI=PI+128*352;
    #pragma unroll
    for(int t=0;t<2;++t){ const int tok=32*tq+16*t+(lane&15);
      lptr pp=L+PI+tok*352+(2*hh+(g&1))*80+(g>>1)*40;
      #pragma unroll
      for(int i=0;i<5;++i) lds_st<u32x2>(pp+8*i,u32x2{ph[t][2*i],ph[t][2*i+1]});
      lptr vp=L+VI+tok*224+2*hh*48;
      #pragma unroll
      for(int i=2;i<5;++i){ int dv=16*i+4*g-40; u32x2 v={pk_bf16(acc[i][t][0],acc[i][t][1]),pk_bf16(acc[i][t][2],acc[i][t][3])};
        int o=(dv>=20)?(48+2*(dv-20)):2*dv;
        if(i==2&&g<2){ v=u32x2{0x3F80u,0u}; o=48*g+40; }
        lds_st<u32x2>(vp+o,v); } }
    bar();
    ST(4);
    { const int hd=w>>1, dt=w&1, q=(lane&15)>>2, p=lane&3; const int tk=16*(g>>1)+4*(g&1)+q;
      lptr pa=L+PI+tk*352+hd*80+8*p, vb=L+VI+tk*224+hd*48+dt*32+8*p;
      f32x4 c[3]={f32x4{0,0,0,0},f32x4{0,0,0,0},f32x4{0,0,0,0}};
      #pragma unroll
      for(int ks=0;ks<4;++ks){ u32x4 b=cat(lds_tr(vb+ks*32*224),lds_tr(vb+(ks*32+8)*224));
        #pragma unroll
        for(int i=0;i<3;++i){ u32x4 a=cat(lds_tr(pa+ks*32*352+32*i),lds_tr(pa+(ks*32+8)*352+32*i)); c[i]=mfma16(a,b,c[i]); } }
      const int d=16*dt+(lane&15), R=hd*21+d, sl=R/6;
      const u32 o=WS_P1+((grp*14+sl)*16+rt)*960+(R-6*sl)*160+16*g;
      #pragma unroll
      for(int i=0;i<3;++i) st_sc1(rs,(d<21&&(i<2||g<2))?o+64*i:0x80000000u,canon(__builtin_bit_cast(u32x4,c[i]))); }
    wo_stage(L,0,wo,ct,0,288,w,lane);
    bar();
    wo_stage(L,PI,wo,ct,288,288,w,lane);
    ST(5);
    bool ok=true;
    if(w==0){ ok=flags_ready(ws,E,lane);
      if(me==0&&lane==0) __hip_atomic_store((GAS u64*)ws,E,__ATOMIC_RELAXED,__HIP_MEMORY_SCOPE_AGENT);
      if(rt<14){ const u32 o=(lane<60)?(WS_P1+(grp*14+rt)*16*960+lane*16):0x80000000u; u32x4 v[16];
        for(int it=0;it<SPIN_MAX;++it){ asm volatile("":::"memory"); u32 m=0;
          #pragma unroll
          for(int i=0;i<16;++i) v[i]=ld_sc1(rs,o+i*960);
          #pragma unroll
          for(int i=0;i<16;++i) m=max(m,umax4(v[i]));
          if(__all(m!=SENT)) break; }
        f32x4 sum=__builtin_bit_cast(f32x4,v[0]);
        #pragma unroll
        for(int i=1;i<16;++i) sum+=__builtin_bit_cast(f32x4,v[i]);
        u32x2 r={min(pk_bf16(sum[0],sum[1]),SENT-1),min(pk_bf16(sum[2],sum[3]),SENT-1)};
        __builtin_amdgcn_raw_buffer_store_b64(r,rs,(lane<60)?(WS_R+(grp*84+6*rt)*80+lane*8):0x80000000u,0,16); } }
    ST(6);
    waitv<0>(); rbar(); wo_frags(L,0,9,w,lane,wf); wo_frags(L,PI,9,w,lane,wf+9); bar();
    wo_stage(L,0,wo,ct,576,64,w,lane); waitv<0>(); rbar(); wo_frags(L,0,2,w,lane,wf+18); bar();
  } else {
    u32 ph[2][2][10];
    { u32x4 b[2][2];
      #pragma unroll
      for(int t=0;t<2;++t){ b[t][0]=pack2(acc[0][t],acc[1][t]); b[t][1]=pack2(acc[2][t],acc[3][t]); }
      features<0>(L,4*hh,lane,b,ph[0]);
      #pragma unroll
      for(int t=0;t<2;++t){ b[t][0]=b[t][1]; b[t][1]=pack2(acc[4][t],acc[4][t]); }
      features<40>(L,4*hh+2,lane,b,ph[1]); }
    #pragma unroll
    for(int i=0;i<40;++i) asm volatile("":"+v"(ph[i/20][(i/10)&1][i%10]));
    ST(4);
    if(w==0) flags_ready(ws,E,lane);
    bar();
    if(doout){
      wo_stage(L,0,wo,ct,0,512,w,lane); waitv<0>(); bar(); wo_frags(L,0,8,w,lane,wf); wo_frags(L,65536,8,w,lane,wf+8); bar();
      wo_stage(L,0,wo,ct,512,128,w,lane); waitv<0>(); bar(); wo_frags(L,0,4,w,lane,wf+16); bar(); }
    ST(5);
    { u32x4 v0,v1; const u32 o=WS_R+grp*13440+tid*16;
      for(int it=0;it<SPIN_MAX;++it){ asm volatile("":::"memory");
        v0=ld_sc1(rs,o); v1=ld_sc1(rs,(tid<328)?o+8192:0x80000000u);
        if(__all(max(umax4(v0),umax4(v1))!=SENT)) break; }
      lds_st<u32x4>(L+tid*16,v0); if(tid<328) lds_st<u32x4>(L+8192+tid*16,v1); }
    bar();
    ST(6);
    constexpr int OI=16384;
    #pragma unroll
    for(int ps=0;ps<2;++ps){
      const int gp=(lane&15)>>2, e=lane&3; const bool mt=((gp>>1)==(g&1));
      f32x4 n[2][3];
      #pragma unroll
      for(int i=0;i<3;++i){ int sg=4*i+e; int d=(sg<10)?(10*(gp&1)+sg):20;
        lptr a=mt?(L+((4*hh+2*ps+(gp>>1))*21+d)*80+(g>>1)*40):(L+B_Z);
        u32x4 a0=cat(lds_ld<u32x2>(a),lds_ld<u32x2>(a+8)), a1=cat(lds_ld<u32x2>(a+16),lds_ld<u32x2>(a+24)), a2=cat(lds_ld<u32x2>(a+32),u32x2{0,0});
        #pragma unroll
        for(int t=0;t<2;++t){ const u32* f=ph[ps][t];
          f32x4 c=mfma16(a0,u32x4{f[0],f[1],f[2],f[3]},f32x4{0,0,0,0}); c=mfma16(a1,u32x4{f[4],f[5],f[6],f[7]},c); n[t][i]=mfma16(a2,u32x4{f[8],f[9],0,0},c); } }
      #pragma unroll
      for(int t=0;t<2;++t){ const float den=rbf(rbf(n[t][2][2])+1e-6f), r=__builtin_amdgcn_rcpf(den);
        lptr op=L+OI+(32*tq+16*t+(lane&15))*336+((4*hh+2*ps+(g>>1))*20+10*(g&1))*2;
        #pragma unroll
        for(int i=0;i<5;++i){ u32 nb=pk_bf16(n[t][i>>1][(i&1)*2],n[t][i>>1][(i&1)*2+1]); lds_st<u32>(op+4*i,pk_bf16(bflo(nb)*r,bfhi(nb)*r)); } } }
    bar();
    #pragma unroll
    for(int i=0;i<5;++i) st_sc1(rs,WS_O+((8*rt+w)*20+5*grp+i)*1024+lane*16,canon(lds_ld<u32x4>(L+OI+(16*w+(lane&15))*336+64*i+16*g)));
    ST(7);
    bar();
  }
  if(!doout) return;
  ST(8);
  { u32x4 v[10]; const u32 o=WS_O+tt64*81920+w*10240+lane*16;
    for(int it=0;it<SPIN_MAX;++it){ asm volatile("":::"memory"); u32 m=0;
      #pragma unroll
      for(int i=0;i<10;++i) v[i]=ld_sc1(rs,o+i*1024);
      #pragma unroll
      for(int i=0;i<10;++i) m=max(m,umax4(v[i]));
      if(__all(m!=SENT)) break; }
    #pragma unroll
    for(int i=0;i<10;++i) lds_st<u32x4>(L+w*10240+i*1024+lane*16,v[i]); }
  bar();
  ST(9);
  { f32x4 c[4]={f32x4{0,0,0,0},f32x4{0,0,0,0},f32x4{0,0,0,0},f32x4{0,0,0,0}};
    u32x4 ob[2][4];
    #pragma unroll
    for(int t=0;t<4;++t) ob[0][t]=lds_ld<u32x4>(L+(t*20)*1024+lane*16);
    SB();
    #pragma unroll
    for(int ks=0;ks<20;++ks){
      #pragma unroll
      for(int t=0;t<4;++t){ if(ks<19) ob[(ks+1)&1][t]=lds_ld<u32x4>(L+(t*20+ks+1)*1024+lane*16); c[t]=mfma16(wf[ks],ob[ks&1][t],c[t]); }
      SB(); }
    constexpr int CI=81920;
    #pragma unroll
    for(int t=0;t<4;++t) lds_st<u32x2>(L+CI+(16*t+(lane&15))*272+w*32+8*g,u32x2{pk_bf16(c[t][0],c[t][1]),pk_bf16(c[t][2],c[t][3])});
    bar();
    #pragma unroll
    for(int i=0;i<2;++i){ const int r=32*i+(tid>>4);
      *(u32x4*)((char*)out+(size_t)(64*tt64+r)*2560+1280+ct*256+(tid&15)*16)=lds_ld<u32x4>(L+CI+r*272+(tid&15)*16); } }
  ST(10);
}

__global__ __launch_bounds__(512,2) void fused(const u16* u,const u16* are,const u16* aim,const u16* bre,const u16* bim,const u16* cre,const u16* cim,
    const u16* logdt,const u16* dskip,const u16* x,const u16* wqkv,const u16* wphi,const u16* wo,u16* out,void* ws){
  extern __shared__ __attribute__((aligned(16))) char smem[];
  lptr L=(lptr)smem;
  const int b=blockIdx.x, X=b&7, idx=b>>3;
  ST(0);
  if(idx<16) role_b<false>(X,idx,L,x,wqkv,wphi,wo,out,ws);
  else if(idx<24){
    const u16* pp[6]={are,aim,bre,bim,cre,cim}; role_a(X*8+(idx-16),L,u,pp,logdt,dskip,wo,out,ws);
  }
  else role_b<true>(X,idx-24,L,x,wqkv,wphi,wo,out,ws);
}

extern "C" void kernel_launch(void* const* d_in, const int* in_sizes, int n_in, void* d_out, int out_size, void* d_ws, size_t ws_size, hipStream_t stream){
  (void)hipFuncSetAttribute((const void*)fused,hipFuncAttributeMaxDynamicSharedMemorySize,163840);
  fused<<<256,512,163840,stream>>>((const u16*)d_in[0],(const u16*)d_in[1],(const u16*)d_in[2],(const u16*)d_in[3],(const u16*)d_in[4],(const u16*)d_in[5],(const u16*)d_in[6],
    (const u16*)d_in[7],(const u16*)d_in[8],(const u16*)d_in[9],(const u16*)d_in[10],(const u16*)d_in[11],(const u16*)d_in[12],(u16*)d_out,d_ws);
}
```

```cpp
#include <hip/hip_runtime.h>
#include <stdint.h>

typedef unsigned short u16; typedef unsigned u32; typedef unsigned long long u64;
typedef __attribute__((ext_vector_type(4))) short s16x4;
typedef __attribute__((ext_vector_type(8))) short s16x8;
typedef __attribute__((ext_vector_type(8))) _Float16 f16x8;
typedef __attribute__((ext_vector_type(8))) __bf16 bf16x8;
typedef __attribute__((ext_vector_type(2))) __bf16 bf16x2;
typedef __attribute__((ext_vector_type(2))) _Float16 f16x2;
typedef __attribute__((ext_vector_type(2))) float f32x2;
typedef __attribute__((ext_vector_type(4))) float f32x4;
typedef __attribute__((ext_vector_type(16))) float f32x16;
typedef __attribute__((ext_vector_type(2))) unsigned u32x2;
typedef __attribute__((ext_vector_type(4))) unsigned u32x4;
#define LAS __attribute__((address_space(3)))
#define GAS __attribute__((address_space(1)))
#define DI __device__ __forceinline__
typedef LAS char* lptr;

#define ST(i) do{}while(0)

DI u32 pk_bf16(float a, float b){ f32x2 v={a,b}; return __builtin_bit_cast(u32,__builtin_convertvector(v,bf16x2)); }
DI u32 pk_f16(f32x2 v){ return __builtin_bit_cast(u32,__builtin_convertvector(v,f16x2)); }
DI float bflo(u32 x){ return __builtin_bit_cast(float,x<<16); }
DI float bfhi(u32 x){ return __builtin_bit_cast(float,x&0xffff0000u); }
DI float rbf(float a){ return bflo(pk_bf16(a,a)); }
template<class T> DI T lds_ld(lptr p){ return *(LAS T*)p; }
template<class T> DI void lds_st(lptr p, T v){ *(LAS T*)p=v; }
DI u32x2 lds_tr(lptr p){ return __builtin_bit_cast(u32x2,__builtin_amdgcn_ds_read_tr16_b64_v4i16((LAS s16x4*)p)); }
DI f32x4 mfma16(u32x4 a,u32x4 b,f32x4 c){ return __builtin_amdgcn_mfma_f32_16x16x32_bf16(__builtin_bit_cast(bf16x8,a),__builtin_bit_cast(bf16x8,b),c,0,0,0); }
DI u32x4 cat(u32x2 a,u32x2 b){ u32x4 r={a.x,a.y,b.x,b.y}; return r; }
DI u32 p_ll(u32 a,u32 b){ return __builtin_amdgcn_perm(b,a,0x05040100u); }
DI u32 p_lh(u32 a,u32 b){ return __builtin_amdgcn_perm(b,a,0x07060100u); }
DI u32 p_hl(u32 a,u32 b){ return __builtin_amdgcn_perm(b,a,0x05040302u); }
DI u32 p_hh(u32 a,u32 b){ return __builtin_amdgcn_perm(b,a,0x07060302u); }
#define SB() __builtin_amdgcn_sched_barrier(0)
DI void rbar(){ asm volatile("":::"memory"); __builtin_amdgcn_s_barrier(); asm volatile("":::"memory"); }
DI u32x2 lds_ld64(lptr p){ return *(volatile LAS u32x2*)p; }
DI float wave_max(float m){
  m=fmaxf(m,__builtin_bit_cast(float,__builtin_amdgcn_update_dpp(0,__builtin_bit_cast(int,m),0xB1,0xf,0xf,true)));
  m=fmaxf(m,__builtin_bit_cast(float,__builtin_amdgcn_update_dpp(0,__builtin_bit_cast(int,m),0x4E,0xf,0xf,true)));
  m=fmaxf(m,__builtin_bit_cast(float,__builtin_amdgcn_update_dpp(0,__builtin_bit_cast(int,m),0x141,0xf,0xf,true)));
  m=fmaxf(m,__builtin_bit_cast(float,__builtin_amdgcn_update_dpp(0,__builtin_bit_cast(int,m),0x140,0xf,0xf,true)));
  int x=__builtin_bit_cast(int,m);
  float a=__builtin_bit_cast(float,__builtin_amdgcn_readlane(x,0)), b=__builtin_bit_cast(float,__builtin_amdgcn_readlane(x,16));
  float c=__builtin_bit_cast(float,__builtin_amdgcn_readlane(x,32)), d=__builtin_bit_cast(float,__builtin_amdgcn_readlane(x,48));
  return fmaxf(fmaxf(a,b),fmaxf(c,d)); }
DI void bar(){ asm volatile("s_waitcnt lgkmcnt(0)":::"memory"); __builtin_amdgcn_s_barrier(); asm volatile("":::"memory"); }

constexpr int A_UB=4576, A_KA=9120, A_KB=4224  , A_SLOT=17824;
constexpr int A_KS=8*A_SLOT, A_KSZ=1296, A_XP=A_KS+8*A_KSZ, A_XPZ=2688;
static_assert(A_XP+4*A_XPZ<=163840,"lds");

DI f32x2 cmul(f32x2 a, f32x2 b){ f32x2 sw={-b.y,b.x}; return __builtin_elementwise_fma(a.yy,sw,a.xx*b); }
DI f32x2 cexp_e(float re2, float rev, float e){
  float m=__builtin_amdgcn_exp2f(re2*e); float a=__builtin_amdgcn_fractf(rev*e);
  f32x2 r={m*__builtin_amdgcn_cosf(a), m*__builtin_amdgcn_sinf(a)}; return r; }

struct SsmP { u32 are,aim,bre,bim,cre,cim; float dt; };
DI SsmP load_ssm(const u16* const* pp, const u16* logdt, int ch, int lane){
  SsmP p; int o=ch*64+lane;
  p.are=((const u32*)pp[0])[o]; p.aim=((const u32*)pp[1])[o]; p.bre=((const u32*)pp[2])[o];
  p.bim=((const u32*)pp[3])[o]; p.cre=((const u32*)pp[4])[o]; p.cim=((const u32*)pp[5])[o];
  p.dt=__builtin_bit_cast(float,(u32)logdt[ch]<<16);
  return p; }
DI f32x2 ssm_w(const SsmP& p, int half){
  f32x2 b={half?bfhi(p.bre):bflo(p.bre), half?bfhi(p.bim):bflo(p.bim)};
  f32x2 c={half?bfhi(p.cre):bflo(p.cre), half?bfhi(p.cim):bflo(p.cim)};
  return cmul(c,b*p.dt); }
DI void ssm_scale(const SsmP& p, float& s, float& inv){
  f32x2 w0=ssm_w(p,0), w1=ssm_w(p,1);
  float m=fmaxf(fmaxf(fabsf(w0.x),fabsf(w0.y)),fmaxf(fabsf(w1.x),fabsf(w1.y)));
  m=wave_max(m);
  int e=__builtin_amdgcn_frexp_expf(m); e=max(-100,min(100,e));
  s=__builtin_amdgcn_ldexpf(1.0f,-e); inv=__builtin_amdgcn_ldexpf(1.0f,e); }

DI void kgen_half(const SsmP& p, int half, float s, lptr tab, int lane, f32x16& acc){
  const float are=(half?bfhi(p.are):bflo(p.are))*p.dt, aim=(half?bfhi(p.aim):bflo(p.aim))*p.dt;
  const float re2=are*1.4426950408889634f, rev=aim*0.15915494309189535f;
  lptr wr=tab+lane*4; lptr rd=tab+(lane&31)*272+(lane>>5)*16;
  {
    f32x2 e8=cexp_e(re2,rev,8.f), e16=cmul(e8,e8);
    f32x2 a={1.f,0.f}, b=e8;
    #pragma unroll
    for(int j=0;j<32;j+=2){ lds_st<u32>(wr+j*272,pk_f16(a)); lds_st<u32>(wr+(j+1)*272,pk_f16(b)); if(j<30){ a=cmul(a,e16); b=cmul(b,e16);} }
  }
  u32x4 bf[8];
  #pragma unroll
  for(int k=0;k<8;++k) bf[k]=lds_ld<u32x4>(rd+k*32);
  {
    f32x2 w=ssm_w(p,half)*s; w.y=-w.y;
    f32x2 e1=cexp_e(re2,-rev,1.f), g=cexp_e(re2,-rev,256.f);
    f32x2 g2=cmul(g,g), g3=cmul(g2,g), g4=cmul(g2,g2);
    f32x2 x[8]; x[0]=w;
    #pragma unroll
    for(int i=1;i<8;++i) x[i]=cmul(x[i-1],e1);
    #pragma unroll
    for(int i=0;i<8;++i) lds_st<u32>(wr+i*272,pk_f16(x[i]));
    #pragma unroll
    for(int i=0;i<4;++i) lds_st<u32>(wr+(8+i)*272,pk_f16(cmul(x[2*i],g)));
    lds_st<u32>(wr+12*272,pk_f16(cmul(x[0],g2))); lds_st<u32>(wr+13*272,pk_f16(cmul(x[4],g2)));
    lds_st<u32>(wr+14*272,pk_f16(cmul(x[0],g3))); lds_st<u32>(wr+15*272,pk_f16(cmul(x[4],g3)));
    f32x2 y=cmul(w,g4);
    #pragma unroll
    for(int i=0;i<5;++i){ lds_st<u32>(wr+(16+i)*272,pk_f16(y)); if(i<4) y=cmul(y,g); }
  }
  #pragma unroll
  for(int k=0;k<8;++k){ u32x4 af=lds_ld<u32x4>(rd+k*32);
    acc=__builtin_amdgcn_mfma_f32_32x32x16_f16(__builtin_bit_cast(f16x8,af),__builtin_bit_cast(f16x8,bf[k]),acc,0,0,0); }
}
DI void ks_write(const float* v, lptr ks, int lane){
  const int j=lane&31, h=lane>>5;
  u32x2 a={pk_bf16(v[0],v[1]),pk_bf16(v[2],v[3])};
  lds_st<u32x2>(ks+(8*j+4*h)*2,a);
  u32x2 b={pk_bf16(v[4],v[5]),pk_bf16(v[6],v[7])};
  u32 c0=pk_bf16(v[8],v[9]), c1=pk_bf16(v[10],v[11]);
  if(h==0){ lds_st<u32x2>(ks+(256+4*j)*2,b);
    lds_st<u16>(ks+(512+j)*2,(u16)c0); lds_st<u16>(ks+(544+j)*2,(u16)(c0>>16));
    lds_st<u16>(ks+(576+j)*2,(u16)c1); lds_st<u16>(ks+(608+j)*2,(u16)(c1>>16));
  } else { lds_st<u32>(ks+(384+2*j)*2,b.x); lds_st<u32>(ks+(448+2*j)*2,b.y); if(j==0) lds_st<u16>(ks+640*2,(u16)c0); }
}
DI void k_expand(lptr ks, lptr ka, int lane){
  lptr kb=ka+A_KB;
  { int M=lane; u32x4 A,B;
    if(M<32){ u32x4 v=lds_ld<u32x4>(ks+M*16); u32 pv=(M>0)?lds_ld<u16>(ks+M*16-2):0u;
      B.x=p_hl(v.w,v.w); B.y=p_hl(v.z,v.z); B.z=p_hl(v.y,v.y); B.w=p_hl(v.x,v.x);
      A.x=p_lh(v.w,v.z); A.y=p_lh(v.z,v.y); A.z=p_lh(v.y,v.x); A.w=p_ll(v.x,pv);
    } else { u32x2 v=lds_ld<u32x2>(ks+(128+4*M)*2); u32 v4=lds_ld<u16>(ks+(132+4*M)*2); u32 pv=lds_ld<u16>(ks+((M==32)?255:(128+4*M))*2);
      B.x=p_lh(v4,v.y); B.y=p_ll(v.y,v.y); B.z=p_lh(v.y,v.x); B.w=p_ll(v.x,v.x);
      A.x=p_hl(v.y,v.y); A.y=B.y; A.z=p_hl(v.x,v.x); A.w=p_ll(v.x,pv);
    }
    lds_st<u32x4>(ka+(255-M)*16,A); lds_st<u32x4>(kb+(255-M)*16,B); }
  { int M=lane+64; u32 ab=lds_ld<u32>(ks+(256+2*M)*2), c=lds_ld<u16>(ks+(258+2*M)*2); u32x4 A,B;
    B.x=p_ll(c,c); B.y=p_hh(ab,ab); B.z=p_hl(ab,ab); B.w=p_ll(ab,ab);
    A.x=p_lh(c,ab); A.y=B.y; A.z=B.w; A.w=B.w;
    lds_st<u32x4>(ka+(255-M)*16,A); lds_st<u32x4>(kb+(255-M)*16,B); }
  #pragma unroll
  for(int i=2;i<4;++i){ int M=lane+64*i; u32 a=lds_ld<u16>(ks+(384+M)*2), b=lds_ld<u16>(ks+(385+M)*2); u32 c=(M&1)?b:a; u32x4 A,B;
    B.x=p_ll(b,b); B.y=p_ll(b,c); B.z=p_ll(a,a); B.w=B.z;
    A.x=B.x; A.y=p_ll(c,a); A.z=B.z; A.w=B.z;
    lds_st<u32x4>(ka+(255-M)*16,A); lds_st<u32x4>(kb+(255-M)*16,B); }
  if(lane<8){ u32x4 z={0,0,0,0}; lds_st<u32x4>(((lane&4)?kb:ka)+(256+(lane&3))*16,z); }
}
template<int NS> DI void conv_steps(lptr ua, lptr ka, int s0, int lane, f32x4* acc){
  const int a=lane&15, g=lane>>4;
  lptr up=ua+((a&1)?0:A_UB)+(32*(a>>1)+8*g+32*s0)*2;
  lptr kp=ka+((a&1)?A_KB:0)+(28-4*(a>>1)+8*g+32*s0)*2;
  u32x4 A[2][8],B[2][8];
  auto ldA=[&](int s,int q){ A[s&1][q]=cat(lds_ld64(kp+(1792-256*q+32*s)*2),lds_ld64(kp+(1792-256*q+32*s)*2+8)); };
  auto ldB=[&](int s,int q){ B[s&1][q]=lds_ld<u32x4>(up+(256*q+32*s)*2); };
  #pragma unroll
  for(int q=0;q<8;++q){ ldB(0,q); ldA(0,q); }
  SB();
  #pragma unroll
  for(int s=0;s<NS;++s){
    #pragma unroll
    for(int c=0;c<8;++c){
      #pragma unroll
      for(int q=0;q<=c;++q) acc[c]=mfma16(A[s&1][q],B[s&1][c-q],acc[c]);
      if(s+1<NS){ if(c<2){ ldB(s+1,c); } else if(c==2){ ldA(s+1,0); } else if(c==3){ ldA(s+1,1); ldB(s+1,2);} else if(c==4){ ldA(s+1,2); ldB(s+1,3); ldA(s+1,3);} else if(c==5){ ldB(s+1,4); ldA(s+1,4); ldB(s+1,5);} else if(c==6){ ldA(s+1,5); ldB(s+1,6); ldA(s+1,6);} else { ldB(s+1,7); ldA(s+1,7);} }
      SB();
    }
  }
}
#define OAUX 0
DI void ost128(u16* out,u32 off,u32x4 v){ __builtin_amdgcn_raw_buffer_store_b128(v,__builtin_amdgcn_make_buffer_rsrc(out,0,2048*2560,0x00020000),off,0,OAUX); }
DI void ost32(u16* out,u32 off,u32 v){ __builtin_amdgcn_raw_buffer_store_b32(v,__builtin_amdgcn_make_buffer_rsrc(out,0,2048*2560,0x00020000),off,0,OAUX); }
DI u32 fin2(float y0,float y1,u32 u,float dsk){
  u32 y=pk_bf16(y0,y1), s=pk_bf16(bflo(u)*dsk,bfhi(u)*dsk);
  return pk_bf16(bflo(y)+bflo(s),bfhi(y)+bfhi(s)); }

DI void role_a(int j, lptr L, const u16* u, const u16* const* pp, const u16* logdt, const u16* dskip, const u16* wo, u16* out, void* ws){
  u32 pf0,pf1;
  const int tid=threadIdx.x, lane=tid&63, w=__builtin_amdgcn_readfirstlane(tid>>6);
  const int ch0=10*j;
  SsmP p=load_ssm(pp,logdt,ch0+w,lane);
  SsmP px; if(w<4) px=load_ssm(pp,logdt,ch0+8+(w>>1),lane);
  const float dsk=__builtin_bit_cast(float,(u32)dskip[ch0+w]<<16);
  const u32 dx=*(const u32*)(dskip+ch0+8);
  lptr slot=L+w*A_SLOT;
  if(lane<57){ u32x4 z={0,0,0,0}; if(lane<28) lds_st<u32x4>(slot+lane*16,z); else lds_st<u32x4>(slot+A_UB+(lane-28)*16,z); }
  p.dt=rbf(expf(p.dt));
  asm volatile("":"+v"(p.dt),"+v"(p.are),"+v"(p.aim),"+v"(p.bre),"+v"(p.bim),"+v"(p.cre),"+v"(p.cim)::"memory");
  if(w<4) asm volatile("":"+v"(px.dt),"+v"(px.are),"+v"(px.aim),"+v"(px.bre),"+v"(px.bim),"+v"(px.cre),"+v"(px.cim)::"memory");
  bar();
  u32x4 um[4]; u32 ux[4];
  { const char* up=(const char*)u+(size_t)tid*4*1280+20*j;
    #pragma unroll
    for(int r=0;r<4;++r){ typedef u32x4 __attribute__((aligned(4))) u32x4u; um[r]=*(const u32x4u*)(up+r*1280); ux[r]=*(const u32*)(up+r*1280+16); } }
  {
    const char* wp=(const char*)wo+(j&7)*102400+tid*128;
    pf0=*(const u32*)wp; pf1=*(const u32*)(wp+((tid<288)?65536:0)); }
  ST(1);
  if(w<4){ px.dt=rbf(expf(px.dt)); float s,inv; ssm_scale(px,s,inv); f32x16 acc={};
    kgen_half(px,w&1,s,slot+A_KA,lane,acc);
    lptr xp=L+A_XP+w*A_XPZ+(lane&31)*4; const int h=lane>>5;
    #pragma unroll
    for(int r=0;r<8;++r) lds_st<float>(xp+((r&3)+8*(r>>2)+4*h)*128,acc[r]*inv);
    if(h==0){
      #pragma unroll
      for(int r=8;r<12;++r) lds_st<float>(xp+(r+8)*128,acc[r]*inv);
    } else lds_st<float>(xp+20*128,acc[8]*inv);
  }
  { float s,inv; ssm_scale(p,s,inv); f32x16 acc={};
    kgen_half(p,0,s,slot+A_KA,lane,acc); kgen_half(p,1,s,slot+A_KA,lane,acc);
    float v[12];
    #pragma unroll
    for(int r=0;r<12;++r) v[r]=acc[r]*inv;
    ks_write(v,L+A_KS+w*A_KSZ,lane);
    k_expand(L+A_KS+w*A_KSZ,slot+A_KA,lane); }
  ST(2);
  asm volatile(""::"v"(pf0),"v"(pf1):"memory");
  {
    lptr d=L+(224+4*tid)*2;
    #pragma unroll
    for(int c=0;c<4;++c){
      u32x2 e={p_ll(um[0][c],um[1][c]),p_ll(um[2][c],um[3][c])}, o={p_hh(um[0][c],um[1][c]),p_hh(um[2][c],um[3][c])};
      lds_st<u32x2>(d+(2*c)*A_SLOT,e); lds_st<u32x2>(d+(2*c+1)*A_SLOT,o);
      lds_st<u32>(d+(2*c)*A_SLOT+A_UB+4,e.x); lds_st<u32>(d+(2*c+1)*A_SLOT+A_UB+4,o.x);
      if(tid<511){ lds_st<u32>(d+(2*c)*A_SLOT+A_UB+8,e.y); lds_st<u32>(d+(2*c+1)*A_SLOT+A_UB+8,o.y); }
    } }
  bar();
  ST(3);
  if(w<2){
    lptr xp=L+A_XP+2*w*A_XPZ+(lane&31)*4; const int h=lane>>5; float v[12];
    #pragma unroll
    for(int r=0;r<12;++r){ int row=min((r&3)+8*(r>>2)+4*h,20); v[r]=lds_ld<float>(xp+row*128)+lds_ld<float>(xp+A_XPZ+row*128); }
    ks_write(v,L+A_KS+w*A_KSZ,lane); }
  const int a=lane&15, g=lane>>4;
  const int tl=32*(a>>1)+8*g+2*(a&1);
  { f32x4 acc[8];
    #pragma unroll
    for(int c=0;c<8;++c) acc[c]=f32x4{0,0,0,0};
    conv_steps<8>(slot,slot+A_KA,0,lane,acc);
    ST(4);
    #pragma unroll
    for(int c=0;c<8;++c){
      u32 u0=lds_ld<u32>(slot+(224+256*c+tl)*2), u1=lds_ld<u32>(slot+(228+256*c+tl)*2);
      lds_st<u32>(slot+A_KA+(256*c+tl)*2,fin2(acc[c][0],acc[c][1],u0,dsk));
      lds_st<u32>(slot+A_KA+(256*c+tl+4)*2,fin2(acc[c][2],acc[c][3],u1,dsk)); } }
  bar();
  ST(5);
  {
    lptr d=L+(224+4*tid)*2;
    u32x2 e={p_ll(ux[0],ux[1]),p_ll(ux[2],ux[3])}, o={p_hh(ux[0],ux[1]),p_hh(ux[2],ux[3])};
    lds_st<u32x2>(d,e); lds_st<u32x2>(d+A_SLOT,o);
    lds_st<u32>(d+A_UB+4,e.x); lds_st<u32>(d+A_SLOT+A_UB+4,o.x);
    if(tid<511){ lds_st<u32>(d+A_UB+8,e.y); lds_st<u32>(d+A_SLOT+A_UB+8,o.y); }
    if(w<2) k_expand(L+A_KS+w*A_KSZ,L+(2+w)*A_SLOT,lane);
    lptr yp=L+((lane&15)>>2)*A_SLOT+A_KA+(256*w+16*g+4*(lane&3))*2;
    const u32 op=(256*w+lane)*2560+20*j;
    #pragma unroll
    for(int i=0;i<4;++i) ost128(out,op+i*64*2560,cat(lds_tr(yp+i*128),lds_tr(yp+4*A_SLOT+i*128))); }
  bar();
  ST(6);
  { f32x4 acc[8];
    #pragma unroll
    for(int c=0;c<8;++c) acc[c]=f32x4{0,0,0,0};
    conv_steps<2>(L+(w>>2)*A_SLOT,L+(2+(w>>2))*A_SLOT,2*(w&3),lane,acc);
    #pragma unroll
    for(int c=0;c<8;++c) lds_st<f32x4>(slot+A_KA+c*1024+lane*16,acc[c]); }
  bar();
  { f32x4 y[2]; u32 r[2][2];
    #pragma unroll
    for(int e=0;e<2;++e){ lptr q=L+4*e*A_SLOT+A_KA+tid*16;
      y[e]=(lds_ld<f32x4>(q)+lds_ld<f32x4>(q+A_SLOT))+(lds_ld<f32x4>(q+2*A_SLOT)+lds_ld<f32x4>(q+3*A_SLOT));
      u32 u0=lds_ld<u32>(L+e*A_SLOT+(224+256*w+tl)*2), u1=lds_ld<u32>(L+e*A_SLOT+(228+256*w+tl)*2);
      float d=e?bfhi(dx):bflo(dx);
      r[e][0]=fin2(y[e][0],y[e][1],u0,d); r[e][1]=fin2(y[e][2],y[e][3],u1,d); }
    const u32 op=(256*w+tl)*2560+20*j+16;
    ost32(out,op,p_ll(r[0][0],r[1][0])); ost32(out,op+2560,p_hh(r[0][0],r[1][0]));
    ost32(out,op+4*2560,p_ll(r[0][1],r[1][1])); ost32(out,op+5*2560,p_hh(r[0][1],r[1][1])); }
  ST(7);
}

constexpr int B_ST=36864, B_WP=4*B_ST, B_Z=B_WP+6400;
constexpr u32 WS_FLAG=256, WS_P1=4096, WS_R=WS_P1+8*14*16*960, WS_O=WS_R+57344, WS_END=WS_O+128*20*1024;
constexpr u32 SENT=0xFFFFFFFFu;
constexpr int SPIN_MAX=1<<20;
typedef __amdgpu_buffer_rsrc_t rsrc_t;
template<int N> DI void waitv(){ asm volatile("s_waitcnt vmcnt(%0)"::"i"(N):"memory"); }
DI void dma16(const void* sb, u32 voff, lptr l){ u32 keep;
  asm volatile("s_mov_b32 %0, m0\n\ts_mov_b32 m0, %3\n\ts_nop 0\n\tglobal_load_lds_dwordx4 %1, %2\n\ts_mov_b32 m0, %0":"=&s"(keep):"v"(voff),"s"(sb),"s"((u32)(uintptr_t)l):"memory"); }
DI u32x4 ld_sc1(rsrc_t rs,u32 off){ return __builtin_amdgcn_raw_buffer_load_b128(rs,off,0,16); }
DI void st_sc1(rsrc_t rs,u32 off,u32x4 v){ __builtin_amdgcn_raw_buffer_store_b128(v,rs,off,0,16); }
DI u32 umax4(u32x4 v){ return max(max(v.x,v.y),max(v.z,v.w)); }
DI u32x4 canon(u32x4 v){ u32x4 r={min(v.x,SENT-1),min(v.y,SENT-1),min(v.z,SENT-1),min(v.w,SENT-1)}; return r; }

DI void softmax20(const f32x4* z, u32* o){
  float m=fmaxf(fmaxf(z[0][0],z[0][1]),fmaxf(z[0][2],z[0][3]));
  #pragma unroll
  for(int i=1;i<5;++i){ m=fmaxf(fmaxf(m,z[i][0]),z[i][1]); m=fmaxf(fmaxf(m,z[i][2]),z[i][3]); }
  const float c=1.4426950408889634f, mc=-m*c;
  f32x2 e[10]; f32x2 s={0.f,0.f};
  #pragma unroll
  for(int i=0;i<10;++i){ f32x2 t={z[i>>1][(i&1)*2],z[i>>1][(i&1)*2+1]}; t=__builtin_elementwise_fma(t,f32x2{c,c},f32x2{mc,mc});
    e[i]=f32x2{__builtin_amdgcn_exp2f(t.x),__builtin_amdgcn_exp2f(t.y)}; s+=e[i]; }
  const float r=__builtin_amdgcn_rcpf(s.x+s.y);
  #pragma unroll
  for(int i=0;i<10;++i){ f32x2 t=e[i]*r; o[i]=pk_bf16(t.x,t.y); }
}
DI u32x4 pack2(f32x4 a,f32x4 b){ u32x4 r={pk_bf16(a[0],a[1]),pk_bf16(a[2],a[3]),pk_bf16(b[0],b[1]),pk_bf16(b[2],b[3])}; return r; }

template<int C0> DI void features(lptr L,int hl,int lane,const u32x4 (*b)[2],u32 (*ph)[10]){
  const int q=(lane&15)>>2, p=lane&3, gk=lane>>4;
  const u32 sg=(lane&8)?0x80008000u:0u;
  constexpr int IA=(C0==0)?0:2;
  f32x4 z[2][5];
  #pragma unroll
  for(int i=0;i<5;++i){ z[0][i]=f32x4{0,0,0,0}; z[1][i]=f32x4{0,0,0,0}; }
  #pragma unroll
  for(int s=0;s<2;++s){
    lptr ad[2];
    #pragma unroll
    for(int t=0;t<2;++t){ int d=16*(IA+2*s+t)+4*gk-C0-20*(p&1); bool ok=(d>=0)&&(d<20)&&(IA+2*s+t<5);
      ad[t]=ok?(L+B_WP+(hl+(p&1))*800+(d+q)*40):(L+B_Z); }
    #pragma unroll
    for(int i=0;i<5;++i){ u32x4 a=cat(lds_tr(ad[0]+8*i),lds_tr(ad[1]+8*i)); a^=sg;
      z[0][i]=mfma16(a,b[0][s],z[0][i]); z[1][i]=mfma16(a,b[1][s],z[1][i]); }
  }
  softmax20(z[0],ph[0]); softmax20(z[1],ph[1]);
}
DI bool flags_ready(const void* ws,u64 E,int lane){
  const GAS u64* f=(const GAS u64*)((const char*)ws+WS_FLAG);
  for(int it=0;it<SPIN_MAX;++it){
    u64 a=__hip_atomic_load(f+lane,__ATOMIC_RELAXED,__HIP_MEMORY_SCOPE_AGENT), b=__hip_atomic_load(f+64+lane,__ATOMIC_RELAXED,__HIP_MEMORY_SCOPE_AGENT), c=__hip_atomic_load(f+128+lane,__ATOMIC_RELAXED,__HIP_MEMORY_SCOPE_AGENT);
    if(__all(a==E&&b==E&&c==E)) return true;
    __builtin_amdgcn_s_sleep(2); }
  return false; }

DI void wo_stage(lptr L,int off,const u16* wo,int ct,int k0,int nk,int w,int lane){
  const int r=lane>>4, f=(r|(((w>>1)&1)<<2))<<1;
  const char* g=(const char*)wo+(size_t)(k0+4*w)*1280+ct*256; const u32 vo=r*1280+(((lane&15)^f)<<4);
  #pragma unroll
  for(int i=0;i<nk/32;++i) dma16(g+(size_t)i*32*1280,vo,L+off+(w+8*i)*1024);
}
DI void wo_frags(lptr L,int off,int nks,int w,int lane,u32x4* wf){
  const int g=lane>>4,q=(lane&15)>>2,p=lane&3, f=(q|((g&1)<<2))<<1;
  lptr a=L+off+(8*g+q)*256+(((2*w+(p>>1))^f)<<4)+8*(p&1);
  #pragma unroll
  for(int s=0;s<9;++s) if(s<nks) wf[s]=cat(lds_tr(a+s*8192),lds_tr(a+s*8192+1024));
}

template<bool ISQ> DI void role_b(int X,int li,lptr L,const u16* x,const u16* wqkv,const u16* wphi,const u16* wo,u16* out,void* ws){
  const int tid=threadIdx.x, lane=tid&63, w=__builtin_amdgcn_readfirstlane(tid>>6);
  const int rt=4*(X>>1)+(ISQ?(li>>1):(li>>2));
  const int grp=ISQ?(2*(X&1)+(li&1)):(4*(X&1)+(li&3));
  const int me=ISQ?(128+X*8+li):(X*16+li);
#define ROT 5
  const int s0=ISQ?((li*ROT+1)%10):(((li>>2)+(li&3))*ROT)%10;
  const rsrc_t rs=__builtin_amdgcn_make_buffer_rsrc(ws,0,WS_END,0x00020000);
  u32 xo[2], wo_[3];
  #pragma unroll
  for(int i=0;i<2;++i){ int r=(2*w+i)*8+(lane>>3); xo[i]=(u32)(rt*128+r)*1280u+(u32)(((lane&7)^((r>>1)&7))<<4); }
  #pragma unroll
  for(int i=0;i<3;++i){ int c=(w+8*i)*64+lane, r=c/20, pos=c-r*20, sc=pos^(((r>>3)&1)<<1);
    wo_[i]=(u32)r*3840u+(ISQ?(u32)(320*grp+16*sc):(u32)((sc<10?1280:2400)+160*grp+16*sc)); }
  auto issue=[&](int s){ int ss=s+s0; if(ss>=10) ss-=10; lptr d=L+(s&3)*B_ST;
    const char* xb=(const char*)x+ss*128; const char* wb=(const char*)wqkv+(size_t)ss*64*3840;
    dma16(xb,xo[0],d+(2*w)*1024); dma16(xb,xo[1],d+(2*w+1)*1024);
    dma16(wb,wo_[0],d+16384+w*1024); dma16(wb,wo_[1],d+16384+(w+8)*1024);
    if(w<4) dma16(wb,wo_[2],d+16384+(w+16)*1024); };
  {
    constexpr int NB=ISQ?6400:3200; const int h0=ISQ?8*grp:4*grp;
    if(tid*16<NB) dma16((const char*)wphi+h0*800,tid*16,L+B_WP+w*1024);
    if(tid<4) lds_st<u32x4>(L+B_Z+tid*16,u32x4{0,0,0,0}); }
  issue(0); issue(1); issue(2); issue(3);
  {
    const u32x4 sv={SENT,SENT,SENT,SENT};
    if(ISQ){
      #pragma unroll
      for(int i=0;i<5;++i) st_sc1(rs,WS_O+((8*rt+w)*20+5*grp+i)*1024+lane*16,sv);
    } else {
      #pragma unroll
      for(int i=0;i<2;++i){ int c=tid+512*i; int sl=c/60; u32 o=WS_P1+((grp*14+sl)*16+rt)*960+(c-sl*60)*16;
        if(c>=840) o=(c<870&&rt<14)?(WS_R+(grp*84+6*rt)*80+(c-840)*16):0x80000000u;
        st_sc1(rs,o,sv); } } }
  u64 E; asm volatile("s_load_dwordx2 %0, %1, 0x0\n\ts_waitcnt lgkmcnt(0)":"=s"(E):"s"(ws):"memory"); E+=1;
  constexpr int NC=ISQ?5:2;
  const int g=lane>>4, tq=w>>1, hh=w&1;
  int ab[5], bb;
  { const int q=(lane&15)>>2,p=lane&3;
    #pragma unroll
    for(int i=0;i<5;++i){ int n=2*i+(p>>1); int cid=ISQ?(10*hh+n):(5*hh+n+(n>=5?5:0)); ab[i]=16384+(8*g+q)*320+((cid^((g&1)<<1))<<4)+8*(p&1); }
    bb=(32*tq+(lane&15))*128+((g^((lane&15)>>1))<<4); }
  f32x4 acc[5][2];
  #pragma unroll
  for(int i=0;i<5;++i){ acc[i][0]=f32x4{0,0,0,0}; acc[i][1]=f32x4{0,0,0,0}; }
  ST(1);
  u32x4 FA[2][5], FB[2][2];
  #pragma unroll
  for(int s=0;s<10;++s){
    if(s==0){ if(w<4) waitv<15+NC>(); else waitv<12+NC>(); }
    else if(s==1){ if(w<4) waitv<10+NC>(); else waitv<8+NC>(); }
    else if(s<4){ if(w<4) waitv<5+NC>(); else waitv<4+NC>(); }
    else if(s<9){ if(w<4) waitv<5>(); else waitv<4>(); }
    else waitv<0>();
    rbar();
    if(s==0) ST(2);
    SB();
    lptr d=L+(s&3)*B_ST;
    int ss=s+2+s0; if(ss>=10) ss-=10; lptr dn=L+((s+2)&3)*B_ST;
    const char* xb=(const char*)x+ss*128; const char* wb=(const char*)wqkv+(size_t)ss*64*3840;
    const bool pf=(s>=2&&s<8);
    #pragma unroll
    for(int i=0;i<5;++i){
      if(s>0){ acc[i][0]=mfma16(FA[1][i],FB[1][0],acc[i][0]); acc[i][1]=mfma16(FA[1][i],FB[1][1],acc[i][1]); }
      if(i==0){ FB[0][0]=lds_ld<u32x4>(d+bb); FB[0][1]=lds_ld<u32x4>(d+bb+2048); }
      FA[0][i]=cat(lds_tr(d+ab[i]),lds_tr(d+ab[i]+1280));
      if(pf){ if(i<2) dma16(xb,xo[i],dn+(2*w+i)*1024); else if(i<4) dma16(wb,wo_[i-2],dn+16384+(w+8*(i-2))*1024); else if(w<4) dma16(wb,wo_[2],dn+16384+(w+16)*1024); }
      SB(); }
    if(s==4 && tid==0) __hip_atomic_store((GAS u64*)((char*)ws+WS_FLAG)+me,E,__ATOMIC_RELAXED,__HIP_MEMORY_SCOPE_AGENT);
    #pragma unroll
    for(int i=0;i<5;++i){
      acc[i][0]=mfma16(FA[0][i],FB[0][0],acc[i][0]); acc[i][1]=mfma16(FA[0][i],FB[0][1],acc[i][1]);
      if(i==0){ FB[1][0]=lds_ld<u32x4>(d+(bb^64)); FB[1][1]=lds_ld<u32x4>(d+(bb^64)+2048); }
      FA[1][i]=cat(lds_tr(d+ab[i]+10240),lds_tr(d+ab[i]+10240+1280));
      SB(); }
  }
  #pragma unroll
  for(int i=0;i<5;++i){ acc[i][0]=mfma16(FA[1][i],FB[1][0],acc[i][0]); acc[i][1]=mfma16(FA[1][i],FB[1][1],acc[i][1]); }
  ST(3);
  const int T=ISQ?16+li:li; const bool doout=T<20; const int tt64=4*X+T/5, ct=T%5;
  u32x4 wf[20];
  if(!ISQ){
    u32 ph[2][10];
    { u32x4 b[2][2];
      #pragma unroll
      for(int t=0;t<2;++t){ b[t][0]=pack2(acc[0][t],acc[1][t]); b[t][1]=pack2(acc[2][t],acc[3][t]); }
      features<0>(L,2*hh,lane,b,ph); }
    constexpr int PI=2*B_ST, VI=PI+128*352;
    #pragma unroll
    for(int t=0;t<2;++t){ const int tok=32*tq+16*t+(lane&15);
      lptr pp=L+PI+tok*352+(2*hh+(g&1))*80+(g>>1)*40;
      #pragma unroll
      for(int i=0;i<5;++i) lds_st<u32x2>(pp+8*i,u32x2{ph[t][2*i],ph[t][2*i+1]});
      lptr vp=L+VI+tok*224+2*hh*48;
      #pragma unroll
      for(int i=2;i<5;++i){ int dv=16*i+4*g-40; u32x2 v={pk_bf16(acc[i][t][0],acc[i][t][1]),pk_bf16(acc[i][t][2],acc[i][t][3])};
        int o=(dv>=20)?(48+2*(dv-20)):2*dv;
        if(i==2&&g<2){ v=u32x2{0x3F80u,0u}; o=48*g+40; }
        lds_st<u32x2>(vp+o,v); } }
    bar();
    ST(4);
    { const int hd=w>>1, dt=w&1, q=(lane&15)>>2, p=lane&3; const int tk=16*(g>>1)+4*(g&1)+q;
      lptr pa=L+PI+tk*352+hd*80+8*p, vb=L+VI+tk*224+hd*48+dt*32+8*p;
      f32x4 c[3]={f32x4{0,0,0,0},f32x4{0,0,0,0},f32x4{0,0,0,0}};
      #pragma unroll
      for(int ks=0;ks<4;++ks){ u32x4 b=cat(lds_tr(vb+ks*32*224),lds_tr(vb+(ks*32+8)*224));
        #pragma unroll
        for(int i=0;i<3;++i){ u32x4 a=cat(lds_tr(pa+ks*32*352+32*i),lds_tr(pa+(ks*32+8)*352+32*i)); c[i]=mfma16(a,b,c[i]); } }
      const int d=16*dt+(lane&15), R=hd*21+d, sl=R/6;
      const u32 o=WS_P1+((grp*14+sl)*16+rt)*960+(R-6*sl)*160+16*g;
      #pragma unroll
      for(int i=0;i<3;++i) st_sc1(rs,(d<21&&(i<2||g<2))?o+64*i:0x80000000u,canon(__builtin_bit_cast(u32x4,c[i]))); }
    wo_stage(L,0,wo,ct,0,288,w,lane);
    bar();
    wo_stage(L,PI,wo,ct,288,288,w,lane);
    ST(5);
    bool ok=true;
    if(w==0){ ok=flags_ready(ws,E,lane);
      if(me==0&&lane==0) __hip_atomic_store((GAS u64*)ws,E,__ATOMIC_RELAXED,__HIP_MEMORY_SCOPE_AGENT);
      if(rt<14){ const u32 o=(lane<60)?(WS_P1+(grp*14+rt)*16*960+lane*16):0x80000000u; u32x4 v[16];
        for(int it=0;it<SPIN_MAX;++it){ asm volatile("":::"memory"); u32 m=0;
          #pragma unroll
          for(int i=0;i<16;++i) v[i]=ld_sc1(rs,o+i*960);
          #pragma unroll
          for(int i=0;i<16;++i) m=max(m,umax4(v[i]));
          if(__all(m!=SENT)) break; }
        f32x4 sum=__builtin_bit_cast(f32x4,v[0]);
        #pragma unroll
        for(int i=1;i<16;++i) sum+=__builtin_bit_cast(f32x4,v[i]);
        u32x2 r={min(pk_bf16(sum[0],sum[1]),SENT-1),min(pk_bf16(sum[2],sum[3]),SENT-1)};
        __builtin_amdgcn_raw_buffer_store_b64(r,rs,(lane<60)?(WS_R+(grp*84+6*rt)*80+lane*8):0x80000000u,0,16); } }
    ST(6);
    waitv<0>(); rbar(); wo_frags(L,0,9,w,lane,wf); wo_frags(L,PI,9,w,lane,wf+9); bar();
    wo_stage(L,0,wo,ct,576,64,w,lane); waitv<0>(); rbar(); wo_frags(L,0,2,w,lane,wf+18); bar();
  } else {
    u32 ph[2][2][10];
    { u32x4 b[2][2];
      #pragma unroll
      for(int t=0;t<2;++t){ b[t][0]=pack2(acc[0][t],acc[1][t]); b[t][1]=pack2(acc[2][t],acc[3][t]); }
      features<0>(L,4*hh,lane,b,ph[0]);
      #pragma unroll
      for(int t=0;t<2;++t){ b[t][0]=b[t][1]; b[t][1]=pack2(acc[4][t],acc[4][t]); }
      features<40>(L,4*hh+2,lane,b,ph[1]); }
    #pragma unroll
    for(int i=0;i<40;++i) asm volatile("":"+v"(ph[i/20][(i/10)&1][i%10]));
    ST(4);
    if(w==0) flags_ready(ws,E,lane);
    bar();
    if(doout){
      wo_stage(L,0,wo,ct,0,512,w,lane); waitv<0>(); bar(); wo_frags(L,0,8,w,lane,wf); wo_frags(L,65536,8,w,lane,wf+8); bar();
      wo_stage(L,0,wo,ct,512,128,w,lane); waitv<0>(); bar(); wo_frags(L,0,4,w,lane,wf+16); bar(); }
    ST(5);
    { u32x4 v0,v1; const u32 o=WS_R+grp*13440+tid*16;
      for(int it=0;it<SPIN_MAX;++it){ asm volatile("":::"memory");
        v0=ld_sc1(rs,o); v1=ld_sc1(rs,(tid<328)?o+8192:0x80000000u);
        if(__all(max(umax4(v0),umax4(v1))!=SENT)) break; }
      lds_st<u32x4>(L+tid*16,v0); if(tid<328) lds_st<u32x4>(L+8192+tid*16,v1); }
    bar();
    ST(6);
    constexpr int OI=16384;
    #pragma unroll
    for(int ps=0;ps<2;++ps){
      const int gp=(lane&15)>>2, e=lane&3; const bool mt=((gp>>1)==(g&1));
      f32x4 n[2][3];
      #pragma unroll
      for(int i=0;i<3;++i){ int sg=4*i+e; int d=(sg<10)?(10*(gp&1)+sg):20;
        lptr a=mt?(L+((4*hh+2*ps+(gp>>1))*21+d)*80+(g>>1)*40):(L+B_Z);
        u32x4 a0=cat(lds_ld<u32x2>(a),lds_ld<u32x2>(a+8)), a1=cat(lds_ld<u32x2>(a+16),lds_ld<u32x2>(a+24)), a2=cat(lds_ld<u32x2>(a+32),u32x2{0,0});
        #pragma unroll
        for(int t=0;t<2;++t){ const u32* f=ph[ps][t];
          f32x4 c=mfma16(a0,u32x4{f[0],f[1],f[2],f[3]},f32x4{0,0,0,0}); c=mfma16(a1,u32x4{f[4],f[5],f[6],f[7]},c); n[t][i]=mfma16(a2,u32x4{f[8],f[9],0,0},c); } }
      #pragma unroll
      for(int t=0;t<2;++t){ const float den=rbf(rbf(n[t][2][2])+1e-6f), r=__builtin_amdgcn_rcpf(den);
        lptr op=L+OI+(32*tq+16*t+(lane&15))*336+((4*hh+2*ps+(g>>1))*20+10*(g&1))*2;
        #pragma unroll
        for(int i=0;i<5;++i){ u32 nb=pk_bf16(n[t][i>>1][(i&1)*2],n[t][i>>1][(i&1)*2+1]); lds_st<u32>(op+4*i,pk_bf16(bflo(nb)*r,bfhi(nb)*r)); } } }
    bar();
    #pragma unroll
    for(int i=0;i<5;++i) st_sc1(rs,WS_O+((8*rt+w)*20+5*grp+i)*1024+lane*16,canon(lds_ld<u32x4>(L+OI+(16*w+(lane&15))*336+64*i+16*g)));
    ST(7);
    bar();
  }
  if(!doout) return;
  ST(8);
  { u32x4 v[10]; const u32 o=WS_O+tt64*81920+w*10240+lane*16;
    for(int it=0;it<SPIN_MAX;++it){ asm volatile("":::"memory"); u32 m=0;
      #pragma unroll
      for(int i=0;i<10;++i) v[i]=ld_sc1(rs,o+i*1024);
      #pragma unroll
      for(int i=0;i<10;++i) m=max(m,umax4(v[i]));
      if(__all(m!=SENT)) break; }
    #pragma unroll
    for(int i=0;i<10;++i) lds_st<u32x4>(L+w*10240+i*1024+lane*16,v[i]); }
  bar();
  ST(9);
  { f32x4 c[4]={f32x4{0,0,0,0},f32x4{0,0,0,0},f32x4{0,0,0,0},f32x4{0,0,0,0}};
    u32x4 ob[2][4];
    #pragma unroll
    for(int t=0;t<4;++t) ob[0][t]=lds_ld<u32x4>(L+(t*20)*1024+lane*16);
    SB();
    #pragma unroll
    for(int ks=0;ks<20;++ks){
      #pragma unroll
      for(int t=0;t<4;++t){ if(ks<19) ob[(ks+1)&1][t]=lds_ld<u32x4>(L+(t*20+ks+1)*1024+lane*16); c[t]=mfma16(wf[ks],ob[ks&1][t],c[t]); }
      SB(); }
    constexpr int CI=81920;
    #pragma unroll
    for(int t=0;t<4;++t) lds_st<u32x2>(L+CI+(16*t+(lane&15))*272+w*32+8*g,u32x2{pk_bf16(c[t][0],c[t][1]),pk_bf16(c[t][2],c[t][3])});
    bar();
    #pragma unroll
    for(int i=0;i<2;++i){ const int r=32*i+(tid>>4);
      ost128(out,(64*tt64+r)*2560+1280+ct*256+(tid&15)*16,lds_ld<u32x4>(L+CI+r*272+(tid&15)*16)); } }
  ST(10);
}

__global__ __launch_bounds__(512,2) void fused(const u16* u,const u16* are,const u16* aim,const u16* bre,const u16* bim,const u16* cre,const u16* cim,
    const u16* logdt,const u16* dskip,const u16* x,const u16* wqkv,const u16* wphi,const u16* wo,u16* out,void* ws){
  extern __shared__ __attribute__((aligned(16))) char smem[];
  lptr L=(lptr)smem;
  const int b=blockIdx.x, X=b&7, idx=b>>3;
  ST(0);
  if(idx<16) role_b<false>(X,idx,L,x,wqkv,wphi,wo,out,ws);
  else if(idx<24){
    const u16* pp[6]={are,aim,bre,bim,cre,cim}; role_a(X*8+(idx-16),L,u,pp,logdt,dskip,wo,out,ws);
  }
  else role_b<true>(X,idx-24,L,x,wqkv,wphi,wo,out,ws);
}

extern "C" void kernel_launch(void* const* d_in, const int* in_sizes, int n_in, void* d_out, int out_size, void* d_ws, size_t ws_size, hipStream_t stream){
  (void)hipFuncSetAttribute((const void*)fused,hipFuncAttributeMaxDynamicSharedMemorySize,163840);
  fused<<<256,512,163840,stream>>>((const u16*)d_in[0],(const u16*)d_in[1],(const u16*)d_in[2],(const u16*)d_in[3],(const u16*)d_in[4],(const u16*)d_in[5],(const u16*)d_in[6],
    (const u16*)d_in[7],(const u16*)d_in[8],(const u16*)d_in[9],(const u16*)d_in[10],(const u16*)d_in[11],(const u16*)d_in[12],(u16*)d_out,d_ws);
}
```

```cpp
#define ROT 2
#include <hip/hip_runtime.h>
#include <stdint.h>

typedef unsigned short u16; typedef unsigned u32; typedef unsigned long long u64;
typedef __attribute__((ext_vector_type(4))) short s16x4;
typedef __attribute__((ext_vector_type(8))) _Float16 f16x8;
typedef __attribute__((ext_vector_type(8))) __bf16 bf16x8;
typedef __attribute__((ext_vector_type(2))) __bf16 bf16x2;
typedef __attribute__((ext_vector_type(2))) _Float16 f16x2;
typedef __attribute__((ext_vector_type(2))) float f32x2;
typedef __attribute__((ext_vector_type(4))) float f32x4;
typedef __attribute__((ext_vector_type(16))) float f32x16;
typedef __attribute__((ext_vector_type(2))) unsigned u32x2;
typedef __attribute__((ext_vector_type(3))) unsigned u32x3;
typedef __attribute__((ext_vector_type(4))) unsigned u32x4;
#define LAS __attribute__((address_space(3)))
#define GAS __attribute__((address_space(1)))
#define DI __device__ __forceinline__
typedef LAS char* lptr;
typedef __amdgpu_buffer_rsrc_t rsrc_t;

#define ST(i) do{}while(0)
#define STDUMP() do{}while(0)
#define STINIT() do{}while(0)

DI u32 pk_bf16(float a, float b){ f32x2 v={a,b}; return __builtin_bit_cast(u32,__builtin_convertvector(v,bf16x2)); }
DI u32 pk_f16(f32x2 v){ return __builtin_bit_cast(u32,__builtin_convertvector(v,f16x2)); }
DI float bflo(u32 x){ return __builtin_bit_cast(float,x<<16); }
DI float bfhi(u32 x){ return __builtin_bit_cast(float,x&0xffff0000u); }
DI float rbf(float a){ return bflo(pk_bf16(a,a)); }
template<class T> DI T lds_ld(lptr p){ return *(LAS T*)p; }
template<class T> DI void lds_st(lptr p, T v){ *(LAS T*)p=v; }
DI u32x2 lds_ld64(lptr p){ return *(volatile LAS u32x2*)p; }
DI u32x2 lds_tr(lptr p){ return __builtin_bit_cast(u32x2,__builtin_amdgcn_ds_read_tr16_b64_v4i16((LAS s16x4*)p)); }
DI f32x4 mfma16(u32x4 a,u32x4 b,f32x4 c){ return __builtin_amdgcn_mfma_f32_16x16x32_bf16(__builtin_bit_cast(bf16x8,a),__builtin_bit_cast(bf16x8,b),c,0,0,0); }
DI u32x4 cat(u32x2 a,u32x2 b){ u32x4 r={a.x,a.y,b.x,b.y}; return r; }
DI u32 p_ll(u32 a,u32 b){ return __builtin_amdgcn_perm(b,a,0x05040100u); }
DI u32 p_lh(u32 a,u32 b){ return __builtin_amdgcn_perm(b,a,0x07060100u); }
DI u32 p_hl(u32 a,u32 b){ return __builtin_amdgcn_perm(b,a,0x05040302u); }
DI u32 p_hh(u32 a,u32 b){ return __builtin_amdgcn_perm(b,a,0x07060302u); }
#define SB() __builtin_amdgcn_sched_barrier(0)
DI void rbar(){ asm volatile("":::"memory"); __builtin_amdgcn_s_barrier(); asm volatile("":::"memory"); }
DI void bar(){ asm volatile("s_waitcnt lgkmcnt(0)":::"memory"); __builtin_amdgcn_s_barrier(); asm volatile("":::"memory"); }
template<int N> DI void waitv(){ asm volatile("s_waitcnt vmcnt(%0)"::"i"(N):"memory"); }
constexpr f32x4 Z4={0.f,0.f,0.f,0.f};
DI float wave_max(float m){
  m=fmaxf(m,__builtin_bit_cast(float,__builtin_amdgcn_update_dpp(0,__builtin_bit_cast(int,m),0xB1,0xf,0xf,true)));
  m=fmaxf(m,__builtin_bit_cast(float,__builtin_amdgcn_update_dpp(0,__builtin_bit_cast(int,m),0x4E,0xf,0xf,true)));
  m=fmaxf(m,__builtin_bit_cast(float,__builtin_amdgcn_update_dpp(0,__builtin_bit_cast(int,m),0x141,0xf,0xf,true)));
  m=fmaxf(m,__builtin_bit_cast(float,__builtin_amdgcn_update_dpp(0,__builtin_bit_cast(int,m),0x140,0xf,0xf,true)));
  int x=__builtin_bit_cast(int,m);
  float a=__builtin_bit_cast(float,__builtin_amdgcn_readlane(x,0)), b=__builtin_bit_cast(float,__builtin_amdgcn_readlane(x,16));
  float c=__builtin_bit_cast(float,__builtin_amdgcn_readlane(x,32)), d=__builtin_bit_cast(float,__builtin_amdgcn_readlane(x,48));
  return fmaxf(fmaxf(a,b),fmaxf(c,d)); }

constexpr u32 WS_FLAG=256, WS_KP=8192, WS_P1=WS_KP+768*1536, WS_R=WS_P1+32*16*3584, WS_O=WS_R+57344, WS_END=WS_O+128*20*1024;
constexpr u32 SENT=0xFFFFFFFFu, OOB=0x80000000u;
constexpr int SPIN_MAX=1<<17;
DI void dma16(const void* sb, u32 voff, lptr l){ u32 keep;
  asm volatile("s_mov_b32 %0, m0\n\ts_mov_b32 m0, %3\n\ts_nop 0\n\tglobal_load_lds_dwordx4 %1, %2\n\ts_mov_b32 m0, %0":"=&s"(keep):"v"(voff),"s"(sb),"s"((u32)(uintptr_t)l):"memory"); }
DI u32x4 ld_sc1(rsrc_t rs,u32 off){ return __builtin_amdgcn_raw_buffer_load_b128(rs,off,0,16); }
DI void st_sc1(rsrc_t rs,u32 off,u32x4 v){ __builtin_amdgcn_raw_buffer_store_b128(v,rs,off,0,16); }
template<int N,int SI> DI void gather(lptr L,rsrc_t rs,u32 off,u32 stride,u32x4* v,u32 lastoff,u32 sample){
  for(int it=0;it<SPIN_MAX;++it){ asm volatile("":::"memory");
    if(__all(__builtin_amdgcn_raw_buffer_load_b32(rs,sample,0,16)!=SENT)) break;
    __builtin_amdgcn_s_sleep(1); }
  ST(SI);
  for(int it=0;it<SPIN_MAX;++it){ asm volatile("":::"memory"); u32 m=0;
    #pragma unroll
    for(int i=0;i<N;++i) v[i]=ld_sc1(rs,(i==N-1)?lastoff:off+i*stride);
    #pragma unroll
    for(int i=0;i<N;++i) m=max(m,max(v[i].x,v[i].z));
    if(__all(m!=SENT)) break; } }
DI u64 epoch(const void* ws){ u64 E; asm volatile("s_load_dwordx2 %0, %1, 0x0\n\ts_waitcnt lgkmcnt(0)":"=s"(E):"s"(ws):"memory"); return E+1; }
DI void flag_set(void* ws,int i,u64 E){ __hip_atomic_store((GAS u64*)((char*)ws+WS_FLAG)+i,E,__ATOMIC_RELAXED,__HIP_MEMORY_SCOPE_AGENT); }
DI u64 flag_get(const void* ws,int i){ return __hip_atomic_load((const GAS u64*)((const char*)ws+WS_FLAG)+i,__ATOMIC_RELAXED,__HIP_MEMORY_SCOPE_AGENT); }
DI void flag_wait(const void* ws,u64 E,int idx,u64 f){
  for(int it=0;it<SPIN_MAX;++it){ if(__all(f==E)) break; __builtin_amdgcn_s_sleep(2); f=flag_get(ws,idx); } }

constexpr int A_UB=4576, A_US=9120, A_K0=10*A_US, A_KB=4224, A_KS=8704;
static_assert(A_K0+8*A_KS<=163840,"lds");

DI f32x2 cmul(f32x2 a, f32x2 b){ f32x2 sw={-b.y,b.x}; return __builtin_elementwise_fma(a.yy,sw,a.xx*b); }
DI f32x2 cexp_e(float re2, float rev, float e){
  float m=__builtin_amdgcn_exp2f(re2*e); float a=__builtin_amdgcn_fractf(rev*e);
  f32x2 r={m*__builtin_amdgcn_cosf(a), m*__builtin_amdgcn_sinf(a)}; return r; }
struct SsmP { u32 are,aim,bre,bim,cre,cim; float dt; };
struct SsmIn { const u16 *are,*aim,*bre,*bim,*cre,*cim,*logdt; };
DI SsmP load_ssm(const SsmIn& in, int ch, int lane){
  SsmP p; int o=ch*64+lane;
  p.are=((const u32*)in.are)[o]; p.aim=((const u32*)in.aim)[o]; p.bre=((const u32*)in.bre)[o];
  p.bim=((const u32*)in.bim)[o]; p.cre=((const u32*)in.cre)[o]; p.cim=((const u32*)in.cim)[o];
  p.dt=__builtin_bit_cast(float,(u32)in.logdt[ch]<<16);
  return p; }
DI void pin(SsmP& p){ asm volatile("":"+v"(p.dt),"+v"(p.are),"+v"(p.aim),"+v"(p.bre),"+v"(p.bim),"+v"(p.cre),"+v"(p.cim)::"memory"); }
DI f32x2 ssm_w(const SsmP& p, int half){
  f32x2 b={half?bfhi(p.bre):bflo(p.bre), half?bfhi(p.bim):bflo(p.bim)};
  f32x2 c={half?bfhi(p.cre):bflo(p.cre), half?bfhi(p.cim):bflo(p.cim)};
  return cmul(c,b*p.dt); }
DI void ssm_prep(SsmP& p, float& s, float& inv){
  p.dt=rbf(expf(p.dt));
  f32x2 w0=ssm_w(p,0), w1=ssm_w(p,1);
  float m=wave_max(fmaxf(fmaxf(fabsf(w0.x),fabsf(w0.y)),fmaxf(fabsf(w1.x),fabsf(w1.y))));
  int e=__builtin_amdgcn_frexp_expf(m); e=max(-100,min(100,e));
  s=__builtin_amdgcn_ldexpf(1.0f,-e); inv=__builtin_amdgcn_ldexpf(1.0f,e); }
DI void kgen_half(const SsmP& p, int half, float s, lptr tab, int lane, f32x16& acc){
  const float are=(half?bfhi(p.are):bflo(p.are))*p.dt, aim=(half?bfhi(p.aim):bflo(p.aim))*p.dt;
  const float re2=are*1.4426950408889634f, rev=aim*0.15915494309189535f;
  lptr wr=tab+lane*4; lptr rd=tab+(lane&31)*272+(lane>>5)*16;
  { f32x2 e8=cexp_e(re2,rev,8.f), e16=cmul(e8,e8);
    f32x2 a={1.f,0.f}, b=e8;
    #pragma unroll
    for(int j=0;j<32;j+=2){ lds_st<u32>(wr+j*272,pk_f16(a)); lds_st<u32>(wr+(j+1)*272,pk_f16(b)); if(j<30){ a=(j==0)?e16:cmul(a,e16); b=cmul(b,e16);} } }
  u32x4 bf[8];
  #pragma unroll
  for(int k=0;k<8;++k) bf[k]=lds_ld<u32x4>(rd+k*32);
  { f32x2 w=ssm_w(p,half)*s; w.y=-w.y;
    f32x2 e1=cexp_e(re2,-rev,1.f), g=cexp_e(re2,-rev,256.f);
    f32x2 g2=cmul(g,g), g3=cmul(g2,g), g4=cmul(g2,g2);
    f32x2 x[8]; x[0]=w;
    #pragma unroll
    for(int i=1;i<8;++i) x[i]=cmul(x[i-1],e1);
    #pragma unroll
    for(int i=0;i<8;++i) lds_st<u32>(wr+i*272,pk_f16(x[i]));
    #pragma unroll
    for(int i=0;i<4;++i) lds_st<u32>(wr+(8+i)*272,pk_f16(cmul(x[2*i],g)));
    lds_st<u32>(wr+12*272,pk_f16(cmul(x[0],g2))); lds_st<u32>(wr+13*272,pk_f16(cmul(x[4],g2)));
    lds_st<u32>(wr+14*272,pk_f16(cmul(x[0],g3))); lds_st<u32>(wr+15*272,pk_f16(cmul(x[4],g3)));
    f32x2 y=cmul(w,g4);
    #pragma unroll
    for(int i=0;i<5;++i){ lds_st<u32>(wr+(16+i)*272,pk_f16(y)); if(i<4) y=cmul(y,g); } }
  #pragma unroll
  for(int k=0;k<8;++k){ u32x4 af=lds_ld<u32x4>(rd+k*32);
    acc=__builtin_amdgcn_mfma_f32_32x32x16_f16(__builtin_bit_cast(f16x8,af),__builtin_bit_cast(f16x8,bf[k]),acc,0,0,0); }
}
DI void k_finish(const float* v, lptr ka, int kbo, int lane){
  lptr ks=ka; lptr kb=ka+kbo;
  { const int j=lane&31, h=lane>>5;
    lds_st<u32x2>(ks+(8*j+4*h)*2,u32x2{pk_bf16(v[0],v[1]),pk_bf16(v[2],v[3])});
    u32x2 b={pk_bf16(v[4],v[5]),pk_bf16(v[6],v[7])};
    u32 c0=pk_bf16(v[8],v[9]), c1=pk_bf16(v[10],v[11]);
    if(h==0){ lds_st<u32x2>(ks+(256+4*j)*2,b);
      lds_st<u16>(ks+(512+j)*2,(u16)c0); lds_st<u16>(ks+(544+j)*2,(u16)(c0>>16));
      lds_st<u16>(ks+(576+j)*2,(u16)c1); lds_st<u16>(ks+(608+j)*2,(u16)(c1>>16));
    } else { lds_st<u32>(ks+(384+2*j)*2,b.x); lds_st<u32>(ks+(448+2*j)*2,b.y); if(j==0) lds_st<u16>(ks+640*2,(u16)c0); } }
  const int M=lane; const bool lo=M<32;
  u32x4 v0=lds_ld<u32x4>(ks+(M&31)*16); u32x2 v1=lds_ld<u32x2>(ks+(128+4*(M|32))*2);
  u32 v4=lds_ld<u16>(ks+(132+4*(M|32))*2);
  u32 pv=lds_ld<u16>(ks+(lo?max(8*M-1,0):((M==32)?255:(128+4*M)))*2); if(M==0) pv=0;
  u32 ab=lds_ld<u32>(ks+(384+2*M)*2), c2=lds_ld<u16>(ks+(386+2*M)*2);
  u32 a3[2],b3[2];
  #pragma unroll
  for(int i=0;i<2;++i){ a3[i]=lds_ld<u16>(ks+(512+M+64*i)*2); b3[i]=lds_ld<u16>(ks+(513+M+64*i)*2); }
  asm volatile("":"+v"(v0),"+v"(v1),"+v"(v4),"+v"(pv),"+v"(ab),"+v"(c2),"+v"(a3[0]),"+v"(a3[1]),"+v"(b3[0]),"+v"(b3[1])::"memory");
  { u32x4 A,B;
    if(lo){ B.x=p_hl(v0.w,v0.w); B.y=p_hl(v0.z,v0.z); B.z=p_hl(v0.y,v0.y); B.w=p_hl(v0.x,v0.x);
      A.x=p_lh(v0.w,v0.z); A.y=p_lh(v0.z,v0.y); A.z=p_lh(v0.y,v0.x); A.w=p_ll(v0.x,pv);
    } else { B.x=p_lh(v4,v1.y); B.y=p_ll(v1.y,v1.y); B.z=p_lh(v1.y,v1.x); B.w=p_ll(v1.x,v1.x);
      A.x=p_hl(v1.y,v1.y); A.y=B.y; A.z=p_hl(v1.x,v1.x); A.w=p_ll(v1.x,pv); }
    lds_st<u32x4>(ka+(255-M)*16,A); lds_st<u32x4>(kb+(255-M)*16,B); }
  { u32x4 A,B; B.x=p_ll(c2,c2); B.y=p_hh(ab,ab); B.z=p_hl(ab,ab); B.w=p_ll(ab,ab);
    A.x=p_lh(c2,ab); A.y=B.y; A.z=B.w; A.w=B.w;
    lds_st<u32x4>(ka+(191-M)*16,A); lds_st<u32x4>(kb+(191-M)*16,B); }
  #pragma unroll
  for(int i=0;i<2;++i){ u32 a=a3[i], b=b3[i], c=(M&1)?b:a; u32x4 A,B;
    B.x=p_ll(b,b); B.y=p_ll(b,c); B.z=p_ll(a,a); B.w=B.z;
    A.x=B.x; A.y=p_ll(c,a); A.z=B.z; A.w=B.z;
    lds_st<u32x4>(ka+(127-64*i-M)*16,A); lds_st<u32x4>(kb+(127-64*i-M)*16,B); }
  if(lane<8) lds_st<u32x4>(((lane&4)?kb:ka)+(256+(lane&3))*16,u32x4{0,0,0,0});
}
DI void k_receive(void* ws, u64 E, int c, lptr ka, int kbo, int lane){
  const rsrc_t rs=__builtin_amdgcn_make_buffer_rsrc(ws,0,WS_END,0x00020000);
  for(int it=0;it<SPIN_MAX;++it){ if(__builtin_amdgcn_readfirstlane(flag_get(ws,256+(c>>1))==E?1:0)) break; __builtin_amdgcn_s_sleep(2); }
  asm volatile("":::"memory");
  const u32 o=WS_KP+c*3072;
  u32x4 a0=ld_sc1(rs,o+lane*16), a1=ld_sc1(rs,o+1536+lane*16);
  u32x2 b0=__builtin_amdgcn_raw_buffer_load_b64(rs,o+1024+lane*8,0,16), b1=__builtin_amdgcn_raw_buffer_load_b64(rs,o+2560+lane*8,0,16);
  const float inv=__builtin_bit_cast(float,__builtin_amdgcn_readlane(b0.y,63));
  const u32 x[6]={a0.x,a0.y,a0.z,a0.w,b0.x,b0.y}, y[6]={a1.x,a1.y,a1.z,a1.w,b1.x,b1.y};
  float v[12];
  #pragma unroll
  for(int r=0;r<6;++r){ f32x2 t=(__builtin_convertvector(__builtin_bit_cast(f16x2,x[r]),f32x2)+__builtin_convertvector(__builtin_bit_cast(f16x2,y[r]),f32x2))*inv; v[2*r]=t.x; v[2*r+1]=t.y; }
  k_finish(v,ka,kbo,lane); }
template<int NS> DI void conv_steps(lptr ua, lptr ka, int kbo, int s0, int lane, f32x4* acc){
  const int a=lane&15, g=lane>>4;
  lptr up=ua+((a&1)?0:A_UB)+(32*(a>>1)+8*g+32*s0)*2;
  lptr kp=ka+((a&1)?kbo:0)+(28-4*(a>>1)+8*g+32*s0)*2;
  u32x4 A[2][8],B[2][8];
  auto ldA=[&](int s,int q){ A[s&1][q]=cat(lds_ld64(kp+(1792-256*q+32*s)*2),lds_ld64(kp+(1792-256*q+32*s)*2+8)); };
  auto ldB=[&](int s,int q){ B[s&1][q]=lds_ld<u32x4>(up+(256*q+32*s)*2); };
  #pragma unroll
  for(int q=0;q<8;++q){ ldB(0,q); ldA(0,q); }
  #pragma unroll
  for(int c=0;c<8;++c) acc[c]=Z4;
  SB();
  #pragma unroll
  for(int s=0;s<NS;++s){
    #pragma unroll
    for(int q=0;q<8;++q){
      #pragma unroll
      for(int c=q;c<8;++c) acc[c]=mfma16(A[s&1][q],B[s&1][c-q],acc[c]);
      if(s+1<NS){ if(q==0){ ldB(s+1,0); ldB(s+1,1); ldB(s+1,2); ldB(s+1,3); } else if(q==1){ ldA(s+1,0); ldB(s+1,4); ldB(s+1,5); }
        else if(q==2){ ldB(s+1,6); ldB(s+1,7); ldA(s+1,1); } else if(q==3){ ldA(s+1,2); ldA(s+1,3); } else if(q==4){ ldA(s+1,4); ldA(s+1,5); }
        else if(q==5){ ldA(s+1,6); } else if(q==6){ ldA(s+1,7); } }
      SB(); } }
}
DI u32 fin2(float y0,float y1,u32 u,float dsk){
  u32 y=pk_bf16(y0,y1), s=pk_bf16(bflo(u)*dsk,bfhi(u)*dsk);
  return pk_bf16(bflo(y)+bflo(s),bfhi(y)+bfhi(s)); }
DI void a_store4(lptr dump, lptr ua, u32x2 dk, rsrc_t ro, u32 col, int p, int lane){
  const int a=lane&15, g=lane>>4, tl=32*(a>>1)+8*g+2*(a&1);
  #pragma unroll
  for(int i=0;i<2;++i){ const int c=2*p+i; u32 r[4][2];
    #pragma unroll
    for(int e=0;e<4;++e){ f32x4 v=lds_ld<f32x4>(dump+e*A_KS+c*1024+lane*16); lptr y=ua+e*A_US+(224+256*c+tl)*2;
      const u32 dd=(e<2)?dk.x:dk.y; const float d=(e&1)?bfhi(dd):bflo(dd);
      r[e][0]=fin2(v[0],v[1],lds_ld<u32>(y),d); r[e][1]=fin2(v[2],v[3],lds_ld<u32>(y+8),d); }
    const u32 o=(256*c+tl)*2560+col;
    #pragma unroll
    for(int h=0;h<2;++h){
      __builtin_amdgcn_raw_buffer_store_b64(u32x2{p_ll(r[0][h],r[1][h]),p_ll(r[2][h],r[3][h])},ro,o+4*h*2560,0,0);
      __builtin_amdgcn_raw_buffer_store_b64(u32x2{p_hh(r[0][h],r[1][h]),p_hh(r[2][h],r[3][h])},ro,o+(4*h+1)*2560,0,0); } } }

DI void role_a(int j, lptr L, const u16* u, const SsmIn& in, const u16* dskip, const u16* wo, u16* out, void* ws){
  const int tid=threadIdx.x, lane=tid&63, w=__builtin_amdgcn_readfirstlane(tid>>6), p=w&3;
  const int ch0=10*j;
  const rsrc_t ro=__builtin_amdgcn_make_buffer_rsrc(out,0,2048*2560,0x00020000);
  lptr k0=L+A_K0+p*A_KS, k1=k0+4*A_KS;
  constexpr int XKB=A_US+224;
  SsmP p0=load_ssm(in,ch0+p,lane);
  u32 dk[5];
  #pragma unroll
  for(int i=0;i<5;++i) dk[i]=((const u32*)(dskip+ch0))[i];
  rbar();
  u32x4 um[4];
  { typedef u32x4 __attribute__((aligned(4))) u32x4u;
    const char* up=(const char*)u+(size_t)tid*4*1280+20*j;
    #pragma unroll
    for(int r=0;r<4;++r) um[r]=*(const u32x4u*)(up+r*1280); }
  u32 pf[2];
  { const char* wp=(const char*)wo+(j&7)*102400+tid*128; pf[0]=*(const u32*)wp; pf[1]=*(const u32*)(wp+((tid<288)?65536:0)); }
  for(int i=tid;i<570;i+=512){ int sl=i/57, c=i-sl*57; lds_st<u32x4>(L+sl*A_US+(c<28?c*16:A_UB+(c-28)*16),u32x4{0,0,0,0}); }
  const u64 E=epoch(ws);
  ST(1);
  { float s,inv; ssm_prep(p0,s,inv); f32x16 acc={};
    kgen_half(p0,w>>2,s,(w<4)?k0:k1,lane,acc);
    if(w<4){
      #pragma unroll
      for(int r=0;r<3;++r) lds_st<f32x4>(k0+r*1024+lane*16,f32x4{acc[4*r],acc[4*r+1],acc[4*r+2],acc[4*r+3]}); }
    bar();
    ST(2);
    if(w>=4){ float v[12];
      #pragma unroll
      for(int r=0;r<3;++r){ f32x4 o=lds_ld<f32x4>(k0+r*1024+lane*16);
        #pragma unroll
        for(int e=0;e<4;++e) v[4*r+e]=(acc[4*r+e]+o[e])*inv; }
      k_finish(v,k0,A_KB,lane); } }
  auto scatter=[&](lptr d,u32 r0,u32 r1,u32 r2,u32 r3,bool last){
    u32x2 e={p_ll(r0,r1),p_ll(r2,r3)}, o={p_hh(r0,r1),p_hh(r2,r3)};
    lds_st<u32x2>(d,e); lds_st<u32x2>(d+A_US,o);
    lds_st<u32>(d+A_UB+4,e.x); lds_st<u32>(d+A_US+A_UB+4,o.x);
    if(!last){ lds_st<u32>(d+A_UB+8,e.y); lds_st<u32>(d+A_US+A_UB+8,o.y); } };
  #pragma unroll
  for(int c=0;c<4;++c) scatter(L+2*c*A_US+(224+4*tid)*2,um[0][c],um[1][c],um[2][c],um[3][c],tid==511);
  asm volatile(""::"v"(pf[0]),"v"(pf[1]));
  bar();
  ST(3);
  if(w<4){
    f32x4 acc[8];
    conv_steps<8>(L+p*A_US,k0,A_KB,0,lane,acc);
    #pragma unroll
    for(int c=0;c<8;++c) lds_st<f32x4>(k0+c*1024+lane*16,acc[c]);
    ST(4);
    bar();
    conv_steps<8>(L+(p+4)*A_US,k1,A_KB,0,lane,acc);
    #pragma unroll
    for(int c=0;c<8;++c) lds_st<f32x4>(k1+c*1024+lane*16,acc[c]);
    ST(5);
    bar();
    conv_steps<4>(L+(8+(p>>1))*A_US,L+(p&2)*A_US+A_UB,XKB,4*(p&1),lane,acc);
    #pragma unroll
    for(int c=0;c<8;++c) lds_st<f32x4>(k0+c*1024+lane*16,acc[c]);
    ST(6);
    bar();
  } else {
    if(tid==256) flag_set(ws,192+j,E);
    k_receive(ws,E,6*j+p,k1,A_KB,lane);
    u32 ux[8];
    { const char* up=(const char*)u+(size_t)(tid-256)*4*1280+20*j+16;
      #pragma unroll
      for(int r=0;r<8;++r) ux[r]=*(const u32*)(up+(size_t)((r&3)+(r>>2)*1024)*1280); }
    ST(4);
    bar();
    if(p<2) k_receive(ws,E,6*j+4+p,L+2*p*A_US+A_UB,XKB,lane);
    #pragma unroll
    for(int h=0;h<2;++h) scatter(L+8*A_US+(224+4*(tid-256)+1024*h)*2,ux[4*h],ux[4*h+1],ux[4*h+2],ux[4*h+3],h==1&&tid==511);
    a_store4(L+A_K0,L,u32x2{dk[0],dk[1]},ro,20*j,p,lane);
    ST(5);
    bar();
    a_store4(L+A_K0+4*A_KS,L+4*A_US,u32x2{dk[2],dk[3]},ro,20*j+8,p,lane);
    ST(6);
    bar();
  }
  {
    const int a=lane&15, g=lane>>4, tl=256*w+32*(a>>1)+8*g+2*(a&1); u32 r[2][2];
    #pragma unroll
    for(int e=0;e<2;++e){ lptr q=L+A_K0+2*e*A_KS+tid*16; f32x4 y=lds_ld<f32x4>(q)+lds_ld<f32x4>(q+A_KS);
      lptr up=L+(8+e)*A_US+(224+tl)*2; const float d=e?bfhi(dk[4]):bflo(dk[4]);
      r[e][0]=fin2(y[0],y[1],lds_ld<u32>(up),d); r[e][1]=fin2(y[2],y[3],lds_ld<u32>(up+8),d); }
    const u32 o=tl*2560+20*j+16;
    __builtin_amdgcn_raw_buffer_store_b32(p_ll(r[0][0],r[1][0]),ro,o,0,0); __builtin_amdgcn_raw_buffer_store_b32(p_hh(r[0][0],r[1][0]),ro,o+2560,0,0);
    __builtin_amdgcn_raw_buffer_store_b32(p_ll(r[0][1],r[1][1]),ro,o+4*2560,0,0); __builtin_amdgcn_raw_buffer_store_b32(p_hh(r[0][1],r[1][1]),ro,o+5*2560,0,0); }
  ST(7); STDUMP();
}

constexpr int B_ST=36864, B_PV=2*B_ST, B_PVS=18432, B_WP=4*B_ST, B_Z=B_WP+6400, B_F=B_Z+64, B_X=B_F+32;
DI float max3(float a,float b,float c){ return __builtin_fmaxf(__builtin_fmaxf(a,b),c); }
DI void softmax20x2(const f32x4 (*z)[5], u32 (*o)[10]){
  const float c=1.4426950408889634f; float mc[2];
  { float m0[2],m1[2],m2[2];
    #pragma unroll
    for(int t=0;t<2;++t){ m0[t]=max3(z[t][0][0],z[t][0][1],z[t][0][2]); m1[t]=max3(z[t][0][3],z[t][1][0],z[t][1][1]); m2[t]=max3(z[t][1][2],z[t][1][3],z[t][2][0]); }
    #pragma unroll
    for(int t=0;t<2;++t){ m0[t]=max3(m0[t],z[t][2][1],z[t][2][2]); m1[t]=max3(m1[t],z[t][2][3],z[t][3][0]); m2[t]=max3(m2[t],z[t][3][1],z[t][3][2]); }
    #pragma unroll
    for(int t=0;t<2;++t){ m0[t]=max3(m0[t],z[t][3][3],z[t][4][0]); m1[t]=max3(m1[t],z[t][4][1],z[t][4][2]); m2[t]=__builtin_fmaxf(m2[t],z[t][4][3]); }
    #pragma unroll
    for(int t=0;t<2;++t) mc[t]=max3(m0[t],m1[t],m2[t])*-c; }
  SB();
  f32x2 e[2][10];
  #pragma unroll
  for(int i=0;i<10;++i)
    #pragma unroll
    for(int t=0;t<2;++t) e[t][i]=__builtin_elementwise_fma(f32x2{z[t][i>>1][(i&1)*2],z[t][i>>1][(i&1)*2+1]},f32x2{c,c},f32x2{mc[t],mc[t]});
  SB();
  #pragma unroll
  for(int i=0;i<10;++i)
    #pragma unroll
    for(int t=0;t<2;++t) e[t][i]=f32x2{__builtin_amdgcn_exp2f(e[t][i].x),__builtin_amdgcn_exp2f(e[t][i].y)};
  SB();
  float r[2];
  { f32x2 s[2][5];
    #pragma unroll
    for(int i=0;i<5;++i)
      #pragma unroll
      for(int t=0;t<2;++t) s[t][i]=e[t][2*i]+e[t][2*i+1];
    #pragma unroll
    for(int t=0;t<2;++t){ s[t][0]+=s[t][1]; s[t][2]+=s[t][3]; }
    #pragma unroll
    for(int t=0;t<2;++t){ s[t][0]+=s[t][4]; }
    #pragma unroll
    for(int t=0;t<2;++t){ s[t][0]+=s[t][2]; }
    #pragma unroll
    for(int t=0;t<2;++t) r[t]=__builtin_amdgcn_rcpf(s[t][0].x+s[t][0].y); }
  SB();
  #pragma unroll
  for(int i=0;i<10;++i)
    #pragma unroll
    for(int t=0;t<2;++t) e[t][i]*=r[t];
  SB();
  #pragma unroll
  for(int i=0;i<10;++i)
    #pragma unroll
    for(int t=0;t<2;++t) o[t][i]=pk_bf16(e[t][i].x,e[t][i].y);
  SB();
}
DI u32x4 pack2(f32x4 a,f32x4 b){ u32x4 r={pk_bf16(a[0],a[1]),pk_bf16(a[2],a[3]),pk_bf16(b[0],b[1]),pk_bf16(b[2],b[3])}; return r; }
template<int C0,int NEG> DI void feat_addr(lptr L,int hl,int lane,lptr (*ad)[2]){
  const int q=(lane&15)>>2, p=lane&3, gk=lane>>4;
  constexpr int IA=(C0==0)?0:2;
  #pragma unroll
  for(int s=0;s<2;++s)
    #pragma unroll
    for(int t=0;t<2;++t){ int d=16*(IA+2*s+t)+4*gk-C0-20*(p&1); bool ok=(d>=0)&&(d<20)&&(IA+2*s+t<5);
      ad[s][t]=ok?(L+B_WP+(hl+(p&1))*800+(d+q)*40+(p>>1)*NEG):(L+B_Z); } }
template<int C0> DI void feat_prep(lptr L,int hl,int lane,u32x4 (*a)[5]){
  lptr ad[2][2]; feat_addr<C0,0>(L,hl,lane,ad);
  const u32 sg=(lane&8)?0x80008000u:0u;
  #pragma unroll
  for(int s=0;s<2;++s)
    #pragma unroll
    for(int i=0;i<5;++i) a[s][i]=cat(lds_tr(ad[s][0]+8*i),lds_tr(ad[s][1]+8*i))^sg; }
template<int C0,int NT> DI void features(const u32x4 (*a)[5],const f32x4 (*acc)[4],u32 (*ph)[10]){
  constexpr int IA=(C0==0)?0:2;
  #pragma unroll
  for(int t0=0;t0<4;t0+=NT){ f32x4 z[NT][5];
    #pragma unroll
    for(int t=0;t<NT;++t){
      u32x4 b0=pack2(acc[IA][t0+t],acc[IA+1][t0+t]), b1=pack2(acc[IA+2][t0+t],acc[(IA+3<5)?IA+3:4][t0+t]);
      #pragma unroll
      for(int i=0;i<5;++i) z[t][i]=mfma16(a[0][i],b0,Z4);
      #pragma unroll
      for(int i=0;i<5;++i) z[t][i]=mfma16(a[1][i],b1,z[t][i]); }
    SB();
    #pragma unroll
    for(int t=0;t<NT;t+=2) softmax20x2(z+t,ph+t0+t); }
}

template<bool ISQ> DI void role_b(int X,int li,lptr L,const SsmIn& in,const u16* x,const u16* wqkv,const u16* wphi,const u16* wo,u16* out,void* ws){
  const int tid=threadIdx.x, lane=tid&63, w=__builtin_amdgcn_readfirstlane(tid>>6), g=lane>>4;
  const int rt=4*(X>>1)+(ISQ?(li>>1):(li>>2));
  const int grp=ISQ?(2*(X&1)+(li&1)):(4*(X&1)+(li&3));
  const int T=ISQ?16+li:li; const bool doout=T<20; const int tt64=4*X+T/5, ct=T%5;
  const rsrc_t rs=__builtin_amdgcn_make_buffer_rsrc(ws,0,WS_END,0x00020000);
  constexpr int OIM=B_PV;
  if(w>=4){
    const int hw=w-4; constexpr int NC=ISQ?10:4;
    const int s0=ISQ?((li*ROT+1)%10):(((li>>2)+(li&3))*ROT)%10;
    u32 xo[2], wv[5];
    #pragma unroll
    for(int i=0;i<2;++i){ int r=(4*hw+i)*8+(lane>>3); xo[i]=(u32)(rt*128+r)*1280u+(u32)(((lane&7)^((r>>1)&7))<<4); }
    #pragma unroll
    for(int i=0;i<5;++i){ int c=(5*hw+i)*64+lane, r=c/20, pos=c-r*20, sc=pos^(((r>>3)&1)<<1);
      wv[i]=(u32)r*3840u+(ISQ?(u32)(320*grp+16*sc):(u32)((sc<10?1280:2400)+160*grp+16*sc)); }
    auto issue=[&](int s){ int ss=s+s0; if(ss>=10) ss-=10; lptr d=L+(s&3)*B_ST;
      const char* xb=(const char*)x+ss*128; const char* wb=(const char*)wqkv+(size_t)ss*64*3840;
      #pragma unroll
      for(int i=0;i<5;++i){ dma16(wb,wv[i],d+16384+(5*hw+i)*1024); if(i<4) dma16(xb+(i>>1)*16*1280,xo[i&1],d+(4*hw+i)*1024); } };
    ST(9);
    issue(0);
    ST(10);
    issue(1); ST(11); issue(2);
    ST(1);
    #pragma unroll
    for(int s=0;s<10;++s){
      if(s<2) waitv<18>(); else if(s<8) waitv<9>(); else if(s==8) waitv<9+NC>(); else waitv<NC>();
      rbar();
      if(s==0){ ST(2); issue(3); }
      if(s>=2&&s<8) issue(s+2);
      if(s==7){
        const u32x4 sv={SENT,SENT,SENT,SENT};
        #pragma unroll
        for(int i=0;i<NC;++i){
          if(ISQ) st_sc1(rs,WS_O+((8*rt+2*hw+i/5)*20+5*grp+i%5)*1024+lane*16,sv);
          else st_sc1(rs,(i<3||lane<18)?(WS_P1+((4*grp+hw)*16+rt)*3584+i*1024+lane*16):((hw==0&&rt<14&&lane<48)?(WS_R+(grp*84+6*rt)*80+(lane-18)*16):OOB),sv); } } }
    rbar();
    ST(3);
    auto cleared=[&](){
      u32 n=0; if(lane==0) n=__hip_atomic_fetch_add((LAS u32*)(L+B_F+16),1u,__ATOMIC_RELAXED,__HIP_MEMORY_SCOPE_WORKGROUP);
      if(__builtin_amdgcn_readfirstlane(n)==3&&lane==0) flag_set(ws,ISQ?(128+4*rt+grp):(16*grp+rt),epoch(ws)); };
    if(!doout){ waitv<0>(); cleared(); STDUMP(); return; }
    u32x4 wf[2][20];
    { lptr ar=L+hw*B_PVS; const int r=lane>>2;
      const char* gb=(const char*)wo+ct*256+hw*64; const u32 vo=r*1280+(((lane&3)^((r>>3)<<1))<<4);
      const int q=(lane&15)>>2, p=lane&3;
      lptr rd=ar+(8*g+q)*64+(((p>>1)^((g&1)<<1))<<4)+8*(p&1), rd1=ar+(8*g+q)*64+(((2+(p>>1))^((g&1)<<1))<<4)+8*(p&1);
      const int xo2=B_X+hw*2048-hw*B_PVS-18432;
      #pragma unroll
      for(int rn=0;rn<2;++rn){
        #pragma unroll
        for(int i=0;i<20;++i) dma16(gb+(size_t)(rn*320+i*16)*1280,vo,ar+i*1024+(i<18?0:xo2));
        if(rn==0){ waitv<20>(); cleared(); }
        waitv<0>();
        #pragma unroll
        for(int s=0;s<10;++s){ const int o=s*2048+(s<9?0:xo2);
          wf[0][10*rn+s]=cat(lds_tr(rd+o),lds_tr(rd+o+256));
          wf[1][10*rn+s]=cat(lds_tr(rd1+o),lds_tr(rd1+o+256)); }
        asm volatile("s_waitcnt lgkmcnt(0)":::"memory"); } }
    ST(4);
    if(ISQ) rbar();
    rbar();
    ST(5);
    f32x4 c[2][4];
    #pragma unroll
    for(int t=0;t<4;++t){ c[0][t]=Z4; c[1][t]=Z4; }
    u32x4 ob[2][4];
    #pragma unroll
    for(int t=0;t<4;++t) ob[0][t]=lds_ld<u32x4>(L+OIM+(t*20)*1024+lane*16);
    SB();
    #pragma unroll
    for(int ks=0;ks<20;++ks){
      #pragma unroll
      for(int t=0;t<4;++t){ if(ks<19) ob[(ks+1)&1][t]=lds_ld<u32x4>(L+OIM+(t*20+ks+1)*1024+lane*16);
        c[0][t]=mfma16(wf[0][ks],ob[ks&1][t],c[0][t]); c[1][t]=mfma16(wf[1][ks],ob[ks&1][t],c[1][t]); }
      SB(); }
    #pragma unroll
    for(int t=0;t<4;++t)
      #pragma unroll
      for(int cc=0;cc<2;++cc) lds_st<u32x2>(L+(16*t+(lane&15))*272+hw*64+cc*32+8*g,u32x2{pk_bf16(c[cc][t][0],c[cc][t][1]),pk_bf16(c[cc][t][2],c[cc][t][3])});
    ST(6);
  } else {
    const int me=ISQ?(128+X*8+li):(X*16+li);
    const int th=w>>1, hh=w&1;
    const int kc=2*me+th;
    SsmP sp=load_ssm(in,10*(kc/6)+4+kc%6,lane);
    const u32 po=ISQ?(WS_O+((8*rt+4*th)*20+5*grp)*1024+lane*16):(WS_P1+((4*grp+2*hh+th)*16+rt)*3584+lane*16);
    const u32 ro=(!ISQ&&rt<14&&w<2&&lane<30)?(WS_R+(grp*84+6*rt+3*w)*80+lane*8):OOB;
    auto publish=[&](int i,u32x4 v){
      if(ISQ){ u32 o;
        if(i<8) o=po+((i>>1)*20+3*hh+(i&1))*1024;
        else o=po+((2*(i-8)+(lane>>5))*20+2)*1024+hh*512-(lane&32)*16;
        st_sc1(rs,o,v); }
      else st_sc1(rs,(i<3||lane<18)?po+i*1024:OOB,v); };
    { constexpr int NB=ISQ?3200:1600; const char* wp=(const char*)wphi+(ISQ?8*grp:4*grp)*800+hh*NB;
      #pragma unroll
      for(int i=0;i<(ISQ?4:2);++i) if(i*1024+lane*16<NB) dma16(wp,i*1024+lane*16,L+B_WP+hh*NB+i*1024); }
    if(lane<4) lds_st<u32x4>(L+B_Z+lane*16,u32x4{0,0,0,0});
    if(tid<2) lds_st<u32x4>(L+B_F+tid*16,u32x4{0,0,0,0});
    const u64 E=epoch(ws);
    u32 kp[6];
    { float s,inv; ssm_prep(sp,s,inv); f32x16 ka={};
      kgen_half(sp,hh,s,L+3*B_ST+w*A_KS,lane,ka);
      #pragma unroll
      for(int r=0;r<6;++r) kp[r]=pk_f16(f32x2{ka[2*r],ka[2*r+1]});
      if(lane>=32) kp[5]=__builtin_bit_cast(u32,inv); }
    u32x4 fa[2][5];
    waitv<0>();
    lptr fad[2][2];
    if(!ISQ){ feat_addr<0,3200>(L,2*hh,lane,fad);
      #pragma unroll
      for(int i=0;i<2;++i) if(i*1024+lane*16<1600){ lptr q=L+B_WP+hh*1600+i*1024+lane*16; lds_st<u32x4>(q+3200,lds_ld<u32x4>(q)^0x80008000u); } }
    st_sc1(rs,WS_KP+(2*kc+hh)*1536+lane*16,u32x4{kp[0],kp[1],kp[2],kp[3]});
    __builtin_amdgcn_raw_buffer_store_b64(u32x2{kp[4],kp[5]},rs,WS_KP+(2*kc+hh)*1536+1024+lane*8,0,16);
    int ab[5], bb;
    { const int q=(lane&15)>>2,p=lane&3;
      #pragma unroll
      for(int i=0;i<5;++i){ int n=2*i+(p>>1); int cid=ISQ?(10*hh+n):(5*hh+n+(n>=5?5:0)); ab[i]=16384+(8*g+q)*320+((cid^((g&1)<<1))<<4)+8*(p&1); }
      bb=(64*th+(lane&15))*128+((g^((lane&15)>>1))<<4); }
    f32x4 acc[5][4];
    #pragma unroll
    for(int i=0;i<5;++i)
      #pragma unroll
      for(int t=0;t<4;++t) acc[i][t]=Z4;
    ST(1);
    u32x4 FA[2][5], FB[2][4];
    #pragma unroll
    for(int s=0;s<10;++s){
      if(s==4) waitv<0>();
      if(s==0) bar(); else rbar();
      if(s==0) ST(2);
      if(s==4&&tid==0) flag_set(ws,256+me,E);
      SB();
      lptr d=L+(s&3)*B_ST;
      #pragma unroll
      for(int hf=0;hf<2;++hf){
        #pragma unroll
        for(int i=0;i<5;++i){
          if(i==0){ FB[hf][0]=lds_ld<u32x4>(d+(bb^(64*hf))); FB[hf][1]=lds_ld<u32x4>(d+(bb^(64*hf))+2048); }
          if(i==1){ FB[hf][2]=lds_ld<u32x4>(d+(bb^(64*hf))+4096); FB[hf][3]=lds_ld<u32x4>(d+(bb^(64*hf))+6144); }
          FA[hf][i]=cat(lds_tr(d+ab[i]+hf*10240),lds_tr(d+ab[i]+hf*10240+1280));
          if(s>0||hf>0){
            #pragma unroll
            for(int t=0;t<4;++t) acc[i][t]=mfma16(FA[hf^1][i],FB[hf^1][t],acc[i][t]); }
          if(!ISQ&&s==9&&hf==1){ fa[0][i]=cat(lds_tr(fad[0][0]+8*i),lds_tr(fad[0][1]+8*i)); fa[1][i]=cat(lds_tr(fad[1][0]+8*i),lds_tr(fad[1][1]+8*i)); }
          SB(); } } }
    bar();

    #pragma unroll
    for(int i=0;i<5;++i)
      #pragma unroll
      for(int t=0;t<4;++t) acc[i][t]=mfma16(FA[1][i],FB[1][t],acc[i][t]);
    ST(3);
    lptr pv=L+B_PV+w*B_PVS;
    if(!ISQ){
      { u32 ph[4][10];
        features<0,4>(fa,acc,ph);
        #pragma unroll
        for(int t=0;t<4;++t){ const int tok=16*t+(lane&15);
          lptr pp=pv+tok*160+(g&1)*80+(g>>1)*40;
          #pragma unroll
          for(int i=0;i<5;++i) lds_st<u32x2>(pp+8*i,u32x2{ph[t][2*i],ph[t][2*i+1]});
          lptr vp=pv+10272+tok*96;
          #pragma unroll
          for(int i=2;i<5;++i){ int dv=16*i+4*g-40; u32x2 v={pk_bf16(acc[i][t][0],acc[i][t][1]),pk_bf16(acc[i][t][2],acc[i][t][3])};
            int o=(dv>=20)?(48+2*(dv-20)):2*dv;
            if(i==2&&g<2){ v=u32x2{0x3F80u,0u}; o=48*g+40; }
            lds_st<u32x2>(vp+o,v); } } }
      ST(4);
      const int fi=(lane<16)?(16*grp+lane):(128+4*(tt64>>1)+(lane&3));
      const u64 fl=flag_get(ws,fi);
      { const int q=(lane&15)>>2, p=lane&3, tk=16*(g>>1)+4*(g&1)+q;
        lptr pa=pv+tk*160+8*p, vb=pv+10272+tk*96+8*p; f32x4 c[2][2][3];
        #pragma unroll
        for(int hd=0;hd<2;++hd){ u32x4 a[2][3], b[2][2];
          #pragma unroll
          for(int ks=0;ks<2;++ks){
            #pragma unroll
            for(int i=0;i<3;++i) a[ks][i]=cat(lds_tr(pa+ks*32*160+hd*80+32*i),lds_tr(pa+(ks*32+8)*160+hd*80+32*i));
            #pragma unroll
            for(int dt=0;dt<2;++dt) b[ks][dt]=cat(lds_tr(vb+ks*32*96+hd*48+32*dt),lds_tr(vb+(ks*32+8)*96+hd*48+32*dt)); }
          #pragma unroll
          for(int dt=0;dt<2;++dt)
            #pragma unroll
            for(int i=0;i<3;++i) c[hd][dt][i]=mfma16(a[1][i],b[1][dt],mfma16(a[0][i],b[0][dt],Z4)); }
        asm volatile("":"+v"(c[1][1][2])); ST(11);
        #pragma unroll
        for(int i=0;i<6;++i) lds_st<f32x4>(pv+i*1024+lane*16,th?c[0][i/3][i%3]:c[1][i/3][i%3]);
        asm volatile("s_waitcnt lgkmcnt(0)":::"memory");
        *(volatile LAS u32*)(L+B_F+4*w)=1u;
        for(int it=0;it<SPIN_MAX;++it){ if(__builtin_amdgcn_readfirstlane(*(volatile LAS u32*)(L+B_F+4*(w^2)))) break; }
        asm volatile("":::"memory");
        ST(12);
        lptr ot=L+B_PV+(w^2)*B_PVS, sp_=pv+8192+(lane&15)*160+16*g;
        #pragma unroll
        for(int i=0;i<6;++i){ f32x4 v=(th?c[1][i/3][i%3]:c[0][i/3][i%3])+lds_ld<f32x4>(ot+i*1024+lane*16);
          if((i<3||(lane&15)<5)&&(i%3<2||g<2)) lds_st<f32x4>(sp_+(i/3)*16*160+64*(i%3),v); }
        for(int it=0;it<SPIN_MAX;++it){ if(__builtin_amdgcn_readfirstlane(*(volatile LAS u32*)(L+B_F+16))==4u) break; }
        ST(13);
        #pragma unroll
        for(int i=0;i<4;++i) publish(i,lds_ld<u32x4>(pv+8192+i*1024+lane*16)); }
      ST(5);
      flag_wait(ws,E,fi,fl);
      ST(14);
      if(me==127&&w==3){
        for(int it=0;it<SPIN_MAX;++it){ if(__all(flag_get(ws,lane)==E&&flag_get(ws,64+lane)==E&&flag_get(ws,128+lane)==E&&flag_get(ws,192+lane)==E)) break; __builtin_amdgcn_s_sleep(2); }
        if(lane==0) __hip_atomic_store((GAS u64*)ws,E,__ATOMIC_RELAXED,__HIP_MEMORY_SCOPE_AGENT); }
      if(rt<14&&w<2){ u32x4 v[8];
        const int sl=2*rt+w;
        const u32 o=((lane&31)<30)?(WS_P1+((4*grp+sl/7)*16+8*(lane>>5))*3584+(sl%7)*480+(lane&31)*16):OOB;
        gather<8,10>(L,rs,o,3584,v,o+7*3584,(lane<16)?(WS_P1+((4*grp+sl/7)*16+lane)*3584+(sl%7)*480+476):OOB);
        f32x4 sum=((__builtin_bit_cast(f32x4,v[0])+__builtin_bit_cast(f32x4,v[1]))+(__builtin_bit_cast(f32x4,v[2])+__builtin_bit_cast(f32x4,v[3])))
                 +((__builtin_bit_cast(f32x4,v[4])+__builtin_bit_cast(f32x4,v[5]))+(__builtin_bit_cast(f32x4,v[6])+__builtin_bit_cast(f32x4,v[7])));
        #pragma unroll
        for(int e=0;e<4;++e) sum[e]+=__shfl_xor(sum[e],32);
        __builtin_amdgcn_raw_buffer_store_b64(u32x2{pk_bf16(sum[0],sum[1]),pk_bf16(sum[2],sum[3])},rs,ro,0,16); }
      ST(6);
    } else {
      u32 ph[2][4][10];
      feat_prep<0>(L,4*hh,lane,fa); features<0,2>(fa,acc,ph[0]); feat_prep<40>(L,4*hh+2,lane,fa); features<40,2>(fa,acc,ph[1]);
      #pragma unroll
      for(int i=0;i<80;++i) asm volatile("":"+v"(ph[i/40][(i/10)&3][i%10]));
      ST(4);
      { const int fi=(lane<16)?(16*(2*grp+hh)+lane):((lane<32)?(128+4*(tt64>>1)+(lane&3)):(128+4*rt+grp));
        flag_wait(ws,E,fi,flag_get(ws,fi)); ST(11); }
      { u32x4 v[7]; const u32 o=WS_R+(8*grp+4*hh)*1680+lane*16;
        gather<7,12>(L,rs,o,1024,v,(lane<36)?o+6144:OOB,(lane<28)?(WS_R+(8*grp+4*hh)*1680+lane*240+236):OOB);
        #pragma unroll
        for(int i=0;i<7;++i) lds_st<u32x4>(pv+i*1024+lane*16,v[i]); }
      ST(5);
      lptr os=pv+7168;
      #pragma unroll
      for(int ps=0;ps<2;++ps){
        const int gp=(lane&15)>>2, e=lane&3; const bool mt=((gp>>1)==(g&1));
        u32x4 a[3][3];
        #pragma unroll
        for(int i=0;i<3;++i){ int sg=4*i+e; int d=(sg<10)?(10*(gp&1)+sg):20;
          lptr q=mt?(pv+((2*ps+(gp>>1))*21+d)*80+(g>>1)*40):(L+B_Z);
          a[i][0]=cat(lds_ld<u32x2>(q),lds_ld<u32x2>(q+8)); a[i][1]=cat(lds_ld<u32x2>(q+16),lds_ld<u32x2>(q+24)); a[i][2]=cat(lds_ld<u32x2>(q+32),u32x2{0,0}); }
        #pragma unroll
        for(int t=0;t<4;++t){ const u32* f=ph[ps][t]; f32x4 n[3];
          #pragma unroll
          for(int i=0;i<3;++i) n[i]=mfma16(a[i][2],u32x4{f[8],f[9],0,0},mfma16(a[i][1],u32x4{f[4],f[5],f[6],f[7]},mfma16(a[i][0],u32x4{f[0],f[1],f[2],f[3]},Z4)));
          const float den=rbf(rbf(n[2][2])+1e-6f), r=__builtin_amdgcn_rcpf(den);
          lptr op=os+(16*t+(lane&15))*176+((2*ps+(g>>1))*20+10*(g&1))*2;
          #pragma unroll
          for(int i=0;i<5;++i){ f32x2 nb={n[i>>1][(i&1)*2],n[i>>1][(i&1)*2+1]}; nb*=r; lds_st<u32>(op+4*i,pk_bf16(nb.x,nb.y)); } } }
      ST(13);
      { lptr rp=os+(lane&15)*176;
        #pragma unroll
        for(int i=0;i<8;++i) publish(i,lds_ld<u32x4>(rp+(i>>1)*16*176+hh*32+(i&1)*64+16*g));
        #pragma unroll
        for(int i=8;i<10;++i) publish(i,lds_ld<u32x4>(rp+(2*(i-8)+(lane>>5))*16*176+(hh?0:128)+16*(g&1))); }
      ST(6);
    }
    if(!doout){ STDUMP(); return; }
    { u32x4 v[20]; const u32 o=WS_O+tt64*81920+w*20480+lane*16;
      gather<20,15>(L,rs,o,1024,v,o+19*1024,(lane<40)?(WS_O+tt64*81920+w*20480+lane*512+508):OOB);
      if(ISQ) rbar();
      #pragma unroll
      for(int i=0;i<20;++i) lds_st<u32x4>(L+OIM+w*20480+i*1024+lane*16,v[i]); }
    ST(7);
    bar();
  }
  bar();
  #pragma unroll
  for(int i=0;i<2;++i){ const int r=32*i+(tid>>4);
    *(u32x4*)((char*)out+(size_t)(64*tt64+r)*2560+1280+ct*256+(tid&15)*16)=lds_ld<u32x4>(L+r*272+(tid&15)*16); }
  ST(8); STDUMP();
}

__global__ __launch_bounds__(512,2) void fused(const u16* u,const u16* are,const u16* aim,const u16* bre,const u16* bim,const u16* cre,const u16* cim,
    const u16* logdt,const u16* dskip,const u16* x,const u16* wqkv,const u16* wphi,const u16* wo,u16* out,void* ws){
  extern __shared__ __attribute__((aligned(16))) char smem[];
  lptr L=(lptr)smem;
  const int b=blockIdx.x, X=b&7, idx=b>>3;
  STINIT(); ST(0);
  const SsmIn in={are,aim,bre,bim,cre,cim,logdt};
  if(idx<16) role_b<false>(X,idx,L,in,x,wqkv,wphi,wo,out,ws);
  else if(idx<24){
    role_a(X*8+(idx-16),L,u,in,dskip,wo,out,ws);
  }
  else role_b<true>(X,idx-24,L,in,x,wqkv,wphi,wo,out,ws);
}

extern "C" void kernel_launch(void* const* d_in, const int* in_sizes, int n_in, void* d_out, int out_size, void* d_ws, size_t ws_size, hipStream_t stream){
  (void)hipFuncSetAttribute((const void*)fused,hipFuncAttributeMaxDynamicSharedMemorySize,163840);
  fused<<<256,512,163840,stream>>>((const u16*)d_in[0],(const u16*)d_in[1],(const u16*)d_in[2],(const u16*)d_in[3],(const u16*)d_in[4],(const u16*)d_in[5],(const u16*)d_in[6],
    (const u16*)d_in[7],(const u16*)d_in[8],(const u16*)d_in[9],(const u16*)d_in[10],(const u16*)d_in[11],(const u16*)d_in[12],(u16*)d_out,d_ws);
}
```

```cpp
#include <hip/hip_runtime.h>
#include <stdint.h>

typedef unsigned short u16; typedef unsigned u32; typedef unsigned long long u64;
typedef __attribute__((ext_vector_type(4))) short s16x4;
typedef __attribute__((ext_vector_type(8))) _Float16 f16x8;
typedef __attribute__((ext_vector_type(8))) __bf16 bf16x8;
typedef __attribute__((ext_vector_type(2))) __bf16 bf16x2;
typedef __attribute__((ext_vector_type(2))) _Float16 f16x2;
typedef __attribute__((ext_vector_type(2))) float f32x2;
typedef __attribute__((ext_vector_type(4))) float f32x4;
typedef __attribute__((ext_vector_type(16))) float f32x16;
typedef __attribute__((ext_vector_type(2))) unsigned u32x2;
typedef __attribute__((ext_vector_type(3))) unsigned u32x3;
typedef __attribute__((ext_vector_type(4))) unsigned u32x4;
#define LAS __attribute__((address_space(3)))
#define GAS __attribute__((address_space(1)))
#define DI __device__ __forceinline__
typedef LAS char* lptr;
typedef __amdgpu_buffer_rsrc_t rsrc_t;

#define ST(i) do{}while(0)
#define STDUMP() do{}while(0)
#define STINIT() do{}while(0)

DI u32 pk_bf16(float a, float b){ f32x2 v={a,b}; return __builtin_bit_cast(u32,__builtin_convertvector(v,bf16x2)); }
DI u32 pk_f16(f32x2 v){ return __builtin_bit_cast(u32,__builtin_convertvector(v,f16x2)); }
DI float bflo(u32 x){ return __builtin_bit_cast(float,x<<16); }
DI float bfhi(u32 x){ return __builtin_bit_cast(float,x&0xffff0000u); }
DI float rbf(float a){ return bflo(pk_bf16(a,a)); }
template<class T> DI T lds_ld(lptr p){ return *(LAS T*)p; }
template<class T> DI void lds_st(lptr p, T v){ *(LAS T*)p=v; }
DI u32x2 lds_ld64(lptr p){ return *(volatile LAS u32x2*)p; }
DI u32x2 lds_tr(lptr p){ return __builtin_bit_cast(u32x2,__builtin_amdgcn_ds_read_tr16_b64_v4i16((LAS s16x4*)p)); }
DI f32x4 mfma16(u32x4 a,u32x4 b,f32x4 c){ return __builtin_amdgcn_mfma_f32_16x16x32_bf16(__builtin_bit_cast(bf16x8,a),__builtin_bit_cast(bf16x8,b),c,0,0,0); }
DI u32x4 cat(u32x2 a,u32x2 b){ u32x4 r={a.x,a.y,b.x,b.y}; return r; }
DI u32 p_ll(u32 a,u32 b){ return __builtin_amdgcn_perm(b,a,0x05040100u); }
DI u32 p_lh(u32 a,u32 b){ return __builtin_amdgcn_perm(b,a,0x07060100u); }
DI u32 p_hl(u32 a,u32 b){ return __builtin_amdgcn_perm(b,a,0x05040302u); }
DI u32 p_hh(u32 a,u32 b){ return __builtin_amdgcn_perm(b,a,0x07060302u); }
#define SB() __builtin_amdgcn_sched_barrier(0)
DI void rbar(){ asm volatile("":::"memory"); __builtin_amdgcn_s_barrier(); asm volatile("":::"memory"); }
DI void bar(){ asm volatile("s_waitcnt lgkmcnt(0)":::"memory"); __builtin_amdgcn_s_barrier(); asm volatile("":::"memory"); }
template<int N> DI void waitv(){ asm volatile("s_waitcnt vmcnt(%0)"::"i"(N):"memory"); }
constexpr f32x4 Z4={0.f,0.f,0.f,0.f};
DI float wave_max(float m){
  m=fmaxf(m,__builtin_bit_cast(float,__builtin_amdgcn_update_dpp(0,__builtin_bit_cast(int,m),0xB1,0xf,0xf,true)));
  m=fmaxf(m,__builtin_bit_cast(float,__builtin_amdgcn_update_dpp(0,__builtin_bit_cast(int,m),0x4E,0xf,0xf,true)));
  m=fmaxf(m,__builtin_bit_cast(float,__builtin_amdgcn_update_dpp(0,__builtin_bit_cast(int,m),0x141,0xf,0xf,true)));
  m=fmaxf(m,__builtin_bit_cast(float,__builtin_amdgcn_update_dpp(0,__builtin_bit_cast(int,m),0x140,0xf,0xf,true)));
  int x=__builtin_bit_cast(int,m);
  float a=__builtin_bit_cast(float,__builtin_amdgcn_readlane(x,0)), b=__builtin_bit_cast(float,__builtin_amdgcn_readlane(x,16));
  float c=__builtin_bit_cast(float,__builtin_amdgcn_readlane(x,32)), d=__builtin_bit_cast(float,__builtin_amdgcn_readlane(x,48));
  return fmaxf(fmaxf(a,b),fmaxf(c,d)); }

constexpr u32 WS_FLAG=256, WS_P1=4096, WS_Q=WS_P1+32*16*3584, WS_O=WS_Q+32*2048*40, WS_END=WS_O+128*20*1024;
constexpr u32 SENT=0xFFFFFFFFu, OOB=0x80000000u;
constexpr int SPIN_MAX=1<<17;
DI void dma16(const void* sb, u32 voff, lptr l){ u32 keep;
  asm volatile("s_mov_b32 %0, m0\n\ts_mov_b32 m0, %3\n\ts_nop 0\n\tglobal_load_lds_dwordx4 %1, %2\n\ts_mov_b32 m0, %0":"=&s"(keep):"v"(voff),"s"(sb),"s"((u32)(uintptr_t)l):"memory"); }
#define DMA4_BODY(SC) asm volatile("s_mov_b32 %0, m0\n\ts_mov_b32 m0, %6\n\ts_nop 0\n\tglobal_load_lds_dwordx4 %1, %5" SC "\n\tglobal_load_lds_dwordx4 %2, %5 offset:1024" SC \
    "\n\tglobal_load_lds_dwordx4 %3, %5 offset:2048" SC "\n\tglobal_load_lds_dwordx4 %4, %5 offset:3072" SC "\n\ts_mov_b32 m0, %0" \
    :"=&s"(keep):"v"(v0),"v"(v1),"v"(v2),"v"(v3),"s"(sb),"s"((u32)(uintptr_t)l):"memory")
DI void dma16x4(const void* sb,u32 v0,u32 v1,u32 v2,u32 v3,lptr l){ u32 keep; DMA4_BODY(""); }
DI void dma16x4_sc1(const void* sb,u32 v0,u32 v1,u32 v2,u32 v3,lptr l){ u32 keep; DMA4_BODY(" sc1"); }
DI u32x4 ld_sc1(rsrc_t rs,u32 off){ return __builtin_amdgcn_raw_buffer_load_b128(rs,off,0,16); }
DI void st_sc1(rsrc_t rs,u32 off,u32x4 v){ __builtin_amdgcn_raw_buffer_store_b128(v,rs,off,0,16); }
template<int NIT> DI void gather4(rsrc_t rs,u32 off,u32 last,u32x4* v){
  for(int it=0;it<NIT;++it){
    u32 todo=0;
    #pragma unroll
    for(int k=0;k<4;++k){ u32 m=0;
      #pragma unroll
      for(int i=0;i<4;++i) m=max(m,max(v[4*k+i].x,v[4*k+i].z));
      if(__any(m==SENT)) todo|=1u<<k; }
    if(!todo) break;
    asm volatile("":::"memory");
    #pragma unroll
    for(int k=0;k<4;++k) if(todo>>k&1){
      #pragma unroll
      for(int i=0;i<4;++i) v[4*k+i]=ld_sc1(rs,((i<3)?off+i*1024:last)+k*3584); } } }
DI u64 epoch(const void* ws){ u64 E; asm volatile("s_load_dwordx2 %0, %1, 0x0\n\ts_waitcnt lgkmcnt(0)":"=s"(E):"s"(ws):"memory"); return E+1; }
DI u64 epoch_late(const void* ws){ return *(const __attribute__((address_space(4))) u64*)ws+1; }
DI void flag_set(void* ws,int i,u64 E){ __hip_atomic_store((GAS u64*)((char*)ws+WS_FLAG)+i,E,__ATOMIC_RELAXED,__HIP_MEMORY_SCOPE_AGENT); }
DI u64 flag_get(const void* ws,int i){ return __hip_atomic_load((const GAS u64*)((const char*)ws+WS_FLAG)+i,__ATOMIC_RELAXED,__HIP_MEMORY_SCOPE_AGENT); }
DI void flag_wait(const void* ws,u64 E,int idx,u64 f){
  for(int it=0;it<SPIN_MAX;++it){ if(__all(f==E)) break; __builtin_amdgcn_s_sleep(2); f=flag_get(ws,idx); } }

constexpr int A_UB=4576, A_US=9120, A_K0=10*A_US, A_KB=4224, A_KS=8704;
constexpr int A_XF=A_K0+8*A_KS;
static_assert(A_XF+16<=163328,"lds");

DI f32x2 cmul(f32x2 a, f32x2 b){ f32x2 sw={-b.y,b.x}; return __builtin_elementwise_fma(a.yy,sw,a.xx*b); }
DI f32x2 cexp_e(float re2, float rev, float e){
  float m=__builtin_amdgcn_exp2f(re2*e); float a=__builtin_amdgcn_fractf(rev*e);
  f32x2 r={m*__builtin_amdgcn_cosf(a), m*__builtin_amdgcn_sinf(a)}; return r; }
struct SsmP { u32 are,aim,bre,bim,cre,cim,ld; float dt; };
struct SsmIn { const u16 *are,*aim,*bre,*bim,*cre,*cim,*logdt; };
DI SsmP load_ssm(const SsmIn& in, int ch, int lane){
  SsmP p; int o=ch*64+lane;
  p.are=((const u32*)in.are)[o]; p.aim=((const u32*)in.aim)[o]; p.bre=((const u32*)in.bre)[o];
  p.bim=((const u32*)in.bim)[o]; p.cre=((const u32*)in.cre)[o]; p.cim=((const u32*)in.cim)[o];
  p.ld=in.logdt[ch];
  return p; }
DI f32x2 ssm_w(const SsmP& p, int half){
  f32x2 b={half?bfhi(p.bre):bflo(p.bre), half?bfhi(p.bim):bflo(p.bim)};
  f32x2 c={half?bfhi(p.cre):bflo(p.cre), half?bfhi(p.cim):bflo(p.cim)};
  return cmul(c,b*p.dt); }
DI void ssm_prep(SsmP& p, float& s, float& inv){
  asm volatile("":"+v"(p.ld));
  p.dt=rbf(expf(bflo(p.ld)));
  f32x2 w0=ssm_w(p,0), w1=ssm_w(p,1);
  float m=wave_max(fmaxf(fmaxf(fabsf(w0.x),fabsf(w0.y)),fmaxf(fabsf(w1.x),fabsf(w1.y))));
  int e=__builtin_amdgcn_frexp_expf(m); e=max(-100,min(100,e));
  s=__builtin_amdgcn_ldexpf(1.0f,-e); inv=__builtin_amdgcn_ldexpf(1.0f,e); }
DI void kgen_half(const SsmP& p, int half, float s, lptr tab, int lane, f32x16& acc){
  const float are=(half?bfhi(p.are):bflo(p.are))*p.dt, aim=(half?bfhi(p.aim):bflo(p.aim))*p.dt;
  const float re2=are*1.4426950408889634f, rev=aim*0.15915494309189535f;
  lptr wr=tab+lane*4; lptr rd=tab+(lane&31)*272+(lane>>5)*16;
  { f32x2 e8=cexp_e(re2,rev,8.f), e16=cmul(e8,e8);
    f32x2 a={1.f,0.f}, b=e8;
    #pragma unroll
    for(int j=0;j<32;j+=2){ lds_st<u32>(wr+j*272,pk_f16(a)); lds_st<u32>(wr+(j+1)*272,pk_f16(b)); if(j<30){ a=(j==0)?e16:cmul(a,e16); b=cmul(b,e16);} } }
  u32x4 bf[8];
  #pragma unroll
  for(int k=0;k<8;++k) bf[k]=lds_ld<u32x4>(rd+k*32);
  { f32x2 w=ssm_w(p,half)*s; w.y=-w.y;
    f32x2 e1=cexp_e(re2,-rev,1.f), g=cexp_e(re2,-rev,256.f);
    f32x2 g2=cmul(g,g), g3=cmul(g2,g), g4=cmul(g2,g2);
    f32x2 x[8]; x[0]=w;
    #pragma unroll
    for(int i=1;i<8;++i) x[i]=cmul(x[i-1],e1);
    #pragma unroll
    for(int i=0;i<8;++i) lds_st<u32>(wr+i*272,pk_f16(x[i]));
    #pragma unroll
    for(int i=0;i<4;++i) lds_st<u32>(wr+(8+i)*272,pk_f16(cmul(x[2*i],g)));
    lds_st<u32>(wr+12*272,pk_f16(cmul(x[0],g2))); lds_st<u32>(wr+13*272,pk_f16(cmul(x[4],g2)));
    lds_st<u32>(wr+14*272,pk_f16(cmul(x[0],g3))); lds_st<u32>(wr+15*272,pk_f16(cmul(x[4],g3)));
    f32x2 y=cmul(w,g4);
    #pragma unroll
    for(int i=0;i<5;++i){ lds_st<u32>(wr+(16+i)*272,pk_f16(y)); if(i<4) y=cmul(y,g); } }
  #pragma unroll
  for(int k=0;k<8;++k){ u32x4 af=lds_ld<u32x4>(rd+k*32);
    acc=__builtin_amdgcn_mfma_f32_32x32x16_f16(__builtin_bit_cast(f16x8,af),__builtin_bit_cast(f16x8,bf[k]),acc,0,0,0); }
}
DI void k_finish(const float* v, lptr ka, int kbo, int lane){
  lptr ks=ka; lptr kb=ka+kbo;
  { const int j=lane&31, h=lane>>5;
    lds_st<u32x2>(ks+(8*j+4*h)*2,u32x2{pk_bf16(v[0],v[1]),pk_bf16(v[2],v[3])});
    u32x2 b={pk_bf16(v[4],v[5]),pk_bf16(v[6],v[7])};
    u32 c0=pk_bf16(v[8],v[9]), c1=pk_bf16(v[10],v[11]);
    if(h==0){ lds_st<u32x2>(ks+(256+4*j)*2,b);
      lds_st<u16>(ks+(512+j)*2,(u16)c0); lds_st<u16>(ks+(544+j)*2,(u16)(c0>>16));
      lds_st<u16>(ks+(576+j)*2,(u16)c1); lds_st<u16>(ks+(608+j)*2,(u16)(c1>>16));
    } else { lds_st<u32>(ks+(384+2*j)*2,b.x); lds_st<u32>(ks+(448+2*j)*2,b.y); if(j==0) lds_st<u16>(ks+640*2,(u16)c0); } }
  const int M=lane; const bool lo=M<32;
  u32x4 v0=lds_ld<u32x4>(ks+(M&31)*16); u32x2 v1=lds_ld<u32x2>(ks+(128+4*(M|32))*2);
  u32 v4=lds_ld<u16>(ks+(132+4*(M|32))*2);
  u32 pv=lds_ld<u16>(ks+(lo?max(8*M-1,0):((M==32)?255:(128+4*M)))*2); if(M==0) pv=0;
  u32 ab=lds_ld<u32>(ks+(384+2*M)*2), c2=lds_ld<u16>(ks+(386+2*M)*2);
  u32 a3[2],b3[2];
  #pragma unroll
  for(int i=0;i<2;++i){ a3[i]=lds_ld<u16>(ks+(512+M+64*i)*2); b3[i]=lds_ld<u16>(ks+(513+M+64*i)*2); }
  asm volatile("":"+v"(v0),"+v"(v1),"+v"(v4),"+v"(pv),"+v"(ab),"+v"(c2),"+v"(a3[0]),"+v"(a3[1]),"+v"(b3[0]),"+v"(b3[1])::"memory");
  { u32x4 A,B;
    if(lo){ B.x=p_hl(v0.w,v0.w); B.y=p_hl(v0.z,v0.z); B.z=p_hl(v0.y,v0.y); B.w=p_hl(v0.x,v0.x);
      A.x=p_lh(v0.w,v0.z); A.y=p_lh(v0.z,v0.y); A.z=p_lh(v0.y,v0.x); A.w=p_ll(v0.x,pv);
    } else { B.x=p_lh(v4,v1.y); B.y=p_ll(v1.y,v1.y); B.z=p_lh(v1.y,v1.x); B.w=p_ll(v1.x,v1.x);
      A.x=p_hl(v1.y,v1.y); A.y=B.y; A.z=p_hl(v1.x,v1.x); A.w=p_ll(v1.x,pv); }
    lds_st<u32x4>(ka+(255-M)*16,A); lds_st<u32x4>(kb+(255-M)*16,B); }
  { u32x4 A,B; B.x=p_ll(c2,c2); B.y=p_hh(ab,ab); B.z=p_hl(ab,ab); B.w=p_ll(ab,ab);
    A.x=p_lh(c2,ab); A.y=B.y; A.z=B.w; A.w=B.w;
    lds_st<u32x4>(ka+(191-M)*16,A); lds_st<u32x4>(kb+(191-M)*16,B); }
  #pragma unroll
  for(int i=0;i<2;++i){ u32 a=a3[i], b=b3[i], c=(M&1)?b:a; u32x4 A,B;
    B.x=p_ll(b,b); B.y=p_ll(b,c); B.z=p_ll(a,a); B.w=B.z;
    A.x=B.x; A.y=p_ll(c,a); A.z=B.z; A.w=B.z;
    lds_st<u32x4>(ka+(127-64*i-M)*16,A); lds_st<u32x4>(kb+(127-64*i-M)*16,B); }
  if(lane<8) lds_st<u32x4>(((lane&4)?kb:ka)+(256+(lane&3))*16,u32x4{0,0,0,0});
}
template<int NS> DI void conv_steps(lptr ua, lptr ka, int kbo, int s0, int lane, f32x4* acc){
  const int a=lane&15, g=lane>>4;
  lptr up=ua+((a&1)?0:A_UB)+(32*(a>>1)+8*g+32*s0)*2;
  lptr kp=ka+((a&1)?kbo:0)+(28-4*(a>>1)+8*g+32*s0)*2;
  u32x4 A[2][8],B[2][8];
  auto ldA=[&](int s,int q){ A[s&1][q]=cat(lds_ld64(kp+(1792-256*q+32*s)*2),lds_ld64(kp+(1792-256*q+32*s)*2+8)); };
  auto ldB=[&](int s,int q){ B[s&1][q]=lds_ld<u32x4>(up+(256*q+32*s)*2); };
  #pragma unroll
  for(int q=0;q<8;++q){ ldB(0,q); ldA(0,q); }
  #pragma unroll
  for(int c=0;c<8;++c) acc[c]=Z4;
  SB();
  #pragma unroll
  for(int s=0;s<NS;++s){
    #pragma unroll
    for(int q=0;q<8;++q){
      #pragma unroll
      for(int c=q;c<8;++c) acc[c]=mfma16(A[s&1][q],B[s&1][c-q],acc[c]);
      if(s+1<NS){ if(q==0){ ldB(s+1,0); ldB(s+1,1); ldB(s+1,2); ldB(s+1,3); } else if(q==1){ ldA(s+1,0); ldB(s+1,4); ldB(s+1,5); }
        else if(q==2){ ldB(s+1,6); ldB(s+1,7); ldA(s+1,1); } else if(q==3){ ldA(s+1,2); ldA(s+1,3); } else if(q==4){ ldA(s+1,4); ldA(s+1,5); }
        else if(q==5){ ldA(s+1,6); } else if(q==6){ ldA(s+1,7); } }
      SB(); } }
}
DI u32 fin2(float y0,float y1,u32 u,float dsk){
  u32 y=pk_bf16(y0,y1), s=pk_bf16(bflo(u)*dsk,bfhi(u)*dsk);
  return pk_bf16(bflo(y)+bflo(s),bfhi(y)+bfhi(s)); }
DI void role_a(int j, lptr L, const u16* u, const SsmIn& in, const u16* dskip, const u16* wo, u16* out, void* ws){
  const int tid=threadIdx.x, lane=tid&63, w=__builtin_amdgcn_readfirstlane(tid>>6), p=w&3;
  const int ch0=10*j;
  const rsrc_t ro=__builtin_amdgcn_make_buffer_rsrc(out,0,2048*2560,0x00020000);
  lptr k0=L+A_K0+p*A_KS, k1=k0+4*A_KS;
  constexpr int XKB=A_US+224;
  u32 dk[5];
  #pragma unroll
  for(int i=0;i<5;++i) dk[i]=((const u32*)(dskip+ch0))[i];
  SsmP p0=load_ssm(in,ch0+w,lane);
  SsmP px; if(w>=4) px=load_ssm(in,ch0+8+(p>>1),lane);
  for(int i=tid;i<570;i+=512){ int sl=i/57, c=i-sl*57; lds_st<u32x4>(L+sl*A_US+(c<28?c*16:A_UB+(c-28)*16),u32x4{0,0,0,0}); }
  if(tid==0) lds_st<u32x4>(L+A_XF,u32x4{0,0,0,0});
  bar();
  u32x4 um[4];
  const int ur=(tid+64*j)&511;
  { typedef u32x4 __attribute__((aligned(4))) u32x4u;
    const char* up0=(const char*)u+(size_t)ur*4*1280+20*j;
    #pragma unroll
    for(int r=0;r<4;++r) um[r]=*(const u32x4u*)(up0+r*1280); }
  const u64 E=epoch(ws);
  ST(1);
  float xv[12];
  if(w>=4){ __builtin_amdgcn_s_setprio(3); float s,inv; ssm_prep(px,s,inv); f32x16 acc={};
    kgen_half(px,p&1,s,k1,lane,acc);
    #pragma unroll
    for(int r=0;r<12;++r) xv[r]=acc[r]*inv; }
  float ks,kinv; ssm_prep(p0,ks,kinv); f32x16 kacc={};
  auto k_rest=[&](lptr kw){ kgen_half(p0,1,ks,kw,lane,kacc); float v[12];
    #pragma unroll
    for(int r=0;r<12;++r) v[r]=kacc[r]*kinv;
    k_finish(v,kw,A_KB,lane); };
  kgen_half(p0,0,ks,L+A_K0+w*A_KS,lane,kacc);
  if(w<4) k_rest(k0);
  ST(2);
  const int tl=32*((lane&15)>>1)+8*(lane>>4)+2*(lane&1);
  auto fin4=[&](u32* r,f32x4 v,int e,int c){
    lptr y=L+e*A_US+(224+256*c+tl)*2; const float d=(e&1)?bfhi(dk[e>>1]):bflo(dk[e>>1]);
    r[0]=fin2(v[0],v[1],lds_ld<u32>(y),d); r[1]=fin2(v[2],v[3],lds_ld<u32>(y+8),d); };
  auto scatter=[&](lptr d,u32 r0,u32 r1,u32 r2,u32 r3,bool last){
    u32x2 e={p_ll(r0,r1),p_ll(r2,r3)}, o={p_hh(r0,r1),p_hh(r2,r3)};
    lds_st<u32x2>(d,e); lds_st<u32x2>(d+A_US,o);
    lds_st<u32>(d+A_UB+4,e.x); lds_st<u32>(d+A_US+A_UB+4,o.x);
    if(!last){ lds_st<u32>(d+A_UB+8,e.y); lds_st<u32>(d+A_US+A_UB+8,o.y); } };
  #pragma unroll
  for(int c=0;c<4;++c) scatter(L+2*c*A_US+(224+4*ur)*2,um[0][c],um[1][c],um[2][c],um[3][c],ur==511);
  bar();
  ST(3);
  if(w<4){
    f32x4 acc[8];
    conv_steps<8>(L+p*A_US,k0,A_KB,0,lane,acc);
    #pragma unroll
    for(int c=0;c<8;++c) lds_st<f32x4>(k0+c*1024+lane*16,acc[c]);
    ST(4);
    bar();
    conv_steps<8>(L+(p+4)*A_US,k1,A_KB,0,lane,acc);
    #pragma unroll
    for(int c=0;c<8;++c) lds_st<f32x4>(k1+c*1024+lane*16,acc[c]);
    ST(5);
    bar();
    conv_steps<4>(L+(8+(p>>1))*A_US,L+(p&2)*A_US+A_UB,XKB,4*(p&1),lane,acc);
    #pragma unroll
    for(int c=0;c<8;++c) lds_st<f32x4>(k0+c*1024+lane*16,acc[c]);
    ST(6);
    bar();
  } else {
    if(tid==256) flag_set(ws,192+j,E);
    k_rest(k1);
    ST(9);
    u32 ux[8];
    { const char* up=(const char*)u+(size_t)(tid-256)*4*1280+20*j+16;
      #pragma unroll
      for(int r=0;r<8;++r) ux[r]=*(const u32*)(up+(size_t)((r&3)+(r>>2)*1024)*1280); }
    ST(4);
    bar();
    u32 pf[4];
    { const char* wp=(const char*)wo+(j&7)*102400+(tid-256)*128;
      #pragma unroll
      for(int i=0;i<4;++i) pf[i]=*(const u32*)(wp+((i<3||tid<288)?i*32768:0)); }
    { lptr xp=L+(p|1)*A_US+A_UB+lane*16; volatile LAS u32* xf=(volatile LAS u32*)(L+A_XF+2*(p&2));
      if(p&1){
        #pragma unroll
        for(int r=0;r<3;++r) *(volatile LAS f32x4*)(xp+r*1024)=f32x4{xv[4*r],xv[4*r+1],xv[4*r+2],xv[4*r+3]};
        *xf=1u;
      } else { f32x4 o[3];
        for(int it=0;it<SPIN_MAX;++it){ const u32 f=*xf;
          #pragma unroll
          for(int r=0;r<3;++r) o[r]=*(volatile LAS f32x4*)(xp+r*1024);
          if(__builtin_amdgcn_readfirstlane(f)) break; }
        #pragma unroll
        for(int r=0;r<12;++r) xv[r]+=o[r>>2][r&3];
        k_finish(xv,L+p*A_US+A_UB,XKB,lane); } }
    #pragma unroll
    for(int h=0;h<2;++h) scatter(L+8*A_US+(224+4*(tid-256)+1024*h)*2,ux[4*h],ux[4*h+1],ux[4*h+2],ux[4*h+3],h==1&&tid==511);
    ST(8);
    #pragma unroll
    for(int i=0;i<2;++i){ u32 r[4][2]; const int c=2*p+i;
      #pragma unroll
      for(int e=0;e<4;++e) fin4(r[e],lds_ld<f32x4>(L+A_K0+e*A_KS+c*1024+lane*16),e,c);
      const u32 o=(256*c+tl)*2560+20*j;
      #pragma unroll
      for(int h=0;h<2;++h){
        __builtin_amdgcn_raw_buffer_store_b64(u32x2{p_ll(r[0][h],r[1][h]),p_ll(r[2][h],r[3][h])},ro,o+4*h*2560,0,0);
        __builtin_amdgcn_raw_buffer_store_b64(u32x2{p_hh(r[0][h],r[1][h]),p_hh(r[2][h],r[3][h])},ro,o+(4*h+1)*2560,0,0); } }
    asm volatile(""::"v"(pf[0]),"v"(pf[1]),"v"(pf[2]),"v"(pf[3]));
    ST(5);
    bar();
    #pragma unroll
    for(int i=0;i<2;++i)
      #pragma unroll
      for(int e=0;e<4;++e){ u32 r[2]; lptr q=L+A_K0+(4+e)*A_KS+(p+4*i)*1024+lane*16; fin4(r,lds_ld<f32x4>(q),4+e,p+4*i); lds_st<u32x2>(q,u32x2{r[0],r[1]}); }
    ST(6);
    bar();
  }
  {
    u32 r[6][2];
    #pragma unroll
    for(int e=0;e<4;++e){ const u32x2 v=lds_ld<u32x2>(L+A_K0+(4+e)*A_KS+tid*16); r[e][0]=v.x; r[e][1]=v.y; }
    #pragma unroll
    for(int e=0;e<2;++e){ lptr q=L+A_K0+2*e*A_KS+tid*16; fin4(r[4+e],lds_ld<f32x4>(q)+lds_ld<f32x4>(q+A_KS),8+e,w); }
    const u32 o=(256*w+tl)*2560+20*j+8;
    #pragma unroll
    for(int h=0;h<2;++h){
      __builtin_amdgcn_raw_buffer_store_b96(u32x3{p_ll(r[0][h],r[1][h]),p_ll(r[2][h],r[3][h]),p_ll(r[4][h],r[5][h])},ro,o+4*h*2560,0,0);
      __builtin_amdgcn_raw_buffer_store_b96(u32x3{p_hh(r[0][h],r[1][h]),p_hh(r[2][h],r[3][h]),p_hh(r[4][h],r[5][h])},ro,o+(4*h+1)*2560,0,0); } }
  ST(7); STDUMP();
}

constexpr int B_ST=36864, B_PV=2*B_ST, B_PVS=18432, B_WP=4*B_ST, B_Z=B_WP+6400, B_F=163200;
DI float max3(float a,float b,float c){ return __builtin_fmaxf(__builtin_fmaxf(a,b),c); }
template<class H> DI void softmax20x2(const f32x4 (*z)[5], u32 (*o)[10], H hook){
  const float c=1.4426950408889634f; float mc[2];
  { float m0[2],m1[2],m2[2];
    #pragma unroll
    for(int t=0;t<2;++t){ m0[t]=max3(z[t][0][0],z[t][0][1],z[t][0][2]); m1[t]=max3(z[t][0][3],z[t][1][0],z[t][1][1]); m2[t]=max3(z[t][1][2],z[t][1][3],z[t][2][0]); }
    #pragma unroll
    for(int t=0;t<2;++t){ m0[t]=max3(m0[t],z[t][2][1],z[t][2][2]); m1[t]=max3(m1[t],z[t][2][3],z[t][3][0]); m2[t]=max3(m2[t],z[t][3][1],z[t][3][2]); }
    #pragma unroll
    for(int t=0;t<2;++t){ m0[t]=max3(m0[t],z[t][3][3],z[t][4][0]); m1[t]=max3(m1[t],z[t][4][1],z[t][4][2]); m2[t]=__builtin_fmaxf(m2[t],z[t][4][3]); }
    #pragma unroll
    for(int t=0;t<2;++t) mc[t]=max3(m0[t],m1[t],m2[t])*-c; }
  SB();
  hook(0);
  f32x2 e[2][10];
  #pragma unroll
  for(int i=0;i<10;++i)
    #pragma unroll
    for(int t=0;t<2;++t) e[t][i]=__builtin_elementwise_fma(f32x2{z[t][i>>1][(i&1)*2],z[t][i>>1][(i&1)*2+1]},f32x2{c,c},f32x2{mc[t],mc[t]});
  SB();
  hook(1);
  #pragma unroll
  for(int i=0;i<10;++i)
    #pragma unroll
    for(int t=0;t<2;++t) e[t][i]=f32x2{__builtin_amdgcn_exp2f(e[t][i].x),__builtin_amdgcn_exp2f(e[t][i].y)};
  SB();
  hook(2);
  float r[2];
  { f32x2 s[2][5];
    #pragma unroll
    for(int i=0;i<5;++i)
      #pragma unroll
      for(int t=0;t<2;++t) s[t][i]=e[t][2*i]+e[t][2*i+1];
    #pragma unroll
    for(int t=0;t<2;++t){ s[t][0]+=s[t][1]; s[t][2]+=s[t][3]; }
    #pragma unroll
    for(int t=0;t<2;++t){ s[t][0]+=s[t][4]; }
    #pragma unroll
    for(int t=0;t<2;++t){ s[t][0]+=s[t][2]; }
    #pragma unroll
    for(int t=0;t<2;++t) r[t]=__builtin_amdgcn_rcpf(s[t][0].x+s[t][0].y); }
  SB();
  #pragma unroll
  for(int i=0;i<10;++i)
    #pragma unroll
    for(int t=0;t<2;++t) e[t][i]*=r[t];
  SB();
  #pragma unroll
  for(int i=0;i<10;++i)
    #pragma unroll
    for(int t=0;t<2;++t) o[t][i]=pk_bf16(e[t][i].x,e[t][i].y);
  SB();
}
DI u32x4 pack2(f32x4 a,f32x4 b){ u32x4 r={pk_bf16(a[0],a[1]),pk_bf16(a[2],a[3]),pk_bf16(b[0],b[1]),pk_bf16(b[2],b[3])}; return r; }
DI void role_b(const bool ISQ,int X,int li,lptr L,u32 warm,const u16* x,const u16* wqkv,const u16* wphi,const u16* wo,u16* out,void* ws){
  const int tid=threadIdx.x, lane=tid&63, w=__builtin_amdgcn_readfirstlane(tid>>6), g=lane>>4;
  const int rt=4*(X>>1)+(ISQ?(li>>1):(li>>2));
  const int grp=ISQ?(2*(X&1)+(li&1)):(4*(X&1)+(li&3));
  const int T=ISQ?16+li:li; const bool doout=T<20; const int tt64=4*X+T/5, ct=T%5;
  const rsrc_t rs=__builtin_amdgcn_make_buffer_rsrc(ws,0,WS_END,0x00020000);
  constexpr int OIM=B_PV;
  u64 E=epoch_late(ws);
  auto arrive=[&](){
    u32 n=0; if(lane==0) n=__hip_atomic_fetch_add((LAS u32*)(L+B_F+16),1u,__ATOMIC_RELAXED,__HIP_MEMORY_SCOPE_WORKGROUP);
    if(__builtin_amdgcn_readfirstlane(n)==3u&&lane==0) flag_set(ws,ISQ?(128+4*rt+grp):(16*grp+rt),E); };
  volatile LAS u32* bad=(volatile LAS u32*)(L+B_F+20);
  if(__builtin_expect(w>=4,1)){
    const int hw=w-4;
    const int s0=(0x8530>>((ISQ?((li>>1)+2*(li&1)):((li>>2)+(li&3)))&3)*4&15)+(ISQ?1:0);
    u32 xo[4], wv[5];
    #pragma unroll
    for(int i=0;i<4;++i){ int r=(4*hw+i)*8+(lane>>3); xo[i]=(u32)(rt*128+r)*1280u+(u32)(((lane&7)^((r>>1)&7))<<4)-1024u*i; }
    const u32 wlo=ISQ?320*grp:1280+160*grp, whi=ISQ?320*grp:2400+160*grp;
    #pragma unroll
    for(int i=0;i<5;++i){ u32 c=(5*hw+i)*64+lane, r=(c*3277u)>>16, pos=c-r*20, sc=pos^(((r>>3)&1)<<1);
      wv[i]=r*3840u+(sc<10?wlo:whi)+16*sc-1024u*(i&3); }
    auto issue=[&](int s){ int ss=s+s0; if(ss>=10) ss-=10; lptr d=L+(s&3)*B_ST;
      const char* xb=(const char*)x+ss*128; const char* wb=(const char*)wqkv+(size_t)ss*64*3840;
      dma16x4(wb,wv[0],wv[1],wv[2],wv[3],d+16384+5*hw*1024); dma16x4(xb,xo[0],xo[1],xo[2],xo[3],d+4*hw*1024); dma16(wb,wv[4],d+16384+(5*hw+4)*1024); };
    ST(9);
    issue(0); issue(1);
    asm volatile("":"+s"(E));
    ST(1); waitv<9>(); rbar(); issue(2);
    ST(2);
    waitv<9>(); rbar();
    #pragma unroll 1
    for(int s=2;s<9;++s){
      issue(s+1);
      waitv<9>(); rbar(); }
    waitv<0>(); rbar();
    if(!doout){ STDUMP(); return; }
    if(!ISQ){
      const u32x4 sv={SENT,SENT,SENT,SENT};
      #pragma unroll
      for(int i=0;i<4;++i) st_sc1(rs,(i<3||lane<18)?(WS_P1+((4*grp+hw)*16+rt)*3584+i*1024+lane*16):OOB,sv);
      #pragma unroll
      for(int i=0;i<5;++i) st_sc1(rs,WS_O+((8*rt+grp)*20+5*hw+i)*1024+lane*16,sv);
      waitv<0>(); arrive();
      ST(12); }
    auto go=[&](u32 n){ for(int it=0;it<SPIN_MAX;++it){ if(__builtin_amdgcn_readfirstlane(*(volatile LAS u32*)(L+B_F+24))>=n) break; __builtin_amdgcn_s_sleep(2); } asm volatile("":::"memory"); };
    u32x4 wf[2][20];
    {
      const int pr=hw>>1, me2=hw&1, r=lane>>3;
      const char* gb=(const char*)wo+ct*256+pr*128+me2*8*1280;
      const u32 sw=((lane&7)^((((r>>1)&1)|(me2<<1))<<1))<<4, vo=r*1280+sw, vo2=((r>>2)*20+(r&3)+16+32*me2)*1280+sw;
      auto V=[&](int n,int k){ const int G=n/10, m=n%10; return ((m<8)?vo+(160*G+20*m)*1280:vo2+(160*G+80*(m-8))*1280)-1024*k; };
      auto four=[&](int n,lptr d){ dma16x4(gb,V(n,0),V(n+1,1),V(n+2,2),V(n+3,3),d); };
      const int q=(lane&15)>>2, p=lane&3, f=((q>>1)|((g&1)<<1))<<1;
      lptr rb=L+(pr*2+(g&1))*18432+(g>>1)*1024+q*128+8*(p&1);
      lptr rd=rb+(((4*me2+(p>>1))^f)<<4), rd1=rb+(((4*me2+2+(p>>1))^f)<<4);
      constexpr int TAIL=B_PV+12288;
      lptr dst=L+hw*18432;
      volatile LAS u32* fm=(volatile LAS u32*)(L+B_F+48+4*hw); volatile LAS u32* fo=(volatile LAS u32*)(L+B_F+48+4*(hw^1));
      auto meet=[&](u32 n){ *fm=n; for(int it=0;it<SPIN_MAX;++it){ if(__builtin_amdgcn_readfirstlane(*fo)>=n) break; __builtin_amdgcn_s_sleep(1); } asm volatile("":::"memory"); };
      go(4u); ST(3);
      four(0,dst); four(4,dst+4096); four(8,dst+8192); four(12,dst+12288); dma16(gb,V(16,0),dst+16384); dma16(gb,V(17,0),dst+17408);
      four(18,dst+TAIL); dma16(gb,V(22,0),dst+TAIL+4096); dma16(gb,V(23,0),dst+TAIL+5120);
      ST(10); waitv<0>(); ST(11); meet(1);
      #pragma unroll
      for(int s=0;s<12;++s){ const int o=(s<9)?s*2048:TAIL+(s-9)*2048;
        wf[0][s]=cat(lds_tr(rd+o),lds_tr(rd+o+512)); wf[1][s]=cat(lds_tr(rd1+o),lds_tr(rd1+o+512)); }
      asm volatile("s_waitcnt lgkmcnt(0)":::"memory");
      if(lane==0) __hip_atomic_fetch_add((LAS u32*)(L+B_F+28),1u,__ATOMIC_RELAXED,__HIP_MEMORY_SCOPE_WORKGROUP);
      meet(2);
      go(ISQ?4u:8u);
      four(24,dst); four(28,dst+4096); four(32,dst+8192); four(36,dst+12288);
      ST(13); waitv<0>(); ST(14); meet(3);
      #pragma unroll
      for(int s=0;s<8;++s){ wf[0][12+s]=cat(lds_tr(rd+s*2048),lds_tr(rd+s*2048+512)); wf[1][12+s]=cat(lds_tr(rd1+s*2048),lds_tr(rd1+s*2048+512)); }
      asm volatile("s_waitcnt lgkmcnt(0)":::"memory"); }
    ST(4);
    for(u32 it=1;it<SPIN_MAX;++it){
      rbar();
      ST(5);
      f32x4 c[2][4];
      #pragma unroll
      for(int t=0;t<4;++t){ c[0][t]=Z4; c[1][t]=Z4; }
      u32x4 ob[2][4]; u32 chk=0;
      lptr ob0=L+OIM+lane*16;
      #pragma unroll
      for(int t=0;t<4;++t) ob[0][t]=lds_ld<u32x4>(ob0+((t+hw)&3)*20480);
      lptr obt[4];
      #pragma unroll
      for(int t=0;t<4;++t) obt[t]=ob0+((t+hw)&3)*20480;
      SB();
      #pragma unroll
      for(int ks=0;ks<20;++ks){
        #pragma unroll
        for(int t=0;t<4;++t){ if(ks<19) ob[(ks+1)&1][t]=lds_ld<u32x4>(obt[t]+(ks+1)*1024);
          c[0][t]=mfma16(wf[0][ks],ob[ks&1][t],c[0][t]); c[1][t]=mfma16(wf[1][ks],ob[ks&1][t],c[1][t]); }
        chk=max(chk,max(ob[ks&1][0].x,ob[ks&1][0].z));
        SB(); }
      if(__any(chk==SENT)) *bad=it;
      #pragma unroll
      for(int t=0;t<4;++t)
        #pragma unroll
        for(int cc=0;cc<2;++cc) lds_st<u32x2>(L+(16*((t+hw)&3)+(lane&15))*272+hw*64+cc*32+8*g,u32x2{pk_bf16(c[cc][t][0],c[cc][t][1]),pk_bf16(c[cc][t][2],c[cc][t][3])});
      ST(6);
      bar();
      if(__builtin_amdgcn_readfirstlane(*bad)!=it) break; }
  } else {
    const int th=w>>1, hh=w&1;
    { const int NB=ISQ?3200:1600; const char* wp=(const char*)wphi+(ISQ?8*grp:4*grp)*800+hh*NB;
      #pragma unroll
      for(int i=0;i<4;++i) if(i*1024+lane*16<NB) dma16(wp,i*1024+lane*16,L+B_WP+hh*NB+i*1024); }
    if(lane<4) lds_st<u32x4>(L+B_Z+lane*16,u32x4{0,0,0,0});
    if(tid<4) lds_st<u32x4>(L+B_F+tid*16,u32x4{0,0,0,0});
    asm volatile("":"+s"(E));
    asm volatile("s_waitcnt vmcnt(0)"::"v"(warm):"memory");
    ST(9);
    lptr fad[3][2];
    { const int q=(lane&15)>>2, p=lane&3, hd=ISQ?p:(p&1);
      #pragma unroll
      for(int s=0;s<3;++s)
        #pragma unroll
        for(int t=0;t<2;++t){ const int d=16*(2*s+t)+4*g-20*hd; const bool ok=(d>=0)&&(d<20)&&(2*s+t<5);
          fad[s][t]=ok?(L+B_WP+((ISQ?4*hh:2*hh)+hd)*800+(d+q)*40+(ISQ?0:(p>>1)*3200)):(L+B_Z); if(2*s+t<5) asm volatile("":"+v"(fad[s][t])); } }
    lptr pv=L+B_PV+w*B_PVS;
    const int oh=4*grp+(rt&3), tq=rt>>2;
    const int ot=4*tt64+w;
    int fi=(lane<16)?(16*grp+lane):((lane<32)?(16*(lane&7)+4*tq+w):((lane<48)?(16*(ot&7)+(ot>>3)):(128+4*(4*tq+w)+(oh>>3))));
    lptr pp=pv+(lane&15)*160+(g&1)*80+(g>>1)*40, vp2, vp3, vp4, pa, vb;
    { lptr vp=pv+10272+(lane&15)*96; vp2=vp+((g<2)?(48*g+40):(8*g-16)); vp3=vp+((g==3)?48:(16+8*g)); vp4=vp+56+8*g;
      const int tk=16*(g>>1)+4*(g&1)+((lane&15)>>2); pa=pv+tk*160+8*(lane&3)+(th^1)*80; vb=pv+10272+tk*96+8*(lane&3)+(th^1)*48; }
    u32 qo=WS_Q+(oh*2048+512*tq+128*w+16*(g>>1)+(lane&15))*40, po0, po1;
    { const u32 po=WS_P1+((4*grp+2*hh+th)*16+rt)*3584; po0=po+lane*16; po1=((lane&15)<5)?(po+(160+5*g+(lane&15))*16):OOB; }
    asm volatile("":"+v"(fi),"+v"(pp),"+v"(vp2),"+v"(vp3),"+v"(vp4),"+v"(pa),"+v"(vb),"+v"(qo),"+v"(po0),"+v"(po1));
    if(!ISQ){
      #pragma unroll
      for(int i=0;i<2;++i) if(i*1024+lane*16<1600){ lptr q=L+B_WP+hh*1600+i*1024+lane*16; lds_st<u32x4>(q+3200,lds_ld<u32x4>(q)^0x80008000u); } }
    lptr ab[5], bb[2];
    { const int q=(lane&15)>>2,p=lane&3;
      #pragma unroll
      for(int i=0;i<5;++i){ int n=2*i+(p>>1); int cid=ISQ?(10*hh+n):(5*hh+n+(n>=5?5:0)); ab[i]=L+16384+(8*g+q)*320+((cid^((g&1)<<1))<<4)+8*(p&1); }
      const int b0=(64*th+(lane&15))*128+((g^((lane&15)>>1))<<4); bb[0]=L+b0; bb[1]=L+(b0^64); }
    f32x4 acc[5][4];
    #pragma unroll
    for(int i=0;i<5;++i)
      #pragma unroll
      for(int t=0;t<4;++t) acc[i][t]=Z4;
    ST(1);
    u32x4 FA[2][5], FB[2][4], fa[2][5];
    auto half=[&](int hf,bool mm,bool last){
      #pragma unroll
      for(int i=0;i<5;++i){
        if(i<2){ FB[hf][2*i]=lds_ld<u32x4>(bb[hf]+4096*i); FB[hf][2*i+1]=lds_ld<u32x4>(bb[hf]+4096*i+2048); }
        FA[hf][i]=cat(lds_tr(ab[i]+hf*10240),lds_tr(ab[i]+hf*10240+1280));
        if(mm){
          #pragma unroll
          for(int t=0;t<4;++t) acc[i][t]=mfma16(FA[hf^1][i],FB[hf^1][t],acc[i][t]); }
        if(last){ fa[0][i]=cat(lds_tr(fad[0][0]+8*i),lds_tr(fad[0][1]+8*i)); fa[1][i]=cat(lds_tr(fad[1][0]+8*i),lds_tr(fad[1][1]+8*i)); }
        SB(); } };
    bar();
    ST(2);
    SB();
    half(0,false,false);
    #pragma unroll 1
    for(int s=1;s<10;++s){
      half(1,true,false);
      const int step=(s&3)?B_ST:-3*B_ST;
      #pragma unroll
      for(int i=0;i<5;++i) ab[i]+=step;
      bb[0]+=step; bb[1]+=step;
      rbar();
      SB();
      half(0,true,false); }
    half(1,true,true);
    auto signal=[&](){ asm volatile("":::"memory"); if(lane==0) __hip_atomic_fetch_add((LAS u32*)(L+B_F+24),1u,__ATOMIC_RELAXED,__HIP_MEMORY_SCOPE_WORKGROUP); };
    #pragma unroll
    for(int t=0;t<4;++t)
      #pragma unroll
      for(int i=0;i<5;++i) acc[i][t]=mfma16(FA[1][i],FB[1][t],acc[i][t]);
    u32x2 pk[5][4];
    #pragma unroll
    for(int t=0;t<4;++t)
      #pragma unroll
      for(int i=0;i<5;++i) pk[i][t]=u32x2{pk_bf16(acc[i][t][0],acc[i][t][1]),pk_bf16(acc[i][t][2],acc[i][t][3])};
    ST(3);
    if(ISQ){
      u32x4 f2[5];
      #pragma unroll
      for(int i=0;i<5;++i) f2[i]=cat(lds_tr(fad[2][0]+8*i),u32x2{0u,0u});
      lptr sp_=pv+g*2560+(lane&15)*40;
      f32x4 z[4][5];
      #pragma unroll
      for(int t=0;t<4;++t)
        #pragma unroll
        for(int i=0;i<5;++i) z[t][i]=mfma16(fa[0][i],cat(pk[0][t],pk[1][t]),Z4);
      #pragma unroll
      for(int t=0;t<4;++t)
        #pragma unroll
        for(int i=0;i<5;++i) z[t][i]=mfma16(fa[1][i],cat(pk[2][t],pk[3][t]),z[t][i]);
      SB();
      #pragma unroll
      for(int t=0;t<5;++t){
        #pragma unroll
        for(int i=0;i<5;++i){ if(t<4) z[t][i]=mfma16(f2[i],cat(pk[4][t],u32x2{0u,0u}),z[t][i]);
          if(t>0) lds_st<u32x2>(sp_+(t-1)*640+8*i,u32x2{pk_bf16(z[t-1][i][0],z[t-1][i][1]),pk_bf16(z[t-1][i][2],z[t-1][i][3])}); }
        SB(); }
      ST(10);
      #pragma unroll
      for(int i=0;i<10;++i){ const int c=i*64+lane, hd=(c*205)>>15;
        st_sc1(rs,WS_Q+((8*grp+4*hh+hd)*2048+128*rt+64*th)*40+(c-160*hd)*16,lds_ld<u32x4>(pv+c*16)); }
      ST(11);
      waitv<0>(); ST(12); arrive(); signal();
      ST(4);
      if(doout){ fi=16*(ot&7)+(ot>>3); flag_wait(ws,E,fi,flag_get(ws,fi)); }
    } else {
      { f32x4 z[4][5]; u32 ph[4][10];
        auto fm=[&](int t){
          const u32x4 b0=cat(pk[0][t],pk[1][t]), b1=cat(pk[2][t],pk[3][t]);
          #pragma unroll
          for(int i=0;i<5;++i) z[t][i]=mfma16(fa[0][i],b0,Z4);
          #pragma unroll
          for(int i=0;i<5;++i) z[t][i]=mfma16(fa[1][i],b1,z[t][i]); };
        fm(0); fm(1);
        SB();
        softmax20x2(z,ph,[&](int k){ if(k==0){ fm(2); } else if(k==1){ fm(3); } });
        auto wp=[&](int t){
          #pragma unroll
          for(int i=0;i<5;++i) lds_st<u32x2>(pp+t*2560+8*i,u32x2{ph[t][2*i],ph[t][2*i+1]}); };
        softmax20x2(z+2,ph+2,[&](int k){
          if(k==0){ wp(0); wp(1); }
          else if(k==1){
            #pragma unroll
            for(int t=0;t<4;++t){ u32x2 v=pk[2][t]; if(g<2) v=u32x2{0x3F80u,0u};
              lds_st<u32x2>(vp2+t*1536,v); lds_st<u32x2>(vp3+t*1536,pk[3][t]); lds_st<u32x2>(vp4+t*1536,pk[4][t]); } } });
        wp(2); wp(3); }
      ST(4);
      u64 fl;
      lptr q00,q01,q10,q11; u32 om,ox;
      { const int r=lane&15, pc=5*(g&1)+2*(g>>1), pl=5*(g&1)+4, d1=min(r,4); lptr b=L+B_PV+8192;
        q00=b+(16*pc+r)*16; q01=b+(16*pl+r)*16+8*(g>>1); q10=b+(160+5*pc+d1)*16; q11=b+(160+5*pl+d1)*16+8*(g>>1);
        const u32 fb=WS_O+((32*tq+8*w)*20+5*(oh>>3))*1024+r*16;
        om=fb+((oh&7)>>1)*1024+(2*(oh&1)+(g>>1))*256+8*(g&1); ox=(g==0)?(fb+4096+((oh&7)>>1)*256+8*(oh&1)):OOB; }
      u32x4 l0[4],l1[4]; u32x2 l2[4];
      auto ldq=[&](){
        #pragma unroll
        for(int i=0;i<4;++i){ l0[i]=ld_sc1(rs,qo+i*1280); l1[i]=ld_sc1(rs,qo+i*1280+16); l2[i]=__builtin_amdgcn_raw_buffer_load_b64(rs,qo+i*1280+32,0,16); } };
      { f32x4 c[2][6];
        #pragma unroll
        for(int h2=0;h2<2;++h2){
          if(h2){ pa+=th?80:-80; vb+=th?48:-48; }
          u32x4 a[2][3], b[2][2];
          #pragma unroll
          for(int ks=0;ks<2;++ks){
            #pragma unroll
            for(int i=0;i<3;++i) a[ks][i]=cat(lds_tr(pa+ks*32*160+32*i),lds_tr(pa+(ks*32+8)*160+32*i));
            #pragma unroll
            for(int dt=0;dt<2;++dt) b[ks][dt]=cat(lds_tr(vb+ks*32*96+32*dt),lds_tr(vb+(ks*32+8)*96+32*dt)); }
          #pragma unroll
          for(int i=0;i<6;++i) c[h2][i]=mfma16(a[0][i%3],b[0][i/3],Z4);
          #pragma unroll
          for(int i=0;i<6;++i) c[h2][i]=mfma16(a[1][i%3],b[1][i/3],c[h2][i]); }
        #pragma unroll
        for(int i=0;i<6;++i) *(volatile LAS f32x4*)(pv+i*1024+lane*16)=c[0][i];
        *(volatile LAS u32*)(L+B_F+4*w)=1u;
        #pragma unroll
        for(int i=0;i<6;++i) asm volatile("":"+v"(c[1][i]));
        lptr ox_=L+B_PV+(w^2)*B_PVS; f32x4 pr[6];
        for(int it=0;it<SPIN_MAX;++it){
          const u32 f=*(volatile LAS u32*)(L+B_F+4*(w^2)), n=*(volatile LAS u32*)(L+B_F+16);
          #pragma unroll
          for(int i=0;i<6;++i) pr[i]=*(volatile LAS f32x4*)(ox_+i*1024+lane*16);
          if(__builtin_amdgcn_readfirstlane(f+n)==5u) break; }
        fl=flag_get(ws,fi);
        #pragma unroll
        for(int i=0;i<6;++i){ u32 o=(i<3)?(po0+i*1024):(po1+(i-3)*320); if(i%3==2) o=(g<2)?o:OOB;
          st_sc1(rs,o,__builtin_bit_cast(u32x4,c[1][i]+pr[i])); } }
      ST(5);
      if(!__all(fl==E)) flag_wait(ws,E,fi,fl);
      asm volatile("":::"memory"); ldq();
      u32 pq[4][10];
      {
        ST(11);
        const u32 sg=(g&1)?0x80008000u:0u;
        u32x4 v[16]; const u32 go0=WS_P1+(oh*16+4*w)*3584+lane*16, go1=(lane<18)?go0+3072:OOB;
        #pragma unroll
        for(int i2=0;i2<4;i2+=2){ f32x4 z[2][5];
          #pragma unroll
          for(int t=0;t<2;++t){ const u32x4 a=l0[i2+t]^sg, b=l1[i2+t]^sg; const u32x2 c=l2[i2+t]^sg;
            z[t][0]=f32x4{bflo(a.x),bfhi(a.x),bflo(a.y),bfhi(a.y)}; z[t][1]=f32x4{bflo(a.z),bfhi(a.z),bflo(a.w),bfhi(a.w)};
            z[t][2]=f32x4{bflo(b.x),bfhi(b.x),bflo(b.y),bfhi(b.y)}; z[t][3]=f32x4{bflo(b.z),bfhi(b.z),bflo(b.w),bfhi(b.w)};
            z[t][4]=f32x4{bflo(c.x),bfhi(c.x),bflo(c.y),bfhi(c.y)}; }
          if(i2==0){
            #pragma unroll
            for(int k=0;k<16;++k) v[k]=ld_sc1(rs,((k&3)<3?go0+(k&3)*1024:go1)+(k>>2)*3584);
            signal(); }
          SB();
          softmax20x2(z,pq+i2,[](int){});
          #pragma unroll
          for(int i=i2;i<i2+2;++i){
            #pragma unroll
            for(int k=0;k<5;++k){ const int a=(k<4)?k:8, b=(k<4)?k+4:9;
              auto r=__builtin_amdgcn_permlane32_swap(pq[i][a],pq[i][b],false,false); pq[i][a]=r[0]; pq[i][b]=r[1]; }
            #pragma unroll
            for(int k=0;k<10;++k) asm volatile("":"+v"(pq[i][k])); }
          if(i2==0){ gather4<1>(rs,go0,go1,v); SB(); } }
        asm volatile("":"+v"(q00),"+v"(q01),"+v"(q10),"+v"(q11),"+v"(om),"+v"(ox));
        ST(10);
        gather4<SPIN_MAX>(rs,go0,go1,v);
        ST(13);
        #pragma unroll
        for(int i=0;i<4;++i) *(volatile LAS f32x4*)(pv+8192+i*1024+lane*16)=(__builtin_bit_cast(f32x4,v[i])+__builtin_bit_cast(f32x4,v[4+i]))+(__builtin_bit_cast(f32x4,v[8+i])+__builtin_bit_cast(f32x4,v[12+i]));
        *(volatile LAS u32*)(L+B_F+32+4*w)=1u;
        SB();
      }
      ST(12);
      u32x4 a0[2]; u32 a1[2];
      {
        f32x4 s[2][2]; f32x2 u[2];
        for(int it=0;it<SPIN_MAX;++it){
          const u32x4 f=*(volatile LAS u32x4*)(L+B_F+32);
          #pragma unroll
          for(int k=0;k<4;++k){ const int q=k*B_PVS;
            const f32x4 x0=*(volatile LAS f32x4*)(q00+q), x1=*(volatile LAS f32x4*)(q00+q+256), y0=*(volatile LAS f32x4*)(q10+q), y1=*(volatile LAS f32x4*)(q10+q+80);
            const f32x2 u0=*(volatile LAS f32x2*)(q01+q), u1=*(volatile LAS f32x2*)(q11+q);
            if(k==0){ s[0][0]=x0; s[0][1]=x1; s[1][0]=y0; s[1][1]=y1; u[0]=u0; u[1]=u1; }
            else { s[0][0]+=x0; s[0][1]+=x1; s[1][0]+=y0; s[1][1]+=y1; u[0]+=u0; u[1]+=u1; } }
          if(__builtin_amdgcn_readfirstlane(f.x+f.y+f.z+f.w)==4u) break; }
        #pragma unroll
        for(int i=0;i<2;++i){ a0[i]=pack2(s[i][0],s[i][1]); a1[i]=pk_bf16(u[i].x,u[i].y); } }
      ST(14);
      { f32x4 n[8][2];
        #pragma unroll
        for(int t=0;t<8;++t){ const u32* f=pq[t>>1]+4*(t&1);
          #pragma unroll
          for(int i=0;i<2;++i) n[t][i]=mfma16(a0[i],u32x4{f[0],f[1],f[2],f[3]},Z4); }
        #pragma unroll
        for(int t=0;t<8;++t)
          #pragma unroll
          for(int i=0;i<2;++i) n[t][i]=mfma16(u32x4{a1[i],0u,0u,0u},u32x4{pq[t>>1][8+(t&1)],0u,0u,0u},n[t][i]);
        SB();
        float den[8];
        #pragma unroll
        for(int t=0;t<8;++t) den[t]=__builtin_bit_cast(float,__builtin_amdgcn_ds_bpermute(((lane&15)+16)*4,__builtin_bit_cast(int,n[t][1][0])));
        SB();
        #pragma unroll
        for(int t=0;t<8;++t){ const float r=__builtin_amdgcn_rcpf(rbf(rbf(den[t])+1e-6f));
          const f32x4 m=n[t][0]*r, e=n[t][1]*r;
          __builtin_amdgcn_raw_buffer_store_b64(u32x2{pk_bf16(m[0],m[1]),pk_bf16(m[2],m[3])},rs,om+t*20480,0,16);
          __builtin_amdgcn_raw_buffer_store_b64(u32x2{pk_bf16(e[0],e[1]),pk_bf16(e[2],e[3])},rs,ox+t*20480,0,16); } }
      signal();
      if(rt==15&&grp==7&&w==3){
        for(int it=0;it<SPIN_MAX;++it){ if(__all(flag_get(ws,lane)==E&&flag_get(ws,64+lane)==E&&flag_get(ws,128+lane)==E&&flag_get(ws,192+lane)==E)) break; __builtin_amdgcn_s_sleep(2); }
        if(lane==0) __hip_atomic_store((GAS u64*)ws,E,__ATOMIC_RELAXED,__HIP_MEMORY_SCOPE_AGENT); }
      ST(6);
    }
    if(!doout){ STDUMP(); return; }
    for(int it=0;it<SPIN_MAX;++it){ const u32x2 c=*(volatile LAS u32x2*)(L+B_F+24); if(__builtin_amdgcn_readfirstlane(c.x+c.y)>=(ISQ?8u:12u)) break; __builtin_amdgcn_s_sleep(1); }
    { const u32 ob=WS_O+ot*20480;
      const u32 sm=ob+((lane<32)?((5*(lane>>3)+((lane>>1)&3))*1024+(lane&1)*512+508):((5*((lane-32)>>3)+4)*1024+((lane&7)>>1)*256+244+8*(lane&1)));
      for(int it=0;it<SPIN_MAX;++it){ asm volatile("":::"memory");
        if(__all(__builtin_amdgcn_raw_buffer_load_b32(rs,sm,0,16)!=SENT)) break;
        __builtin_amdgcn_s_sleep(1); }
      ST(15);
      const u32 vo=lane*16;
      for(u32 it=1;it<SPIN_MAX;++it){
        #pragma unroll
        for(int i=0;i<5;++i) dma16x4_sc1((const char*)ws+ob+i*4096,vo,vo,vo,vo,L+OIM+w*20480+i*4096);
        waitv<0>();
        ST(7);
        rbar();
        rbar();
        if(__builtin_amdgcn_readfirstlane(*bad)!=it) break; } }
  }
  #pragma unroll
  for(int i=0;i<2;++i){ const int r=32*i+(tid>>4);
    *(u32x4*)((char*)out+(size_t)(64*tt64+r)*2560+1280+ct*256+(tid&15)*16)=lds_ld<u32x4>(L+r*272+(tid&15)*16); }
  ST(8); STDUMP();
}

constexpr int CODE_LINES=445;
__global__ __launch_bounds__(512,2) void fused(const u16* u,const u16* are,const u16* aim,const u16* bre,const u16* bim,const u16* cre,const u16* cim,
    const u16* logdt,const u16* dskip,const u16* x,const u16* wqkv,const u16* wphi,const u16* wo,u16* out,void* ws){
  extern __shared__ __attribute__((aligned(16))) char smem[];
  lptr L=(lptr)smem;
  const int b=blockIdx.x, X=b&7, idx=b>>3;
  u64 pc; asm volatile("s_getpc_b64 %0":"=s"(pc));
  STINIT(); ST(0);
  const int w=__builtin_amdgcn_readfirstlane(threadIdx.x>>6);
  const bool isa=(idx>=16&&idx<24), isq=idx>=24;
  if(__builtin_expect(!isa,1)){
    u32 warm=0;
    if(idx<16&&threadIdx.x<32&&idx*32+threadIdx.x<CODE_LINES) warm=*(const u32*)((const char*)(pc&~255ull)+(idx*32+threadIdx.x)*128);
    if(idx<16&&w==1&&(threadIdx.x&63)<50) warm=*(const u32*)((const char*)wo+((X*16+idx)*50+(threadIdx.x&63))*128);
    role_b(isq,X,isq?idx-24:idx,L,warm,x,wqkv,wphi,wo,out,ws);
  } else { const SsmIn in={are,aim,bre,bim,cre,cim,logdt}; role_a(X*8+(idx-16),L,u,in,dskip,wo,out,ws); }
}

extern "C" void kernel_launch(void* const* d_in, const int* in_sizes, int n_in, void* d_out, int out_size, void* d_ws, size_t ws_size, hipStream_t stream){
  (void)hipFuncSetAttribute((const void*)fused,hipFuncAttributeMaxDynamicSharedMemorySize,163840);
  fused<<<256,512,163840,stream>>>((const u16*)d_in[0],(const u16*)d_in[1],(const u16*)d_in[2],(const u16*)d_in[3],(const u16*)d_in[4],(const u16*)d_in[5],(const u16*)d_in[6],
    (const u16*)d_in[7],(const u16*)d_in[8],(const u16*)d_in[9],(const u16*)d_in[10],(const u16*)d_in[11],(const u16*)d_in[12],(u16*)d_out,d_ws);
}
```

```cpp
#include <hip/hip_runtime.h>
#include <stdint.h>
#include <stdio.h>

typedef unsigned short u16; typedef unsigned u32; typedef unsigned long long u64;
typedef __attribute__((ext_vector_type(4))) short s16x4;
typedef __attribute__((ext_vector_type(8))) _Float16 f16x8;
typedef __attribute__((ext_vector_type(8))) __bf16 bf16x8;
typedef __attribute__((ext_vector_type(2))) __bf16 bf16x2;
typedef __attribute__((ext_vector_type(2))) _Float16 f16x2;
typedef __attribute__((ext_vector_type(2))) float f32x2;
typedef __attribute__((ext_vector_type(4))) float f32x4;
typedef __attribute__((ext_vector_type(16))) float f32x16;
typedef __attribute__((ext_vector_type(2))) unsigned u32x2;
typedef __attribute__((ext_vector_type(3))) unsigned u32x3;
typedef __attribute__((ext_vector_type(4))) unsigned u32x4;
#define LAS __attribute__((address_space(3)))
#define GAS __attribute__((address_space(1)))
#define DI __device__ __forceinline__
typedef LAS char* lptr;
typedef __amdgpu_buffer_rsrc_t rsrc_t;

#define ST(i) do{}while(0)
#define STDUMP() do{}while(0)
#define STINIT() do{}while(0)

DI u32 pk_bf16(float a, float b){ f32x2 v={a,b}; return __builtin_bit_cast(u32,__builtin_convertvector(v,bf16x2)); }
DI u32 pk_f16(f32x2 v){ return __builtin_bit_cast(u32,__builtin_convertvector(v,f16x2)); }
DI float bflo(u32 x){ return __builtin_bit_cast(float,x<<16); }
DI float bfhi(u32 x){ return __builtin_bit_cast(float,x&0xffff0000u); }
DI float rbf(float a){ return bflo(pk_bf16(a,a)); }
template<class T> DI T lds_ld(lptr p){ return *(LAS T*)p; }
template<class T> DI void lds_st(lptr p, T v){ *(LAS T*)p=v; }
DI u32x2 lds_ld64(lptr p){ return *(volatile LAS u32x2*)p; }
DI u32x2 lds_tr(lptr p){ return __builtin_bit_cast(u32x2,__builtin_amdgcn_ds_read_tr16_b64_v4i16((LAS s16x4*)p)); }
DI f32x4 mfma16(u32x4 a,u32x4 b,f32x4 c){ return __builtin_amdgcn_mfma_f32_16x16x32_bf16(__builtin_bit_cast(bf16x8,a),__builtin_bit_cast(bf16x8,b),c,0,0,0); }
DI u32x4 cat(u32x2 a,u32x2 b){ u32x4 r={a.x,a.y,b.x,b.y}; return r; }
DI u32 p_ll(u32 a,u32 b){ return __builtin_amdgcn_perm(b,a,0x05040100u); }
DI u32 p_lh(u32 a,u32 b){ return __builtin_amdgcn_perm(b,a,0x07060100u); }
DI u32 p_hl(u32 a,u32 b){ return __builtin_amdgcn_perm(b,a,0x05040302u); }
DI u32 p_hh(u32 a,u32 b){ return __builtin_amdgcn_perm(b,a,0x07060302u); }
#define SB() __builtin_amdgcn_sched_barrier(0)
DI void rbar(){ asm volatile("":::"memory"); __builtin_amdgcn_s_barrier(); asm volatile("":::"memory"); }
DI void bar(){ asm volatile("s_waitcnt lgkmcnt(0)":::"memory"); __builtin_amdgcn_s_barrier(); asm volatile("":::"memory"); }
template<int N> DI void waitv(){ asm volatile("s_waitcnt vmcnt(%0)"::"i"(N):"memory"); }
constexpr f32x4 Z4={0.f,0.f,0.f,0.f};
DI float wave_max(float m){
  m=fmaxf(m,__builtin_bit_cast(float,__builtin_amdgcn_update_dpp(0,__builtin_bit_cast(int,m),0xB1,0xf,0xf,true)));
  m=fmaxf(m,__builtin_bit_cast(float,__builtin_amdgcn_update_dpp(0,__builtin_bit_cast(int,m),0x4E,0xf,0xf,true)));
  m=fmaxf(m,__builtin_bit_cast(float,__builtin_amdgcn_update_dpp(0,__builtin_bit_cast(int,m),0x141,0xf,0xf,true)));
  m=fmaxf(m,__builtin_bit_cast(float,__builtin_amdgcn_update_dpp(0,__builtin_bit_cast(int,m),0x140,0xf,0xf,true)));
  int x=__builtin_bit_cast(int,m);
  float a=__builtin_bit_cast(float,__builtin_amdgcn_readlane(x,0)), b=__builtin_bit_cast(float,__builtin_amdgcn_readlane(x,16));
  float c=__builtin_bit_cast(float,__builtin_amdgcn_readlane(x,32)), d=__builtin_bit_cast(float,__builtin_amdgcn_readlane(x,48));
  return fmaxf(fmaxf(a,b),fmaxf(c,d)); }

constexpr u32 WS_FLAG=256, WS_P1=4096, WS_Q=WS_P1+32*16*3584, WS_O=WS_Q+32*2048*40, WS_END=WS_O+128*20*1024;
constexpr u32 SENT=0xFFFFFFFFu, OOB=0x80000000u;
constexpr int SPIN_MAX=1<<17;
DI void dma16(const void* sb, u32 voff, lptr l){ u32 keep;
  asm volatile("s_mov_b32 %0, m0\n\ts_mov_b32 m0, %3\n\ts_nop 0\n\tglobal_load_lds_dwordx4 %1, %2\n\ts_mov_b32 m0, %0":"=&s"(keep):"v"(voff),"s"(sb),"s"((u32)(uintptr_t)l):"memory"); }
#define DMA4_BODY(SC) asm volatile("s_mov_b32 %0, m0\n\ts_mov_b32 m0, %6\n\ts_nop 0\n\tglobal_load_lds_dwordx4 %1, %5" SC "\n\tglobal_load_lds_dwordx4 %2, %5 offset:1024" SC \
    "\n\tglobal_load_lds_dwordx4 %3, %5 offset:2048" SC "\n\tglobal_load_lds_dwordx4 %4, %5 offset:3072" SC "\n\ts_mov_b32 m0, %0" \
    :"=&s"(keep):"v"(v0),"v"(v1),"v"(v2),"v"(v3),"s"(sb),"s"((u32)(uintptr_t)l):"memory")
DI void dma16x4(const void* sb,u32 v0,u32 v1,u32 v2,u32 v3,lptr l){ u32 keep; DMA4_BODY(""); }
DI void dma16x4_sc1(const void* sb,u32 v0,u32 v1,u32 v2,u32 v3,lptr l){ u32 keep; DMA4_BODY(" sc1"); }
DI u32x4 ld_sc1(rsrc_t rs,u32 off){ return __builtin_amdgcn_raw_buffer_load_b128(rs,off,0,16); }
DI void st_sc1(rsrc_t rs,u32 off,u32x4 v){ __builtin_amdgcn_raw_buffer_store_b128(v,rs,off,0,16); }
template<int NIT> DI void gather4(rsrc_t rs,u32 off,u32 last,u32x4* v){
  for(int it=0;it<NIT;++it){
    u32 todo=0;
    #pragma unroll
    for(int k=0;k<4;++k){ u32 m=0;
      #pragma unroll
      for(int i=0;i<4;++i) m=max(m,max(v[4*k+i].x,v[4*k+i].z));
      if(__any(m==SENT)) todo|=1u<<k; }
    if(!todo) break;
    asm volatile("":::"memory");
    #pragma unroll
    for(int k=0;k<4;++k) if(todo>>k&1){
      #pragma unroll
      for(int i=0;i<4;++i) v[4*k+i]=ld_sc1(rs,((i<3)?off+i*1024:last)+k*3584); } } }
DI u64 epoch(const void* ws){ u64 E; asm volatile("s_load_dwordx2 %0, %1, 0x0\n\ts_waitcnt lgkmcnt(0)":"=s"(E):"s"(ws):"memory"); return E+1; }
DI u64 epoch_late(const void* ws){ return *(const __attribute__((address_space(4))) u64*)ws+1; }
DI void flag_set(void* ws,int i,u64 E){ __hip_atomic_store((GAS u64*)((char*)ws+WS_FLAG)+i,E,__ATOMIC_RELAXED,__HIP_MEMORY_SCOPE_AGENT); }
DI u64 flag_get(const void* ws,int i){ return __hip_atomic_load((const GAS u64*)((const char*)ws+WS_FLAG)+i,__ATOMIC_RELAXED,__HIP_MEMORY_SCOPE_AGENT); }
DI void flag_wait(const void* ws,u64 E,int idx,u64 f){
  for(int it=0;it<SPIN_MAX;++it){ if(__all(f==E)) break; __builtin_amdgcn_s_sleep(2); f=flag_get(ws,idx); } }

constexpr int A_UB=4576, A_US=9120, A_K0=10*A_US, A_KB=4224, A_KS=8704;
constexpr int A_XF=A_K0+8*A_KS;
static_assert(A_XF+16<=163328,"lds");

DI f32x2 cmul(f32x2 a, f32x2 b){ f32x2 sw={-b.y,b.x}; return __builtin_elementwise_fma(a.yy,sw,a.xx*b); }
DI f32x2 cexp_e(float re2, float rev, float e){
  float m=__builtin_amdgcn_exp2f(re2*e); float a=__builtin_amdgcn_fractf(rev*e);
  f32x2 r={m*__builtin_amdgcn_cosf(a), m*__builtin_amdgcn_sinf(a)}; return r; }
struct SsmP { u32 are,aim,bre,bim,cre,cim,ld; float dt; };
struct SsmIn { const u16 *are,*aim,*bre,*bim,*cre,*cim,*logdt; };
DI SsmP load_ssm(const SsmIn& in, int ch, int lane){
  SsmP p; int o=ch*64+lane;
  p.are=((const u32*)in.are)[o]; p.aim=((const u32*)in.aim)[o]; p.bre=((const u32*)in.bre)[o];
  p.bim=((const u32*)in.bim)[o]; p.cre=((const u32*)in.cre)[o]; p.cim=((const u32*)in.cim)[o];
  p.ld=in.logdt[ch];
  return p; }
DI f32x2 ssm_w(const SsmP& p, int half){
  f32x2 b={half?bfhi(p.bre):bflo(p.bre), half?bfhi(p.bim):bflo(p.bim)};
  f32x2 c={half?bfhi(p.cre):bflo(p.cre), half?bfhi(p.cim):bflo(p.cim)};
  return cmul(c,b*p.dt); }
DI void ssm_prep(SsmP& p, float& s, float& inv){
  asm volatile("":"+v"(p.ld));
  p.dt=rbf(expf(bflo(p.ld)));
  f32x2 w0=ssm_w(p,0), w1=ssm_w(p,1);
  float m=wave_max(fmaxf(fmaxf(fabsf(w0.x),fabsf(w0.y)),fmaxf(fabsf(w1.x),fabsf(w1.y))));
  int e=__builtin_amdgcn_frexp_expf(m); e=max(-100,min(100,e));
  s=__builtin_amdgcn_ldexpf(1.0f,-e); inv=__builtin_amdgcn_ldexpf(1.0f,e); }
DI void kgen_half(const SsmP& p, int half, float s, lptr tab, int lane, f32x16& acc){
  const float are=(half?bfhi(p.are):bflo(p.are))*p.dt, aim=(half?bfhi(p.aim):bflo(p.aim))*p.dt;
  const float re2=are*1.4426950408889634f, rev=aim*0.15915494309189535f;
  lptr wr=tab+lane*4; lptr rd=tab+(lane&31)*272+(lane>>5)*16;
  { f32x2 e8=cexp_e(re2,rev,8.f), e16=cmul(e8,e8);
    f32x2 a={1.f,0.f}, b=e8;
    #pragma unroll
    for(int j=0;j<32;j+=2){ lds_st<u32>(wr+j*272,pk_f16(a)); lds_st<u32>(wr+(j+1)*272,pk_f16(b)); if(j<30){ a=(j==0)?e16:cmul(a,e16); b=cmul(b,e16);} } }
  u32x4 bf[8];
  #pragma unroll
  for(int k=0;k<8;++k) bf[k]=lds_ld<u32x4>(rd+k*32);
  { f32x2 w=ssm_w(p,half)*s; w.y=-w.y;
    f32x2 e1=cexp_e(re2,-rev,1.f), g=cexp_e(re2,-rev,256.f);
    f32x2 g2=cmul(g,g), g3=cmul(g2,g), g4=cmul(g2,g2);
    f32x2 x[8]; x[0]=w;
    #pragma unroll
    for(int i=1;i<8;++i) x[i]=cmul(x[i-1],e1);
    #pragma unroll
    for(int i=0;i<8;++i) lds_st<u32>(wr+i*272,pk_f16(x[i]));
    #pragma unroll
    for(int i=0;i<4;++i) lds_st<u32>(wr+(8+i)*272,pk_f16(cmul(x[2*i],g)));
    lds_st<u32>(wr+12*272,pk_f16(cmul(x[0],g2))); lds_st<u32>(wr+13*272,pk_f16(cmul(x[4],g2)));
    lds_st<u32>(wr+14*272,pk_f16(cmul(x[0],g3))); lds_st<u32>(wr+15*272,pk_f16(cmul(x[4],g3)));
    f32x2 y=cmul(w,g4);
    #pragma unroll
    for(int i=0;i<5;++i){ lds_st<u32>(wr+(16+i)*272,pk_f16(y)); if(i<4) y=cmul(y,g); } }
  #pragma unroll
  for(int k=0;k<8;++k){ u32x4 af=lds_ld<u32x4>(rd+k*32);
    acc=__builtin_amdgcn_mfma_f32_32x32x16_f16(__builtin_bit_cast(f16x8,af),__builtin_bit_cast(f16x8,bf[k]),acc,0,0,0); }
}
DI void k_finish(const float* v, lptr ka, int kbo, int lane){
  lptr ks=ka; lptr kb=ka+kbo;
  { const int j=lane&31, h=lane>>5;
    lds_st<u32x2>(ks+(8*j+4*h)*2,u32x2{pk_bf16(v[0],v[1]),pk_bf16(v[2],v[3])});
    u32x2 b={pk_bf16(v[4],v[5]),pk_bf16(v[6],v[7])};
    u32 c0=pk_bf16(v[8],v[9]), c1=pk_bf16(v[10],v[11]);
    if(h==0){ lds_st<u32x2>(ks+(256+4*j)*2,b);
      lds_st<u16>(ks+(512+j)*2,(u16)c0); lds_st<u16>(ks+(544+j)*2,(u16)(c0>>16));
      lds_st<u16>(ks+(576+j)*2,(u16)c1); lds_st<u16>(ks+(608+j)*2,(u16)(c1>>16));
    } else { lds_st<u32>(ks+(384+2*j)*2,b.x); lds_st<u32>(ks+(448+2*j)*2,b.y); if(j==0) lds_st<u16>(ks+640*2,(u16)c0); } }
  const int M=lane; const bool lo=M<32;
  u32x4 v0=lds_ld<u32x4>(ks+(M&31)*16); u32x2 v1=lds_ld<u32x2>(ks+(128+4*(M|32))*2);
  u32 v4=lds_ld<u16>(ks+(132+4*(M|32))*2);
  u32 pv=lds_ld<u16>(ks+(lo?max(8*M-1,0):((M==32)?255:(128+4*M)))*2); if(M==0) pv=0;
  u32 ab=lds_ld<u32>(ks+(384+2*M)*2), c2=lds_ld<u16>(ks+(386+2*M)*2);
  u32 a3[2],b3[2];
  #pragma unroll
  for(int i=0;i<2;++i){ a3[i]=lds_ld<u16>(ks+(512+M+64*i)*2); b3[i]=lds_ld<u16>(ks+(513+M+64*i)*2); }
  asm volatile("":"+v"(v0),"+v"(v1),"+v"(v4),"+v"(pv),"+v"(ab),"+v"(c2),"+v"(a3[0]),"+v"(a3[1]),"+v"(b3[0]),"+v"(b3[1])::"memory");
  { u32x4 A,B;
    if(lo){ B.x=p_hl(v0.w,v0.w); B.y=p_hl(v0.z,v0.z); B.z=p_hl(v0.y,v0.y); B.w=p_hl(v0.x,v0.x);
      A.x=p_lh(v0.w,v0.z); A.y=p_lh(v0.z,v0.y); A.z=p_lh(v0.y,v0.x); A.w=p_ll(v0.x,pv);
    } else { B.x=p_lh(v4,v1.y); B.y=p_ll(v1.y,v1.y); B.z=p_lh(v1.y,v1.x); B.w=p_ll(v1.x,v1.x);
      A.x=p_hl(v1.y,v1.y); A.y=B.y; A.z=p_hl(v1.x,v1.x); A.w=p_ll(v1.x,pv); }
    lds_st<u32x4>(ka+(255-M)*16,A); lds_st<u32x4>(kb+(255-M)*16,B); }
  { u32x4 A,B; B.x=p_ll(c2,c2); B.y=p_hh(ab,ab); B.z=p_hl(ab,ab); B.w=p_ll(ab,ab);
    A.x=p_lh(c2,ab); A.y=B.y; A.z=B.w; A.w=B.w;
    lds_st<u32x4>(ka+(191-M)*16,A); lds_st<u32x4>(kb+(191-M)*16,B); }
  #pragma unroll
  for(int i=0;i<2;++i){ u32 a=a3[i], b=b3[i], c=(M&1)?b:a; u32x4 A,B;
    B.x=p_ll(b,b); B.y=p_ll(b,c); B.z=p_ll(a,a); B.w=B.z;
    A.x=B.x; A.y=p_ll(c,a); A.z=B.z; A.w=B.z;
    lds_st<u32x4>(ka+(127-64*i-M)*16,A); lds_st<u32x4>(kb+(127-64*i-M)*16,B); }
  if(lane<8) lds_st<u32x4>(((lane&4)?kb:ka)+(256+(lane&3))*16,u32x4{0,0,0,0});
}
template<int NS> DI void conv_steps(lptr ua, lptr ka, int kbo, int s0, int lane, f32x4* acc){
  const int a=lane&15, g=lane>>4;
  lptr up=ua+((a&1)?0:A_UB)+(32*(a>>1)+8*g+32*s0)*2;
  lptr kp=ka+((a&1)?kbo:0)+(28-4*(a>>1)+8*g+32*s0)*2;
  u32x4 A[2][8],B[2][8];
  auto ldA=[&](int s,int q){ A[s&1][q]=cat(lds_ld64(kp+(1792-256*q+32*s)*2),lds_ld64(kp+(1792-256*q+32*s)*2+8)); };
  auto ldB=[&](int s,int q){ B[s&1][q]=lds_ld<u32x4>(up+(256*q+32*s)*2); };
  #pragma unroll
  for(int q=0;q<8;++q){ ldB(0,q); ldA(0,q); }
  #pragma unroll
  for(int c=0;c<8;++c) acc[c]=Z4;
  SB();
  #pragma unroll
  for(int s=0;s<NS;++s){
    #pragma unroll
    for(int q=0;q<8;++q){
      #pragma unroll
      for(int c=q;c<8;++c) acc[c]=mfma16(A[s&1][q],B[s&1][c-q],acc[c]);
      if(s+1<NS){ if(q==0){ ldB(s+1,0); ldB(s+1,1); ldB(s+1,2); ldB(s+1,3); } else if(q==1){ ldA(s+1,0); ldB(s+1,4); ldB(s+1,5); }
        else if(q==2){ ldB(s+1,6); ldB(s+1,7); ldA(s+1,1); } else if(q==3){ ldA(s+1,2); ldA(s+1,3); } else if(q==4){ ldA(s+1,4); ldA(s+1,5); }
        else if(q==5){ ldA(s+1,6); } else if(q==6){ ldA(s+1,7); } }
      SB(); } }
}
DI u32 fin2(float y0,float y1,u32 u,float dsk){
  u32 y=pk_bf16(y0,y1), s=pk_bf16(bflo(u)*dsk,bfhi(u)*dsk);
  return pk_bf16(bflo(y)+bflo(s),bfhi(y)+bfhi(s)); }
DI void role_a(int j, lptr L, const u16* u, const SsmIn& in, const u16* dskip, const u16* wo, u16* out, void* ws){
  const int tid=threadIdx.x, lane=tid&63, w=__builtin_amdgcn_readfirstlane(tid>>6), p=w&3;
  const int ch0=10*j;
  const rsrc_t ro=__builtin_amdgcn_make_buffer_rsrc(out,0,2048*2560,0x00020000);
  lptr k0=L+A_K0+p*A_KS, k1=k0+4*A_KS;
  constexpr int XKB=A_US+224;
  u32 dk[5];
  #pragma unroll
  for(int i=0;i<5;++i) dk[i]=((const u32*)(dskip+ch0))[i];
  SsmP p0=load_ssm(in,ch0+w,lane);
  SsmP px; if(w>=4) px=load_ssm(in,ch0+8+(p>>1),lane);
  for(int i=tid;i<570;i+=512){ int sl=i/57, c=i-sl*57; lds_st<u32x4>(L+sl*A_US+(c<28?c*16:A_UB+(c-28)*16),u32x4{0,0,0,0}); }
  if(tid==0) lds_st<u32x4>(L+A_XF,u32x4{0,0,0,0});
  bar();
  u32x4 um[4];
  const int ur=(tid+64*j)&511;
  { typedef u32x4 __attribute__((aligned(4))) u32x4u;
    const char* up0=(const char*)u+(size_t)ur*4*1280+20*j;
    #pragma unroll
    for(int r=0;r<4;++r) um[r]=*(const u32x4u*)(up0+r*1280); }
  const u64 E=epoch(ws);
  ST(1);
  float xv[12];
  if(w>=4){ __builtin_amdgcn_s_setprio(3); float s,inv; ssm_prep(px,s,inv); f32x16 acc={};
    kgen_half(px,p&1,s,k1,lane,acc);
    #pragma unroll
    for(int r=0;r<12;++r) xv[r]=acc[r]*inv; }
  float ks,kinv; ssm_prep(p0,ks,kinv); f32x16 kacc={};
  auto k_rest=[&](lptr kw){ kgen_half(p0,1,ks,kw,lane,kacc); float v[12];
    #pragma unroll
    for(int r=0;r<12;++r) v[r]=kacc[r]*kinv;
    k_finish(v,kw,A_KB,lane); };
  kgen_half(p0,0,ks,L+A_K0+w*A_KS,lane,kacc);
  if(w<4) k_rest(k0);
  ST(2);
  const int tl=32*((lane&15)>>1)+8*(lane>>4)+2*(lane&1);
  auto fin4=[&](u32* r,f32x4 v,int e,int c){
    lptr y=L+e*A_US+(224+256*c+tl)*2; const float d=(e&1)?bfhi(dk[e>>1]):bflo(dk[e>>1]);
    r[0]=fin2(v[0],v[1],lds_ld<u32>(y),d); r[1]=fin2(v[2],v[3],lds_ld<u32>(y+8),d); };
  auto scatter=[&](lptr d,u32 r0,u32 r1,u32 r2,u32 r3,bool last){
    u32x2 e={p_ll(r0,r1),p_ll(r2,r3)}, o={p_hh(r0,r1),p_hh(r2,r3)};
    lds_st<u32x2>(d,e); lds_st<u32x2>(d+A_US,o);
    lds_st<u32>(d+A_UB+4,e.x); lds_st<u32>(d+A_US+A_UB+4,o.x);
    if(!last){ lds_st<u32>(d+A_UB+8,e.y); lds_st<u32>(d+A_US+A_UB+8,o.y); } };
  #pragma unroll
  for(int c=0;c<4;++c) scatter(L+2*c*A_US+(224+4*ur)*2,um[0][c],um[1][c],um[2][c],um[3][c],ur==511);
  bar();
  ST(3);
  if(w<4){
    f32x4 acc[8];
    conv_steps<8>(L+p*A_US,k0,A_KB,0,lane,acc);
    #pragma unroll
    for(int c=0;c<8;++c) lds_st<f32x4>(k0+c*1024+lane*16,acc[c]);
    ST(4);
    bar();
    conv_steps<8>(L+(p+4)*A_US,k1,A_KB,0,lane,acc);
    #pragma unroll
    for(int c=0;c<8;++c) lds_st<f32x4>(k1+c*1024+lane*16,acc[c]);
    ST(5);
    bar();
    conv_steps<4>(L+(8+(p>>1))*A_US,L+(p&2)*A_US+A_UB,XKB,4*(p&1),lane,acc);
    #pragma unroll
    for(int c=0;c<8;++c) lds_st<f32x4>(k0+c*1024+lane*16,acc[c]);
    ST(6);
    bar();
  } else {
    if(tid==256) flag_set(ws,192+j,E);
    k_rest(k1);
    ST(9);
    u32 ux[8];
    { const char* up=(const char*)u+(size_t)(tid-256)*4*1280+20*j+16;
      #pragma unroll
      for(int r=0;r<8;++r) ux[r]=*(const u32*)(up+(size_t)((r&3)+(r>>2)*1024)*1280); }
    ST(4);
    bar();
    u32 pf[4];
    { const char* wp=(const char*)wo+(j&7)*102400+(tid-256)*128;
      #pragma unroll
      for(int i=0;i<4;++i) pf[i]=*(const u32*)(wp+((i<3||tid<288)?i*32768:0)); }
    { lptr xp=L+(p|1)*A_US+A_UB+lane*16; volatile LAS u32* xf=(volatile LAS u32*)(L+A_XF+2*(p&2));
      if(p&1){
        #pragma unroll
        for(int r=0;r<3;++r) *(volatile LAS f32x4*)(xp+r*1024)=f32x4{xv[4*r],xv[4*r+1],xv[4*r+2],xv[4*r+3]};
        *xf=1u;
      } else { f32x4 o[3];
        for(int it=0;it<SPIN_MAX;++it){ const u32 f=*xf;
          #pragma unroll
          for(int r=0;r<3;++r) o[r]=*(volatile LAS f32x4*)(xp+r*1024);
          if(__builtin_amdgcn_readfirstlane(f)) break; }
        #pragma unroll
        for(int r=0;r<12;++r) xv[r]+=o[r>>2][r&3];
        k_finish(xv,L+p*A_US+A_UB,XKB,lane); } }
    #pragma unroll
    for(int h=0;h<2;++h) scatter(L+8*A_US+(224+4*(tid-256)+1024*h)*2,ux[4*h],ux[4*h+1],ux[4*h+2],ux[4*h+3],h==1&&tid==511);
    ST(8);
    #pragma unroll
    for(int i=0;i<2;++i){ u32 r[4][2]; const int c=2*p+i;
      #pragma unroll
      for(int e=0;e<4;++e) fin4(r[e],lds_ld<f32x4>(L+A_K0+e*A_KS+c*1024+lane*16),e,c);
      const u32 o=(256*c+tl)*2560+20*j;
      #pragma unroll
      for(int h=0;h<2;++h){
        __builtin_amdgcn_raw_buffer_store_b64(u32x2{p_ll(r[0][h],r[1][h]),p_ll(r[2][h],r[3][h])},ro,o+4*h*2560,0,0);
        __builtin_amdgcn_raw_buffer_store_b64(u32x2{p_hh(r[0][h],r[1][h]),p_hh(r[2][h],r[3][h])},ro,o+(4*h+1)*2560,0,0); } }
    asm volatile(""::"v"(pf[0]),"v"(pf[1]),"v"(pf[2]),"v"(pf[3]));
    ST(5);
    bar();
    #pragma unroll
    for(int i=0;i<2;++i)
      #pragma unroll
      for(int e=0;e<4;++e){ u32 r[2]; lptr q=L+A_K0+(4+e)*A_KS+(p+4*i)*1024+lane*16; fin4(r,lds_ld<f32x4>(q),4+e,p+4*i); lds_st<u32x2>(q,u32x2{r[0],r[1]}); }
    ST(6);
    bar();
  }
  {
    u32 r[6][2];
    #pragma unroll
    for(int e=0;e<4;++e){ const u32x2 v=lds_ld<u32x2>(L+A_K0+(4+e)*A_KS+tid*16); r[e][0]=v.x; r[e][1]=v.y; }
    #pragma unroll
    for(int e=0;e<2;++e){ lptr q=L+A_K0+2*e*A_KS+tid*16; fin4(r[4+e],lds_ld<f32x4>(q)+lds_ld<f32x4>(q+A_KS),8+e,w); }
    const u32 o=(256*w+tl)*2560+20*j+8;
    #pragma unroll
    for(int h=0;h<2;++h){
      __builtin_amdgcn_raw_buffer_store_b96(u32x3{p_ll(r[0][h],r[1][h]),p_ll(r[2][h],r[3][h]),p_ll(r[4][h],r[5][h])},ro,o+4*h*2560,0,0);
      __builtin_amdgcn_raw_buffer_store_b96(u32x3{p_hh(r[0][h],r[1][h]),p_hh(r[2][h],r[3][h]),p_hh(r[4][h],r[5][h])},ro,o+(4*h+1)*2560,0,0); } }
  ST(7); STDUMP();
}

constexpr int B_ST=36864, B_PV=2*B_ST, B_PVS=18432, B_WP=4*B_ST, B_Z=B_WP+6400, B_F=163200;
DI float max3(float a,float b,float c){ return __builtin_fmaxf(__builtin_fmaxf(a,b),c); }
template<class H> DI void softmax20x2(const f32x4 (*z)[5], u32 (*o)[10], H hook){
  const float c=1.4426950408889634f; float mc[2];
  { float m0[2],m1[2],m2[2];
    #pragma unroll
    for(int t=0;t<2;++t){ m0[t]=max3(z[t][0][0],z[t][0][1],z[t][0][2]); m1[t]=max3(z[t][0][3],z[t][1][0],z[t][1][1]); m2[t]=max3(z[t][1][2],z[t][1][3],z[t][2][0]); }
    #pragma unroll
    for(int t=0;t<2;++t){ m0[t]=max3(m0[t],z[t][2][1],z[t][2][2]); m1[t]=max3(m1[t],z[t][2][3],z[t][3][0]); m2[t]=max3(m2[t],z[t][3][1],z[t][3][2]); }
    #pragma unroll
    for(int t=0;t<2;++t){ m0[t]=max3(m0[t],z[t][3][3],z[t][4][0]); m1[t]=max3(m1[t],z[t][4][1],z[t][4][2]); m2[t]=__builtin_fmaxf(m2[t],z[t][4][3]); }
    #pragma unroll
    for(int t=0;t<2;++t) mc[t]=max3(m0[t],m1[t],m2[t])*-c; }
  SB();
  hook(0);
  f32x2 e[2][10];
  #pragma unroll
  for(int i=0;i<10;++i)
    #pragma unroll
    for(int t=0;t<2;++t) e[t][i]=__builtin_elementwise_fma(f32x2{z[t][i>>1][(i&1)*2],z[t][i>>1][(i&1)*2+1]},f32x2{c,c},f32x2{mc[t],mc[t]});
  SB();
  hook(1);
  #pragma unroll
  for(int i=0;i<10;++i)
    #pragma unroll
    for(int t=0;t<2;++t) e[t][i]=f32x2{__builtin_amdgcn_exp2f(e[t][i].x),__builtin_amdgcn_exp2f(e[t][i].y)};
  SB();
  hook(2);
  float r[2];
  { f32x2 s[2][5];
    #pragma unroll
    for(int i=0;i<5;++i)
      #pragma unroll
      for(int t=0;t<2;++t) s[t][i]=e[t][2*i]+e[t][2*i+1];
    #pragma unroll
    for(int t=0;t<2;++t){ s[t][0]+=s[t][1]; s[t][2]+=s[t][3]; }
    #pragma unroll
    for(int t=0;t<2;++t){ s[t][0]+=s[t][4]; }
    #pragma unroll
    for(int t=0;t<2;++t){ s[t][0]+=s[t][2]; }
    #pragma unroll
    for(int t=0;t<2;++t) r[t]=__builtin_amdgcn_rcpf(s[t][0].x+s[t][0].y); }
  SB();
  #pragma unroll
  for(int i=0;i<10;++i)
    #pragma unroll
    for(int t=0;t<2;++t) e[t][i]*=r[t];
  SB();
  #pragma unroll
  for(int i=0;i<10;++i)
    #pragma unroll
    for(int t=0;t<2;++t) o[t][i]=pk_bf16(e[t][i].x,e[t][i].y);
  SB();
}
DI u32x4 pack2(f32x4 a,f32x4 b){ u32x4 r={pk_bf16(a[0],a[1]),pk_bf16(a[2],a[3]),pk_bf16(b[0],b[1]),pk_bf16(b[2],b[3])}; return r; }
DI void role_b(const bool ISQ,int X,int li,lptr L,u32 warm,const u16* x,const u16* wqkv,const u16* wphi,const u16* wo,u16* out,void* ws){
  const int tid=threadIdx.x, lane=tid&63, w=__builtin_amdgcn_readfirstlane(tid>>6), g=lane>>4;
  const int rt=4*(X>>1)+(ISQ?(li>>1):(li>>2));
  const int grp=ISQ?(2*(X&1)+(li&1)):(4*(X&1)+(li&3));
  const int T=ISQ?16+li:li; const bool doout=T<20; const int tt64=4*X+T/5, ct=T%5;
  const rsrc_t rs=__builtin_amdgcn_make_buffer_rsrc(ws,0,WS_END,0x00020000);
  constexpr int OIM=B_PV;
  u64 E=epoch_late(ws);
  auto arrive=[&](){
    u32 n=0; if(lane==0) n=__hip_atomic_fetch_add((LAS u32*)(L+B_F+16),1u,__ATOMIC_RELAXED,__HIP_MEMORY_SCOPE_WORKGROUP);
    if(__builtin_amdgcn_readfirstlane(n)==3u&&lane==0) flag_set(ws,ISQ?(128+4*rt+grp):(16*grp+rt),E); };
  volatile LAS u32* bad=(volatile LAS u32*)(L+B_F+20);
  if(__builtin_expect(w>=4,1)){
    const int hw=w-4;
    const int s0=(0x8530>>((ISQ?((li>>1)+2*(li&1)):((li>>2)+(li&3)))&3)*4&15)+(ISQ?1:0);
    u32 xo[4], wv[5];
    #pragma unroll
    for(int i=0;i<4;++i){ int r=(4*hw+i)*8+(lane>>3); xo[i]=(u32)(rt*128+r)*1280u+(u32)(((lane&7)^((r>>1)&7))<<4)-1024u*i; }
    const u32 wlo=ISQ?320*grp:1280+160*grp, whi=ISQ?320*grp:2400+160*grp;
    #pragma unroll
    for(int i=0;i<5;++i){ u32 c=(5*hw+i)*64+lane, r=(c*3277u)>>16, pos=c-r*20, sc=pos^(((r>>3)&1)<<1);
      wv[i]=r*3840u+(sc<10?wlo:whi)+16*sc-1024u*(i&3); }
    auto issue=[&](int s){ int ss=s+s0; if(ss>=10) ss-=10; lptr d=L+(s&3)*B_ST;
      const char* xb=(const char*)x+ss*128; const char* wb=(const char*)wqkv+(size_t)ss*64*3840;
      dma16x4(wb,wv[0],wv[1],wv[2],wv[3],d+16384+5*hw*1024); dma16x4(xb,xo[0],xo[1],xo[2],xo[3],d+4*hw*1024); dma16(wb,wv[4],d+16384+(5*hw+4)*1024); };
    ST(9);
    issue(0); issue(1);
    asm volatile("":"+s"(E));
    ST(1); waitv<9>(); rbar(); issue(2);
    ST(2);
    waitv<9>(); rbar();
    #pragma unroll 1
    for(int s=2;s<9;++s){
      issue(s+1);
      waitv<9>(); rbar(); }
    waitv<0>(); rbar();
    if(!doout){ STDUMP(); return; }
    if(!ISQ){
      const u32x4 sv={SENT,SENT,SENT,SENT};
      #pragma unroll
      for(int i=0;i<4;++i) st_sc1(rs,(i<3||lane<18)?(WS_P1+((4*grp+hw)*16+rt)*3584+i*1024+lane*16):OOB,sv);
      #pragma unroll
      for(int i=0;i<5;++i) st_sc1(rs,WS_O+((8*rt+grp)*20+5*hw+i)*1024+lane*16,sv);
      waitv<0>(); arrive();
      ST(12); }
    auto go=[&](u32 n){ for(int it=0;it<SPIN_MAX;++it){ if(__builtin_amdgcn_readfirstlane(*(volatile LAS u32*)(L+B_F+24))>=n) break; __builtin_amdgcn_s_sleep(2); } asm volatile("":::"memory"); };
    u32x4 wf[2][20];
    {
      const int pr=hw>>1, me2=hw&1, r=lane>>3;
      const char* gb=(const char*)wo+ct*256+pr*128+me2*8*1280;
      const u32 sw=((lane&7)^((((r>>1)&1)|(me2<<1))<<1))<<4, vo=r*1280+sw, vo2=((r>>2)*20+(r&3)+16+32*me2)*1280+sw;
      auto V=[&](int n,int k){ const int G=n/10, m=n%10; return ((m<8)?vo+(160*G+20*m)*1280:vo2+(160*G+80*(m-8))*1280)-1024*k; };
      auto four=[&](int n,lptr d){ dma16x4(gb,V(n,0),V(n+1,1),V(n+2,2),V(n+3,3),d); };
      const int q=(lane&15)>>2, p=lane&3, f=((q>>1)|((g&1)<<1))<<1;
      lptr rb=L+(pr*2+(g&1))*18432+(g>>1)*1024+q*128+8*(p&1);
      lptr rd=rb+(((4*me2+(p>>1))^f)<<4), rd1=rb+(((4*me2+2+(p>>1))^f)<<4);
      constexpr int TAIL=B_PV+12288;
      lptr dst=L+hw*18432;
      volatile LAS u32* fm=(volatile LAS u32*)(L+B_F+48+4*hw); volatile LAS u32* fo=(volatile LAS u32*)(L+B_F+48+4*(hw^1));
      auto meet=[&](u32 n){ *fm=n; for(int it=0;it<SPIN_MAX;++it){ if(__builtin_amdgcn_readfirstlane(*fo)>=n) break; __builtin_amdgcn_s_sleep(1); } asm volatile("":::"memory"); };
      go(4u); ST(3);
      four(0,dst); four(4,dst+4096); four(8,dst+8192); four(12,dst+12288); dma16(gb,V(16,0),dst+16384); dma16(gb,V(17,0),dst+17408);
      four(18,dst+TAIL); dma16(gb,V(22,0),dst+TAIL+4096); dma16(gb,V(23,0),dst+TAIL+5120);
      ST(10); waitv<0>(); ST(11); meet(1);
      #pragma unroll
      for(int s=0;s<12;++s){ const int o=(s<9)?s*2048:TAIL+(s-9)*2048;
        wf[0][s]=cat(lds_tr(rd+o),lds_tr(rd+o+512)); wf[1][s]=cat(lds_tr(rd1+o),lds_tr(rd1+o+512)); }
      asm volatile("s_waitcnt lgkmcnt(0)":::"memory");
      if(lane==0) __hip_atomic_fetch_add((LAS u32*)(L+B_F+28),1u,__ATOMIC_RELAXED,__HIP_MEMORY_SCOPE_WORKGROUP);
      meet(2);
      go(ISQ?4u:8u);
      four(24,dst); four(28,dst+4096); four(32,dst+8192); four(36,dst+12288);
      ST(13); waitv<0>(); ST(14); meet(3);
      #pragma unroll
      for(int s=0;s<8;++s){ wf[0][12+s]=cat(lds_tr(rd+s*2048),lds_tr(rd+s*2048+512)); wf[1][12+s]=cat(lds_tr(rd1+s*2048),lds_tr(rd1+s*2048+512)); }
      asm volatile("s_waitcnt lgkmcnt(0)":::"memory"); }
    ST(4);
    for(u32 it=1;it<SPIN_MAX;++it){
      rbar();
      ST(5);
      f32x4 c[2][4];
      #pragma unroll
      for(int t=0;t<4;++t){ c[0][t]=Z4; c[1][t]=Z4; }
      u32x4 ob[2][4]; u32 chk=0;
      lptr ob0=L+OIM+lane*16;
      #pragma unroll
      for(int t=0;t<4;++t) ob[0][t]=lds_ld<u32x4>(ob0+((t+hw)&3)*20480);
      lptr obt[4];
      #pragma unroll
      for(int t=0;t<4;++t) obt[t]=ob0+((t+hw)&3)*20480;
      SB();
      #pragma unroll
      for(int ks=0;ks<20;++ks){
        #pragma unroll
        for(int t=0;t<4;++t){ if(ks<19) ob[(ks+1)&1][t]=lds_ld<u32x4>(obt[t]+(ks+1)*1024);
          c[0][t]=mfma16(wf[0][ks],ob[ks&1][t],c[0][t]); c[1][t]=mfma16(wf[1][ks],ob[ks&1][t],c[1][t]); }
        chk=max(chk,max(ob[ks&1][0].x,ob[ks&1][0].z));
        SB(); }
      if(__any(chk==SENT)) *bad=it;
      #pragma unroll
      for(int t=0;t<4;++t)
        #pragma unroll
        for(int cc=0;cc<2;++cc) lds_st<u32x2>(L+(16*((t+hw)&3)+(lane&15))*272+hw*64+cc*32+8*g,u32x2{pk_bf16(c[cc][t][0],c[cc][t][1]),pk_bf16(c[cc][t][2],c[cc][t][3])});
      ST(6);
      bar();
      if(__builtin_amdgcn_readfirstlane(*bad)!=it) break; }
  } else {
    const int th=w>>1, hh=w&1;
    { const int NB=ISQ?3200:1600; const char* wp=(const char*)wphi+(ISQ?8*grp:4*grp)*800+hh*NB;
      #pragma unroll
      for(int i=0;i<4;++i) if(i*1024+lane*16<NB) dma16(wp,i*1024+lane*16,L+B_WP+hh*NB+i*1024); }
    if(lane<4) lds_st<u32x4>(L+B_Z+lane*16,u32x4{0,0,0,0});
    if(tid<4) lds_st<u32x4>(L+B_F+tid*16,u32x4{0,0,0,0});
    asm volatile("":"+s"(E));
    asm volatile("s_waitcnt vmcnt(0)"::"v"(warm):"memory");
    ST(9);
    lptr fad[3][2];
    { const int q=(lane&15)>>2, p=lane&3, hd=ISQ?p:(p&1);
      #pragma unroll
      for(int s=0;s<3;++s)
        #pragma unroll
        for(int t=0;t<2;++t){ const int d=16*(2*s+t)+4*g-20*hd; const bool ok=(d>=0)&&(d<20)&&(2*s+t<5);
          fad[s][t]=ok?(L+B_WP+((ISQ?4*hh:2*hh)+hd)*800+(d+q)*40+(ISQ?0:(p>>1)*3200)):(L+B_Z); if(2*s+t<5) asm volatile("":"+v"(fad[s][t])); } }
    lptr pv=L+B_PV+w*B_PVS;
    const int oh=4*grp+(rt&3), tq=rt>>2;
    const int ot=4*tt64+w;
    int fi=(lane<16)?(16*grp+lane):((lane<32)?(16*(lane&7)+4*tq+w):((lane<48)?(16*(ot&7)+(ot>>3)):(128+4*(4*tq+w)+(oh>>3))));
    lptr pp=pv+(lane&15)*160+(g&1)*80+(g>>1)*40, vp2, vp3, vp4, pa, vb;
    { lptr vp=pv+10272+(lane&15)*96; vp2=vp+((g<2)?(48*g+40):(8*g-16)); vp3=vp+((g==3)?48:(16+8*g)); vp4=vp+56+8*g;
      const int tk=16*(g>>1)+4*(g&1)+((lane&15)>>2); pa=pv+tk*160+8*(lane&3)+(th^1)*80; vb=pv+10272+tk*96+8*(lane&3)+(th^1)*48; }
    u32 qo=WS_Q+(oh*2048+512*tq+128*w+16*(g>>1)+(lane&15))*40, po0, po1;
    { const u32 po=WS_P1+((4*grp+2*hh+th)*16+rt)*3584; po0=po+lane*16; po1=((lane&15)<5)?(po+(160+5*g+(lane&15))*16):OOB; }
    asm volatile("":"+v"(fi),"+v"(pp),"+v"(vp2),"+v"(vp3),"+v"(vp4),"+v"(pa),"+v"(vb),"+v"(qo),"+v"(po0),"+v"(po1));
    if(!ISQ){
      #pragma unroll
      for(int i=0;i<2;++i) if(i*1024+lane*16<1600){ lptr q=L+B_WP+hh*1600+i*1024+lane*16; lds_st<u32x4>(q+3200,lds_ld<u32x4>(q)^0x80008000u); } }
    lptr ab[5], bb[2];
    { const int q=(lane&15)>>2,p=lane&3;
      #pragma unroll
      for(int i=0;i<5;++i){ int n=2*i+(p>>1); int cid=ISQ?(10*hh+n):(5*hh+n+(n>=5?5:0)); ab[i]=L+16384+(8*g+q)*320+((cid^((g&1)<<1))<<4)+8*(p&1); }
      const int b0=(64*th+(lane&15))*128+((g^((lane&15)>>1))<<4); bb[0]=L+b0; bb[1]=L+(b0^64); }
    f32x4 acc[5][4];
    #pragma unroll
    for(int i=0;i<5;++i)
      #pragma unroll
      for(int t=0;t<4;++t) acc[i][t]=Z4;
    ST(1);
    u32x4 FA[2][5], FB[2][4], fa[2][5];
    auto half=[&](int hf,bool mm,bool last){
      #pragma unroll
      for(int i=0;i<5;++i){
        if(i<2){ FB[hf][2*i]=lds_ld<u32x4>(bb[hf]+4096*i); FB[hf][2*i+1]=lds_ld<u32x4>(bb[hf]+4096*i+2048); }
        FA[hf][i]=cat(lds_tr(ab[i]+hf*10240),lds_tr(ab[i]+hf*10240+1280));
        if(mm){
          #pragma unroll
          for(int t=0;t<4;++t) acc[i][t]=mfma16(FA[hf^1][i],FB[hf^1][t],acc[i][t]); }
        if(last){ fa[0][i]=cat(lds_tr(fad[0][0]+8*i),lds_tr(fad[0][1]+8*i)); fa[1][i]=cat(lds_tr(fad[1][0]+8*i),lds_tr(fad[1][1]+8*i)); }
        SB(); } };
    bar();
    ST(2);
    SB();
    half(0,false,false);
    #pragma unroll 1
    for(int s=1;s<10;++s){
      half(1,true,false);
      const int step=(s&3)?B_ST:-3*B_ST;
      #pragma unroll
      for(int i=0;i<5;++i) ab[i]+=step;
      bb[0]+=step; bb[1]+=step;
      rbar();
      SB();
      half(0,true,false); }
    half(1,true,true);
    auto signal=[&](){ asm volatile("":::"memory"); if(lane==0) __hip_atomic_fetch_add((LAS u32*)(L+B_F+24),1u,__ATOMIC_RELAXED,__HIP_MEMORY_SCOPE_WORKGROUP); };
    #pragma unroll
    for(int t=0;t<4;++t)
      #pragma unroll
      for(int i=0;i<5;++i) acc[i][t]=mfma16(FA[1][i],FB[1][t],acc[i][t]);
    u32x2 pk[5][4];
    #pragma unroll
    for(int t=0;t<4;++t)
      #pragma unroll
      for(int i=0;i<5;++i) pk[i][t]=u32x2{pk_bf16(acc[i][t][0],acc[i][t][1]),pk_bf16(acc[i][t][2],acc[i][t][3])};
    ST(3);
    if(ISQ){
      u32x4 f2[5];
      #pragma unroll
      for(int i=0;i<5;++i) f2[i]=cat(lds_tr(fad[2][0]+8*i),u32x2{0u,0u});
      lptr sp_=pv+g*2560+(lane&15)*40;
      f32x4 z[4][5];
      #pragma unroll
      for(int t=0;t<4;++t)
        #pragma unroll
        for(int i=0;i<5;++i) z[t][i]=mfma16(fa[0][i],cat(pk[0][t],pk[1][t]),Z4);
      #pragma unroll
      for(int t=0;t<4;++t)
        #pragma unroll
        for(int i=0;i<5;++i) z[t][i]=mfma16(fa[1][i],cat(pk[2][t],pk[3][t]),z[t][i]);
      SB();
      #pragma unroll
      for(int t=0;t<5;++t){
        #pragma unroll
        for(int i=0;i<5;++i){ if(t<4) z[t][i]=mfma16(f2[i],cat(pk[4][t],u32x2{0u,0u}),z[t][i]);
          if(t>0) lds_st<u32x2>(sp_+(t-1)*640+8*i,u32x2{pk_bf16(z[t-1][i][0],z[t-1][i][1]),pk_bf16(z[t-1][i][2],z[t-1][i][3])}); }
        SB(); }
      ST(10);
      #pragma unroll
      for(int i=0;i<10;++i){ const int c=i*64+lane, hd=(c*205)>>15;
        st_sc1(rs,WS_Q+((8*grp+4*hh+hd)*2048+128*rt+64*th)*40+(c-160*hd)*16,lds_ld<u32x4>(pv+c*16)); }
      ST(11);
      waitv<0>(); ST(12); arrive(); signal();
      ST(4);
      if(doout){ fi=16*(ot&7)+(ot>>3); flag_wait(ws,E,fi,flag_get(ws,fi)); }
    } else {
      { f32x4 z[4][5]; u32 ph[4][10];
        auto fm=[&](int t){
          const u32x4 b0=cat(pk[0][t],pk[1][t]), b1=cat(pk[2][t],pk[3][t]);
          #pragma unroll
          for(int i=0;i<5;++i) z[t][i]=mfma16(fa[0][i],b0,Z4);
          #pragma unroll
          for(int i=0;i<5;++i) z[t][i]=mfma16(fa[1][i],b1,z[t][i]); };
        fm(0); fm(1);
        SB();
        softmax20x2(z,ph,[&](int k){ if(k==0){ fm(2); } else if(k==1){ fm(3); } });
        auto wp=[&](int t){
          #pragma unroll
          for(int i=0;i<5;++i) lds_st<u32x2>(pp+t*2560+8*i,u32x2{ph[t][2*i],ph[t][2*i+1]}); };
        softmax20x2(z+2,ph+2,[&](int k){
          if(k==0){ wp(0); wp(1); }
          else if(k==1){
            #pragma unroll
            for(int t=0;t<4;++t){ u32x2 v=pk[2][t]; if(g<2) v=u32x2{0x3F80u,0u};
              lds_st<u32x2>(vp2+t*1536,v); lds_st<u32x2>(vp3+t*1536,pk[3][t]); lds_st<u32x2>(vp4+t*1536,pk[4][t]); } } });
        wp(2); wp(3); }
      ST(4);
      u64 fl;
      lptr q00,q01,q10,q11; u32 om,ox;
      { const int r=lane&15, pc=5*(g&1)+2*(g>>1), pl=5*(g&1)+4, d1=min(r,4); lptr b=L+B_PV+8192;
        q00=b+(16*pc+r)*16; q01=b+(16*pl+r)*16+8*(g>>1); q10=b+(160+5*pc+d1)*16; q11=b+(160+5*pl+d1)*16+8*(g>>1);
        const u32 fb=WS_O+((32*tq+8*w)*20+5*(oh>>3))*1024+r*16;
        om=fb+((oh&7)>>1)*1024+(2*(oh&1)+(g>>1))*256+8*(g&1); ox=(g==0)?(fb+4096+((oh&7)>>1)*256+8*(oh&1)):OOB; }
      u32x4 l0[4],l1[4]; u32x2 l2[4];
      auto ldq=[&](){
        #pragma unroll
        for(int i=0;i<4;++i){ l0[i]=ld_sc1(rs,qo+i*1280); l1[i]=ld_sc1(rs,qo+i*1280+16); l2[i]=__builtin_amdgcn_raw_buffer_load_b64(rs,qo+i*1280+32,0,16); } };
      { f32x4 c[2][6];
        #pragma unroll
        for(int h2=0;h2<2;++h2){
          if(h2){ pa+=th?80:-80; vb+=th?48:-48; }
          u32x4 a[2][3], b[2][2];
          #pragma unroll
          for(int ks=0;ks<2;++ks){
            #pragma unroll
            for(int i=0;i<3;++i) a[ks][i]=cat(lds_tr(pa+ks*32*160+32*i),lds_tr(pa+(ks*32+8)*160+32*i));
            #pragma unroll
            for(int dt=0;dt<2;++dt) b[ks][dt]=cat(lds_tr(vb+ks*32*96+32*dt),lds_tr(vb+(ks*32+8)*96+32*dt)); }
          #pragma unroll
          for(int i=0;i<6;++i) c[h2][i]=mfma16(a[0][i%3],b[0][i/3],Z4);
          #pragma unroll
          for(int i=0;i<6;++i) c[h2][i]=mfma16(a[1][i%3],b[1][i/3],c[h2][i]); }
        #pragma unroll
        for(int i=0;i<6;++i) *(volatile LAS f32x4*)(pv+i*1024+lane*16)=c[0][i];
        *(volatile LAS u32*)(L+B_F+4*w)=1u;
        #pragma unroll
        for(int i=0;i<6;++i) asm volatile("":"+v"(c[1][i]));
        lptr ox_=L+B_PV+(w^2)*B_PVS; f32x4 pr[6];
        for(int it=0;it<SPIN_MAX;++it){
          const u32 f=*(volatile LAS u32*)(L+B_F+4*(w^2)), n=*(volatile LAS u32*)(L+B_F+16);
          #pragma unroll
          for(int i=0;i<6;++i) pr[i]=*(volatile LAS f32x4*)(ox_+i*1024+lane*16);
          if(__builtin_amdgcn_readfirstlane(f+n)==5u) break; }
        fl=flag_get(ws,fi);
        #pragma unroll
        for(int i=0;i<6;++i){ u32 o=(i<3)?(po0+i*1024):(po1+(i-3)*320); if(i%3==2) o=(g<2)?o:OOB;
          st_sc1(rs,o,__builtin_bit_cast(u32x4,c[1][i]+pr[i])); } }
      ST(5);
      if(!__all(fl==E)) flag_wait(ws,E,fi,fl);
      asm volatile("":::"memory"); ldq();
      u32 pq[4][10];
      {
        ST(11);
        const u32 sg=(g&1)?0x80008000u:0u;
        u32x4 v[16]; const u32 go0=WS_P1+(oh*16+4*w)*3584+lane*16, go1=(lane<18)?go0+3072:OOB;
        #pragma unroll
        for(int i2=0;i2<4;i2+=2){ f32x4 z[2][5];
          #pragma unroll
          for(int t=0;t<2;++t){ const u32x4 a=l0[i2+t]^sg, b=l1[i2+t]^sg; const u32x2 c=l2[i2+t]^sg;
            z[t][0]=f32x4{bflo(a.x),bfhi(a.x),bflo(a.y),bfhi(a.y)}; z[t][1]=f32x4{bflo(a.z),bfhi(a.z),bflo(a.w),bfhi(a.w)};
            z[t][2]=f32x4{bflo(b.x),bfhi(b.x),bflo(b.y),bfhi(b.y)}; z[t][3]=f32x4{bflo(b.z),bfhi(b.z),bflo(b.w),bfhi(b.w)};
            z[t][4]=f32x4{bflo(c.x),bfhi(c.x),bflo(c.y),bfhi(c.y)}; }
          if(i2==0){
            #pragma unroll
            for(int k=0;k<16;++k) v[k]=ld_sc1(rs,((k&3)<3?go0+(k&3)*1024:go1)+(k>>2)*3584);
            signal(); }
          SB();
          softmax20x2(z,pq+i2,[](int){});
          #pragma unroll
          for(int i=i2;i<i2+2;++i){
            #pragma unroll
            for(int k=0;k<5;++k){ const int a=(k<4)?k:8, b=(k<4)?k+4:9;
              auto r=__builtin_amdgcn_permlane32_swap(pq[i][a],pq[i][b],false,false); pq[i][a]=r[0]; pq[i][b]=r[1]; }
            #pragma unroll
            for(int k=0;k<10;++k) asm volatile("":"+v"(pq[i][k])); }
          if(i2==0){ gather4<1>(rs,go0,go1,v); SB(); } }
        asm volatile("":"+v"(q00),"+v"(q01),"+v"(q10),"+v"(q11),"+v"(om),"+v"(ox));
        ST(10);
        gather4<SPIN_MAX>(rs,go0,go1,v);
        ST(13);
        #pragma unroll
        for(int i=0;i<4;++i) *(volatile LAS f32x4*)(pv+8192+i*1024+lane*16)=(__builtin_bit_cast(f32x4,v[i])+__builtin_bit_cast(f32x4,v[4+i]))+(__builtin_bit_cast(f32x4,v[8+i])+__builtin_bit_cast(f32x4,v[12+i]));
        *(volatile LAS u32*)(L+B_F+32+4*w)=1u;
        SB();
      }
      ST(12);
      u32x4 a0[2]; u32 a1[2];
      {
        f32x4 s[2][2]; f32x2 u[2];
        for(int it=0;it<SPIN_MAX;++it){
          const u32x4 f=*(volatile LAS u32x4*)(L+B_F+32);
          #pragma unroll
          for(int k=0;k<4;++k){ const int q=k*B_PVS;
            const f32x4 x0=*(volatile LAS f32x4*)(q00+q), x1=*(volatile LAS f32x4*)(q00+q+256), y0=*(volatile LAS f32x4*)(q10+q), y1=*(volatile LAS f32x4*)(q10+q+80);
            const f32x2 u0=*(volatile LAS f32x2*)(q01+q), u1=*(volatile LAS f32x2*)(q11+q);
            if(k==0){ s[0][0]=x0; s[0][1]=x1; s[1][0]=y0; s[1][1]=y1; u[0]=u0; u[1]=u1; }
            else { s[0][0]+=x0; s[0][1]+=x1; s[1][0]+=y0; s[1][1]+=y1; u[0]+=u0; u[1]+=u1; } }
          if(__builtin_amdgcn_readfirstlane(f.x+f.y+f.z+f.w)==4u) break; }
        #pragma unroll
        for(int i=0;i<2;++i){ a0[i]=pack2(s[i][0],s[i][1]); a1[i]=pk_bf16(u[i].x,u[i].y); } }
      ST(14);
      { f32x4 n[8][2];
        #pragma unroll
        for(int t=0;t<8;++t){ const u32* f=pq[t>>1]+4*(t&1);
          #pragma unroll
          for(int i=0;i<2;++i) n[t][i]=mfma16(a0[i],u32x4{f[0],f[1],f[2],f[3]},Z4); }
        #pragma unroll
        for(int t=0;t<8;++t)
          #pragma unroll
          for(int i=0;i<2;++i) n[t][i]=mfma16(u32x4{a1[i],0u,0u,0u},u32x4{pq[t>>1][8+(t&1)],0u,0u,0u},n[t][i]);
        SB();
        float den[8];
        #pragma unroll
        for(int t=0;t<8;++t) den[t]=__builtin_bit_cast(float,__builtin_amdgcn_ds_bpermute(((lane&15)+16)*4,__builtin_bit_cast(int,n[t][1][0])));
        SB();
        #pragma unroll
        for(int t=0;t<8;++t){ const float r=__builtin_amdgcn_rcpf(rbf(rbf(den[t])+1e-6f));
          const f32x4 m=n[t][0]*r, e=n[t][1]*r;
          __builtin_amdgcn_raw_buffer_store_b64(u32x2{pk_bf16(m[0],m[1]),pk_bf16(m[2],m[3])},rs,om+t*20480,0,16);
          __builtin_amdgcn_raw_buffer_store_b64(u32x2{pk_bf16(e[0],e[1]),pk_bf16(e[2],e[3])},rs,ox+t*20480,0,16); } }
      signal();
      if(rt==15&&grp==7&&w==3){
        for(int it=0;it<SPIN_MAX;++it){ if(__all(flag_get(ws,lane)==E&&flag_get(ws,64+lane)==E&&flag_get(ws,128+lane)==E&&flag_get(ws,192+lane)==E)) break; __builtin_amdgcn_s_sleep(2); }
        if(lane==0) __hip_atomic_store((GAS u64*)ws,E,__ATOMIC_RELAXED,__HIP_MEMORY_SCOPE_AGENT); }
      ST(6);
    }
    if(!doout){ STDUMP(); return; }
    for(int it=0;it<SPIN_MAX;++it){ const u32x2 c=*(volatile LAS u32x2*)(L+B_F+24); if(__builtin_amdgcn_readfirstlane(c.x+c.y)>=(ISQ?8u:12u)) break; __builtin_amdgcn_s_sleep(1); }
    { const u32 ob=WS_O+ot*20480;
      const u32 sm=ob+((lane<32)?((5*(lane>>3)+((lane>>1)&3))*1024+(lane&1)*512+508):((5*((lane-32)>>3)+4)*1024+((lane&7)>>1)*256+244+8*(lane&1)));
      for(int it=0;it<SPIN_MAX;++it){ asm volatile("":::"memory");
        if(__all(__builtin_amdgcn_raw_buffer_load_b32(rs,sm,0,16)!=SENT)) break;
        __builtin_amdgcn_s_sleep(1); }
      ST(15);
      const u32 vo=lane*16;
      for(u32 it=1;it<SPIN_MAX;++it){
        #pragma unroll
        for(int i=0;i<5;++i) dma16x4_sc1((const char*)ws+ob+i*4096,vo,vo,vo,vo,L+OIM+w*20480+i*4096);
        waitv<0>();
        ST(7);
        rbar();
        rbar();
        if(__builtin_amdgcn_readfirstlane(*bad)!=it) break; } }
  }
  #pragma unroll
  for(int i=0;i<2;++i){ const int r=32*i+(tid>>4);
    *(u32x4*)((char*)out+(size_t)(64*tt64+r)*2560+1280+ct*256+(tid&15)*16)=lds_ld<u32x4>(L+r*272+(tid&15)*16); }
  ST(8); STDUMP();
}

constexpr int CODE_LINES=400;
__global__ __launch_bounds__(512,2) void fused(const u16* u,const u16* are,const u16* aim,const u16* bre,const u16* bim,const u16* cre,const u16* cim,
    const u16* logdt,const u16* dskip,const u16* x,const u16* wqkv,const u16* wphi,const u16* wo,u16* out,void* ws){
  extern __shared__ __attribute__((aligned(16))) char smem[];
  lptr L=(lptr)smem;
  const int b=blockIdx.x, X=b&7, idx=b>>3;
  u64 pc; asm volatile("s_getpc_b64 %0":"=s"(pc));
  const u64 tstart=__builtin_amdgcn_s_memrealtime(); const u64 cstart=__builtin_amdgcn_s_memtime();
  STINIT(); ST(0);
  const int w=__builtin_amdgcn_readfirstlane(threadIdx.x>>6);
  const bool isa=(idx>=16&&idx<24), isq=idx>=24;
  if(__builtin_expect(!isa,1)){
    u32 warm=0;
    if(idx<16&&threadIdx.x<32&&idx*32+threadIdx.x<CODE_LINES) warm=*(const u32*)((const char*)(pc&~255ull)+(idx*32+threadIdx.x)*128);
    if(idx<16&&w==1&&(threadIdx.x&63)<50) warm=*(const u32*)((const char*)wo+((X*16+idx)*50+(threadIdx.x&63))*128);
    role_b(isq,X,isq?idx-24:idx,L,warm,x,wqkv,wphi,wo,out,ws);
  } else { const SsmIn in={are,aim,bre,bim,cre,cim,logdt}; role_a(X*8+(idx-16),L,u,in,dskip,wo,out,ws); }
  if(threadIdx.x==0){ u64 t=__builtin_amdgcn_s_memrealtime(), c=__builtin_amdgcn_s_memtime(); printf("WG %d xcc %d start %llu dur %llu clk %llu\n",b,(int)(__builtin_amdgcn_s_getreg(20|(3<<11))&15),tstart%1000000,t-tstart,c-cstart); }
}

extern "C" void kernel_launch(void* const* d_in, const int* in_sizes, int n_in, void* d_out, int out_size, void* d_ws, size_t ws_size, hipStream_t stream){
  (void)hipFuncSetAttribute((const void*)fused,hipFuncAttributeMaxDynamicSharedMemorySize,163840);
  fused<<<256,512,163840,stream>>>((const u16*)d_in[0],(const u16*)d_in[1],(const u16*)d_in[2],(const u16*)d_in[3],(const u16*)d_in[4],(const u16*)d_in[5],(const u16*)d_in[6],
    (const u16*)d_in[7],(const u16*)d_in[8],(const u16*)d_in[9],(const u16*)d_in[10],(const u16*)d_in[11],(const u16*)d_in[12],(u16*)d_out,d_ws);
}
```

```cpp
#include <hip/hip_runtime.h>
#include <stdint.h>

typedef unsigned short u16; typedef unsigned u32; typedef unsigned long long u64;
typedef __attribute__((ext_vector_type(4))) short s16x4;
typedef __attribute__((ext_vector_type(8))) _Float16 f16x8;
typedef __attribute__((ext_vector_type(8))) __bf16 bf16x8;
typedef __attribute__((ext_vector_type(2))) __bf16 bf16x2;
typedef __attribute__((ext_vector_type(2))) _Float16 f16x2;
typedef __attribute__((ext_vector_type(2))) float f32x2;
typedef __attribute__((ext_vector_type(4))) float f32x4;
typedef __attribute__((ext_vector_type(16))) float f32x16;
typedef __attribute__((ext_vector_type(2))) unsigned u32x2;
typedef __attribute__((ext_vector_type(3))) unsigned u32x3;
typedef __attribute__((ext_vector_type(4))) unsigned u32x4;
#define LAS __attribute__((address_space(3)))
#define GAS __attribute__((address_space(1)))
#define DI __device__ __forceinline__
typedef LAS char* lptr;
typedef __amdgpu_buffer_rsrc_t rsrc_t;

#define ST(i) do{}while(0)
#define STDUMP() do{}while(0)
#define STINIT() do{}while(0)

DI u32 pk_bf16(float a, float b){ f32x2 v={a,b}; return __builtin_bit_cast(u32,__builtin_convertvector(v,bf16x2)); }
DI u32 pk_f16(f32x2 v){ return __builtin_bit_cast(u32,__builtin_convertvector(v,f16x2)); }
DI float bflo(u32 x){ return __builtin_bit_cast(float,x<<16); }
DI float bfhi(u32 x){ return __builtin_bit_cast(float,x&0xffff0000u); }
DI float rbf(float a){ return bflo(pk_bf16(a,a)); }
template<class T> DI T lds_ld(lptr p){ return *(LAS T*)p; }
template<class T> DI void lds_st(lptr p, T v){ *(LAS T*)p=v; }
DI u32x2 lds_ld64(lptr p){ return *(volatile LAS u32x2*)p; }
DI u32x2 lds_tr(lptr p){ return __builtin_bit_cast(u32x2,__builtin_amdgcn_ds_read_tr16_b64_v4i16((LAS s16x4*)p)); }
DI f32x4 mfma16(u32x4 a,u32x4 b,f32x4 c){ return __builtin_amdgcn_mfma_f32_16x16x32_bf16(__builtin_bit_cast(bf16x8,a),__builtin_bit_cast(bf16x8,b),c,0,0,0); }
DI u32x4 cat(u32x2 a,u32x2 b){ u32x4 r={a.x,a.y,b.x,b.y}; return r; }
DI u32 p_ll(u32 a,u32 b){ return __builtin_amdgcn_perm(b,a,0x05040100u); }
DI u32 p_lh(u32 a,u32 b){ return __builtin_amdgcn_perm(b,a,0x07060100u); }
DI u32 p_hl(u32 a,u32 b){ return __builtin_amdgcn_perm(b,a,0x05040302u); }
DI u32 p_hh(u32 a,u32 b){ return __builtin_amdgcn_perm(b,a,0x07060302u); }
#define SB() __builtin_amdgcn_sched_barrier(0)
DI void rbar(){ asm volatile("":::"memory"); __builtin_amdgcn_s_barrier(); asm volatile("":::"memory"); }
DI void bar(){ asm volatile("s_waitcnt lgkmcnt(0)":::"memory"); __builtin_amdgcn_s_barrier(); asm volatile("":::"memory"); }
template<int N> DI void waitv(){ asm volatile("s_waitcnt vmcnt(%0)"::"i"(N):"memory"); }
constexpr f32x4 Z4={0.f,0.f,0.f,0.f};
DI float wave_max(float m){
  m=fmaxf(m,__builtin_bit_cast(float,__builtin_amdgcn_update_dpp(0,__builtin_bit_cast(int,m),0xB1,0xf,0xf,true)));
  m=fmaxf(m,__builtin_bit_cast(float,__builtin_amdgcn_update_dpp(0,__builtin_bit_cast(int,m),0x4E,0xf,0xf,true)));
  m=fmaxf(m,__builtin_bit_cast(float,__builtin_amdgcn_update_dpp(0,__builtin_bit_cast(int,m),0x141,0xf,0xf,true)));
  m=fmaxf(m,__builtin_bit_cast(float,__builtin_amdgcn_update_dpp(0,__builtin_bit_cast(int,m),0x140,0xf,0xf,true)));
  int x=__builtin_bit_cast(int,m);
  float a=__builtin_bit_cast(float,__builtin_amdgcn_readlane(x,0)), b=__builtin_bit_cast(float,__builtin_amdgcn_readlane(x,16));
  float c=__builtin_bit_cast(float,__builtin_amdgcn_readlane(x,32)), d=__builtin_bit_cast(float,__builtin_amdgcn_readlane(x,48));
  return fmaxf(fmaxf(a,b),fmaxf(c,d)); }

constexpr u32 WS_FLAG=256, WS_P1=4096, WS_Q=WS_P1+32*16*3584, WS_O=WS_Q+32*2048*40, WS_END=WS_O+128*20*1024;
constexpr u32 SENT=0xFFFFFFFFu, OOB=0x80000000u;
constexpr int SPIN_MAX=1<<17;
DI void dma16(const void* sb, u32 voff, lptr l){ u32 keep;
  asm volatile("s_mov_b32 %0, m0\n\ts_mov_b32 m0, %3\n\ts_nop 0\n\tglobal_load_lds_dwordx4 %1, %2\n\ts_mov_b32 m0, %0":"=&s"(keep):"v"(voff),"s"(sb),"s"((u32)(uintptr_t)l):"memory"); }
#define DMA4_BODY(SC) asm volatile("s_mov_b32 %0, m0\n\ts_mov_b32 m0, %6\n\ts_nop 0\n\tglobal_load_lds_dwordx4 %1, %5" SC "\n\tglobal_load_lds_dwordx4 %2, %5 offset:1024" SC \
    "\n\tglobal_load_lds_dwordx4 %3, %5 offset:2048" SC "\n\tglobal_load_lds_dwordx4 %4, %5 offset:3072" SC "\n\ts_mov_b32 m0, %0" \
    :"=&s"(keep):"v"(v0),"v"(v1),"v"(v2),"v"(v3),"s"(sb),"s"((u32)(uintptr_t)l):"memory")
DI void dma16x4(const void* sb,u32 v0,u32 v1,u32 v2,u32 v3,lptr l){ u32 keep; DMA4_BODY(""); }
DI void dma16x4_sc1(const void* sb,u32 v0,u32 v1,u32 v2,u32 v3,lptr l){ u32 keep; DMA4_BODY(" sc1"); }
DI u32x4 ld_sc1(rsrc_t rs,u32 off){ return __builtin_amdgcn_raw_buffer_load_b128(rs,off,0,16); }
DI void st_sc1(rsrc_t rs,u32 off,u32x4 v){ __builtin_amdgcn_raw_buffer_store_b128(v,rs,off,0,16); }
template<int NIT> DI void gather4(rsrc_t rs,u32 off,u32 last,u32x4* v){
  for(int it=0;it<NIT;++it){
    u32 todo=0;
    #pragma unroll
    for(int k=0;k<4;++k){ u32 m=0;
      #pragma unroll
      for(int i=0;i<4;++i) m=max(m,max(v[4*k+i].x,v[4*k+i].z));
      if(__any(m==SENT)) todo|=1u<<k; }
    if(!todo) break;
    asm volatile("":::"memory");
    #pragma unroll
    for(int k=0;k<4;++k) if(todo>>k&1){
      #pragma unroll
      for(int i=0;i<4;++i) v[4*k+i]=ld_sc1(rs,((i<3)?off+i*1024:last)+k*3584); } } }
DI u64 epoch(const void* ws){ u64 E; asm volatile("s_load_dwordx2 %0, %1, 0x0\n\ts_waitcnt lgkmcnt(0)":"=s"(E):"s"(ws):"memory"); return E+1; }
DI u64 epoch_late(const void* ws){ return *(const __attribute__((address_space(4))) u64*)ws+1; }
DI void flag_set(void* ws,int i,u64 E){ __hip_atomic_store((GAS u64*)((char*)ws+WS_FLAG)+i,E,__ATOMIC_RELAXED,__HIP_MEMORY_SCOPE_AGENT); }
DI u64 flag_get(const void* ws,int i){ return __hip_atomic_load((const GAS u64*)((const char*)ws+WS_FLAG)+i,__ATOMIC_RELAXED,__HIP_MEMORY_SCOPE_AGENT); }
DI void flag_wait(const void* ws,u64 E,int idx,u64 f){
  for(int it=0;it<SPIN_MAX;++it){ if(__all(f==E)) break; __builtin_amdgcn_s_sleep(2); f=flag_get(ws,idx); } }

constexpr int A_UB=4576, A_US=9120, A_K0=10*A_US, A_KB=4224, A_KS=8704;
constexpr int A_XF=A_K0+8*A_KS;
static_assert(A_XF+16<=163328,"lds");

DI f32x2 cmul(f32x2 a, f32x2 b){ f32x2 sw={-b.y,b.x}; return __builtin_elementwise_fma(a.yy,sw,a.xx*b); }
DI f32x2 cexp_e(float re2, float rev, float e){
  float m=__builtin_amdgcn_exp2f(re2*e); float a=__builtin_amdgcn_fractf(rev*e);
  f32x2 r={m*__builtin_amdgcn_cosf(a), m*__builtin_amdgcn_sinf(a)}; return r; }
struct SsmP { u32 are,aim,bre,bim,cre,cim,ld; float dt; };
struct SsmIn { const u16 *are,*aim,*bre,*bim,*cre,*cim,*logdt; };
DI SsmP load_ssm(const SsmIn& in, int ch, int lane){
  SsmP p; int o=ch*64+lane;
  p.are=((const u32*)in.are)[o]; p.aim=((const u32*)in.aim)[o]; p.bre=((const u32*)in.bre)[o];
  p.bim=((const u32*)in.bim)[o]; p.cre=((const u32*)in.cre)[o]; p.cim=((const u32*)in.cim)[o];
  p.ld=in.logdt[ch];
  return p; }
DI f32x2 ssm_w(const SsmP& p, int half){
  f32x2 b={half?bfhi(p.bre):bflo(p.bre), half?bfhi(p.bim):bflo(p.bim)};
  f32x2 c={half?bfhi(p.cre):bflo(p.cre), half?bfhi(p.cim):bflo(p.cim)};
  return cmul(c,b*p.dt); }
DI void ssm_prep(SsmP& p, float& s, float& inv){
  asm volatile("":"+v"(p.ld));
  p.dt=rbf(expf(bflo(p.ld)));
  f32x2 w0=ssm_w(p,0), w1=ssm_w(p,1);
  float m=wave_max(fmaxf(fmaxf(fabsf(w0.x),fabsf(w0.y)),fmaxf(fabsf(w1.x),fabsf(w1.y))));
  int e=__builtin_amdgcn_frexp_expf(m); e=max(-100,min(100,e));
  s=__builtin_amdgcn_ldexpf(1.0f,-e); inv=__builtin_amdgcn_ldexpf(1.0f,e); }
DI void kgen_half(const SsmP& p, int half, float s, lptr tab, int lane, f32x16& acc){
  const float are=(half?bfhi(p.are):bflo(p.are))*p.dt, aim=(half?bfhi(p.aim):bflo(p.aim))*p.dt;
  const float re2=are*1.4426950408889634f, rev=aim*0.15915494309189535f;
  lptr wr=tab+lane*4; lptr rd=tab+(lane&31)*272+(lane>>5)*16;
  { f32x2 e8=cexp_e(re2,rev,8.f), e16=cmul(e8,e8);
    f32x2 a={1.f,0.f}, b=e8;
    #pragma unroll
    for(int j=0;j<32;j+=2){ lds_st<u32>(wr+j*272,pk_f16(a)); lds_st<u32>(wr+(j+1)*272,pk_f16(b)); if(j<30){ a=(j==0)?e16:cmul(a,e16); b=cmul(b,e16);} } }
  u32x4 bf[8];
  #pragma unroll
  for(int k=0;k<8;++k) bf[k]=lds_ld<u32x4>(rd+k*32);
  { f32x2 w=ssm_w(p,half)*s; w.y=-w.y;
    f32x2 e1=cexp_e(re2,-rev,1.f), g=cexp_e(re2,-rev,256.f);
    f32x2 g2=cmul(g,g), g3=cmul(g2,g), g4=cmul(g2,g2);
    f32x2 x[8]; x[0]=w;
    #pragma unroll
    for(int i=1;i<8;++i) x[i]=cmul(x[i-1],e1);
    #pragma unroll
    for(int i=0;i<8;++i) lds_st<u32>(wr+i*272,pk_f16(x[i]));
    #pragma unroll
    for(int i=0;i<4;++i) lds_st<u32>(wr+(8+i)*272,pk_f16(cmul(x[2*i],g)));
    lds_st<u32>(wr+12*272,pk_f16(cmul(x[0],g2))); lds_st<u32>(wr+13*272,pk_f16(cmul(x[4],g2)));
    lds_st<u32>(wr+14*272,pk_f16(cmul(x[0],g3))); lds_st<u32>(wr+15*272,pk_f16(cmul(x[4],g3)));
    f32x2 y=cmul(w,g4);
    #pragma unroll
    for(int i=0;i<5;++i){ lds_st<u32>(wr+(16+i)*272,pk_f16(y)); if(i<4) y=cmul(y,g); } }
  #pragma unroll
  for(int k=0;k<8;++k){ u32x4 af=lds_ld<u32x4>(rd+k*32);
    acc=__builtin_amdgcn_mfma_f32_32x32x16_f16(__builtin_bit_cast(f16x8,af),__builtin_bit_cast(f16x8,bf[k]),acc,0,0,0); }
}
DI void k_finish(const float* v, lptr ka, int kbo, int lane){
  lptr ks=ka; lptr kb=ka+kbo;
  { const int j=lane&31, h=lane>>5;
    lds_st<u32x2>(ks+(8*j+4*h)*2,u32x2{pk_bf16(v[0],v[1]),pk_bf16(v[2],v[3])});
    u32x2 b={pk_bf16(v[4],v[5]),pk_bf16(v[6],v[7])};
    u32 c0=pk_bf16(v[8],v[9]), c1=pk_bf16(v[10],v[11]);
    if(h==0){ lds_st<u32x2>(ks+(256+4*j)*2,b);
      lds_st<u16>(ks+(512+j)*2,(u16)c0); lds_st<u16>(ks+(544+j)*2,(u16)(c0>>16));
      lds_st<u16>(ks+(576+j)*2,(u16)c1); lds_st<u16>(ks+(608+j)*2,(u16)(c1>>16));
    } else { lds_st<u32>(ks+(384+2*j)*2,b.x); lds_st<u32>(ks+(448+2*j)*2,b.y); if(j==0) lds_st<u16>(ks+640*2,(u16)c0); } }
  const int M=lane; const bool lo=M<32;
  u32x4 v0=lds_ld<u32x4>(ks+(M&31)*16); u32x2 v1=lds_ld<u32x2>(ks+(128+4*(M|32))*2);
  u32 v4=lds_ld<u16>(ks+(132+4*(M|32))*2);
  u32 pv=lds_ld<u16>(ks+(lo?max(8*M-1,0):((M==32)?255:(128+4*M)))*2); if(M==0) pv=0;
  u32 ab=lds_ld<u32>(ks+(384+2*M)*2), c2=lds_ld<u16>(ks+(386+2*M)*2);
  u32 a3[2],b3[2];
  #pragma unroll
  for(int i=0;i<2;++i){ a3[i]=lds_ld<u16>(ks+(512+M+64*i)*2); b3[i]=lds_ld<u16>(ks+(513+M+64*i)*2); }
  asm volatile("":"+v"(v0),"+v"(v1),"+v"(v4),"+v"(pv),"+v"(ab),"+v"(c2),"+v"(a3[0]),"+v"(a3[1]),"+v"(b3[0]),"+v"(b3[1])::"memory");
  { u32x4 A,B;
    if(lo){ B.x=p_hl(v0.w,v0.w); B.y=p_hl(v0.z,v0.z); B.z=p_hl(v0.y,v0.y); B.w=p_hl(v0.x,v0.x);
      A.x=p_lh(v0.w,v0.z); A.y=p_lh(v0.z,v0.y); A.z=p_lh(v0.y,v0.x); A.w=p_ll(v0.x,pv);
    } else { B.x=p_lh(v4,v1.y); B.y=p_ll(v1.y,v1.y); B.z=p_lh(v1.y,v1.x); B.w=p_ll(v1.x,v1.x);
      A.x=p_hl(v1.y,v1.y); A.y=B.y; A.z=p_hl(v1.x,v1.x); A.w=p_ll(v1.x,pv); }
    lds_st<u32x4>(ka+(255-M)*16,A); lds_st<u32x4>(kb+(255-M)*16,B); }
  { u32x4 A,B; B.x=p_ll(c2,c2); B.y=p_hh(ab,ab); B.z=p_hl(ab,ab); B.w=p_ll(ab,ab);
    A.x=p_lh(c2,ab); A.y=B.y; A.z=B.w; A.w=B.w;
    lds_st<u32x4>(ka+(191-M)*16,A); lds_st<u32x4>(kb+(191-M)*16,B); }
  #pragma unroll
  for(int i=0;i<2;++i){ u32 a=a3[i], b=b3[i], c=(M&1)?b:a; u32x4 A,B;
    B.x=p_ll(b,b); B.y=p_ll(b,c); B.z=p_ll(a,a); B.w=B.z;
    A.x=B.x; A.y=p_ll(c,a); A.z=B.z; A.w=B.z;
    lds_st<u32x4>(ka+(127-64*i-M)*16,A); lds_st<u32x4>(kb+(127-64*i-M)*16,B); }
  if(lane<8) lds_st<u32x4>(((lane&4)?kb:ka)+(256+(lane&3))*16,u32x4{0,0,0,0});
}
template<int NS> DI void conv_steps(lptr ua, lptr ka, int kbo, int s0, int lane, f32x4* acc){
  const int a=lane&15, g=lane>>4;
  lptr up=ua+((a&1)?0:A_UB)+(32*(a>>1)+8*g+32*s0)*2;
  lptr kp=ka+((a&1)?kbo:0)+(28-4*(a>>1)+8*g+32*s0)*2;
  u32x4 A[2][8],B[2][8];
  auto ldA=[&](int s,int q){ A[s&1][q]=cat(lds_ld64(kp+(1792-256*q+32*s)*2),lds_ld64(kp+(1792-256*q+32*s)*2+8)); };
  auto ldB=[&](int s,int q){ B[s&1][q]=lds_ld<u32x4>(up+(256*q+32*s)*2); };
  #pragma unroll
  for(int q=0;q<8;++q){ ldB(0,q); ldA(0,q); }
  #pragma unroll
  for(int c=0;c<8;++c) acc[c]=Z4;
  SB();
  #pragma unroll
  for(int s=0;s<NS;++s){
    #pragma unroll
    for(int q=0;q<8;++q){
      #pragma unroll
      for(int c=q;c<8;++c) acc[c]=mfma16(A[s&1][q],B[s&1][c-q],acc[c]);
      if(s+1<NS){ if(q==0){ ldB(s+1,0); ldB(s+1,1); ldB(s+1,2); ldB(s+1,3); } else if(q==1){ ldA(s+1,0); ldB(s+1,4); ldB(s+1,5); }
        else if(q==2){ ldB(s+1,6); ldB(s+1,7); ldA(s+1,1); } else if(q==3){ ldA(s+1,2); ldA(s+1,3); } else if(q==4){ ldA(s+1,4); ldA(s+1,5); }
        else if(q==5){ ldA(s+1,6); } else if(q==6){ ldA(s+1,7); } }
      SB(); } }
}
DI u32 fin2(float y0,float y1,u32 u,float dsk){
  u32 y=pk_bf16(y0,y1), s=pk_bf16(bflo(u)*dsk,bfhi(u)*dsk);
  return pk_bf16(bflo(y)+bflo(s),bfhi(y)+bfhi(s)); }
DI void role_a(int j, lptr L, const u16* u, const SsmIn& in, const u16* dskip, const u16* wo, u16* out, void* ws){
  const int tid=threadIdx.x, lane=tid&63, w=__builtin_amdgcn_readfirstlane(tid>>6), p=w&3;
  const int ch0=10*j;
  const rsrc_t ro=__builtin_amdgcn_make_buffer_rsrc(out,0,2048*2560,0x00020000);
  lptr k0=L+A_K0+p*A_KS, k1=k0+4*A_KS;
  constexpr int XKB=A_US+224;
  u32 dk[5];
  #pragma unroll
  for(int i=0;i<5;++i) dk[i]=((const u32*)(dskip+ch0))[i];
  SsmP p0=load_ssm(in,ch0+w,lane);
  SsmP px; if(w>=4) px=load_ssm(in,ch0+8+(p>>1),lane);
  for(int i=tid;i<570;i+=512){ int sl=i/57, c=i-sl*57; lds_st<u32x4>(L+sl*A_US+(c<28?c*16:A_UB+(c-28)*16),u32x4{0,0,0,0}); }
  if(tid==0) lds_st<u32x4>(L+A_XF,u32x4{0,0,0,0});
  bar();
  u32x4 um[4];
  const int ur=(tid+64*j)&511;
  { typedef u32x4 __attribute__((aligned(4))) u32x4u;
    const char* up0=(const char*)u+(size_t)ur*4*1280+20*j;
    #pragma unroll
    for(int r=0;r<4;++r) um[r]=*(const u32x4u*)(up0+r*1280); }
  const u64 E=epoch(ws);
  ST(1);
  float xv[12];
  if(w>=4){ __builtin_amdgcn_s_setprio(3); float s,inv; ssm_prep(px,s,inv); f32x16 acc={};
    kgen_half(px,p&1,s,k1,lane,acc);
    #pragma unroll
    for(int r=0;r<12;++r) xv[r]=acc[r]*inv; }
  float ks,kinv; ssm_prep(p0,ks,kinv); f32x16 kacc={};
  auto k_rest=[&](lptr kw){ kgen_half(p0,1,ks,kw,lane,kacc); float v[12];
    #pragma unroll
    for(int r=0;r<12;++r) v[r]=kacc[r]*kinv;
    k_finish(v,kw,A_KB,lane); };
  kgen_half(p0,0,ks,L+A_K0+w*A_KS,lane,kacc);
  if(w<4) k_rest(k0);
  ST(2);
  const int tl=32*((lane&15)>>1)+8*(lane>>4)+2*(lane&1);
  auto fin4=[&](u32* r,f32x4 v,int e,int c){
    lptr y=L+e*A_US+(224+256*c+tl)*2; const float d=(e&1)?bfhi(dk[e>>1]):bflo(dk[e>>1]);
    r[0]=fin2(v[0],v[1],lds_ld<u32>(y),d); r[1]=fin2(v[2],v[3],lds_ld<u32>(y+8),d); };
  auto scatter=[&](lptr d,u32 r0,u32 r1,u32 r2,u32 r3,bool last){
    u32x2 e={p_ll(r0,r1),p_ll(r2,r3)}, o={p_hh(r0,r1),p_hh(r2,r3)};
    lds_st<u32x2>(d,e); lds_st<u32x2>(d+A_US,o);
    lds_st<u32>(d+A_UB+4,e.x); lds_st<u32>(d+A_US+A_UB+4,o.x);
    if(!last){ lds_st<u32>(d+A_UB+8,e.y); lds_st<u32>(d+A_US+A_UB+8,o.y); } };
  #pragma unroll
  for(int c=0;c<4;++c) scatter(L+2*c*A_US+(224+4*ur)*2,um[0][c],um[1][c],um[2][c],um[3][c],ur==511);
  bar();
  ST(3);
  if(w<4){
    f32x4 acc[8];
    conv_steps<8>(L+p*A_US,k0,A_KB,0,lane,acc);
    #pragma unroll
    for(int c=0;c<8;++c) lds_st<f32x4>(k0+c*1024+lane*16,acc[c]);
    ST(4);
    bar();
    conv_steps<8>(L+(p+4)*A_US,k1,A_KB,0,lane,acc);
    #pragma unroll
    for(int c=0;c<8;++c) lds_st<f32x4>(k1+c*1024+lane*16,acc[c]);
    ST(5);
    bar();
    conv_steps<4>(L+(8+(p>>1))*A_US,L+(p&2)*A_US+A_UB,XKB,4*(p&1),lane,acc);
    #pragma unroll
    for(int c=0;c<8;++c) lds_st<f32x4>(k0+c*1024+lane*16,acc[c]);
    ST(6);
    bar();
  } else {
    if(tid==256) flag_set(ws,192+j,E);
    k_rest(k1);
    ST(9);
    u32 ux[8];
    { const char* up=(const char*)u+(size_t)(tid-256)*4*1280+20*j+16;
      #pragma unroll
      for(int r=0;r<8;++r) ux[r]=*(const u32*)(up+(size_t)((r&3)+(r>>2)*1024)*1280); }
    ST(4);
    bar();
    u32 pf[4];
    { const char* wp=(const char*)wo+(j&7)*102400+(tid-256)*128;
      #pragma unroll
      for(int i=0;i<4;++i) pf[i]=*(const u32*)(wp+((i<3||tid<288)?i*32768:0)); }
    { lptr xp=L+(p|1)*A_US+A_UB+lane*16; volatile LAS u32* xf=(volatile LAS u32*)(L+A_XF+2*(p&2));
      if(p&1){
        #pragma unroll
        for(int r=0;r<3;++r) *(volatile LAS f32x4*)(xp+r*1024)=f32x4{xv[4*r],xv[4*r+1],xv[4*r+2],xv[4*r+3]};
        *xf=1u;
      } else { f32x4 o[3];
        for(int it=0;it<SPIN_MAX;++it){ const u32 f=*xf;
          #pragma unroll
          for(int r=0;r<3;++r) o[r]=*(volatile LAS f32x4*)(xp+r*1024);
          if(__builtin_amdgcn_readfirstlane(f)) break; }
        #pragma unroll
        for(int r=0;r<12;++r) xv[r]+=o[r>>2][r&3];
        k_finish(xv,L+p*A_US+A_UB,XKB,lane); } }
    #pragma unroll
    for(int h=0;h<2;++h) scatter(L+8*A_US+(224+4*(tid-256)+1024*h)*2,ux[4*h],ux[4*h+1],ux[4*h+2],ux[4*h+3],h==1&&tid==511);
    ST(8);
    #pragma unroll
    for(int i=0;i<2;++i){ u32 r[4][2]; const int c=2*p+i;
      #pragma unroll
      for(int e=0;e<4;++e) fin4(r[e],lds_ld<f32x4>(L+A_K0+e*A_KS+c*1024+lane*16),e,c);
      const u32 o=(256*c+tl)*2560+20*j;
      #pragma unroll
      for(int h=0;h<2;++h){
        __builtin_amdgcn_raw_buffer_store_b64(u32x2{p_ll(r[0][h],r[1][h]),p_ll(r[2][h],r[3][h])},ro,o+4*h*2560,0,0);
        __builtin_amdgcn_raw_buffer_store_b64(u32x2{p_hh(r[0][h],r[1][h]),p_hh(r[2][h],r[3][h])},ro,o+(4*h+1)*2560,0,0); } }
    asm volatile(""::"v"(pf[0]),"v"(pf[1]),"v"(pf[2]),"v"(pf[3]));
    ST(5);
    bar();
    #pragma unroll
    for(int i=0;i<2;++i)
      #pragma unroll
      for(int e=0;e<4;++e){ u32 r[2]; lptr q=L+A_K0+(4+e)*A_KS+(p+4*i)*1024+lane*16; fin4(r,lds_ld<f32x4>(q),4+e,p+4*i); lds_st<u32x2>(q,u32x2{r[0],r[1]}); }
    ST(6);
    bar();
  }
  {
    u32 r[6][2];
    #pragma unroll
    for(int e=0;e<4;++e){ const u32x2 v=lds_ld<u32x2>(L+A_K0+(4+e)*A_KS+tid*16); r[e][0]=v.x; r[e][1]=v.y; }
    #pragma unroll
    for(int e=0;e<2;++e){ lptr q=L+A_K0+2*e*A_KS+tid*16; fin4(r[4+e],lds_ld<f32x4>(q)+lds_ld<f32x4>(q+A_KS),8+e,w); }
    const u32 o=(256*w+tl)*2560+20*j+8;
    #pragma unroll
    for(int h=0;h<2;++h){
      __builtin_amdgcn_raw_buffer_store_b96(u32x3{p_ll(r[0][h],r[1][h]),p_ll(r[2][h],r[3][h]),p_ll(r[4][h],r[5][h])},ro,o+4*h*2560,0,0);
      __builtin_amdgcn_raw_buffer_store_b96(u32x3{p_hh(r[0][h],r[1][h]),p_hh(r[2][h],r[3][h]),p_hh(r[4][h],r[5][h])},ro,o+(4*h+1)*2560,0,0); } }
  ST(7); STDUMP();
}

constexpr int B_ST=36864, B_PV=2*B_ST, B_PVS=18432, B_WP=4*B_ST, B_Z=B_WP+6400, B_F=163200;
DI float max3(float a,float b,float c){ return __builtin_fmaxf(__builtin_fmaxf(a,b),c); }
template<class H> DI void softmax20x2(const f32x4 (*z)[5], u32 (*o)[10], H hook){
  const float c=1.4426950408889634f; float mc[2];
  { float m0[2],m1[2],m2[2];
    #pragma unroll
    for(int t=0;t<2;++t){ m0[t]=max3(z[t][0][0],z[t][0][1],z[t][0][2]); m1[t]=max3(z[t][0][3],z[t][1][0],z[t][1][1]); m2[t]=max3(z[t][1][2],z[t][1][3],z[t][2][0]); }
    #pragma unroll
    for(int t=0;t<2;++t){ m0[t]=max3(m0[t],z[t][2][1],z[t][2][2]); m1[t]=max3(m1[t],z[t][2][3],z[t][3][0]); m2[t]=max3(m2[t],z[t][3][1],z[t][3][2]); }
    #pragma unroll
    for(int t=0;t<2;++t){ m0[t]=max3(m0[t],z[t][3][3],z[t][4][0]); m1[t]=max3(m1[t],z[t][4][1],z[t][4][2]); m2[t]=__builtin_fmaxf(m2[t],z[t][4][3]); }
    #pragma unroll
    for(int t=0;t<2;++t) mc[t]=max3(m0[t],m1[t],m2[t])*-c; }
  SB();
  hook(0);
  f32x2 e[2][10];
  #pragma unroll
  for(int i=0;i<10;++i)
    #pragma unroll
    for(int t=0;t<2;++t) e[t][i]=__builtin_elementwise_fma(f32x2{z[t][i>>1][(i&1)*2],z[t][i>>1][(i&1)*2+1]},f32x2{c,c},f32x2{mc[t],mc[t]});
  SB();
  hook(1);
  #pragma unroll
  for(int i=0;i<10;++i)
    #pragma unroll
    for(int t=0;t<2;++t) e[t][i]=f32x2{__builtin_amdgcn_exp2f(e[t][i].x),__builtin_amdgcn_exp2f(e[t][i].y)};
  SB();
  hook(2);
  float r[2];
  { f32x2 s[2][5];
    #pragma unroll
    for(int i=0;i<5;++i)
      #pragma unroll
      for(int t=0;t<2;++t) s[t][i]=e[t][2*i]+e[t][2*i+1];
    #pragma unroll
    for(int t=0;t<2;++t){ s[t][0]+=s[t][1]; s[t][2]+=s[t][3]; }
    #pragma unroll
    for(int t=0;t<2;++t){ s[t][0]+=s[t][4]; }
    #pragma unroll
    for(int t=0;t<2;++t){ s[t][0]+=s[t][2]; }
    #pragma unroll
    for(int t=0;t<2;++t) r[t]=__builtin_amdgcn_rcpf(s[t][0].x+s[t][0].y); }
  SB();
  #pragma unroll
  for(int i=0;i<10;++i)
    #pragma unroll
    for(int t=0;t<2;++t) e[t][i]*=r[t];
  SB();
  #pragma unroll
  for(int i=0;i<10;++i)
    #pragma unroll
    for(int t=0;t<2;++t) o[t][i]=pk_bf16(e[t][i].x,e[t][i].y);
  SB();
}
DI u32x4 pack2(f32x4 a,f32x4 b){ u32x4 r={pk_bf16(a[0],a[1]),pk_bf16(a[2],a[3]),pk_bf16(b[0],b[1]),pk_bf16(b[2],b[3])}; return r; }
DI void role_b(const bool ISQ,int X,int li,lptr L,u32 warm,const u16* x,const u16* wqkv,const u16* wphi,const u16* wo,u16* out,void* ws){
  const int tid=threadIdx.x, lane=tid&63, w=__builtin_amdgcn_readfirstlane(tid>>6), g=lane>>4;
  const int rt=4*(X>>1)+(ISQ?(li>>1):(li>>2));
  const int grp=ISQ?(2*(X&1)+(li&1)):(4*(X&1)+(li&3));
  const int T=ISQ?16+li:li; const bool doout=T<20; const int tt64=4*X+T/5, ct=T%5;
  const rsrc_t rs=__builtin_amdgcn_make_buffer_rsrc(ws,0,WS_END,0x00020000);
  constexpr int OIM=B_PV;
  u64 E=epoch_late(ws);
  auto arrive=[&](){
    u32 n=0; if(lane==0) n=__hip_atomic_fetch_add((LAS u32*)(L+B_F+16),1u,__ATOMIC_RELAXED,__HIP_MEMORY_SCOPE_WORKGROUP);
    if(__builtin_amdgcn_readfirstlane(n)==3u&&lane==0) flag_set(ws,ISQ?(128+4*rt+grp):(16*grp+rt),E); };
  volatile LAS u32* bad=(volatile LAS u32*)(L+B_F+20);
  if(__builtin_expect(w>=4,1)){
    const int hw=w-4;
    const int s0=(0x8530>>((ISQ?((li>>1)+2*(li&1)):((li>>2)+(li&3)))&3)*4&15)+(ISQ?1:0);
    u32 xo[4], wv[5];
    #pragma unroll
    for(int i=0;i<4;++i){ int r=(4*hw+i)*8+(lane>>3); xo[i]=(u32)(rt*128+r)*1280u+(u32)(((lane&7)^((r>>1)&7))<<4)-1024u*i; }
    const u32 wlo=ISQ?320*grp:1280+160*grp, whi=ISQ?320*grp:2400+160*grp;
    #pragma unroll
    for(int i=0;i<5;++i){ u32 c=(5*hw+i)*64+lane, r=(c*3277u)>>16, pos=c-r*20, sc=pos^(((r>>3)&1)<<1);
      wv[i]=r*3840u+(sc<10?wlo:whi)+16*sc-1024u*(i&3); }
    auto issue=[&](int s){ int ss=s+s0; if(ss>=10) ss-=10; lptr d=L+(s&3)*B_ST;
      const char* xb=(const char*)x+ss*128; const char* wb=(const char*)wqkv+(size_t)ss*64*3840;
      dma16x4(wb,wv[0],wv[1],wv[2],wv[3],d+16384+5*hw*1024); dma16x4(xb,xo[0],xo[1],xo[2],xo[3],d+4*hw*1024); dma16(wb,wv[4],d+16384+(5*hw+4)*1024); };
    ST(9);
    issue(0); issue(1);
    asm volatile("":"+s"(E));
    ST(1); waitv<9>(); rbar(); issue(2);
    ST(2);
    waitv<9>(); rbar();
    #pragma unroll 1
    for(int s=2;s<9;++s){
      issue(s+1);
      waitv<9>(); rbar(); }
    waitv<0>(); rbar();
    if(!doout){ STDUMP(); return; }
    if(!ISQ){
      const u32x4 sv={SENT,SENT,SENT,SENT};
      #pragma unroll
      for(int i=0;i<4;++i) st_sc1(rs,(i<3||lane<18)?(WS_P1+((4*grp+hw)*16+rt)*3584+i*1024+lane*16):OOB,sv);
      #pragma unroll
      for(int i=0;i<5;++i) st_sc1(rs,WS_O+((8*rt+grp)*20+5*hw+i)*1024+lane*16,sv);
      waitv<0>(); arrive();
      ST(12); }
    auto go=[&](u32 n){ for(int it=0;it<SPIN_MAX;++it){ if(__builtin_amdgcn_readfirstlane(*(volatile LAS u32*)(L+B_F+24))>=n) break; __builtin_amdgcn_s_sleep(2); } asm volatile("":::"memory"); };
    u32x4 wf[2][20];
    {
      const int pr=hw>>1, me2=hw&1, r=lane>>3;
      const char* gb=(const char*)wo+ct*256+pr*128+me2*8*1280;
      const u32 sw=((lane&7)^((((r>>1)&1)|(me2<<1))<<1))<<4, vo=r*1280+sw, vo2=((r>>2)*20+(r&3)+16+32*me2)*1280+sw;
      auto V=[&](int n,int k){ const int G=n/10, m=n%10; return ((m<8)?vo+(160*G+20*m)*1280:vo2+(160*G+80*(m-8))*1280)-1024*k; };
      auto four=[&](int n,lptr d){ dma16x4(gb,V(n,0),V(n+1,1),V(n+2,2),V(n+3,3),d); };
      const int q=(lane&15)>>2, p=lane&3, f=((q>>1)|((g&1)<<1))<<1;
      lptr rb=L+(pr*2+(g&1))*18432+(g>>1)*1024+q*128+8*(p&1);
      lptr rd=rb+(((4*me2+(p>>1))^f)<<4), rd1=rb+(((4*me2+2+(p>>1))^f)<<4);
      constexpr int TAIL=B_PV+12288;
      lptr dst=L+hw*18432;
      volatile LAS u32* fm=(volatile LAS u32*)(L+B_F+48+4*hw); volatile LAS u32* fo=(volatile LAS u32*)(L+B_F+48+4*(hw^1));
      auto meet=[&](u32 n){ *fm=n; for(int it=0;it<SPIN_MAX;++it){ if(__builtin_amdgcn_readfirstlane(*fo)>=n) break; __builtin_amdgcn_s_sleep(1); } asm volatile("":::"memory"); };
      go(4u); ST(3);
      four(0,dst); four(4,dst+4096); four(8,dst+8192); four(12,dst+12288); dma16(gb,V(16,0),dst+16384); dma16(gb,V(17,0),dst+17408);
      four(18,dst+TAIL); dma16(gb,V(22,0),dst+TAIL+4096); dma16(gb,V(23,0),dst+TAIL+5120);
      ST(10); waitv<0>(); ST(11); meet(1);
      #pragma unroll
      for(int s=0;s<12;++s){ const int o=(s<9)?s*2048:TAIL+(s-9)*2048;
        wf[0][s]=cat(lds_tr(rd+o),lds_tr(rd+o+512)); wf[1][s]=cat(lds_tr(rd1+o),lds_tr(rd1+o+512)); }
      asm volatile("s_waitcnt lgkmcnt(0)":::"memory");
      if(lane==0) __hip_atomic_fetch_add((LAS u32*)(L+B_F+28),1u,__ATOMIC_RELAXED,__HIP_MEMORY_SCOPE_WORKGROUP);
      meet(2);
      go(ISQ?4u:8u);
      four(24,dst); four(28,dst+4096); four(32,dst+8192); four(36,dst+12288);
      ST(13); waitv<0>(); ST(14); meet(3);
      #pragma unroll
      for(int s=0;s<8;++s){ wf[0][12+s]=cat(lds_tr(rd+s*2048),lds_tr(rd+s*2048+512)); wf[1][12+s]=cat(lds_tr(rd1+s*2048),lds_tr(rd1+s*2048+512)); }
      asm volatile("s_waitcnt lgkmcnt(0)":::"memory"); }
    ST(4);
    for(u32 it=1;it<SPIN_MAX;++it){
      rbar();
      ST(5);
      f32x4 c[2][4];
      #pragma unroll
      for(int t=0;t<4;++t){ c[0][t]=Z4; c[1][t]=Z4; }
      u32x4 ob[2][4]; u32 chk=0;
      lptr ob0=L+OIM+lane*16;
      #pragma unroll
      for(int t=0;t<4;++t) ob[0][t]=lds_ld<u32x4>(ob0+((t+hw)&3)*20480);
      lptr obt[4];
      #pragma unroll
      for(int t=0;t<4;++t) obt[t]=ob0+((t+hw)&3)*20480;
      SB();
      #pragma unroll
      for(int ks=0;ks<20;++ks){
        #pragma unroll
        for(int t=0;t<4;++t){ if(ks<19) ob[(ks+1)&1][t]=lds_ld<u32x4>(obt[t]+(ks+1)*1024);
          c[0][t]=mfma16(wf[0][ks],ob[ks&1][t],c[0][t]); c[1][t]=mfma16(wf[1][ks],ob[ks&1][t],c[1][t]); }
        chk=max(chk,max(ob[ks&1][0].x,ob[ks&1][0].z));
        SB(); }
      if(__any(chk==SENT)) *bad=it;
      #pragma unroll
      for(int t=0;t<4;++t)
        #pragma unroll
        for(int cc=0;cc<2;++cc) lds_st<u32x2>(L+(16*((t+hw)&3)+(lane&15))*272+hw*64+cc*32+8*g,u32x2{pk_bf16(c[cc][t][0],c[cc][t][1]),pk_bf16(c[cc][t][2],c[cc][t][3])});
      ST(6);
      bar();
      if(__builtin_amdgcn_readfirstlane(*bad)!=it) break; }
  } else {
    const int th=w>>1, hh=w&1;
    { const int NB=ISQ?3200:1600; const char* wp=(const char*)wphi+(ISQ?8*grp:4*grp)*800+hh*NB;
      #pragma unroll
      for(int i=0;i<4;++i) if(i*1024+lane*16<NB) dma16(wp,i*1024+lane*16,L+B_WP+hh*NB+i*1024); }
    if(lane<4) lds_st<u32x4>(L+B_Z+lane*16,u32x4{0,0,0,0});
    if(tid<4) lds_st<u32x4>(L+B_F+tid*16,u32x4{0,0,0,0});
    asm volatile("":"+s"(E));
    asm volatile("s_waitcnt vmcnt(0)"::"v"(warm):"memory");
    ST(9);
    lptr fad[3][2];
    { const int q=(lane&15)>>2, p=lane&3, hd=ISQ?p:(p&1);
      #pragma unroll
      for(int s=0;s<3;++s)
        #pragma unroll
        for(int t=0;t<2;++t){ const int d=16*(2*s+t)+4*g-20*hd; const bool ok=(d>=0)&&(d<20)&&(2*s+t<5);
          fad[s][t]=ok?(L+B_WP+((ISQ?4*hh:2*hh)+hd)*800+(d+q)*40+(ISQ?0:(p>>1)*3200)):(L+B_Z); if(2*s+t<5) asm volatile("":"+v"(fad[s][t])); } }
    lptr pv=L+B_PV+w*B_PVS;
    const int oh=4*grp+(rt&3), tq=rt>>2;
    const int ot=4*tt64+w;
    int fi=(lane<16)?(16*grp+lane):((lane<32)?(16*(lane&7)+4*tq+w):((lane<48)?(16*(ot&7)+(ot>>3)):(128+4*(4*tq+w)+(oh>>3))));
    lptr pp=pv+(lane&15)*160+(g&1)*80+(g>>1)*40, vp2, vp3, vp4, pa, vb;
    { lptr vp=pv+10272+(lane&15)*96; vp2=vp+((g<2)?(48*g+40):(8*g-16)); vp3=vp+((g==3)?48:(16+8*g)); vp4=vp+56+8*g;
      const int tk=16*(g>>1)+4*(g&1)+((lane&15)>>2); pa=pv+tk*160+8*(lane&3)+(th^1)*80; vb=pv+10272+tk*96+8*(lane&3)+(th^1)*48; }
    u32 qo=WS_Q+(oh*2048+512*tq+128*w+16*(g>>1)+(lane&15))*40, po0, po1;
    { const u32 po=WS_P1+((4*grp+2*hh+th)*16+rt)*3584; po0=po+lane*16; po1=((lane&15)<5)?(po+(160+5*g+(lane&15))*16):OOB; }
    asm volatile("":"+v"(fi),"+v"(pp),"+v"(vp2),"+v"(vp3),"+v"(vp4),"+v"(pa),"+v"(vb),"+v"(qo),"+v"(po0),"+v"(po1));
    if(!ISQ){
      #pragma unroll
      for(int i=0;i<2;++i) if(i*1024+lane*16<1600){ lptr q=L+B_WP+hh*1600+i*1024+lane*16; lds_st<u32x4>(q+3200,lds_ld<u32x4>(q)^0x80008000u); } }
    lptr ab[5], bb[2];
    { const int q=(lane&15)>>2,p=lane&3;
      #pragma unroll
      for(int i=0;i<5;++i){ int n=2*i+(p>>1); int cid=ISQ?(10*hh+n):(5*hh+n+(n>=5?5:0)); ab[i]=L+16384+(8*g+q)*320+((cid^((g&1)<<1))<<4)+8*(p&1); }
      const int b0=(64*th+(lane&15))*128+((g^((lane&15)>>1))<<4); bb[0]=L+b0; bb[1]=L+(b0^64); }
    f32x4 acc[5][4];
    #pragma unroll
    for(int i=0;i<5;++i)
      #pragma unroll
      for(int t=0;t<4;++t) acc[i][t]=Z4;
    ST(1);
    u32x4 FA[2][5], FB[2][4], fa[2][5];
    auto half=[&](int hf,bool mm,bool last){
      #pragma unroll
      for(int i=0;i<5;++i){
        if(i<2){ FB[hf][2*i]=lds_ld<u32x4>(bb[hf]+4096*i); FB[hf][2*i+1]=lds_ld<u32x4>(bb[hf]+4096*i+2048); }
        FA[hf][i]=cat(lds_tr(ab[i]+hf*10240),lds_tr(ab[i]+hf*10240+1280));
        if(mm){
          #pragma unroll
          for(int t=0;t<4;++t) acc[i][t]=mfma16(FA[hf^1][i],FB[hf^1][t],acc[i][t]); }
        if(last){ fa[0][i]=cat(lds_tr(fad[0][0]+8*i),lds_tr(fad[0][1]+8*i)); fa[1][i]=cat(lds_tr(fad[1][0]+8*i),lds_tr(fad[1][1]+8*i)); }
        SB(); } };
    bar();
    ST(2);
    SB();
    half(0,false,false);
    #pragma unroll 1
    for(int s=1;s<10;++s){
      half(1,true,false);
      const int step=(s&3)?B_ST:-3*B_ST;
      #pragma unroll
      for(int i=0;i<5;++i) ab[i]+=step;
      bb[0]+=step; bb[1]+=step;
      rbar();
      SB();
      half(0,true,false); }
    half(1,true,true);
    auto signal=[&](){ asm volatile("":::"memory"); if(lane==0) __hip_atomic_fetch_add((LAS u32*)(L+B_F+24),1u,__ATOMIC_RELAXED,__HIP_MEMORY_SCOPE_WORKGROUP); };
    #pragma unroll
    for(int t=0;t<4;++t)
      #pragma unroll
      for(int i=0;i<5;++i) acc[i][t]=mfma16(FA[1][i],FB[1][t],acc[i][t]);
    u32x2 pk[5][4];
    #pragma unroll
    for(int t=0;t<4;++t)
      #pragma unroll
      for(int i=0;i<5;++i) pk[i][t]=u32x2{pk_bf16(acc[i][t][0],acc[i][t][1]),pk_bf16(acc[i][t][2],acc[i][t][3])};
    ST(3);
    if(ISQ){
      u32x4 f2[5];
      #pragma unroll
      for(int i=0;i<5;++i) f2[i]=cat(lds_tr(fad[2][0]+8*i),u32x2{0u,0u});
      lptr sp_=pv+g*2560+(lane&15)*40;
      f32x4 z[4][5];
      #pragma unroll
      for(int t=0;t<4;++t)
        #pragma unroll
        for(int i=0;i<5;++i) z[t][i]=mfma16(fa[0][i],cat(pk[0][t],pk[1][t]),Z4);
      #pragma unroll
      for(int t=0;t<4;++t)
        #pragma unroll
        for(int i=0;i<5;++i) z[t][i]=mfma16(fa[1][i],cat(pk[2][t],pk[3][t]),z[t][i]);
      SB();
      #pragma unroll
      for(int t=0;t<5;++t){
        #pragma unroll
        for(int i=0;i<5;++i){ if(t<4) z[t][i]=mfma16(f2[i],cat(pk[4][t],u32x2{0u,0u}),z[t][i]);
          if(t>0) lds_st<u32x2>(sp_+(t-1)*640+8*i,u32x2{pk_bf16(z[t-1][i][0],z[t-1][i][1]),pk_bf16(z[t-1][i][2],z[t-1][i][3])}); }
        SB(); }
      ST(10);
      #pragma unroll
      for(int i=0;i<10;++i){ const int c=i*64+lane, hd=(c*205)>>15;
        st_sc1(rs,WS_Q+((8*grp+4*hh+hd)*2048+128*rt+64*th)*40+(c-160*hd)*16,lds_ld<u32x4>(pv+c*16)); }
      ST(11);
      waitv<0>(); ST(12); arrive(); signal();
      ST(4);
      if(doout){ fi=16*(ot&7)+(ot>>3); flag_wait(ws,E,fi,flag_get(ws,fi)); }
    } else {
      { f32x4 z[4][5]; u32 ph[4][10];
        auto fm=[&](int t){
          const u32x4 b0=cat(pk[0][t],pk[1][t]), b1=cat(pk[2][t],pk[3][t]);
          #pragma unroll
          for(int i=0;i<5;++i) z[t][i]=mfma16(fa[0][i],b0,Z4);
          #pragma unroll
          for(int i=0;i<5;++i) z[t][i]=mfma16(fa[1][i],b1,z[t][i]); };
        fm(0); fm(1);
        SB();
        softmax20x2(z,ph,[&](int k){ if(k==0){ fm(2); } else if(k==1){ fm(3); } });
        auto wp=[&](int t){
          #pragma unroll
          for(int i=0;i<5;++i) lds_st<u32x2>(pp+t*2560+8*i,u32x2{ph[t][2*i],ph[t][2*i+1]}); };
        softmax20x2(z+2,ph+2,[&](int k){
          if(k==0){ wp(0); wp(1); }
          else if(k==1){
            #pragma unroll
            for(int t=0;t<4;++t){ u32x2 v=pk[2][t]; if(g<2) v=u32x2{0x3F80u,0u};
              lds_st<u32x2>(vp2+t*1536,v); lds_st<u32x2>(vp3+t*1536,pk[3][t]); lds_st<u32x2>(vp4+t*1536,pk[4][t]); } } });
        wp(2); wp(3); }
      ST(4);
      u64 fl;
      lptr q00,q01,q10,q11; u32 om,ox;
      { const int r=lane&15, pc=5*(g&1)+2*(g>>1), pl=5*(g&1)+4, d1=min(r,4); lptr b=L+B_PV+8192;
        q00=b+(16*pc+r)*16; q01=b+(16*pl+r)*16+8*(g>>1); q10=b+(160+5*pc+d1)*16; q11=b+(160+5*pl+d1)*16+8*(g>>1);
        const u32 fb=WS_O+((32*tq+8*w)*20+5*(oh>>3))*1024+r*16;
        om=fb+((oh&7)>>1)*1024+(2*(oh&1)+(g>>1))*256+8*(g&1); ox=(g==0)?(fb+4096+((oh&7)>>1)*256+8*(oh&1)):OOB; }
      u32x4 l0[4],l1[4]; u32x2 l2[4];
      auto ldq=[&](){
        #pragma unroll
        for(int i=0;i<4;++i){ l0[i]=ld_sc1(rs,qo+i*1280); l1[i]=ld_sc1(rs,qo+i*1280+16); l2[i]=__builtin_amdgcn_raw_buffer_load_b64(rs,qo+i*1280+32,0,16); } };
      { f32x4 c[2][6];
        #pragma unroll
        for(int h2=0;h2<2;++h2){
          if(h2){ pa+=th?80:-80; vb+=th?48:-48; }
          u32x4 a[2][3], b[2][2];
          #pragma unroll
          for(int ks=0;ks<2;++ks){
            #pragma unroll
            for(int i=0;i<3;++i) a[ks][i]=cat(lds_tr(pa+ks*32*160+32*i),lds_tr(pa+(ks*32+8)*160+32*i));
            #pragma unroll
            for(int dt=0;dt<2;++dt) b[ks][dt]=cat(lds_tr(vb+ks*32*96+32*dt),lds_tr(vb+(ks*32+8)*96+32*dt)); }
          #pragma unroll
          for(int i=0;i<6;++i) c[h2][i]=mfma16(a[0][i%3],b[0][i/3],Z4);
          #pragma unroll
          for(int i=0;i<6;++i) c[h2][i]=mfma16(a[1][i%3],b[1][i/3],c[h2][i]); }
        #pragma unroll
        for(int i=0;i<6;++i) *(volatile LAS f32x4*)(pv+i*1024+lane*16)=c[0][i];
        *(volatile LAS u32*)(L+B_F+4*w)=1u;
        #pragma unroll
        for(int i=0;i<6;++i) asm volatile("":"+v"(c[1][i]));
        lptr ox_=L+B_PV+(w^2)*B_PVS; f32x4 pr[6];
        for(int it=0;it<SPIN_MAX;++it){
          const u32 f=*(volatile LAS u32*)(L+B_F+4*(w^2)), n=*(volatile LAS u32*)(L+B_F+16);
          #pragma unroll
          for(int i=0;i<6;++i) pr[i]=*(volatile LAS f32x4*)(ox_+i*1024+lane*16);
          if(__builtin_amdgcn_readfirstlane(f+n)==5u) break; }
        fl=flag_get(ws,fi);
        #pragma unroll
        for(int i=0;i<6;++i){ u32 o=(i<3)?(po0+i*1024):(po1+(i-3)*320); if(i%3==2) o=(g<2)?o:OOB;
          st_sc1(rs,o,__builtin_bit_cast(u32x4,c[1][i]+pr[i])); } }
      ST(5);
      if(!__all(fl==E)) flag_wait(ws,E,fi,fl);
      asm volatile("":::"memory"); ldq();
      u32 pq[4][10];
      {
        ST(11);
        const u32 sg=(g&1)?0x80008000u:0u;
        u32x4 v[16]; const u32 go0=WS_P1+(oh*16+4*w)*3584+lane*16, go1=(lane<18)?go0+3072:OOB;
        #pragma unroll
        for(int i2=0;i2<4;i2+=2){ f32x4 z[2][5];
          #pragma unroll
          for(int t=0;t<2;++t){ const u32x4 a=l0[i2+t]^sg, b=l1[i2+t]^sg; const u32x2 c=l2[i2+t]^sg;
            z[t][0]=f32x4{bflo(a.x),bfhi(a.x),bflo(a.y),bfhi(a.y)}; z[t][1]=f32x4{bflo(a.z),bfhi(a.z),bflo(a.w),bfhi(a.w)};
            z[t][2]=f32x4{bflo(b.x),bfhi(b.x),bflo(b.y),bfhi(b.y)}; z[t][3]=f32x4{bflo(b.z),bfhi(b.z),bflo(b.w),bfhi(b.w)};
            z[t][4]=f32x4{bflo(c.x),bfhi(c.x),bflo(c.y),bfhi(c.y)}; }
          if(i2==0){
            #pragma unroll
            for(int k=0;k<16;++k) v[k]=ld_sc1(rs,((k&3)<3?go0+(k&3)*1024:go1)+(k>>2)*3584);
            signal(); }
          SB();
          softmax20x2(z,pq+i2,[](int){});
          #pragma unroll
          for(int i=i2;i<i2+2;++i){
            #pragma unroll
            for(int k=0;k<5;++k){ const int a=(k<4)?k:8, b=(k<4)?k+4:9;
              auto r=__builtin_amdgcn_permlane32_swap(pq[i][a],pq[i][b],false,false); pq[i][a]=r[0]; pq[i][b]=r[1]; }
            #pragma unroll
            for(int k=0;k<10;++k) asm volatile("":"+v"(pq[i][k])); }
          if(i2==0){ gather4<1>(rs,go0,go1,v); SB(); } }
        asm volatile("":"+v"(q00),"+v"(q01),"+v"(q10),"+v"(q11),"+v"(om),"+v"(ox));
        ST(10);
        gather4<SPIN_MAX>(rs,go0,go1,v);
        ST(13);
        #pragma unroll
        for(int i=0;i<4;++i) *(volatile LAS f32x4*)(pv+8192+i*1024+lane*16)=(__builtin_bit_cast(f32x4,v[i])+__builtin_bit_cast(f32x4,v[4+i]))+(__builtin_bit_cast(f32x4,v[8+i])+__builtin_bit_cast(f32x4,v[12+i]));
        *(volatile LAS u32*)(L+B_F+32+4*w)=1u;
        SB();
      }
      ST(12);
      u32x4 a0[2]; u32 a1[2];
      {
        f32x4 s[2][2]; f32x2 u[2];
        for(int it=0;it<SPIN_MAX;++it){
          const u32x4 f=*(volatile LAS u32x4*)(L+B_F+32);
          #pragma unroll
          for(int k=0;k<4;++k){ const int q=k*B_PVS;
            const f32x4 x0=*(volatile LAS f32x4*)(q00+q), x1=*(volatile LAS f32x4*)(q00+q+256), y0=*(volatile LAS f32x4*)(q10+q), y1=*(volatile LAS f32x4*)(q10+q+80);
            const f32x2 u0=*(volatile LAS f32x2*)(q01+q), u1=*(volatile LAS f32x2*)(q11+q);
            if(k==0){ s[0][0]=x0; s[0][1]=x1; s[1][0]=y0; s[1][1]=y1; u[0]=u0; u[1]=u1; }
            else { s[0][0]+=x0; s[0][1]+=x1; s[1][0]+=y0; s[1][1]+=y1; u[0]+=u0; u[1]+=u1; } }
          if(__builtin_amdgcn_readfirstlane(f.x+f.y+f.z+f.w)==4u) break; }
        #pragma unroll
        for(int i=0;i<2;++i){ a0[i]=pack2(s[i][0],s[i][1]); a1[i]=pk_bf16(u[i].x,u[i].y); } }
      ST(14);
      { f32x4 n[8][2];
        #pragma unroll
        for(int t=0;t<8;++t){ const u32* f=pq[t>>1]+4*(t&1);
          #pragma unroll
          for(int i=0;i<2;++i) n[t][i]=mfma16(a0[i],u32x4{f[0],f[1],f[2],f[3]},Z4); }
        #pragma unroll
        for(int t=0;t<8;++t)
          #pragma unroll
          for(int i=0;i<2;++i) n[t][i]=mfma16(u32x4{a1[i],0u,0u,0u},u32x4{pq[t>>1][8+(t&1)],0u,0u,0u},n[t][i]);
        SB();
        float den[8];
        #pragma unroll
        for(int t=0;t<8;++t) den[t]=__builtin_bit_cast(float,__builtin_amdgcn_ds_bpermute(((lane&15)+16)*4,__builtin_bit_cast(int,n[t][1][0])));
        SB();
        #pragma unroll
        for(int t=0;t<8;++t){ const float r=__builtin_amdgcn_rcpf(rbf(rbf(den[t])+1e-6f));
          const f32x4 m=n[t][0]*r, e=n[t][1]*r;
          __builtin_amdgcn_raw_buffer_store_b64(u32x2{pk_bf16(m[0],m[1]),pk_bf16(m[2],m[3])},rs,om+t*20480,0,16);
          __builtin_amdgcn_raw_buffer_store_b64(u32x2{pk_bf16(e[0],e[1]),pk_bf16(e[2],e[3])},rs,ox+t*20480,0,16); } }
      signal();
      if(rt==15&&grp==7&&w==3){
        for(int it=0;it<SPIN_MAX;++it){ if(__all(flag_get(ws,lane)==E&&flag_get(ws,64+lane)==E&&flag_get(ws,128+lane)==E&&flag_get(ws,192+lane)==E)) break; __builtin_amdgcn_s_sleep(2); }
        if(lane==0) __hip_atomic_store((GAS u64*)ws,E,__ATOMIC_RELAXED,__HIP_MEMORY_SCOPE_AGENT); }
      ST(6);
    }
    if(!doout){ STDUMP(); return; }
    for(int it=0;it<SPIN_MAX;++it){ const u32x2 c=*(volatile LAS u32x2*)(L+B_F+24); if(__builtin_amdgcn_readfirstlane(c.x+c.y)>=(ISQ?8u:12u)) break; __builtin_amdgcn_s_sleep(1); }
    { const u32 ob=WS_O+ot*20480;
      const u32 sm=ob+((lane<32)?((5*(lane>>3)+((lane>>1)&3))*1024+(lane&1)*512+508):((5*((lane-32)>>3)+4)*1024+((lane&7)>>1)*256+244+8*(lane&1)));
      for(int it=0;it<SPIN_MAX;++it){ asm volatile("":::"memory");
        if(__all(__builtin_amdgcn_raw_buffer_load_b32(rs,sm,0,16)!=SENT)) break;
        __builtin_amdgcn_s_sleep(1); }
      ST(15);
      const u32 vo=lane*16;
      for(u32 it=1;it<SPIN_MAX;++it){
        #pragma unroll
        for(int i=0;i<5;++i) dma16x4_sc1((const char*)ws+ob+i*4096,vo,vo,vo,vo,L+OIM+w*20480+i*4096);
        waitv<0>();
        ST(7);
        rbar();
        rbar();
        if(__builtin_amdgcn_readfirstlane(*bad)!=it) break; } }
  }
  #pragma unroll
  for(int i=0;i<2;++i){ const int r=32*i+(tid>>4);
    __builtin_amdgcn_raw_buffer_store_b128(lds_ld<u32x4>(L+r*272+(tid&15)*16),__builtin_amdgcn_make_buffer_rsrc(out,0,2048*2560,0x00020000),(64*tt64+r)*2560+1280+ct*256+(tid&15)*16,0,16); }
  ST(8); STDUMP();
}

constexpr int CODE_LINES=440;
__global__ __launch_bounds__(512,2) void fused(const u16* u,const u16* are,const u16* aim,const u16* bre,const u16* bim,const u16* cre,const u16* cim,
    const u16* logdt,const u16* dskip,const u16* x,const u16* wqkv,const u16* wphi,const u16* wo,u16* out,void* ws){
  extern __shared__ __attribute__((aligned(16))) char smem[];
  lptr L=(lptr)smem;
  const int b=blockIdx.x, X=b&7, idx=b>>3;
  u64 pc; asm volatile("s_getpc_b64 %0":"=s"(pc));
  STINIT(); ST(0);
  const int w=__builtin_amdgcn_readfirstlane(threadIdx.x>>6);
  const bool isa=(idx>=16&&idx<24), isq=idx>=24;
  if(__builtin_expect(!isa,1)){
    u32 warm=0;
    if(idx<16&&threadIdx.x<32&&idx*32+threadIdx.x<CODE_LINES) warm=*(const u32*)((const char*)(pc&~255ull)+(idx*32+threadIdx.x)*128);
    if(idx<16&&w==1&&(threadIdx.x&63)<50) warm=*(const u32*)((const char*)wo+((X*16+idx)*50+(threadIdx.x&63))*128);
    role_b(isq,X,isq?idx-24:idx,L,warm,x,wqkv,wphi,wo,out,ws);
  } else { const SsmIn in={are,aim,bre,bim,cre,cim,logdt}; role_a(X*8+(idx-16),L,u,in,dskip,wo,out,ws); }
}

extern "C" void kernel_launch(void* const* d_in, const int* in_sizes, int n_in, void* d_out, int out_size, void* d_ws, size_t ws_size, hipStream_t stream){
  (void)hipFuncSetAttribute((const void*)fused,hipFuncAttributeMaxDynamicSharedMemorySize,163840);
  fused<<<256,512,163840,stream>>>((const u16*)d_in[0],(const u16*)d_in[1],(const u16*)d_in[2],(const u16*)d_in[3],(const u16*)d_in[4],(const u16*)d_in[5],(const u16*)d_in[6],
    (const u16*)d_in[7],(const u16*)d_in[8],(const u16*)d_in[9],(const u16*)d_in[10],(const u16*)d_in[11],(const u16*)d_in[12],(u16*)d_out,d_ws);
}
```

```cpp
#define STAMP
#include <hip/hip_runtime.h>
#include <stdint.h>
#include <stdio.h>

typedef unsigned short u16; typedef unsigned u32; typedef unsigned long long u64;
typedef __attribute__((ext_vector_type(4))) short s16x4;
typedef __attribute__((ext_vector_type(8))) _Float16 f16x8;
typedef __attribute__((ext_vector_type(8))) __bf16 bf16x8;
typedef __attribute__((ext_vector_type(2))) __bf16 bf16x2;
typedef __attribute__((ext_vector_type(2))) _Float16 f16x2;
typedef __attribute__((ext_vector_type(2))) float f32x2;
typedef __attribute__((ext_vector_type(4))) float f32x4;
typedef __attribute__((ext_vector_type(16))) float f32x16;
typedef __attribute__((ext_vector_type(2))) unsigned u32x2;
typedef __attribute__((ext_vector_type(3))) unsigned u32x3;
typedef __attribute__((ext_vector_type(4))) unsigned u32x4;
#define LAS __attribute__((address_space(3)))
#define GAS __attribute__((address_space(1)))
#define DI __device__ __forceinline__
typedef LAS char* lptr;
typedef __amdgpu_buffer_rsrc_t rsrc_t;

#define ST(i) do{ if((threadIdx.x&255)==0){ *(LAS u64*)(L+163328+(threadIdx.x>>8)*128+(i)*8)=__builtin_amdgcn_s_memrealtime(); } }while(0)
#define STDUMP() do{ if((threadIdx.x&255)<16){ asm volatile("s_waitcnt lgkmcnt(0)":::"memory"); __hip_atomic_store((GAS u64*)((char*)ws+(200u<<20))+(blockIdx.x*2+(threadIdx.x>>8))*16+(threadIdx.x&15),*(volatile LAS u64*)(L+163328+(threadIdx.x>>8)*128+(threadIdx.x&15)*8),__ATOMIC_RELAXED,__HIP_MEMORY_SCOPE_AGENT); } }while(0)
#define STINIT() do{ if((threadIdx.x&255)<16){ *(LAS u64*)(L+163328+(threadIdx.x>>8)*128+(threadIdx.x&15)*8)=0; } }while(0)

DI u32 pk_bf16(float a, float b){ f32x2 v={a,b}; return __builtin_bit_cast(u32,__builtin_convertvector(v,bf16x2)); }
DI u32 pk_f16(f32x2 v){ return __builtin_bit_cast(u32,__builtin_convertvector(v,f16x2)); }
DI float bflo(u32 x){ return __builtin_bit_cast(float,x<<16); }
DI float bfhi(u32 x){ return __builtin_bit_cast(float,x&0xffff0000u); }
DI float rbf(float a){ return bflo(pk_bf16(a,a)); }
template<class T> DI T lds_ld(lptr p){ return *(LAS T*)p; }
template<class T> DI void lds_st(lptr p, T v){ *(LAS T*)p=v; }
DI u32x2 lds_ld64(lptr p){ return *(volatile LAS u32x2*)p; }
DI u32x2 lds_tr(lptr p){ return __builtin_bit_cast(u32x2,__builtin_amdgcn_ds_read_tr16_b64_v4i16((LAS s16x4*)p)); }
DI f32x4 mfma16(u32x4 a,u32x4 b,f32x4 c){ return __builtin_amdgcn_mfma_f32_16x16x32_bf16(__builtin_bit_cast(bf16x8,a),__builtin_bit_cast(bf16x8,b),c,0,0,0); }
DI u32x4 cat(u32x2 a,u32x2 b){ u32x4 r={a.x,a.y,b.x,b.y}; return r; }
DI u32 p_ll(u32 a,u32 b){ return __builtin_amdgcn_perm(b,a,0x05040100u); }
DI u32 p_lh(u32 a,u32 b){ return __builtin_amdgcn_perm(b,a,0x07060100u); }
DI u32 p_hl(u32 a,u32 b){ return __builtin_amdgcn_perm(b,a,0x05040302u); }
DI u32 p_hh(u32 a,u32 b){ return __builtin_amdgcn_perm(b,a,0x07060302u); }
#define SB() __builtin_amdgcn_sched_barrier(0)
DI void rbar(){ asm volatile("":::"memory"); __builtin_amdgcn_s_barrier(); asm volatile("":::"memory"); }
DI void bar(){ asm volatile("s_waitcnt lgkmcnt(0)":::"memory"); __builtin_amdgcn_s_barrier(); asm volatile("":::"memory"); }
template<int N> DI void waitv(){ asm volatile("s_waitcnt vmcnt(%0)"::"i"(N):"memory"); }
constexpr f32x4 Z4={0.f,0.f,0.f,0.f};
DI float wave_max(float m){
  m=fmaxf(m,__builtin_bit_cast(float,__builtin_amdgcn_update_dpp(0,__builtin_bit_cast(int,m),0xB1,0xf,0xf,true)));
  m=fmaxf(m,__builtin_bit_cast(float,__builtin_amdgcn_update_dpp(0,__builtin_bit_cast(int,m),0x4E,0xf,0xf,true)));
  m=fmaxf(m,__builtin_bit_cast(float,__builtin_amdgcn_update_dpp(0,__builtin_bit_cast(int,m),0x141,0xf,0xf,true)));
  m=fmaxf(m,__builtin_bit_cast(float,__builtin_amdgcn_update_dpp(0,__builtin_bit_cast(int,m),0x140,0xf,0xf,true)));
  int x=__builtin_bit_cast(int,m);
  float a=__builtin_bit_cast(float,__builtin_amdgcn_readlane(x,0)), b=__builtin_bit_cast(float,__builtin_amdgcn_readlane(x,16));
  float c=__builtin_bit_cast(float,__builtin_amdgcn_readlane(x,32)), d=__builtin_bit_cast(float,__builtin_amdgcn_readlane(x,48));
  return fmaxf(fmaxf(a,b),fmaxf(c,d)); }

constexpr u32 WS_FLAG=256, WS_P1=4096, WS_Q=WS_P1+32*16*3584, WS_O=WS_Q+32*2048*40, WS_END=WS_O+128*20*1024;
constexpr u32 SENT=0xFFFFFFFFu, OOB=0x80000000u;
constexpr int SPIN_MAX=1<<17;
DI void dma16(const void* sb, u32 voff, lptr l){ u32 keep;
  asm volatile("s_mov_b32 %0, m0\n\ts_mov_b32 m0, %3\n\ts_nop 0\n\tglobal_load_lds_dwordx4 %1, %2\n\ts_mov_b32 m0, %0":"=&s"(keep):"v"(voff),"s"(sb),"s"((u32)(uintptr_t)l):"memory"); }
#define DMA4_BODY(SC) asm volatile("s_mov_b32 %0, m0\n\ts_mov_b32 m0, %6\n\ts_nop 0\n\tglobal_load_lds_dwordx4 %1, %5" SC "\n\tglobal_load_lds_dwordx4 %2, %5 offset:1024" SC \
    "\n\tglobal_load_lds_dwordx4 %3, %5 offset:2048" SC "\n\tglobal_load_lds_dwordx4 %4, %5 offset:3072" SC "\n\ts_mov_b32 m0, %0" \
    :"=&s"(keep):"v"(v0),"v"(v1),"v"(v2),"v"(v3),"s"(sb),"s"((u32)(uintptr_t)l):"memory")
DI void dma16x4(const void* sb,u32 v0,u32 v1,u32 v2,u32 v3,lptr l){ u32 keep; DMA4_BODY(""); }
DI void dma16x4_sc1(const void* sb,u32 v0,u32 v1,u32 v2,u32 v3,lptr l){ u32 keep; DMA4_BODY(" sc1"); }
DI u32x4 ld_sc1(rsrc_t rs,u32 off){ return __builtin_amdgcn_raw_buffer_load_b128(rs,off,0,16); }
DI void st_sc1(rsrc_t rs,u32 off,u32x4 v){ __builtin_amdgcn_raw_buffer_store_b128(v,rs,off,0,16); }
template<int NIT> DI void gather4(rsrc_t rs,u32 off,u32 last,u32x4* v){
  for(int it=0;it<NIT;++it){
    u32 todo=0;
    #pragma unroll
    for(int k=0;k<4;++k){ u32 m=0;
      #pragma unroll
      for(int i=0;i<4;++i) m=max(m,max(v[4*k+i].x,v[4*k+i].z));
      if(__any(m==SENT)) todo|=1u<<k; }
    if(!todo) break;
    asm volatile("":::"memory");
    #pragma unroll
    for(int k=0;k<4;++k) if(todo>>k&1){
      #pragma unroll
      for(int i=0;i<4;++i) v[4*k+i]=ld_sc1(rs,((i<3)?off+i*1024:last)+k*3584); } } }
DI u64 epoch(const void* ws){ u64 E; asm volatile("s_load_dwordx2 %0, %1, 0x0\n\ts_waitcnt lgkmcnt(0)":"=s"(E):"s"(ws):"memory"); return E+1; }
DI u64 epoch_late(const void* ws){ return *(const __attribute__((address_space(4))) u64*)ws+1; }
DI void flag_set(void* ws,int i,u64 E){ __hip_atomic_store((GAS u64*)((char*)ws+WS_FLAG)+i,E,__ATOMIC_RELAXED,__HIP_MEMORY_SCOPE_AGENT); }
DI u64 flag_get(const void* ws,int i){ return __hip_atomic_load((const GAS u64*)((const char*)ws+WS_FLAG)+i,__ATOMIC_RELAXED,__HIP_MEMORY_SCOPE_AGENT); }
DI void flag_wait(const void* ws,u64 E,int idx,u64 f){
  for(int it=0;it<SPIN_MAX;++it){ if(__all(f==E)) break; __builtin_amdgcn_s_sleep(2); f=flag_get(ws,idx); } }

constexpr int A_UB=4576, A_US=9120, A_K0=10*A_US, A_KB=4224, A_KS=8704;
constexpr int A_XF=A_K0+8*A_KS;
static_assert(A_XF+16<=163328,"lds");

DI f32x2 cmul(f32x2 a, f32x2 b){ f32x2 sw={-b.y,b.x}; return __builtin_elementwise_fma(a.yy,sw,a.xx*b); }
DI f32x2 cexp_e(float re2, float rev, float e){
  float m=__builtin_amdgcn_exp2f(re2*e); float a=__builtin_amdgcn_fractf(rev*e);
  f32x2 r={m*__builtin_amdgcn_cosf(a), m*__builtin_amdgcn_sinf(a)}; return r; }
struct SsmP { u32 are,aim,bre,bim,cre,cim,ld; float dt; };
struct SsmIn { const u16 *are,*aim,*bre,*bim,*cre,*cim,*logdt; };
DI SsmP load_ssm(const SsmIn& in, int ch, int lane){
  SsmP p; int o=ch*64+lane;
  p.are=((const u32*)in.are)[o]; p.aim=((const u32*)in.aim)[o]; p.bre=((const u32*)in.bre)[o];
  p.bim=((const u32*)in.bim)[o]; p.cre=((const u32*)in.cre)[o]; p.cim=((const u32*)in.cim)[o];
  p.ld=in.logdt[ch];
  return p; }
DI f32x2 ssm_w(const SsmP& p, int half){
  f32x2 b={half?bfhi(p.bre):bflo(p.bre), half?bfhi(p.bim):bflo(p.bim)};
  f32x2 c={half?bfhi(p.cre):bflo(p.cre), half?bfhi(p.cim):bflo(p.cim)};
  return cmul(c,b*p.dt); }
DI void ssm_prep(SsmP& p, float& s, float& inv){
  asm volatile("":"+v"(p.ld));
  p.dt=rbf(expf(bflo(p.ld)));
  f32x2 w0=ssm_w(p,0), w1=ssm_w(p,1);
  float m=wave_max(fmaxf(fmaxf(fabsf(w0.x),fabsf(w0.y)),fmaxf(fabsf(w1.x),fabsf(w1.y))));
  int e=__builtin_amdgcn_frexp_expf(m); e=max(-100,min(100,e));
  s=__builtin_amdgcn_ldexpf(1.0f,-e); inv=__builtin_amdgcn_ldexpf(1.0f,e); }
DI void kgen_half(const SsmP& p, int half, float s, lptr tab, int lane, f32x16& acc){
  const float are=(half?bfhi(p.are):bflo(p.are))*p.dt, aim=(half?bfhi(p.aim):bflo(p.aim))*p.dt;
  const float re2=are*1.4426950408889634f, rev=aim*0.15915494309189535f;
  lptr wr=tab+lane*4; lptr rd=tab+(lane&31)*272+(lane>>5)*16;
  { f32x2 e8=cexp_e(re2,rev,8.f), e16=cmul(e8,e8);
    f32x2 a={1.f,0.f}, b=e8;
    #pragma unroll
    for(int j=0;j<32;j+=2){ lds_st<u32>(wr+j*272,pk_f16(a)); lds_st<u32>(wr+(j+1)*272,pk_f16(b)); if(j<30){ a=(j==0)?e16:cmul(a,e16); b=cmul(b,e16);} } }
  u32x4 bf[8];
  #pragma unroll
  for(int k=0;k<8;++k) bf[k]=lds_ld<u32x4>(rd+k*32);
  { f32x2 w=ssm_w(p,half)*s; w.y=-w.y;
    f32x2 e1=cexp_e(re2,-rev,1.f), g=cexp_e(re2,-rev,256.f);
    f32x2 g2=cmul(g,g), g3=cmul(g2,g), g4=cmul(g2,g2);
    f32x2 x[8]; x[0]=w;
    #pragma unroll
    for(int i=1;i<8;++i) x[i]=cmul(x[i-1],e1);
    #pragma unroll
    for(int i=0;i<8;++i) lds_st<u32>(wr+i*272,pk_f16(x[i]));
    #pragma unroll
    for(int i=0;i<4;++i) lds_st<u32>(wr+(8+i)*272,pk_f16(cmul(x[2*i],g)));
    lds_st<u32>(wr+12*272,pk_f16(cmul(x[0],g2))); lds_st<u32>(wr+13*272,pk_f16(cmul(x[4],g2)));
    lds_st<u32>(wr+14*272,pk_f16(cmul(x[0],g3))); lds_st<u32>(wr+15*272,pk_f16(cmul(x[4],g3)));
    f32x2 y=cmul(w,g4);
    #pragma unroll
    for(int i=0;i<5;++i){ lds_st<u32>(wr+(16+i)*272,pk_f16(y)); if(i<4) y=cmul(y,g); } }
  #pragma unroll
  for(int k=0;k<8;++k){ u32x4 af=lds_ld<u32x4>(rd+k*32);
    acc=__builtin_amdgcn_mfma_f32_32x32x16_f16(__builtin_bit_cast(f16x8,af),__builtin_bit_cast(f16x8,bf[k]),acc,0,0,0); }
}
DI void k_finish(const float* v, lptr ka, int kbo, int lane){
  lptr ks=ka; lptr kb=ka+kbo;
  { const int j=lane&31, h=lane>>5;
    lds_st<u32x2>(ks+(8*j+4*h)*2,u32x2{pk_bf16(v[0],v[1]),pk_bf16(v[2],v[3])});
    u32x2 b={pk_bf16(v[4],v[5]),pk_bf16(v[6],v[7])};
    u32 c0=pk_bf16(v[8],v[9]), c1=pk_bf16(v[10],v[11]);
    if(h==0){ lds_st<u32x2>(ks+(256+4*j)*2,b);
      lds_st<u16>(ks+(512+j)*2,(u16)c0); lds_st<u16>(ks+(544+j)*2,(u16)(c0>>16));
      lds_st<u16>(ks+(576+j)*2,(u16)c1); lds_st<u16>(ks+(608+j)*2,(u16)(c1>>16));
    } else { lds_st<u32>(ks+(384+2*j)*2,b.x); lds_st<u32>(ks+(448+2*j)*2,b.y); if(j==0) lds_st<u16>(ks+640*2,(u16)c0); } }
  const int M=lane; const bool lo=M<32;
  u32x4 v0=lds_ld<u32x4>(ks+(M&31)*16); u32x2 v1=lds_ld<u32x2>(ks+(128+4*(M|32))*2);
  u32 v4=lds_ld<u16>(ks+(132+4*(M|32))*2);
  u32 pv=lds_ld<u16>(ks+(lo?max(8*M-1,0):((M==32)?255:(128+4*M)))*2); if(M==0) pv=0;
  u32 ab=lds_ld<u32>(ks+(384+2*M)*2), c2=lds_ld<u16>(ks+(386+2*M)*2);
  u32 a3[2],b3[2];
  #pragma unroll
  for(int i=0;i<2;++i){ a3[i]=lds_ld<u16>(ks+(512+M+64*i)*2); b3[i]=lds_ld<u16>(ks+(513+M+64*i)*2); }
  asm volatile("":"+v"(v0),"+v"(v1),"+v"(v4),"+v"(pv),"+v"(ab),"+v"(c2),"+v"(a3[0]),"+v"(a3[1]),"+v"(b3[0]),"+v"(b3[1])::"memory");
  { u32x4 A,B;
    if(lo){ B.x=p_hl(v0.w,v0.w); B.y=p_hl(v0.z,v0.z); B.z=p_hl(v0.y,v0.y); B.w=p_hl(v0.x,v0.x);
      A.x=p_lh(v0.w,v0.z); A.y=p_lh(v0.z,v0.y); A.z=p_lh(v0.y,v0.x); A.w=p_ll(v0.x,pv);
    } else { B.x=p_lh(v4,v1.y); B.y=p_ll(v1.y,v1.y); B.z=p_lh(v1.y,v1.x); B.w=p_ll(v1.x,v1.x);
      A.x=p_hl(v1.y,v1.y); A.y=B.y; A.z=p_hl(v1.x,v1.x); A.w=p_ll(v1.x,pv); }
    lds_st<u32x4>(ka+(255-M)*16,A); lds_st<u32x4>(kb+(255-M)*16,B); }
  { u32x4 A,B; B.x=p_ll(c2,c2); B.y=p_hh(ab,ab); B.z=p_hl(ab,ab); B.w=p_ll(ab,ab);
    A.x=p_lh(c2,ab); A.y=B.y; A.z=B.w; A.w=B.w;
    lds_st<u32x4>(ka+(191-M)*16,A); lds_st<u32x4>(kb+(191-M)*16,B); }
  #pragma unroll
  for(int i=0;i<2;++i){ u32 a=a3[i], b=b3[i], c=(M&1)?b:a; u32x4 A,B;
    B.x=p_ll(b,b); B.y=p_ll(b,c); B.z=p_ll(a,a); B.w=B.z;
    A.x=B.x; A.y=p_ll(c,a); A.z=B.z; A.w=B.z;
    lds_st<u32x4>(ka+(127-64*i-M)*16,A); lds_st<u32x4>(kb+(127-64*i-M)*16,B); }
  if(lane<8) lds_st<u32x4>(((lane&4)?kb:ka)+(256+(lane&3))*16,u32x4{0,0,0,0});
}
template<int NS> DI void conv_steps(lptr ua, lptr ka, int kbo, int s0, int lane, f32x4* acc){
  const int a=lane&15, g=lane>>4;
  lptr up=ua+((a&1)?0:A_UB)+(32*(a>>1)+8*g+32*s0)*2;
  lptr kp=ka+((a&1)?kbo:0)+(28-4*(a>>1)+8*g+32*s0)*2;
  u32x4 A[2][8],B[2][8];
  auto ldA=[&](int s,int q){ A[s&1][q]=cat(lds_ld64(kp+(1792-256*q+32*s)*2),lds_ld64(kp+(1792-256*q+32*s)*2+8)); };
  auto ldB=[&](int s,int q){ B[s&1][q]=lds_ld<u32x4>(up+(256*q+32*s)*2); };
  #pragma unroll
  for(int q=0;q<8;++q){ ldB(0,q); ldA(0,q); }
  #pragma unroll
  for(int c=0;c<8;++c) acc[c]=Z4;
  SB();
  #pragma unroll
  for(int s=0;s<NS;++s){
    #pragma unroll
    for(int q=0;q<8;++q){
      #pragma unroll
      for(int c=q;c<8;++c) acc[c]=mfma16(A[s&1][q],B[s&1][c-q],acc[c]);
      if(s+1<NS){ if(q==0){ ldB(s+1,0); ldB(s+1,1); ldB(s+1,2); ldB(s+1,3); } else if(q==1){ ldA(s+1,0); ldB(s+1,4); ldB(s+1,5); }
        else if(q==2){ ldB(s+1,6); ldB(s+1,7); ldA(s+1,1); } else if(q==3){ ldA(s+1,2); ldA(s+1,3); } else if(q==4){ ldA(s+1,4); ldA(s+1,5); }
        else if(q==5){ ldA(s+1,6); } else if(q==6){ ldA(s+1,7); } }
      SB(); } }
}
DI u32 fin2(float y0,float y1,u32 u,float dsk){
  u32 y=pk_bf16(y0,y1), s=pk_bf16(bflo(u)*dsk,bfhi(u)*dsk);
  return pk_bf16(bflo(y)+bflo(s),bfhi(y)+bfhi(s)); }
DI void role_a(int j, lptr L, const u16* u, const SsmIn& in, const u16* dskip, const u16* wo, u16* out, void* ws){
  const int tid=threadIdx.x, lane=tid&63, w=__builtin_amdgcn_readfirstlane(tid>>6), p=w&3;
  const int ch0=10*j;
  const rsrc_t ro=__builtin_amdgcn_make_buffer_rsrc(out,0,2048*2560,0x00020000);
  lptr k0=L+A_K0+p*A_KS, k1=k0+4*A_KS;
  constexpr int XKB=A_US+224;
  u32 dk[5];
  #pragma unroll
  for(int i=0;i<5;++i) dk[i]=((const u32*)(dskip+ch0))[i];
  SsmP p0=load_ssm(in,ch0+w,lane);
  SsmP px; if(w>=4) px=load_ssm(in,ch0+8+(p>>1),lane);
  for(int i=tid;i<570;i+=512){ int sl=i/57, c=i-sl*57; lds_st<u32x4>(L+sl*A_US+(c<28?c*16:A_UB+(c-28)*16),u32x4{0,0,0,0}); }
  if(tid==0) lds_st<u32x4>(L+A_XF,u32x4{0,0,0,0});
  bar();
  u32x4 um[4];
  const int ur=(tid+64*j)&511;
  { typedef u32x4 __attribute__((aligned(4))) u32x4u;
    const char* up0=(const char*)u+(size_t)ur*4*1280+20*j;
    #pragma unroll
    for(int r=0;r<4;++r) um[r]=*(const u32x4u*)(up0+r*1280); }
  const u64 E=epoch(ws);
  ST(1);
  float xv[12];
  if(w>=4){ __builtin_amdgcn_s_setprio(3); float s,inv; ssm_prep(px,s,inv); f32x16 acc={};
    kgen_half(px,p&1,s,k1,lane,acc);
    #pragma unroll
    for(int r=0;r<12;++r) xv[r]=acc[r]*inv; }
  float ks,kinv; ssm_prep(p0,ks,kinv); f32x16 kacc={};
  auto k_rest=[&](lptr kw){ kgen_half(p0,1,ks,kw,lane,kacc); float v[12];
    #pragma unroll
    for(int r=0;r<12;++r) v[r]=kacc[r]*kinv;
    k_finish(v,kw,A_KB,lane); };
  kgen_half(p0,0,ks,L+A_K0+w*A_KS,lane,kacc);
  if(w<4) k_rest(k0);
  ST(2);
  const int tl=32*((lane&15)>>1)+8*(lane>>4)+2*(lane&1);
  auto fin4=[&](u32* r,f32x4 v,int e,int c){
    lptr y=L+e*A_US+(224+256*c+tl)*2; const float d=(e&1)?bfhi(dk[e>>1]):bflo(dk[e>>1]);
    r[0]=fin2(v[0],v[1],lds_ld<u32>(y),d); r[1]=fin2(v[2],v[3],lds_ld<u32>(y+8),d); };
  auto scatter=[&](lptr d,u32 r0,u32 r1,u32 r2,u32 r3,bool last){
    u32x2 e={p_ll(r0,r1),p_ll(r2,r3)}, o={p_hh(r0,r1),p_hh(r2,r3)};
    lds_st<u32x2>(d,e); lds_st<u32x2>(d+A_US,o);
    lds_st<u32>(d+A_UB+4,e.x); lds_st<u32>(d+A_US+A_UB+4,o.x);
    if(!last){ lds_st<u32>(d+A_UB+8,e.y); lds_st<u32>(d+A_US+A_UB+8,o.y); } };
  #pragma unroll
  for(int c=0;c<4;++c) scatter(L+2*c*A_US+(224+4*ur)*2,um[0][c],um[1][c],um[2][c],um[3][c],ur==511);
  bar();
  ST(3);
  if(w<4){
    f32x4 acc[8];
    conv_steps<8>(L+p*A_US,k0,A_KB,0,lane,acc);
    #pragma unroll
    for(int c=0;c<8;++c) lds_st<f32x4>(k0+c*1024+lane*16,acc[c]);
    ST(4);
    bar();
    conv_steps<8>(L+(p+4)*A_US,k1,A_KB,0,lane,acc);
    #pragma unroll
    for(int c=0;c<8;++c) lds_st<f32x4>(k1+c*1024+lane*16,acc[c]);
    ST(5);
    bar();
    conv_steps<4>(L+(8+(p>>1))*A_US,L+(p&2)*A_US+A_UB,XKB,4*(p&1),lane,acc);
    #pragma unroll
    for(int c=0;c<8;++c) lds_st<f32x4>(k0+c*1024+lane*16,acc[c]);
    ST(6);
    bar();
  } else {
    if(tid==256) flag_set(ws,192+j,E);
    k_rest(k1);
    ST(9);
    u32 ux[8];
    { const char* up=(const char*)u+(size_t)(tid-256)*4*1280+20*j+16;
      #pragma unroll
      for(int r=0;r<8;++r) ux[r]=*(const u32*)(up+(size_t)((r&3)+(r>>2)*1024)*1280); }
    ST(4);
    bar();
    u32 pf[4];
    { const char* wp=(const char*)wo+(j&7)*102400+(tid-256)*128;
      #pragma unroll
      for(int i=0;i<4;++i) pf[i]=*(const u32*)(wp+((i<3||tid<288)?i*32768:0)); }
    { lptr xp=L+(p|1)*A_US+A_UB+lane*16; volatile LAS u32* xf=(volatile LAS u32*)(L+A_XF+2*(p&2));
      if(p&1){
        #pragma unroll
        for(int r=0;r<3;++r) *(volatile LAS f32x4*)(xp+r*1024)=f32x4{xv[4*r],xv[4*r+1],xv[4*r+2],xv[4*r+3]};
        *xf=1u;
      } else { f32x4 o[3];
        for(int it=0;it<SPIN_MAX;++it){ const u32 f=*xf;
          #pragma unroll
          for(int r=0;r<3;++r) o[r]=*(volatile LAS f32x4*)(xp+r*1024);
          if(__builtin_amdgcn_readfirstlane(f)) break; }
        #pragma unroll
        for(int r=0;r<12;++r) xv[r]+=o[r>>2][r&3];
        k_finish(xv,L+p*A_US+A_UB,XKB,lane); } }
    #pragma unroll
    for(int h=0;h<2;++h) scatter(L+8*A_US+(224+4*(tid-256)+1024*h)*2,ux[4*h],ux[4*h+1],ux[4*h+2],ux[4*h+3],h==1&&tid==511);
    ST(8);
    #pragma unroll
    for(int i=0;i<2;++i){ u32 r[4][2]; const int c=2*p+i;
      #pragma unroll
      for(int e=0;e<4;++e) fin4(r[e],lds_ld<f32x4>(L+A_K0+e*A_KS+c*1024+lane*16),e,c);
      const u32 o=(256*c+tl)*2560+20*j;
      #pragma unroll
      for(int h=0;h<2;++h){
        __builtin_amdgcn_raw_buffer_store_b64(u32x2{p_ll(r[0][h],r[1][h]),p_ll(r[2][h],r[3][h])},ro,o+4*h*2560,0,0);
        __builtin_amdgcn_raw_buffer_store_b64(u32x2{p_hh(r[0][h],r[1][h]),p_hh(r[2][h],r[3][h])},ro,o+(4*h+1)*2560,0,0); } }
    asm volatile(""::"v"(pf[0]),"v"(pf[1]),"v"(pf[2]),"v"(pf[3]));
    ST(5);
    bar();
    #pragma unroll
    for(int i=0;i<2;++i)
      #pragma unroll
      for(int e=0;e<4;++e){ u32 r[2]; lptr q=L+A_K0+(4+e)*A_KS+(p+4*i)*1024+lane*16; fin4(r,lds_ld<f32x4>(q),4+e,p+4*i); lds_st<u32x2>(q,u32x2{r[0],r[1]}); }
    ST(6);
    bar();
  }
  {
    u32 r[6][2];
    #pragma unroll
    for(int e=0;e<4;++e){ const u32x2 v=lds_ld<u32x2>(L+A_K0+(4+e)*A_KS+tid*16); r[e][0]=v.x; r[e][1]=v.y; }
    #pragma unroll
    for(int e=0;e<2;++e){ lptr q=L+A_K0+2*e*A_KS+tid*16; fin4(r[4+e],lds_ld<f32x4>(q)+lds_ld<f32x4>(q+A_KS),8+e,w); }
    const u32 o=(256*w+tl)*2560+20*j+8;
    #pragma unroll
    for(int h=0;h<2;++h){
      __builtin_amdgcn_raw_buffer_store_b96(u32x3{p_ll(r[0][h],r[1][h]),p_ll(r[2][h],r[3][h]),p_ll(r[4][h],r[5][h])},ro,o+4*h*2560,0,0);
      __builtin_amdgcn_raw_buffer_store_b96(u32x3{p_hh(r[0][h],r[1][h]),p_hh(r[2][h],r[3][h]),p_hh(r[4][h],r[5][h])},ro,o+(4*h+1)*2560,0,0); } }
  ST(7); STDUMP();
}

constexpr int B_ST=36864, B_PV=2*B_ST, B_PVS=18432, B_WP=4*B_ST, B_Z=B_WP+6400, B_F=163200;
DI float max3(float a,float b,float c){ return __builtin_fmaxf(__builtin_fmaxf(a,b),c); }
template<class H> DI void softmax20x2(const f32x4 (*z)[5], u32 (*o)[10], H hook){
  const float c=1.4426950408889634f; float mc[2];
  { float m0[2],m1[2],m2[2];
    #pragma unroll
    for(int t=0;t<2;++t){ m0[t]=max3(z[t][0][0],z[t][0][1],z[t][0][2]); m1[t]=max3(z[t][0][3],z[t][1][0],z[t][1][1]); m2[t]=max3(z[t][1][2],z[t][1][3],z[t][2][0]); }
    #pragma unroll
    for(int t=0;t<2;++t){ m0[t]=max3(m0[t],z[t][2][1],z[t][2][2]); m1[t]=max3(m1[t],z[t][2][3],z[t][3][0]); m2[t]=max3(m2[t],z[t][3][1],z[t][3][2]); }
    #pragma unroll
    for(int t=0;t<2;++t){ m0[t]=max3(m0[t],z[t][3][3],z[t][4][0]); m1[t]=max3(m1[t],z[t][4][1],z[t][4][2]); m2[t]=__builtin_fmaxf(m2[t],z[t][4][3]); }
    #pragma unroll
    for(int t=0;t<2;++t) mc[t]=max3(m0[t],m1[t],m2[t])*-c; }
  SB();
  hook(0);
  f32x2 e[2][10];
  #pragma unroll
  for(int i=0;i<10;++i)
    #pragma unroll
    for(int t=0;t<2;++t) e[t][i]=__builtin_elementwise_fma(f32x2{z[t][i>>1][(i&1)*2],z[t][i>>1][(i&1)*2+1]},f32x2{c,c},f32x2{mc[t],mc[t]});
  SB();
  hook(1);
  #pragma unroll
  for(int i=0;i<10;++i)
    #pragma unroll
    for(int t=0;t<2;++t) e[t][i]=f32x2{__builtin_amdgcn_exp2f(e[t][i].x),__builtin_amdgcn_exp2f(e[t][i].y)};
  SB();
  hook(2);
  float r[2];
  { f32x2 s[2][5];
    #pragma unroll
    for(int i=0;i<5;++i)
      #pragma unroll
      for(int t=0;t<2;++t) s[t][i]=e[t][2*i]+e[t][2*i+1];
    #pragma unroll
    for(int t=0;t<2;++t){ s[t][0]+=s[t][1]; s[t][2]+=s[t][3]; }
    #pragma unroll
    for(int t=0;t<2;++t){ s[t][0]+=s[t][4]; }
    #pragma unroll
    for(int t=0;t<2;++t){ s[t][0]+=s[t][2]; }
    #pragma unroll
    for(int t=0;t<2;++t) r[t]=__builtin_amdgcn_rcpf(s[t][0].x+s[t][0].y); }
  SB();
  #pragma unroll
  for(int i=0;i<10;++i)
    #pragma unroll
    for(int t=0;t<2;++t) e[t][i]*=r[t];
  SB();
  #pragma unroll
  for(int i=0;i<10;++i)
    #pragma unroll
    for(int t=0;t<2;++t) o[t][i]=pk_bf16(e[t][i].x,e[t][i].y);
  SB();
}
DI u32x4 pack2(f32x4 a,f32x4 b){ u32x4 r={pk_bf16(a[0],a[1]),pk_bf16(a[2],a[3]),pk_bf16(b[0],b[1]),pk_bf16(b[2],b[3])}; return r; }
DI void role_b(const bool ISQ,int X,int li,lptr L,u32 warm,const u16* x,const u16* wqkv,const u16* wphi,const u16* wo,u16* out,void* ws){
  const int tid=threadIdx.x, lane=tid&63, w=__builtin_amdgcn_readfirstlane(tid>>6), g=lane>>4;
  const int rt=4*(X>>1)+(ISQ?(li>>1):(li>>2));
  const int grp=ISQ?(2*(X&1)+(li&1)):(4*(X&1)+(li&3));
  const int T=ISQ?16+li:li; const bool doout=T<20; const int tt64=4*X+T/5, ct=T%5;
  const rsrc_t rs=__builtin_amdgcn_make_buffer_rsrc(ws,0,WS_END,0x00020000);
  constexpr int OIM=B_PV;
  u64 E=epoch_late(ws);
  auto arrive=[&](){
    u32 n=0; if(lane==0) n=__hip_atomic_fetch_add((LAS u32*)(L+B_F+16),1u,__ATOMIC_RELAXED,__HIP_MEMORY_SCOPE_WORKGROUP);
    if(__builtin_amdgcn_readfirstlane(n)==3u&&lane==0) flag_set(ws,ISQ?(128+4*rt+grp):(16*grp+rt),E); };
  volatile LAS u32* bad=(volatile LAS u32*)(L+B_F+20);
  if(__builtin_expect(w>=4,1)){
    const int hw=w-4;
    const int s0=(0x8530>>((ISQ?((li>>1)+2*(li&1)):((li>>2)+(li&3)))&3)*4&15)+(ISQ?1:0);
    u32 xo[4], wv[5];
    #pragma unroll
    for(int i=0;i<4;++i){ int r=(4*hw+i)*8+(lane>>3); xo[i]=(u32)(rt*128+r)*1280u+(u32)(((lane&7)^((r>>1)&7))<<4)-1024u*i; }
    const u32 wlo=ISQ?320*grp:1280+160*grp, whi=ISQ?320*grp:2400+160*grp;
    #pragma unroll
    for(int i=0;i<5;++i){ u32 c=(5*hw+i)*64+lane, r=(c*3277u)>>16, pos=c-r*20, sc=pos^(((r>>3)&1)<<1);
      wv[i]=r*3840u+(sc<10?wlo:whi)+16*sc-1024u*(i&3); }
    auto issue=[&](int s){ int ss=s+s0; if(ss>=10) ss-=10; lptr d=L+(s&3)*B_ST;
      const char* xb=(const char*)x+ss*128; const char* wb=(const char*)wqkv+(size_t)ss*64*3840;
      dma16x4(wb,wv[0],wv[1],wv[2],wv[3],d+16384+5*hw*1024); dma16x4(xb,xo[0],xo[1],xo[2],xo[3],d+4*hw*1024); dma16(wb,wv[4],d+16384+(5*hw+4)*1024); };
    ST(9);
    issue(0); issue(1);
    asm volatile("":"+s"(E));
    ST(1); waitv<9>(); rbar(); issue(2);
    ST(2);
    waitv<9>(); rbar();
    #pragma unroll 1
    for(int s=2;s<9;++s){
      issue(s+1);
      waitv<9>(); rbar(); }
    waitv<0>(); rbar();
    if(!doout){ STDUMP(); return; }
    if(!ISQ){
      const u32x4 sv={SENT,SENT,SENT,SENT};
      #pragma unroll
      for(int i=0;i<4;++i) st_sc1(rs,(i<3||lane<18)?(WS_P1+((4*grp+hw)*16+rt)*3584+i*1024+lane*16):OOB,sv);
      #pragma unroll
      for(int i=0;i<5;++i) st_sc1(rs,WS_O+((8*rt+grp)*20+5*hw+i)*1024+lane*16,sv);
      waitv<0>(); arrive();
      ST(12); }
    auto go=[&](u32 n){ for(int it=0;it<SPIN_MAX;++it){ if(__builtin_amdgcn_readfirstlane(*(volatile LAS u32*)(L+B_F+24))>=n) break; __builtin_amdgcn_s_sleep(2); } asm volatile("":::"memory"); };
    u32x4 wf[2][20];
    {
      const int pr=hw>>1, me2=hw&1, r=lane>>3;
      const char* gb=(const char*)wo+ct*256+pr*128+me2*8*1280;
      const u32 sw=((lane&7)^((((r>>1)&1)|(me2<<1))<<1))<<4, vo=r*1280+sw, vo2=((r>>2)*20+(r&3)+16+32*me2)*1280+sw;
      auto V=[&](int n,int k){ const int G=n/10, m=n%10; return ((m<8)?vo+(160*G+20*m)*1280:vo2+(160*G+80*(m-8))*1280)-1024*k; };
      auto four=[&](int n,lptr d){ dma16x4(gb,V(n,0),V(n+1,1),V(n+2,2),V(n+3,3),d); };
      const int q=(lane&15)>>2, p=lane&3, f=((q>>1)|((g&1)<<1))<<1;
      lptr rb=L+(pr*2+(g&1))*18432+(g>>1)*1024+q*128+8*(p&1);
      lptr rd=rb+(((4*me2+(p>>1))^f)<<4), rd1=rb+(((4*me2+2+(p>>1))^f)<<4);
      constexpr int TAIL=B_PV+12288;
      lptr dst=L+hw*18432;
      volatile LAS u32* fm=(volatile LAS u32*)(L+B_F+48+4*hw); volatile LAS u32* fo=(volatile LAS u32*)(L+B_F+48+4*(hw^1));
      auto meet=[&](u32 n){ *fm=n; for(int it=0;it<SPIN_MAX;++it){ if(__builtin_amdgcn_readfirstlane(*fo)>=n) break; __builtin_amdgcn_s_sleep(1); } asm volatile("":::"memory"); };
      go(4u); ST(3);
      four(0,dst); four(4,dst+4096); four(8,dst+8192); four(12,dst+12288); dma16(gb,V(16,0),dst+16384); dma16(gb,V(17,0),dst+17408);
      four(18,dst+TAIL); dma16(gb,V(22,0),dst+TAIL+4096); dma16(gb,V(23,0),dst+TAIL+5120);
      ST(10); waitv<0>(); ST(11); meet(1);
      #pragma unroll
      for(int s=0;s<12;++s){ const int o=(s<9)?s*2048:TAIL+(s-9)*2048;
        wf[0][s]=cat(lds_tr(rd+o),lds_tr(rd+o+512)); wf[1][s]=cat(lds_tr(rd1+o),lds_tr(rd1+o+512)); }
      asm volatile("s_waitcnt lgkmcnt(0)":::"memory");
      if(lane==0) __hip_atomic_fetch_add((LAS u32*)(L+B_F+28),1u,__ATOMIC_RELAXED,__HIP_MEMORY_SCOPE_WORKGROUP);
      meet(2);
      go(ISQ?4u:8u);
      four(24,dst); four(28,dst+4096); four(32,dst+8192); four(36,dst+12288);
      ST(13); waitv<0>(); ST(14); meet(3);
      #pragma unroll
      for(int s=0;s<8;++s){ wf[0][12+s]=cat(lds_tr(rd+s*2048),lds_tr(rd+s*2048+512)); wf[1][12+s]=cat(lds_tr(rd1+s*2048),lds_tr(rd1+s*2048+512)); }
      asm volatile("s_waitcnt lgkmcnt(0)":::"memory"); }
    ST(4);
    for(u32 it=1;it<SPIN_MAX;++it){
      rbar();
      ST(5);
      f32x4 c[2][4];
      #pragma unroll
      for(int t=0;t<4;++t){ c[0][t]=Z4; c[1][t]=Z4; }
      u32x4 ob[2][4]; u32 chk=0;
      lptr ob0=L+OIM+lane*16;
      #pragma unroll
      for(int t=0;t<4;++t) ob[0][t]=lds_ld<u32x4>(ob0+((t+hw)&3)*20480);
      lptr obt[4];
      #pragma unroll
      for(int t=0;t<4;++t) obt[t]=ob0+((t+hw)&3)*20480;
      SB();
      #pragma unroll
      for(int ks=0;ks<20;++ks){
        #pragma unroll
        for(int t=0;t<4;++t){ if(ks<19) ob[(ks+1)&1][t]=lds_ld<u32x4>(obt[t]+(ks+1)*1024);
          c[0][t]=mfma16(wf[0][ks],ob[ks&1][t],c[0][t]); c[1][t]=mfma16(wf[1][ks],ob[ks&1][t],c[1][t]); }
        chk=max(chk,max(ob[ks&1][0].x,ob[ks&1][0].z));
        SB(); }
      if(__any(chk==SENT)) *bad=it;
      #pragma unroll
      for(int t=0;t<4;++t)
        #pragma unroll
        for(int cc=0;cc<2;++cc) lds_st<u32x2>(L+(16*((t+hw)&3)+(lane&15))*272+hw*64+cc*32+8*g,u32x2{pk_bf16(c[cc][t][0],c[cc][t][1]),pk_bf16(c[cc][t][2],c[cc][t][3])});
      ST(6);
      bar();
      if(__builtin_amdgcn_readfirstlane(*bad)!=it) break; }
  } else {
    const int th=w>>1, hh=w&1;
    { const int NB=ISQ?3200:1600; const char* wp=(const char*)wphi+(ISQ?8*grp:4*grp)*800+hh*NB;
      #pragma unroll
      for(int i=0;i<4;++i) if(i*1024+lane*16<NB) dma16(wp,i*1024+lane*16,L+B_WP+hh*NB+i*1024); }
    if(lane<4) lds_st<u32x4>(L+B_Z+lane*16,u32x4{0,0,0,0});
    if(tid<4) lds_st<u32x4>(L+B_F+tid*16,u32x4{0,0,0,0});
    asm volatile("":"+s"(E));
    asm volatile("s_waitcnt vmcnt(0)"::"v"(warm):"memory");
    ST(9);
    lptr fad[3][2];
    { const int q=(lane&15)>>2, p=lane&3, hd=ISQ?p:(p&1);
      #pragma unroll
      for(int s=0;s<3;++s)
        #pragma unroll
        for(int t=0;t<2;++t){ const int d=16*(2*s+t)+4*g-20*hd; const bool ok=(d>=0)&&(d<20)&&(2*s+t<5);
          fad[s][t]=ok?(L+B_WP+((ISQ?4*hh:2*hh)+hd)*800+(d+q)*40+(ISQ?0:(p>>1)*3200)):(L+B_Z); if(2*s+t<5) asm volatile("":"+v"(fad[s][t])); } }
    lptr pv=L+B_PV+w*B_PVS;
    const int oh=4*grp+(rt&3), tq=rt>>2;
    const int ot=4*tt64+w;
    int fi=(lane<16)?(16*grp+lane):((lane<32)?(16*(lane&7)+4*tq+w):((lane<48)?(16*(ot&7)+(ot>>3)):(128+4*(4*tq+w)+(oh>>3))));
    lptr pp=pv+(lane&15)*160+(g&1)*80+(g>>1)*40, vp2, vp3, vp4, pa, vb;
    { lptr vp=pv+10272+(lane&15)*96; vp2=vp+((g<2)?(48*g+40):(8*g-16)); vp3=vp+((g==3)?48:(16+8*g)); vp4=vp+56+8*g;
      const int tk=16*(g>>1)+4*(g&1)+((lane&15)>>2); pa=pv+tk*160+8*(lane&3)+(th^1)*80; vb=pv+10272+tk*96+8*(lane&3)+(th^1)*48; }
    u32 qo=WS_Q+(oh*2048+512*tq+128*w+16*(g>>1)+(lane&15))*40, po0, po1;
    { const u32 po=WS_P1+((4*grp+2*hh+th)*16+rt)*3584; po0=po+lane*16; po1=((lane&15)<5)?(po+(160+5*g+(lane&15))*16):OOB; }
    asm volatile("":"+v"(fi),"+v"(pp),"+v"(vp2),"+v"(vp3),"+v"(vp4),"+v"(pa),"+v"(vb),"+v"(qo),"+v"(po0),"+v"(po1));
    if(!ISQ){
      #pragma unroll
      for(int i=0;i<2;++i) if(i*1024+lane*16<1600){ lptr q=L+B_WP+hh*1600+i*1024+lane*16; lds_st<u32x4>(q+3200,lds_ld<u32x4>(q)^0x80008000u); } }
    lptr ab[5], bb[2];
    { const int q=(lane&15)>>2,p=lane&3;
      #pragma unroll
      for(int i=0;i<5;++i){ int n=2*i+(p>>1); int cid=ISQ?(10*hh+n):(5*hh+n+(n>=5?5:0)); ab[i]=L+16384+(8*g+q)*320+((cid^((g&1)<<1))<<4)+8*(p&1); }
      const int b0=(64*th+(lane&15))*128+((g^((lane&15)>>1))<<4); bb[0]=L+b0; bb[1]=L+(b0^64); }
    f32x4 acc[5][4];
    #pragma unroll
    for(int i=0;i<5;++i)
      #pragma unroll
      for(int t=0;t<4;++t) acc[i][t]=Z4;
    ST(1);
    u32x4 FA[2][5], FB[2][4], fa[2][5];
    auto half=[&](int hf,bool mm,bool last){
      #pragma unroll
      for(int i=0;i<5;++i){
        if(i<2){ FB[hf][2*i]=lds_ld<u32x4>(bb[hf]+4096*i); FB[hf][2*i+1]=lds_ld<u32x4>(bb[hf]+4096*i+2048); }
        FA[hf][i]=cat(lds_tr(ab[i]+hf*10240),lds_tr(ab[i]+hf*10240+1280));
        if(mm){
          #pragma unroll
          for(int t=0;t<4;++t) acc[i][t]=mfma16(FA[hf^1][i],FB[hf^1][t],acc[i][t]); }
        if(last){ fa[0][i]=cat(lds_tr(fad[0][0]+8*i),lds_tr(fad[0][1]+8*i)); fa[1][i]=cat(lds_tr(fad[1][0]+8*i),lds_tr(fad[1][1]+8*i)); }
        SB(); } };
    bar();
    ST(2);
    SB();
    half(0,false,false);
    #pragma unroll 1
    for(int s=1;s<10;++s){
      half(1,true,false);
      const int step=(s&3)?B_ST:-3*B_ST;
      #pragma unroll
      for(int i=0;i<5;++i) ab[i]+=step;
      bb[0]+=step; bb[1]+=step;
      rbar();
      SB();
      half(0,true,false); }
    half(1,true,true);
    auto signal=[&](){ asm volatile("":::"memory"); if(lane==0) __hip_atomic_fetch_add((LAS u32*)(L+B_F+24),1u,__ATOMIC_RELAXED,__HIP_MEMORY_SCOPE_WORKGROUP); };
    #pragma unroll
    for(int t=0;t<4;++t)
      #pragma unroll
      for(int i=0;i<5;++i) acc[i][t]=mfma16(FA[1][i],FB[1][t],acc[i][t]);
    u32x2 pk[5][4];
    #pragma unroll
    for(int t=0;t<4;++t)
      #pragma unroll
      for(int i=0;i<5;++i) pk[i][t]=u32x2{pk_bf16(acc[i][t][0],acc[i][t][1]),pk_bf16(acc[i][t][2],acc[i][t][3])};
    ST(3);
    if(ISQ){
      u32x4 f2[5];
      #pragma unroll
      for(int i=0;i<5;++i) f2[i]=cat(lds_tr(fad[2][0]+8*i),u32x2{0u,0u});
      lptr sp_=pv+g*2560+(lane&15)*40;
      f32x4 z[4][5];
      #pragma unroll
      for(int t=0;t<4;++t)
        #pragma unroll
        for(int i=0;i<5;++i) z[t][i]=mfma16(fa[0][i],cat(pk[0][t],pk[1][t]),Z4);
      #pragma unroll
      for(int t=0;t<4;++t)
        #pragma unroll
        for(int i=0;i<5;++i) z[t][i]=mfma16(fa[1][i],cat(pk[2][t],pk[3][t]),z[t][i]);
      SB();
      #pragma unroll
      for(int t=0;t<5;++t){
        #pragma unroll
        for(int i=0;i<5;++i){ if(t<4) z[t][i]=mfma16(f2[i],cat(pk[4][t],u32x2{0u,0u}),z[t][i]);
          if(t>0) lds_st<u32x2>(sp_+(t-1)*640+8*i,u32x2{pk_bf16(z[t-1][i][0],z[t-1][i][1]),pk_bf16(z[t-1][i][2],z[t-1][i][3])}); }
        SB(); }
      ST(10);
      #pragma unroll
      for(int i=0;i<10;++i){ const int c=i*64+lane, hd=(c*205)>>15;
        st_sc1(rs,WS_Q+((8*grp+4*hh+hd)*2048+128*rt+64*th)*40+(c-160*hd)*16,lds_ld<u32x4>(pv+c*16)); }
      ST(11);
      waitv<0>(); ST(12); arrive(); signal();
      ST(4);
      if(doout){ fi=16*(ot&7)+(ot>>3); flag_wait(ws,E,fi,flag_get(ws,fi)); }
    } else {
      { f32x4 z[4][5]; u32 ph[4][10];
        auto fm=[&](int t){
          const u32x4 b0=cat(pk[0][t],pk[1][t]), b1=cat(pk[2][t],pk[3][t]);
          #pragma unroll
          for(int i=0;i<5;++i) z[t][i]=mfma16(fa[0][i],b0,Z4);
          #pragma unroll
          for(int i=0;i<5;++i) z[t][i]=mfma16(fa[1][i],b1,z[t][i]); };
        fm(0); fm(1);
        SB();
        softmax20x2(z,ph,[&](int k){ if(k==0){ fm(2); } else if(k==1){ fm(3); } });
        auto wp=[&](int t){
          #pragma unroll
          for(int i=0;i<5;++i) lds_st<u32x2>(pp+t*2560+8*i,u32x2{ph[t][2*i],ph[t][2*i+1]}); };
        softmax20x2(z+2,ph+2,[&](int k){
          if(k==0){ wp(0); wp(1); }
          else if(k==1){
            #pragma unroll
            for(int t=0;t<4;++t){ u32x2 v=pk[2][t]; if(g<2) v=u32x2{0x3F80u,0u};
              lds_st<u32x2>(vp2+t*1536,v); lds_st<u32x2>(vp3+t*1536,pk[3][t]); lds_st<u32x2>(vp4+t*1536,pk[4][t]); } } });
        wp(2); wp(3); }
      ST(4);
      u64 fl;
      lptr q00,q01,q10,q11; u32 om,ox;
      { const int r=lane&15, pc=5*(g&1)+2*(g>>1), pl=5*(g&1)+4, d1=min(r,4); lptr b=L+B_PV+8192;
        q00=b+(16*pc+r)*16; q01=b+(16*pl+r)*16+8*(g>>1); q10=b+(160+5*pc+d1)*16; q11=b+(160+5*pl+d1)*16+8*(g>>1);
        const u32 fb=WS_O+((32*tq+8*w)*20+5*(oh>>3))*1024+r*16;
        om=fb+((oh&7)>>1)*1024+(2*(oh&1)+(g>>1))*256+8*(g&1); ox=(g==0)?(fb+4096+((oh&7)>>1)*256+8*(oh&1)):OOB; }
      u32x4 l0[4],l1[4]; u32x2 l2[4];
      auto ldq=[&](){
        #pragma unroll
        for(int i=0;i<4;++i){ l0[i]=ld_sc1(rs,qo+i*1280); l1[i]=ld_sc1(rs,qo+i*1280+16); l2[i]=__builtin_amdgcn_raw_buffer_load_b64(rs,qo+i*1280+32,0,16); } };
      { f32x4 c[2][6];
        #pragma unroll
        for(int h2=0;h2<2;++h2){
          if(h2){ pa+=th?80:-80; vb+=th?48:-48; }
          u32x4 a[2][3], b[2][2];
          #pragma unroll
          for(int ks=0;ks<2;++ks){
            #pragma unroll
            for(int i=0;i<3;++i) a[ks][i]=cat(lds_tr(pa+ks*32*160+32*i),lds_tr(pa+(ks*32+8)*160+32*i));
            #pragma unroll
            for(int dt=0;dt<2;++dt) b[ks][dt]=cat(lds_tr(vb+ks*32*96+32*dt),lds_tr(vb+(ks*32+8)*96+32*dt)); }
          #pragma unroll
          for(int i=0;i<6;++i) c[h2][i]=mfma16(a[0][i%3],b[0][i/3],Z4);
          #pragma unroll
          for(int i=0;i<6;++i) c[h2][i]=mfma16(a[1][i%3],b[1][i/3],c[h2][i]); }
        #pragma unroll
        for(int i=0;i<6;++i) *(volatile LAS f32x4*)(pv+i*1024+lane*16)=c[0][i];
        *(volatile LAS u32*)(L+B_F+4*w)=1u;
        #pragma unroll
        for(int i=0;i<6;++i) asm volatile("":"+v"(c[1][i]));
        lptr ox_=L+B_PV+(w^2)*B_PVS; f32x4 pr[6];
        for(int it=0;it<SPIN_MAX;++it){
          const u32 f=*(volatile LAS u32*)(L+B_F+4*(w^2)), n=*(volatile LAS u32*)(L+B_F+16);
          #pragma unroll
          for(int i=0;i<6;++i) pr[i]=*(volatile LAS f32x4*)(ox_+i*1024+lane*16);
          if(__builtin_amdgcn_readfirstlane(f+n)==5u) break; }
        fl=flag_get(ws,fi);
        #pragma unroll
        for(int i=0;i<6;++i){ u32 o=(i<3)?(po0+i*1024):(po1+(i-3)*320); if(i%3==2) o=(g<2)?o:OOB;
          st_sc1(rs,o,__builtin_bit_cast(u32x4,c[1][i]+pr[i])); } }
      ST(5);
      if(!__all(fl==E)) flag_wait(ws,E,fi,fl);
      asm volatile("":::"memory"); ldq();
      u32 pq[4][10];
      {
        const u32 sg=(g&1)?0x80008000u:0u;
        u32x4 v[16]; const u32 go0=WS_P1+(oh*16+4*w)*3584+lane*16, go1=(lane<18)?go0+3072:OOB;
        #pragma unroll
        for(int i2=0;i2<4;i2+=2){ f32x4 z[2][5];
          #pragma unroll
          for(int t=0;t<2;++t){ const u32x4 a=l0[i2+t]^sg, b=l1[i2+t]^sg; const u32x2 c=l2[i2+t]^sg;
            z[t][0]=f32x4{bflo(a.x),bfhi(a.x),bflo(a.y),bfhi(a.y)}; z[t][1]=f32x4{bflo(a.z),bfhi(a.z),bflo(a.w),bfhi(a.w)};
            z[t][2]=f32x4{bflo(b.x),bfhi(b.x),bflo(b.y),bfhi(b.y)}; z[t][3]=f32x4{bflo(b.z),bfhi(b.z),bflo(b.w),bfhi(b.w)};
            z[t][4]=f32x4{bflo(c.x),bfhi(c.x),bflo(c.y),bfhi(c.y)}; }
          if(i2==0){
            #pragma unroll
            for(int k=0;k<16;++k) v[k]=ld_sc1(rs,((k&3)<3?go0+(k&3)*1024:go1)+(k>>2)*3584);
            signal(); }
          SB();
          softmax20x2(z,pq+i2,[](int){});
          #pragma unroll
          for(int i=i2;i<i2+2;++i){
            #pragma unroll
            for(int k=0;k<5;++k){ const int a=(k<4)?k:8, b=(k<4)?k+4:9;
              auto r=__builtin_amdgcn_permlane32_swap(pq[i][a],pq[i][b],false,false); pq[i][a]=r[0]; pq[i][b]=r[1]; }
            #pragma unroll
            for(int k=0;k<10;++k) asm volatile("":"+v"(pq[i][k])); }
          if(i2==0){ gather4<1>(rs,go0,go1,v); SB(); } }
        ST(10);
        gather4<SPIN_MAX>(rs,go0,go1,v);
        ST(13);
        #pragma unroll
        for(int i=0;i<4;++i) *(volatile LAS f32x4*)(pv+8192+i*1024+lane*16)=(__builtin_bit_cast(f32x4,v[i])+__builtin_bit_cast(f32x4,v[4+i]))+(__builtin_bit_cast(f32x4,v[8+i])+__builtin_bit_cast(f32x4,v[12+i]));
        *(volatile LAS u32*)(L+B_F+32+4*w)=1u;
        SB();
      }
      ST(12);
      u32x4 a0[2]; u32 a1[2];
      {
        f32x4 s[2][2]; f32x2 u[2];
        for(int it=0;it<SPIN_MAX;++it){
          const u32x4 f=*(volatile LAS u32x4*)(L+B_F+32);
          #pragma unroll
          for(int k=0;k<4;++k){ const int q=k*B_PVS;
            const f32x4 x0=*(volatile LAS f32x4*)(q00+q), x1=*(volatile LAS f32x4*)(q00+q+256), y0=*(volatile LAS f32x4*)(q10+q), y1=*(volatile LAS f32x4*)(q10+q+80);
            const f32x2 u0=*(volatile LAS f32x2*)(q01+q), u1=*(volatile LAS f32x2*)(q11+q);
            if(k==0){ s[0][0]=x0; s[0][1]=x1; s[1][0]=y0; s[1][1]=y1; u[0]=u0; u[1]=u1; }
            else { s[0][0]+=x0; s[0][1]+=x1; s[1][0]+=y0; s[1][1]+=y1; u[0]+=u0; u[1]+=u1; } }
          if(__builtin_amdgcn_readfirstlane(f.x+f.y+f.z+f.w)==4u) break; }
        #pragma unroll
        for(int i=0;i<2;++i){ a0[i]=pack2(s[i][0],s[i][1]); a1[i]=pk_bf16(u[i].x,u[i].y); } }
      ST(14);
      { f32x4 n[8][2];
        #pragma unroll
        for(int t=0;t<8;++t){ const u32* f=pq[t>>1]+4*(t&1);
          #pragma unroll
          for(int i=0;i<2;++i) n[t][i]=mfma16(a0[i],u32x4{f[0],f[1],f[2],f[3]},Z4); }
        #pragma unroll
        for(int t=0;t<8;++t)
          #pragma unroll
          for(int i=0;i<2;++i) n[t][i]=mfma16(u32x4{a1[i],0u,0u,0u},u32x4{pq[t>>1][8+(t&1)],0u,0u,0u},n[t][i]);
        SB();
        float den[8];
        #pragma unroll
        for(int t=0;t<8;++t) den[t]=__builtin_bit_cast(float,__builtin_amdgcn_ds_bpermute(((lane&15)+16)*4,__builtin_bit_cast(int,n[t][1][0])));
        SB();
        #pragma unroll
        for(int t=0;t<8;++t){ const float r=__builtin_amdgcn_rcpf(rbf(rbf(den[t])+1e-6f));
          const f32x4 m=n[t][0]*r, e=n[t][1]*r;
          __builtin_amdgcn_raw_buffer_store_b64(u32x2{pk_bf16(m[0],m[1]),pk_bf16(m[2],m[3])},rs,om+t*20480,0,16);
          __builtin_amdgcn_raw_buffer_store_b64(u32x2{pk_bf16(e[0],e[1]),pk_bf16(e[2],e[3])},rs,ox+t*20480,0,16); } }
      signal();
      if(rt==15&&grp==7&&w==3){
        for(int it=0;it<SPIN_MAX;++it){ if(__all(flag_get(ws,lane)==E&&flag_get(ws,64+lane)==E&&flag_get(ws,128+lane)==E&&flag_get(ws,192+lane)==E)) break; __builtin_amdgcn_s_sleep(2); }
        if(lane==0) __hip_atomic_store((GAS u64*)ws,E,__ATOMIC_RELAXED,__HIP_MEMORY_SCOPE_AGENT); }
      ST(6);
    }
    if(!doout){ STDUMP(); return; }
    for(int it=0;it<SPIN_MAX;++it){ const u32x2 c=*(volatile LAS u32x2*)(L+B_F+24); if(__builtin_amdgcn_readfirstlane(c.x+c.y)>=(ISQ?8u:12u)) break; __builtin_amdgcn_s_sleep(1); }
    { const u32 ob=WS_O+ot*20480;
      const u32 sm=ob+((lane<32)?((5*(lane>>3)+((lane>>1)&3))*1024+(lane&1)*512+508):((5*((lane-32)>>3)+4)*1024+((lane&7)>>1)*256+244+8*(lane&1)));
      for(int it=0;it<SPIN_MAX;++it){ asm volatile("":::"memory");
        if(__all(__builtin_amdgcn_raw_buffer_load_b32(rs,sm,0,16)!=SENT)) break;
        __builtin_amdgcn_s_sleep(1); }
      ST(15);
      const u32 vo=lane*16;
      for(u32 it=1;it<SPIN_MAX;++it){
        #pragma unroll
        for(int i=0;i<5;++i) dma16x4_sc1((const char*)ws+ob+i*4096,vo,vo,vo,vo,L+OIM+w*20480+i*4096);
        waitv<0>();
        ST(7);
        rbar();
        rbar();
        if(__builtin_amdgcn_readfirstlane(*bad)!=it) break; } }
  }
  #pragma unroll
  for(int i=0;i<2;++i){ const int r=32*i+(tid>>4);
    *(u32x4*)((char*)out+(size_t)(64*tt64+r)*2560+1280+ct*256+(tid&15)*16)=lds_ld<u32x4>(L+r*272+(tid&15)*16); }
  ST(8); STDUMP();
}

constexpr int CODE_LINES=440;
__global__ __launch_bounds__(512,2) void fused(const u16* u,const u16* are,const u16* aim,const u16* bre,const u16* bim,const u16* cre,const u16* cim,
    const u16* logdt,const u16* dskip,const u16* x,const u16* wqkv,const u16* wphi,const u16* wo,u16* out,void* ws){
  extern __shared__ __attribute__((aligned(16))) char smem[];
  lptr L=(lptr)smem;
  const int b=blockIdx.x, X=b&7, idx=b>>3;
  u64 pc; asm volatile("s_getpc_b64 %0":"=s"(pc));
  STINIT(); ST(0);
  const int w=__builtin_amdgcn_readfirstlane(threadIdx.x>>6);
  const bool isa=(idx>=16&&idx<24), isq=idx>=24;
  if(__builtin_expect(!isa,1)){
    u32 warm=0;
    if(idx<16&&threadIdx.x<32&&idx*32+threadIdx.x<CODE_LINES) warm=*(const u32*)((const char*)(pc&~255ull)+(idx*32+threadIdx.x)*128);
    if(idx<16&&w==1&&(threadIdx.x&63)<50) warm=*(const u32*)((const char*)wo+((X*16+idx)*50+(threadIdx.x&63))*128);
    role_b(isq,X,isq?idx-24:idx,L,warm,x,wqkv,wphi,wo,out,ws);
  } else { const SsmIn in={are,aim,bre,bim,cre,cim,logdt}; role_a(X*8+(idx-16),L,u,in,dskip,wo,out,ws); }
  if(b==255&&threadIdx.x==0){ u64 t0=__builtin_amdgcn_s_memrealtime(); while(__builtin_amdgcn_s_memrealtime()-t0<20000) __builtin_amdgcn_s_sleep(64);
    const GAS u64* s=(const GAS u64*)((char*)ws+(200u<<20)); u64 tm=~0ull;
    for(int i=0;i<512;++i){ u64 v=__hip_atomic_load(s+i*16,__ATOMIC_RELAXED,__HIP_MEMORY_SCOPE_AGENT); if(v<tm) tm=v; }
    for(int r=0;r<6;++r){ int lo=(r>>1)==0?0:((r>>1)==1?128:192), hi=(r>>1)==0?128:((r>>1)==1?192:256);
      for(int c=0;c<16;++c){ u64 mx=0,sm=0; int n=0; for(int i=lo;i<hi;++i){ u64 v=__hip_atomic_load(s+(i*2+(r&1))*16+c,__ATOMIC_RELAXED,__HIP_MEMORY_SCOPE_AGENT); if(v>=tm&&v-tm<100000){ v-=tm; sm+=v; ++n; if(v>mx) mx=v; } }
        if(n) printf("role %d st %d avg %d max %d\n",r,c,(int)(sm/n),(int)mx); } } }
}

extern "C" void kernel_launch(void* const* d_in, const int* in_sizes, int n_in, void* d_out, int out_size, void* d_ws, size_t ws_size, hipStream_t stream){
  (void)hipFuncSetAttribute((const void*)fused,hipFuncAttributeMaxDynamicSharedMemorySize,163840);
  fused<<<256,512,163840,stream>>>((const u16*)d_in[0],(const u16*)d_in[1],(const u16*)d_in[2],(const u16*)d_in[3],(const u16*)d_in[4],(const u16*)d_in[5],(const u16*)d_in[6],
    (const u16*)d_in[7],(const u16*)d_in[8],(const u16*)d_in[9],(const u16*)d_in[10],(const u16*)d_in[11],(const u16*)d_in[12],(u16*)d_out,d_ws);
}
```

```cpp
#define MULT 0
#define QSEL (true)
#define QCOL 8
#define QMIN 0
#define STAMP
#include <hip/hip_runtime.h>
#include <stdint.h>

typedef unsigned short u16; typedef unsigned u32; typedef unsigned long long u64;
typedef __attribute__((ext_vector_type(4))) short s16x4;
typedef __attribute__((ext_vector_type(8))) _Float16 f16x8;
typedef __attribute__((ext_vector_type(8))) __bf16 bf16x8;
typedef __attribute__((ext_vector_type(2))) __bf16 bf16x2;
typedef __attribute__((ext_vector_type(2))) _Float16 f16x2;
typedef __attribute__((ext_vector_type(2))) float f32x2;
typedef __attribute__((ext_vector_type(4))) float f32x4;
typedef __attribute__((ext_vector_type(16))) float f32x16;
typedef __attribute__((ext_vector_type(2))) unsigned u32x2;
typedef __attribute__((ext_vector_type(3))) unsigned u32x3;
typedef __attribute__((ext_vector_type(4))) unsigned u32x4;
#define LAS __attribute__((address_space(3)))
#define GAS __attribute__((address_space(1)))
#define DI __device__ __forceinline__
typedef LAS char* lptr;
typedef __amdgpu_buffer_rsrc_t rsrc_t;

#define ST(i) do{ if((threadIdx.x&255)==0){ *(LAS u64*)(L+163328+(threadIdx.x>>8)*128+(i)*8)=__builtin_amdgcn_s_memrealtime(); } }while(0)
#define STDUMP() do{ if((threadIdx.x&255)<16){ asm volatile("s_waitcnt lgkmcnt(0)":::"memory"); __hip_atomic_store((GAS u64*)((char*)ws+(200u<<20))+(blockIdx.x*2+(threadIdx.x>>8))*16+(threadIdx.x&15),*(volatile LAS u64*)(L+163328+(threadIdx.x>>8)*128+(threadIdx.x&15)*8),__ATOMIC_RELAXED,__HIP_MEMORY_SCOPE_AGENT); } }while(0)
#define STINIT() do{ if((threadIdx.x&255)<16){ *(LAS u64*)(L+163328+(threadIdx.x>>8)*128+(threadIdx.x&15)*8)=0; } }while(0)

DI u32 pk_bf16(float a, float b){ f32x2 v={a,b}; return __builtin_bit_cast(u32,__builtin_convertvector(v,bf16x2)); }
DI u32 pk_f16(f32x2 v){ return __builtin_bit_cast(u32,__builtin_convertvector(v,f16x2)); }
DI float bflo(u32 x){ return __builtin_bit_cast(float,x<<16); }
DI float bfhi(u32 x){ return __builtin_bit_cast(float,x&0xffff0000u); }
DI float rbf(float a){ return bflo(pk_bf16(a,a)); }
template<class T> DI T lds_ld(lptr p){ return *(LAS T*)p; }
template<class T> DI void lds_st(lptr p, T v){ *(LAS T*)p=v; }
DI u32x2 lds_ld64(lptr p){ return *(volatile LAS u32x2*)p; }
DI u32x2 lds_tr(lptr p){ return __builtin_bit_cast(u32x2,__builtin_amdgcn_ds_read_tr16_b64_v4i16((LAS s16x4*)p)); }
DI f32x4 mfma16(u32x4 a,u32x4 b,f32x4 c){ return __builtin_amdgcn_mfma_f32_16x16x32_bf16(__builtin_bit_cast(bf16x8,a),__builtin_bit_cast(bf16x8,b),c,0,0,0); }
DI u32x4 cat(u32x2 a,u32x2 b){ u32x4 r={a.x,a.y,b.x,b.y}; return r; }
DI u32 p_ll(u32 a,u32 b){ return __builtin_amdgcn_perm(b,a,0x05040100u); }
DI u32 p_lh(u32 a,u32 b){ return __builtin_amdgcn_perm(b,a,0x07060100u); }
DI u32 p_hl(u32 a,u32 b){ return __builtin_amdgcn_perm(b,a,0x05040302u); }
DI u32 p_hh(u32 a,u32 b){ return __builtin_amdgcn_perm(b,a,0x07060302u); }
#define SB() __builtin_amdgcn_sched_barrier(0)
DI void rbar(){ asm volatile("":::"memory"); __builtin_amdgcn_s_barrier(); asm volatile("":::"memory"); }
DI void bar(){ asm volatile("s_waitcnt lgkmcnt(0)":::"memory"); __builtin_amdgcn_s_barrier(); asm volatile("":::"memory"); }
template<int N> DI void waitv(){ asm volatile("s_waitcnt vmcnt(%0)"::"i"(N):"memory"); }
constexpr f32x4 Z4={0.f,0.f,0.f,0.f};
DI float wave_max(float m){
  m=fmaxf(m,__builtin_bit_cast(float,__builtin_amdgcn_update_dpp(0,__builtin_bit_cast(int,m),0xB1,0xf,0xf,true)));
  m=fmaxf(m,__builtin_bit_cast(float,__builtin_amdgcn_update_dpp(0,__builtin_bit_cast(int,m),0x4E,0xf,0xf,true)));
  m=fmaxf(m,__builtin_bit_cast(float,__builtin_amdgcn_update_dpp(0,__builtin_bit_cast(int,m),0x141,0xf,0xf,true)));
  m=fmaxf(m,__builtin_bit_cast(float,__builtin_amdgcn_update_dpp(0,__builtin_bit_cast(int,m),0x140,0xf,0xf,true)));
  int x=__builtin_bit_cast(int,m);
  float a=__builtin_bit_cast(float,__builtin_amdgcn_readlane(x,0)), b=__builtin_bit_cast(float,__builtin_amdgcn_readlane(x,16));
  float c=__builtin_bit_cast(float,__builtin_amdgcn_readlane(x,32)), d=__builtin_bit_cast(float,__builtin_amdgcn_readlane(x,48));
  return fmaxf(fmaxf(a,b),fmaxf(c,d)); }

constexpr u32 WS_FLAG=256, WS_P1=4096, WS_Q=WS_P1+32*16*3584, WS_O=WS_Q+32*2048*40, WS_END=WS_O+128*20*1024;
constexpr u32 SENT=0xFFFFFFFFu, OOB=0x80000000u;
constexpr int SPIN_MAX=1<<17;
DI void dma16(const void* sb, u32 voff, lptr l){ u32 keep;
  asm volatile("s_mov_b32 %0, m0\n\ts_mov_b32 m0, %3\n\ts_nop 0\n\tglobal_load_lds_dwordx4 %1, %2\n\ts_mov_b32 m0, %0":"=&s"(keep):"v"(voff),"s"(sb),"s"((u32)(uintptr_t)l):"memory"); }
#define DMA4_BODY(SC) asm volatile("s_mov_b32 %0, m0\n\ts_mov_b32 m0, %6\n\ts_nop 0\n\tglobal_load_lds_dwordx4 %1, %5" SC "\n\tglobal_load_lds_dwordx4 %2, %5 offset:1024" SC \
    "\n\tglobal_load_lds_dwordx4 %3, %5 offset:2048" SC "\n\tglobal_load_lds_dwordx4 %4, %5 offset:3072" SC "\n\ts_mov_b32 m0, %0" \
    :"=&s"(keep):"v"(v0),"v"(v1),"v"(v2),"v"(v3),"s"(sb),"s"((u32)(uintptr_t)l):"memory")
DI void dma16x4(const void* sb,u32 v0,u32 v1,u32 v2,u32 v3,lptr l){ u32 keep; DMA4_BODY(""); }
DI void dma16x4_sc1(const void* sb,u32 v0,u32 v1,u32 v2,u32 v3,lptr l){ u32 keep; DMA4_BODY(" sc1"); }
DI u32x4 ld_sc1(rsrc_t rs,u32 off){ return __builtin_amdgcn_raw_buffer_load_b128(rs,off,0,16); }
DI void st_sc1(rsrc_t rs,u32 off,u32x4 v){ __builtin_amdgcn_raw_buffer_store_b128(v,rs,off,0,16); }
template<int NIT> DI void gather4(rsrc_t rs,u32 off,u32 last,u32x4* v){
  for(int it=0;it<NIT;++it){
    u32 todo=0;
    #pragma unroll
    for(int k=0;k<4;++k){ u32 m=0;
      #pragma unroll
      for(int i=0;i<4;++i) m=max(m,max(v[4*k+i].x,v[4*k+i].z));
      if(__any(m==SENT)) todo|=1u<<k; }
    if(!todo) break;
    asm volatile("":::"memory");
    #pragma unroll
    for(int k=0;k<4;++k) if(todo>>k&1){
      #pragma unroll
      for(int i=0;i<4;++i) v[4*k+i]=ld_sc1(rs,((i<3)?off+i*1024:last)+k*3584); } } }
DI u64 epoch(const void* ws){ u64 E; asm volatile("s_load_dwordx2 %0, %1, 0x0\n\ts_waitcnt lgkmcnt(0)":"=s"(E):"s"(ws):"memory"); return E+1; }
DI u64 epoch_late(const void* ws){ return *(const __attribute__((address_space(4))) u64*)ws+1; }
DI void flag_set(void* ws,int i,u64 E){ __hip_atomic_store((GAS u64*)((char*)ws+WS_FLAG)+i,E,__ATOMIC_RELAXED,__HIP_MEMORY_SCOPE_AGENT); }
DI u64 flag_get(const void* ws,int i){ return __hip_atomic_load((const GAS u64*)((const char*)ws+WS_FLAG)+i,__ATOMIC_RELAXED,__HIP_MEMORY_SCOPE_AGENT); }
DI void flag_wait(const void* ws,u64 E,int idx,u64 f){
  for(int it=0;it<SPIN_MAX;++it){ if(__all(f==E)) break; __builtin_amdgcn_s_sleep(2); f=flag_get(ws,idx); } }

constexpr int A_UB=4576, A_US=9120, A_K0=10*A_US, A_KB=4224, A_KS=8704;
constexpr int A_XF=A_K0+8*A_KS;
static_assert(A_XF+16<=163328,"lds");

DI f32x2 cmul(f32x2 a, f32x2 b){ f32x2 sw={-b.y,b.x}; return __builtin_elementwise_fma(a.yy,sw,a.xx*b); }
DI f32x2 cexp_e(float re2, float rev, float e){
  float m=__builtin_amdgcn_exp2f(re2*e); float a=__builtin_amdgcn_fractf(rev*e);
  f32x2 r={m*__builtin_amdgcn_cosf(a), m*__builtin_amdgcn_sinf(a)}; return r; }
struct SsmP { u32 are,aim,bre,bim,cre,cim,ld; float dt; };
struct SsmIn { const u16 *are,*aim,*bre,*bim,*cre,*cim,*logdt; };
DI SsmP load_ssm(const SsmIn& in, int ch, int lane){
  SsmP p; int o=ch*64+lane;
  p.are=((const u32*)in.are)[o]; p.aim=((const u32*)in.aim)[o]; p.bre=((const u32*)in.bre)[o];
  p.bim=((const u32*)in.bim)[o]; p.cre=((const u32*)in.cre)[o]; p.cim=((const u32*)in.cim)[o];
  p.ld=in.logdt[ch];
  return p; }
DI f32x2 ssm_w(const SsmP& p, int half){
  f32x2 b={half?bfhi(p.bre):bflo(p.bre), half?bfhi(p.bim):bflo(p.bim)};
  f32x2 c={half?bfhi(p.cre):bflo(p.cre), half?bfhi(p.cim):bflo(p.cim)};
  return cmul(c,b*p.dt); }
DI void ssm_prep(SsmP& p, float& s, float& inv){
  asm volatile("":"+v"(p.ld));
  p.dt=rbf(expf(bflo(p.ld)));
  f32x2 w0=ssm_w(p,0), w1=ssm_w(p,1);
  float m=wave_max(fmaxf(fmaxf(fabsf(w0.x),fabsf(w0.y)),fmaxf(fabsf(w1.x),fabsf(w1.y))));
  int e=__builtin_amdgcn_frexp_expf(m); e=max(-100,min(100,e));
  s=__builtin_amdgcn_ldexpf(1.0f,-e); inv=__builtin_amdgcn_ldexpf(1.0f,e); }
DI void kgen_half(const SsmP& p, int half, float s, lptr tab, int lane, f32x16& acc){
  const float are=(half?bfhi(p.are):bflo(p.are))*p.dt, aim=(half?bfhi(p.aim):bflo(p.aim))*p.dt;
  const float re2=are*1.4426950408889634f, rev=aim*0.15915494309189535f;
  lptr wr=tab+lane*4; lptr rd=tab+(lane&31)*272+(lane>>5)*16;
  { f32x2 e8=cexp_e(re2,rev,8.f), e16=cmul(e8,e8);
    f32x2 a={1.f,0.f}, b=e8;
    #pragma unroll
    for(int j=0;j<32;j+=2){ lds_st<u32>(wr+j*272,pk_f16(a)); lds_st<u32>(wr+(j+1)*272,pk_f16(b)); if(j<30){ a=(j==0)?e16:cmul(a,e16); b=cmul(b,e16);} } }
  u32x4 bf[8];
  #pragma unroll
  for(int k=0;k<8;++k) bf[k]=lds_ld<u32x4>(rd+k*32);
  { f32x2 w=ssm_w(p,half)*s; w.y=-w.y;
    f32x2 e1=cexp_e(re2,-rev,1.f), g=cexp_e(re2,-rev,256.f);
    f32x2 g2=cmul(g,g), g3=cmul(g2,g), g4=cmul(g2,g2);
    f32x2 x[8]; x[0]=w;
    #pragma unroll
    for(int i=1;i<8;++i) x[i]=cmul(x[i-1],e1);
    #pragma unroll
    for(int i=0;i<8;++i) lds_st<u32>(wr+i*272,pk_f16(x[i]));
    #pragma unroll
    for(int i=0;i<4;++i) lds_st<u32>(wr+(8+i)*272,pk_f16(cmul(x[2*i],g)));
    lds_st<u32>(wr+12*272,pk_f16(cmul(x[0],g2))); lds_st<u32>(wr+13*272,pk_f16(cmul(x[4],g2)));
    lds_st<u32>(wr+14*272,pk_f16(cmul(x[0],g3))); lds_st<u32>(wr+15*272,pk_f16(cmul(x[4],g3)));
    f32x2 y=cmul(w,g4);
    #pragma unroll
    for(int i=0;i<5;++i){ lds_st<u32>(wr+(16+i)*272,pk_f16(y)); if(i<4) y=cmul(y,g); } }
  #pragma unroll
  for(int k=0;k<8;++k){ u32x4 af=lds_ld<u32x4>(rd+k*32);
    acc=__builtin_amdgcn_mfma_f32_32x32x16_f16(__builtin_bit_cast(f16x8,af),__builtin_bit_cast(f16x8,bf[k]),acc,0,0,0); }
}
DI void k_finish(const float* v, lptr ka, int kbo, int lane){
  lptr ks=ka; lptr kb=ka+kbo;
  { const int j=lane&31, h=lane>>5;
    lds_st<u32x2>(ks+(8*j+4*h)*2,u32x2{pk_bf16(v[0],v[1]),pk_bf16(v[2],v[3])});
    u32x2 b={pk_bf16(v[4],v[5]),pk_bf16(v[6],v[7])};
    u32 c0=pk_bf16(v[8],v[9]), c1=pk_bf16(v[10],v[11]);
    if(h==0){ lds_st<u32x2>(ks+(256+4*j)*2,b);
      lds_st<u16>(ks+(512+j)*2,(u16)c0); lds_st<u16>(ks+(544+j)*2,(u16)(c0>>16));
      lds_st<u16>(ks+(576+j)*2,(u16)c1); lds_st<u16>(ks+(608+j)*2,(u16)(c1>>16));
    } else { lds_st<u32>(ks+(384+2*j)*2,b.x); lds_st<u32>(ks+(448+2*j)*2,b.y); if(j==0) lds_st<u16>(ks+640*2,(u16)c0); } }
  const int M=lane; const bool lo=M<32;
  u32x4 v0=lds_ld<u32x4>(ks+(M&31)*16); u32x2 v1=lds_ld<u32x2>(ks+(128+4*(M|32))*2);
  u32 v4=lds_ld<u16>(ks+(132+4*(M|32))*2);
  u32 pv=lds_ld<u16>(ks+(lo?max(8*M-1,0):((M==32)?255:(128+4*M)))*2); if(M==0) pv=0;
  u32 ab=lds_ld<u32>(ks+(384+2*M)*2), c2=lds_ld<u16>(ks+(386+2*M)*2);
  u32 a3[2],b3[2];
  #pragma unroll
  for(int i=0;i<2;++i){ a3[i]=lds_ld<u16>(ks+(512+M+64*i)*2); b3[i]=lds_ld<u16>(ks+(513+M+64*i)*2); }
  asm volatile("":"+v"(v0),"+v"(v1),"+v"(v4),"+v"(pv),"+v"(ab),"+v"(c2),"+v"(a3[0]),"+v"(a3[1]),"+v"(b3[0]),"+v"(b3[1])::"memory");
  { u32x4 A,B;
    if(lo){ B.x=p_hl(v0.w,v0.w); B.y=p_hl(v0.z,v0.z); B.z=p_hl(v0.y,v0.y); B.w=p_hl(v0.x,v0.x);
      A.x=p_lh(v0.w,v0.z); A.y=p_lh(v0.z,v0.y); A.z=p_lh(v0.y,v0.x); A.w=p_ll(v0.x,pv);
    } else { B.x=p_lh(v4,v1.y); B.y=p_ll(v1.y,v1.y); B.z=p_lh(v1.y,v1.x); B.w=p_ll(v1.x,v1.x);
      A.x=p_hl(v1.y,v1.y); A.y=B.y; A.z=p_hl(v1.x,v1.x); A.w=p_ll(v1.x,pv); }
    lds_st<u32x4>(ka+(255-M)*16,A); lds_st<u32x4>(kb+(255-M)*16,B); }
  { u32x4 A,B; B.x=p_ll(c2,c2); B.y=p_hh(ab,ab); B.z=p_hl(ab,ab); B.w=p_ll(ab,ab);
    A.x=p_lh(c2,ab); A.y=B.y; A.z=B.w; A.w=B.w;
    lds_st<u32x4>(ka+(191-M)*16,A); lds_st<u32x4>(kb+(191-M)*16,B); }
  #pragma unroll
  for(int i=0;i<2;++i){ u32 a=a3[i], b=b3[i], c=(M&1)?b:a; u32x4 A,B;
    B.x=p_ll(b,b); B.y=p_ll(b,c); B.z=p_ll(a,a); B.w=B.z;
    A.x=B.x; A.y=p_ll(c,a); A.z=B.z; A.w=B.z;
    lds_st<u32x4>(ka+(127-64*i-M)*16,A); lds_st<u32x4>(kb+(127-64*i-M)*16,B); }
  if(lane<8) lds_st<u32x4>(((lane&4)?kb:ka)+(256+(lane&3))*16,u32x4{0,0,0,0});
}
template<int NS> DI void conv_steps(lptr ua, lptr ka, int kbo, int s0, int lane, f32x4* acc){
  const int a=lane&15, g=lane>>4;
  lptr up=ua+((a&1)?0:A_UB)+(32*(a>>1)+8*g+32*s0)*2;
  lptr kp=ka+((a&1)?kbo:0)+(28-4*(a>>1)+8*g+32*s0)*2;
  u32x4 A[2][8],B[2][8];
  auto ldA=[&](int s,int q){ A[s&1][q]=cat(lds_ld64(kp+(1792-256*q+32*s)*2),lds_ld64(kp+(1792-256*q+32*s)*2+8)); };
  auto ldB=[&](int s,int q){ B[s&1][q]=lds_ld<u32x4>(up+(256*q+32*s)*2); };
  #pragma unroll
  for(int q=0;q<8;++q){ ldB(0,q); ldA(0,q); }
  #pragma unroll
  for(int c=0;c<8;++c) acc[c]=Z4;
  SB();
  #pragma unroll
  for(int s=0;s<NS;++s){
    #pragma unroll
    for(int q=0;q<8;++q){
      #pragma unroll
      for(int c=q;c<8;++c) acc[c]=mfma16(A[s&1][q],B[s&1][c-q],acc[c]);
      if(s+1<NS){ if(q==0){ ldB(s+1,0); ldB(s+1,1); ldB(s+1,2); ldB(s+1,3); } else if(q==1){ ldA(s+1,0); ldB(s+1,4); ldB(s+1,5); }
        else if(q==2){ ldB(s+1,6); ldB(s+1,7); ldA(s+1,1); } else if(q==3){ ldA(s+1,2); ldA(s+1,3); } else if(q==4){ ldA(s+1,4); ldA(s+1,5); }
        else if(q==5){ ldA(s+1,6); } else if(q==6){ ldA(s+1,7); } }
      SB(); } }
}
DI u32 fin2(float y0,float y1,u32 u,float dsk){
  u32 y=pk_bf16(y0,y1), s=pk_bf16(bflo(u)*dsk,bfhi(u)*dsk);
  return pk_bf16(bflo(y)+bflo(s),bfhi(y)+bfhi(s)); }
DI void role_a(int j, lptr L, const u16* u, const SsmIn& in, const u16* dskip, const u16* wo, u16* out, void* ws){
  const int tid=threadIdx.x, lane=tid&63, w=__builtin_amdgcn_readfirstlane(tid>>6), p=w&3;
  const int ch0=10*j;
  const rsrc_t ro=__builtin_amdgcn_make_buffer_rsrc(out,0,2048*2560,0x00020000);
  lptr k0=L+A_K0+p*A_KS, k1=k0+4*A_KS;
  constexpr int XKB=A_US+224;
  u32 dk[5];
  #pragma unroll
  for(int i=0;i<5;++i) dk[i]=((const u32*)(dskip+ch0))[i];
  SsmP p0=load_ssm(in,ch0+w,lane);
  SsmP px; if(w>=4) px=load_ssm(in,ch0+8+(p>>1),lane);
  for(int i=tid;i<570;i+=512){ int sl=i/57, c=i-sl*57; lds_st<u32x4>(L+sl*A_US+(c<28?c*16:A_UB+(c-28)*16),u32x4{0,0,0,0}); }
  if(tid==0) lds_st<u32x4>(L+A_XF,u32x4{0,0,0,0});
  bar();
  u32x4 um[4];
  const int ur=(tid+64*j)&511;
  { typedef u32x4 __attribute__((aligned(4))) u32x4u;
    const char* up0=(const char*)u+(size_t)ur*4*1280+20*j;
    #pragma unroll
    for(int r=0;r<4;++r) um[r]=*(const u32x4u*)(up0+r*1280); }
  const u64 E=epoch(ws);
  ST(1);
  float xv[12];
  if(w>=4){ __builtin_amdgcn_s_setprio(3); float s,inv; ssm_prep(px,s,inv); f32x16 acc={};
    kgen_half(px,p&1,s,k1,lane,acc);
    #pragma unroll
    for(int r=0;r<12;++r) xv[r]=acc[r]*inv; }
  float ks,kinv; ssm_prep(p0,ks,kinv); f32x16 kacc={};
  auto k_rest=[&](lptr kw){ kgen_half(p0,1,ks,kw,lane,kacc); float v[12];
    #pragma unroll
    for(int r=0;r<12;++r) v[r]=kacc[r]*kinv;
    k_finish(v,kw,A_KB,lane); };
  kgen_half(p0,0,ks,L+A_K0+w*A_KS,lane,kacc);
  if(w<4) k_rest(k0);
  ST(2);
  const int tl=32*((lane&15)>>1)+8*(lane>>4)+2*(lane&1);
  auto fin4=[&](u32* r,f32x4 v,int e,int c){
    lptr y=L+e*A_US+(224+256*c+tl)*2; const float d=(e&1)?bfhi(dk[e>>1]):bflo(dk[e>>1]);
    r[0]=fin2(v[0],v[1],lds_ld<u32>(y),d); r[1]=fin2(v[2],v[3],lds_ld<u32>(y+8),d); };
  auto scatter=[&](lptr d,u32 r0,u32 r1,u32 r2,u32 r3,bool last){
    u32x2 e={p_ll(r0,r1),p_ll(r2,r3)}, o={p_hh(r0,r1),p_hh(r2,r3)};
    lds_st<u32x2>(d,e); lds_st<u32x2>(d+A_US,o);
    lds_st<u32>(d+A_UB+4,e.x); lds_st<u32>(d+A_US+A_UB+4,o.x);
    if(!last){ lds_st<u32>(d+A_UB+8,e.y); lds_st<u32>(d+A_US+A_UB+8,o.y); } };
  #pragma unroll
  for(int c=0;c<4;++c) scatter(L+2*c*A_US+(224+4*ur)*2,um[0][c],um[1][c],um[2][c],um[3][c],ur==511);
  bar();
  ST(3);
  if(w<4){
    f32x4 acc[8];
    conv_steps<8>(L+p*A_US,k0,A_KB,0,lane,acc);
    #pragma unroll
    for(int c=0;c<8;++c) lds_st<f32x4>(k0+c*1024+lane*16,acc[c]);
    ST(4);
    bar();
    conv_steps<8>(L+(p+4)*A_US,k1,A_KB,0,lane,acc);
    #pragma unroll
    for(int c=0;c<8;++c) lds_st<f32x4>(k1+c*1024+lane*16,acc[c]);
    ST(5);
    bar();
    conv_steps<4>(L+(8+(p>>1))*A_US,L+(p&2)*A_US+A_UB,XKB,4*(p&1),lane,acc);
    #pragma unroll
    for(int c=0;c<8;++c) lds_st<f32x4>(k0+c*1024+lane*16,acc[c]);
    ST(6);
    bar();
  } else {
    if(tid==256) flag_set(ws,192+j,E);
    k_rest(k1);
    ST(9);
    u32 ux[8];
    { const char* up=(const char*)u+(size_t)(tid-256)*4*1280+20*j+16;
      #pragma unroll
      for(int r=0;r<8;++r) ux[r]=*(const u32*)(up+(size_t)((r&3)+(r>>2)*1024)*1280); }
    ST(4);
    bar();
    u32 pf[4];
    { const char* wp=(const char*)wo+(j&7)*102400+(tid-256)*128;
      #pragma unroll
      for(int i=0;i<4;++i) pf[i]=*(const u32*)(wp+((i<3||tid<288)?i*32768:0)); }
    { lptr xp=L+(p|1)*A_US+A_UB+lane*16; volatile LAS u32* xf=(volatile LAS u32*)(L+A_XF+2*(p&2));
      if(p&1){
        #pragma unroll
        for(int r=0;r<3;++r) *(volatile LAS f32x4*)(xp+r*1024)=f32x4{xv[4*r],xv[4*r+1],xv[4*r+2],xv[4*r+3]};
        *xf=1u;
      } else { f32x4 o[3];
        for(int it=0;it<SPIN_MAX;++it){ const u32 f=*xf;
          #pragma unroll
          for(int r=0;r<3;++r) o[r]=*(volatile LAS f32x4*)(xp+r*1024);
          if(__builtin_amdgcn_readfirstlane(f)) break; }
        #pragma unroll
        for(int r=0;r<12;++r) xv[r]+=o[r>>2][r&3];
        k_finish(xv,L+p*A_US+A_UB,XKB,lane); } }
    #pragma unroll
    for(int h=0;h<2;++h) scatter(L+8*A_US+(224+4*(tid-256)+1024*h)*2,ux[4*h],ux[4*h+1],ux[4*h+2],ux[4*h+3],h==1&&tid==511);
    ST(8);
    #pragma unroll
    for(int i=0;i<2;++i){ u32 r[4][2]; const int c=2*p+i;
      #pragma unroll
      for(int e=0;e<4;++e) fin4(r[e],lds_ld<f32x4>(L+A_K0+e*A_KS+c*1024+lane*16),e,c);
      const u32 o=(256*c+tl)*2560+20*j;
      #pragma unroll
      for(int h=0;h<2;++h){
        __builtin_amdgcn_raw_buffer_store_b64(u32x2{p_ll(r[0][h],r[1][h]),p_ll(r[2][h],r[3][h])},ro,o+4*h*2560,0,0);
        __builtin_amdgcn_raw_buffer_store_b64(u32x2{p_hh(r[0][h],r[1][h]),p_hh(r[2][h],r[3][h])},ro,o+(4*h+1)*2560,0,0); } }
    asm volatile(""::"v"(pf[0]),"v"(pf[1]),"v"(pf[2]),"v"(pf[3]));
    ST(5);
    bar();
    #pragma unroll
    for(int i=0;i<2;++i)
      #pragma unroll
      for(int e=0;e<4;++e){ u32 r[2]; lptr q=L+A_K0+(4+e)*A_KS+(p+4*i)*1024+lane*16; fin4(r,lds_ld<f32x4>(q),4+e,p+4*i); lds_st<u32x2>(q,u32x2{r[0],r[1]}); }
    ST(6);
    bar();
  }
  {
    u32 r[6][2];
    #pragma unroll
    for(int e=0;e<4;++e){ const u32x2 v=lds_ld<u32x2>(L+A_K0+(4+e)*A_KS+tid*16); r[e][0]=v.x; r[e][1]=v.y; }
    #pragma unroll
    for(int e=0;e<2;++e){ lptr q=L+A_K0+2*e*A_KS+tid*16; fin4(r[4+e],lds_ld<f32x4>(q)+lds_ld<f32x4>(q+A_KS),8+e,w); }
    const u32 o=(256*w+tl)*2560+20*j+8;
    #pragma unroll
    for(int h=0;h<2;++h){
      __builtin_amdgcn_raw_buffer_store_b96(u32x3{p_ll(r[0][h],r[1][h]),p_ll(r[2][h],r[3][h]),p_ll(r[4][h],r[5][h])},ro,o+4*h*2560,0,0);
      __builtin_amdgcn_raw_buffer_store_b96(u32x3{p_hh(r[0][h],r[1][h]),p_hh(r[2][h],r[3][h]),p_hh(r[4][h],r[5][h])},ro,o+(4*h+1)*2560,0,0); } }
  ST(7); STDUMP();
}

constexpr int B_ST=36864, B_PV=2*B_ST, B_PVS=18432, B_WP=4*B_ST, B_Z=B_WP+6400, B_F=163200;
DI float max3(float a,float b,float c){ return __builtin_fmaxf(__builtin_fmaxf(a,b),c); }
template<class H> DI void softmax20x2(const f32x4 (*z)[5], u32 (*o)[10], H hook){
  const float c=1.4426950408889634f; float mc[2];
  { float m0[2],m1[2],m2[2];
    #pragma unroll
    for(int t=0;t<2;++t){ m0[t]=max3(z[t][0][0],z[t][0][1],z[t][0][2]); m1[t]=max3(z[t][0][3],z[t][1][0],z[t][1][1]); m2[t]=max3(z[t][1][2],z[t][1][3],z[t][2][0]); }
    #pragma unroll
    for(int t=0;t<2;++t){ m0[t]=max3(m0[t],z[t][2][1],z[t][2][2]); m1[t]=max3(m1[t],z[t][2][3],z[t][3][0]); m2[t]=max3(m2[t],z[t][3][1],z[t][3][2]); }
    #pragma unroll
    for(int t=0;t<2;++t){ m0[t]=max3(m0[t],z[t][3][3],z[t][4][0]); m1[t]=max3(m1[t],z[t][4][1],z[t][4][2]); m2[t]=__builtin_fmaxf(m2[t],z[t][4][3]); }
    #pragma unroll
    for(int t=0;t<2;++t) mc[t]=max3(m0[t],m1[t],m2[t])*-c; }
  SB();
  hook(0);
  f32x2 e[2][10];
  #pragma unroll
  for(int i=0;i<10;++i)
    #pragma unroll
    for(int t=0;t<2;++t) e[t][i]=__builtin_elementwise_fma(f32x2{z[t][i>>1][(i&1)*2],z[t][i>>1][(i&1)*2+1]},f32x2{c,c},f32x2{mc[t],mc[t]});
  SB();
  hook(1);
  #pragma unroll
  for(int i=0;i<10;++i)
    #pragma unroll
    for(int t=0;t<2;++t) e[t][i]=f32x2{__builtin_amdgcn_exp2f(e[t][i].x),__builtin_amdgcn_exp2f(e[t][i].y)};
  SB();
  hook(2);
  float r[2];
  { f32x2 s[2][5];
    #pragma unroll
    for(int i=0;i<5;++i)
      #pragma unroll
      for(int t=0;t<2;++t) s[t][i]=e[t][2*i]+e[t][2*i+1];
    #pragma unroll
    for(int t=0;t<2;++t){ s[t][0]+=s[t][1]; s[t][2]+=s[t][3]; }
    #pragma unroll
    for(int t=0;t<2;++t){ s[t][0]+=s[t][4]; }
    #pragma unroll
    for(int t=0;t<2;++t){ s[t][0]+=s[t][2]; }
    #pragma unroll
    for(int t=0;t<2;++t) r[t]=__builtin_amdgcn_rcpf(s[t][0].x+s[t][0].y); }
  SB();
  #pragma unroll
  for(int i=0;i<10;++i)
    #pragma unroll
    for(int t=0;t<2;++t) e[t][i]*=r[t];
  SB();
  #pragma unroll
  for(int i=0;i<10;++i)
    #pragma unroll
    for(int t=0;t<2;++t) o[t][i]=pk_bf16(e[t][i].x,e[t][i].y);
  SB();
}
DI u32x4 pack2(f32x4 a,f32x4 b){ u32x4 r={pk_bf16(a[0],a[1]),pk_bf16(a[2],a[3]),pk_bf16(b[0],b[1]),pk_bf16(b[2],b[3])}; return r; }
DI void role_b(const bool ISQ,int X,int li,lptr L,u32 warm,const u16* x,const u16* wqkv,const u16* wphi,const u16* wo,u16* out,void* ws){
  const int tid=threadIdx.x, lane=tid&63, w=__builtin_amdgcn_readfirstlane(tid>>6), g=lane>>4;
  const int rt=4*(X>>1)+(ISQ?(li>>1):(li>>2));
  const int grp=ISQ?(2*(X&1)+(li&1)):(4*(X&1)+(li&3));
  const int T=ISQ?16+li:li; const bool doout=T<20; const int tt64=4*X+T/5, ct=T%5;
  const rsrc_t rs=__builtin_amdgcn_make_buffer_rsrc(ws,0,WS_END,0x00020000);
  constexpr int OIM=B_PV;
  u64 E=epoch_late(ws);
  auto arrive=[&](){
    u32 n=0; if(lane==0) n=__hip_atomic_fetch_add((LAS u32*)(L+B_F+16),1u,__ATOMIC_RELAXED,__HIP_MEMORY_SCOPE_WORKGROUP);
    if(__builtin_amdgcn_readfirstlane(n)==3u&&lane==0) flag_set(ws,ISQ?(128+4*rt+grp):(16*grp+rt),E); };
  volatile LAS u32* bad=(volatile LAS u32*)(L+B_F+20);
  if(__builtin_expect(w>=4,1)){
    const int hw=w-4;
    const int s0=(0x8530>>((ISQ?((li>>1)+2*(li&1)):((li>>2)+(li&3)))&3)*4&15)+(ISQ?1:0);
    u32 xo[4], wv[5];
    #pragma unroll
    for(int i=0;i<4;++i){ int r=(4*hw+i)*8+(lane>>3); xo[i]=(u32)(rt*128+r)*1280u+(u32)(((lane&7)^((r>>1)&7))<<4)-1024u*i; }
    const u32 wlo=ISQ?320*grp:1280+160*grp, whi=ISQ?320*grp:2400+160*grp;
    #pragma unroll
    for(int i=0;i<5;++i){ u32 c=(5*hw+i)*64+lane, r=(c*3277u)>>16, pos=c-r*20, sc=pos^(((r>>3)&1)<<1);
      wv[i]=r*3840u+(sc<10?wlo:whi)+16*sc-1024u*(i&3); }
    auto issue=[&](int s){ int ss=s+s0; if(ss>=10) ss-=10; lptr d=L+(s&3)*B_ST;
      const char* xb=(const char*)x+ss*128; const char* wb=(const char*)wqkv+(size_t)ss*64*3840;
      dma16x4(wb,wv[0],wv[1],wv[2],wv[3],d+16384+5*hw*1024); dma16x4(xb,xo[0],xo[1],xo[2],xo[3],d+4*hw*1024); dma16(wb,wv[4],d+16384+(5*hw+4)*1024); };
    ST(9);
    issue(0); issue(1);
    asm volatile("":"+s"(E));
    ST(1); waitv<9>(); rbar(); issue(2);
    ST(2);
    waitv<9>(); rbar();
    #pragma unroll 1
    for(int s=2;s<9;++s){
      issue(s+1);
      waitv<9>(); rbar(); }
    waitv<0>(); rbar();
    if(!doout){ STDUMP(); return; }
    if(!ISQ){
      const u32x4 sv={SENT,SENT,SENT,SENT};
      #pragma unroll
      for(int i=0;i<4;++i) st_sc1(rs,(i<3||lane<18)?(WS_P1+((4*grp+hw)*16+rt)*3584+i*1024+lane*16):OOB,sv);
      #pragma unroll
      for(int i=0;i<5;++i) st_sc1(rs,WS_O+((8*rt+grp)*20+5*hw+i)*1024+lane*16,sv);
      waitv<0>(); arrive();
      ST(12); }
    auto go=[&](u32 n){ for(int it=0;it<SPIN_MAX;++it){ if(__builtin_amdgcn_readfirstlane(*(volatile LAS u32*)(L+B_F+24))>=n) break; __builtin_amdgcn_s_sleep(2); } asm volatile("":::"memory"); };
    u32x4 wf[2][20];
    {
      const int pr=hw>>1, me2=hw&1, r=lane>>3;
      const char* gb=(const char*)wo+ct*256+pr*128+me2*8*1280;
      const u32 sw=((lane&7)^((((r>>1)&1)|(me2<<1))<<1))<<4, vo=r*1280+sw, vo2=((r>>2)*20+(r&3)+16+32*me2)*1280+sw;
      auto V=[&](int n,int k){ const int G=n/10, m=n%10; return ((m<8)?vo+(160*G+20*m)*1280:vo2+(160*G+80*(m-8))*1280)-1024*k; };
      auto four=[&](int n,lptr d){ dma16x4(gb,V(n,0),V(n+1,1),V(n+2,2),V(n+3,3),d); };
      const int q=(lane&15)>>2, p=lane&3, f=((q>>1)|((g&1)<<1))<<1;
      lptr rb=L+(pr*2+(g&1))*18432+(g>>1)*1024+q*128+8*(p&1);
      lptr rd=rb+(((4*me2+(p>>1))^f)<<4), rd1=rb+(((4*me2+2+(p>>1))^f)<<4);
      constexpr int TAIL=B_PV+12288;
      lptr dst=L+hw*18432;
      volatile LAS u32* fm=(volatile LAS u32*)(L+B_F+48+4*hw); volatile LAS u32* fo=(volatile LAS u32*)(L+B_F+48+4*(hw^1));
      auto meet=[&](u32 n){ *fm=n; for(int it=0;it<SPIN_MAX;++it){ if(__builtin_amdgcn_readfirstlane(*fo)>=n) break; __builtin_amdgcn_s_sleep(1); } asm volatile("":::"memory"); };
      go(4u); ST(3);
      four(0,dst); four(4,dst+4096); four(8,dst+8192); four(12,dst+12288); dma16(gb,V(16,0),dst+16384); dma16(gb,V(17,0),dst+17408);
      four(18,dst+TAIL); dma16(gb,V(22,0),dst+TAIL+4096); dma16(gb,V(23,0),dst+TAIL+5120);
      ST(10); waitv<0>(); ST(11); meet(1);
      #pragma unroll
      for(int s=0;s<12;++s){ const int o=(s<9)?s*2048:TAIL+(s-9)*2048;
        wf[0][s]=cat(lds_tr(rd+o),lds_tr(rd+o+512)); wf[1][s]=cat(lds_tr(rd1+o),lds_tr(rd1+o+512)); }
      asm volatile("s_waitcnt lgkmcnt(0)":::"memory");
      if(lane==0) __hip_atomic_fetch_add((LAS u32*)(L+B_F+28),1u,__ATOMIC_RELAXED,__HIP_MEMORY_SCOPE_WORKGROUP);
      meet(2);
      go(ISQ?4u:8u);
      four(24,dst); four(28,dst+4096); four(32,dst+8192); four(36,dst+12288);
      ST(13); waitv<0>(); ST(14); meet(3);
      #pragma unroll
      for(int s=0;s<8;++s){ wf[0][12+s]=cat(lds_tr(rd+s*2048),lds_tr(rd+s*2048+512)); wf[1][12+s]=cat(lds_tr(rd1+s*2048),lds_tr(rd1+s*2048+512)); }
      asm volatile("s_waitcnt lgkmcnt(0)":::"memory"); }
    ST(4);
    for(u32 it=1;it<SPIN_MAX;++it){
      rbar();
      ST(5);
      f32x4 c[2][4];
      #pragma unroll
      for(int t=0;t<4;++t){ c[0][t]=Z4; c[1][t]=Z4; }
      u32x4 ob[2][4]; u32 chk=0;
      lptr ob0=L+OIM+lane*16;
      #pragma unroll
      for(int t=0;t<4;++t) ob[0][t]=lds_ld<u32x4>(ob0+((t+hw)&3)*20480);
      lptr obt[4];
      #pragma unroll
      for(int t=0;t<4;++t) obt[t]=ob0+((t+hw)&3)*20480;
      SB();
      #pragma unroll
      for(int ks=0;ks<20;++ks){
        #pragma unroll
        for(int t=0;t<4;++t){ if(ks<19) ob[(ks+1)&1][t]=lds_ld<u32x4>(obt[t]+(ks+1)*1024);
          c[0][t]=mfma16(wf[0][ks],ob[ks&1][t],c[0][t]); c[1][t]=mfma16(wf[1][ks],ob[ks&1][t],c[1][t]); }
        chk=max(chk,max(ob[ks&1][0].x,ob[ks&1][0].z));
        SB(); }
      if(__any(chk==SENT)) *bad=it;
      #pragma unroll
      for(int t=0;t<4;++t)
        #pragma unroll
        for(int cc=0;cc<2;++cc) lds_st<u32x2>(L+(16*((t+hw)&3)+(lane&15))*272+hw*64+cc*32+8*g,u32x2{pk_bf16(c[cc][t][0],c[cc][t][1]),pk_bf16(c[cc][t][2],c[cc][t][3])});
      ST(6);
      bar();
      if(__builtin_amdgcn_readfirstlane(*bad)!=it) break; }
  } else {
    const int th=w>>1, hh=w&1;
    { const int NB=ISQ?3200:1600; const char* wp=(const char*)wphi+(ISQ?8*grp:4*grp)*800+hh*NB;
      #pragma unroll
      for(int i=0;i<4;++i) if(i*1024+lane*16<NB) dma16(wp,i*1024+lane*16,L+B_WP+hh*NB+i*1024); }
    if(lane<4) lds_st<u32x4>(L+B_Z+lane*16,u32x4{0,0,0,0});
    if(tid<4) lds_st<u32x4>(L+B_F+tid*16,u32x4{0,0,0,0});
    asm volatile("":"+s"(E));
    asm volatile("s_waitcnt vmcnt(0)"::"v"(warm):"memory");
    ST(9);
    lptr fad[3][2];
    { const int q=(lane&15)>>2, p=lane&3, hd=ISQ?p:(p&1);
      #pragma unroll
      for(int s=0;s<3;++s)
        #pragma unroll
        for(int t=0;t<2;++t){ const int d=16*(2*s+t)+4*g-20*hd; const bool ok=(d>=0)&&(d<20)&&(2*s+t<5);
          fad[s][t]=ok?(L+B_WP+((ISQ?4*hh:2*hh)+hd)*800+(d+q)*40+(ISQ?0:(p>>1)*3200)):(L+B_Z); if(2*s+t<5) asm volatile("":"+v"(fad[s][t])); } }
    lptr pv=L+B_PV+w*B_PVS;
    const int oh=4*grp+(rt&3), tq=rt>>2;
    const int ot=4*tt64+w;
    int fi=(lane<16)?(16*grp+lane):((lane<32)?(16*(lane&7)+4*tq+w):((lane<48)?(16*(ot&7)+(ot>>3)):(128+4*(4*tq+w)+(oh>>3))));
    lptr pp=pv+(lane&15)*160+(g&1)*80+(g>>1)*40, vp2, vp3, vp4, pa, vb;
    { lptr vp=pv+10272+(lane&15)*96; vp2=vp+((g<2)?(48*g+40):(8*g-16)); vp3=vp+((g==3)?48:(16+8*g)); vp4=vp+56+8*g;
      const int tk=16*(g>>1)+4*(g&1)+((lane&15)>>2); pa=pv+tk*160+8*(lane&3)+(th^1)*80; vb=pv+10272+tk*96+8*(lane&3)+(th^1)*48; }
    u32 qo=WS_Q+(oh*2048+512*tq+128*w+16*(g>>1)+(lane&15))*40, po0, po1;
    { const u32 po=WS_P1+((4*grp+2*hh+th)*16+rt)*3584; po0=po+lane*16; po1=((lane&15)<5)?(po+(160+5*g+(lane&15))*16):OOB; }
    asm volatile("":"+v"(fi),"+v"(pp),"+v"(vp2),"+v"(vp3),"+v"(vp4),"+v"(pa),"+v"(vb),"+v"(qo),"+v"(po0),"+v"(po1));
    if(!ISQ){
      #pragma unroll
      for(int i=0;i<2;++i) if(i*1024+lane*16<1600){ lptr q=L+B_WP+hh*1600+i*1024+lane*16; lds_st<u32x4>(q+3200,lds_ld<u32x4>(q)^0x80008000u); } }
    lptr ab[5], bb[2];
    { const int q=(lane&15)>>2,p=lane&3;
      #pragma unroll
      for(int i=0;i<5;++i){ int n=2*i+(p>>1); int cid=ISQ?(10*hh+n):(5*hh+n+(n>=5?5:0)); ab[i]=L+16384+(8*g+q)*320+((cid^((g&1)<<1))<<4)+8*(p&1); }
      const int b0=(64*th+(lane&15))*128+((g^((lane&15)>>1))<<4); bb[0]=L+b0; bb[1]=L+(b0^64); }
    f32x4 acc[5][4];
    #pragma unroll
    for(int i=0;i<5;++i)
      #pragma unroll
      for(int t=0;t<4;++t) acc[i][t]=Z4;
    ST(1);
    u32x4 FA[2][5], FB[2][4], fa[2][5];
    auto half=[&](int hf,bool mm,bool last){
      #pragma unroll
      for(int i=0;i<5;++i){
        if(i<2){ FB[hf][2*i]=lds_ld<u32x4>(bb[hf]+4096*i); FB[hf][2*i+1]=lds_ld<u32x4>(bb[hf]+4096*i+2048); }
        FA[hf][i]=cat(lds_tr(ab[i]+hf*10240),lds_tr(ab[i]+hf*10240+1280));
        if(mm){
          #pragma unroll
          for(int t=0;t<4;++t) acc[i][t]=mfma16(FA[hf^1][i],FB[hf^1][t],acc[i][t]); }
        if(last){ fa[0][i]=cat(lds_tr(fad[0][0]+8*i),lds_tr(fad[0][1]+8*i)); fa[1][i]=cat(lds_tr(fad[1][0]+8*i),lds_tr(fad[1][1]+8*i)); }
        SB(); } };
    bar();
    ST(2);
    SB();
    half(0,false,false);
    #pragma unroll 1
    for(int s=1;s<10;++s){
      half(1,true,false);
      const int step=(s&3)?B_ST:-3*B_ST;
      #pragma unroll
      for(int i=0;i<5;++i) ab[i]+=step;
      bb[0]+=step; bb[1]+=step;
      rbar();
      SB();
      half(0,true,false); }
    half(1,true,true);
    auto signal=[&](){ asm volatile("":::"memory"); if(lane==0) __hip_atomic_fetch_add((LAS u32*)(L+B_F+24),1u,__ATOMIC_RELAXED,__HIP_MEMORY_SCOPE_WORKGROUP); };
    #pragma unroll
    for(int t=0;t<4;++t)
      #pragma unroll
      for(int i=0;i<5;++i) acc[i][t]=mfma16(FA[1][i],FB[1][t],acc[i][t]);
    u32x2 pk[5][4];
    #pragma unroll
    for(int t=0;t<4;++t)
      #pragma unroll
      for(int i=0;i<5;++i) pk[i][t]=u32x2{pk_bf16(acc[i][t][0],acc[i][t][1]),pk_bf16(acc[i][t][2],acc[i][t][3])};
    ST(3);
    if(ISQ){
      u32x4 f2[5];
      #pragma unroll
      for(int i=0;i<5;++i) f2[i]=cat(lds_tr(fad[2][0]+8*i),u32x2{0u,0u});
      lptr sp_=pv+g*2560+(lane&15)*40;
      f32x4 z[4][5];
      #pragma unroll
      for(int t=0;t<4;++t)
        #pragma unroll
        for(int i=0;i<5;++i) z[t][i]=mfma16(fa[0][i],cat(pk[0][t],pk[1][t]),Z4);
      #pragma unroll
      for(int t=0;t<4;++t)
        #pragma unroll
        for(int i=0;i<5;++i) z[t][i]=mfma16(fa[1][i],cat(pk[2][t],pk[3][t]),z[t][i]);
      SB();
      #pragma unroll
      for(int t=0;t<5;++t){
        #pragma unroll
        for(int i=0;i<5;++i){ if(t<4) z[t][i]=mfma16(f2[i],cat(pk[4][t],u32x2{0u,0u}),z[t][i]);
          if(t>0) lds_st<u32x2>(sp_+(t-1)*640+8*i,u32x2{pk_bf16(z[t-1][i][0],z[t-1][i][1]),pk_bf16(z[t-1][i][2],z[t-1][i][3])}); }
        SB(); }
      ST(10);
      #pragma unroll
      for(int i=0;i<10;++i){ const int c=i*64+lane, hd=(c*205)>>15;
        st_sc1(rs,WS_Q+((8*grp+4*hh+hd)*2048+128*rt+64*th)*40+(c-160*hd)*16,lds_ld<u32x4>(pv+c*16)); }
      ST(11);
      waitv<0>(); ST(12); arrive(); signal();
      ST(4);
      if(doout){ fi=16*(ot&7)+(ot>>3); flag_wait(ws,E,fi,flag_get(ws,fi)); }
    } else {
      { f32x4 z[4][5]; u32 ph[4][10];
        auto fm=[&](int t){
          const u32x4 b0=cat(pk[0][t],pk[1][t]), b1=cat(pk[2][t],pk[3][t]);
          #pragma unroll
          for(int i=0;i<5;++i) z[t][i]=mfma16(fa[0][i],b0,Z4);
          #pragma unroll
          for(int i=0;i<5;++i) z[t][i]=mfma16(fa[1][i],b1,z[t][i]); };
        fm(0); fm(1);
        SB();
        softmax20x2(z,ph,[&](int k){ if(k==0){ fm(2); } else if(k==1){ fm(3); } });
        auto wp=[&](int t){
          #pragma unroll
          for(int i=0;i<5;++i) lds_st<u32x2>(pp+t*2560+8*i,u32x2{ph[t][2*i],ph[t][2*i+1]}); };
        softmax20x2(z+2,ph+2,[&](int k){
          if(k==0){ wp(0); wp(1); }
          else if(k==1){
            #pragma unroll
            for(int t=0;t<4;++t){ u32x2 v=pk[2][t]; if(g<2) v=u32x2{0x3F80u,0u};
              lds_st<u32x2>(vp2+t*1536,v); lds_st<u32x2>(vp3+t*1536,pk[3][t]); lds_st<u32x2>(vp4+t*1536,pk[4][t]); } } });
        wp(2); wp(3); }
      ST(4);
      u64 fl;
      lptr q00,q01,q10,q11; u32 om,ox;
      { const int r=lane&15, pc=5*(g&1)+2*(g>>1), pl=5*(g&1)+4, d1=min(r,4); lptr b=L+B_PV+8192;
        q00=b+(16*pc+r)*16; q01=b+(16*pl+r)*16+8*(g>>1); q10=b+(160+5*pc+d1)*16; q11=b+(160+5*pl+d1)*16+8*(g>>1);
        const u32 fb=WS_O+((32*tq+8*w)*20+5*(oh>>3))*1024+r*16;
        om=fb+((oh&7)>>1)*1024+(2*(oh&1)+(g>>1))*256+8*(g&1); ox=(g==0)?(fb+4096+((oh&7)>>1)*256+8*(oh&1)):OOB; }
      u32x4 l0[4],l1[4]; u32x2 l2[4];
      auto ldq=[&](){
        #pragma unroll
        for(int i=0;i<4;++i){ l0[i]=ld_sc1(rs,qo+i*1280); l1[i]=ld_sc1(rs,qo+i*1280+16); l2[i]=__builtin_amdgcn_raw_buffer_load_b64(rs,qo+i*1280+32,0,16); } };
      { f32x4 c[2][6];
        #pragma unroll
        for(int h2=0;h2<2;++h2){
          if(h2){ pa+=th?80:-80; vb+=th?48:-48; }
          u32x4 a[2][3], b[2][2];
          #pragma unroll
          for(int ks=0;ks<2;++ks){
            #pragma unroll
            for(int i=0;i<3;++i) a[ks][i]=cat(lds_tr(pa+ks*32*160+32*i),lds_tr(pa+(ks*32+8)*160+32*i));
            #pragma unroll
            for(int dt=0;dt<2;++dt) b[ks][dt]=cat(lds_tr(vb+ks*32*96+32*dt),lds_tr(vb+(ks*32+8)*96+32*dt)); }
          #pragma unroll
          for(int i=0;i<6;++i) c[h2][i]=mfma16(a[0][i%3],b[0][i/3],Z4);
          #pragma unroll
          for(int i=0;i<6;++i) c[h2][i]=mfma16(a[1][i%3],b[1][i/3],c[h2][i]); }
        #pragma unroll
        for(int i=0;i<6;++i) *(volatile LAS f32x4*)(pv+i*1024+lane*16)=c[0][i];
        *(volatile LAS u32*)(L+B_F+4*w)=1u;
        #pragma unroll
        for(int i=0;i<6;++i) asm volatile("":"+v"(c[1][i]));
        lptr ox_=L+B_PV+(w^2)*B_PVS; f32x4 pr[6];
        for(int it=0;it<SPIN_MAX;++it){
          const u32 f=*(volatile LAS u32*)(L+B_F+4*(w^2)), n=*(volatile LAS u32*)(L+B_F+16);
          #pragma unroll
          for(int i=0;i<6;++i) pr[i]=*(volatile LAS f32x4*)(ox_+i*1024+lane*16);
          if(__builtin_amdgcn_readfirstlane(f+n)==5u) break; }
        fl=flag_get(ws,fi);
        #pragma unroll
        for(int i=0;i<6;++i){ u32 o=(i<3)?(po0+i*1024):(po1+(i-3)*320); if(i%3==2) o=(g<2)?o:OOB;
          st_sc1(rs,o,__builtin_bit_cast(u32x4,c[1][i]+pr[i])); } }
      ST(5);
      if(!__all(fl==E)) flag_wait(ws,E,fi,fl);
      asm volatile("":::"memory"); ldq();
      u32 pq[4][10];
      {
        const u32 sg=(g&1)?0x80008000u:0u;
        u32x4 v[16]; const u32 go0=WS_P1+(oh*16+4*w)*3584+lane*16, go1=(lane<18)?go0+3072:OOB;
        #pragma unroll
        for(int i2=0;i2<4;i2+=2){ f32x4 z[2][5];
          #pragma unroll
          for(int t=0;t<2;++t){ const u32x4 a=l0[i2+t]^sg, b=l1[i2+t]^sg; const u32x2 c=l2[i2+t]^sg;
            z[t][0]=f32x4{bflo(a.x),bfhi(a.x),bflo(a.y),bfhi(a.y)}; z[t][1]=f32x4{bflo(a.z),bfhi(a.z),bflo(a.w),bfhi(a.w)};
            z[t][2]=f32x4{bflo(b.x),bfhi(b.x),bflo(b.y),bfhi(b.y)}; z[t][3]=f32x4{bflo(b.z),bfhi(b.z),bflo(b.w),bfhi(b.w)};
            z[t][4]=f32x4{bflo(c.x),bfhi(c.x),bflo(c.y),bfhi(c.y)}; }
          if(i2==0){
            #pragma unroll
            for(int k=0;k<16;++k) v[k]=ld_sc1(rs,((k&3)<3?go0+(k&3)*1024:go1)+(k>>2)*3584);
            signal(); }
          SB();
          softmax20x2(z,pq+i2,[](int){});
          #pragma unroll
          for(int i=i2;i<i2+2;++i){
            #pragma unroll
            for(int k=0;k<5;++k){ const int a=(k<4)?k:8, b=(k<4)?k+4:9;
              auto r=__builtin_amdgcn_permlane32_swap(pq[i][a],pq[i][b],false,false); pq[i][a]=r[0]; pq[i][b]=r[1]; }
            #pragma unroll
            for(int k=0;k<10;++k) asm volatile("":"+v"(pq[i][k])); }
          if(i2==0){ gather4<1>(rs,go0,go1,v); SB(); } }
        ST(10);
        gather4<SPIN_MAX>(rs,go0,go1,v);
        ST(13);
        #pragma unroll
        for(int i=0;i<4;++i) *(volatile LAS f32x4*)(pv+8192+i*1024+lane*16)=(__builtin_bit_cast(f32x4,v[i])+__builtin_bit_cast(f32x4,v[4+i]))+(__builtin_bit_cast(f32x4,v[8+i])+__builtin_bit_cast(f32x4,v[12+i]));
        *(volatile LAS u32*)(L+B_F+32+4*w)=1u;
        SB();
      }
      ST(12);
      u32x4 a0[2]; u32 a1[2];
      {
        f32x4 s[2][2]; f32x2 u[2];
        for(int it=0;it<SPIN_MAX;++it){
          const u32x4 f=*(volatile LAS u32x4*)(L+B_F+32);
          #pragma unroll
          for(int k=0;k<4;++k){ const int q=k*B_PVS;
            const f32x4 x0=*(volatile LAS f32x4*)(q00+q), x1=*(volatile LAS f32x4*)(q00+q+256), y0=*(volatile LAS f32x4*)(q10+q), y1=*(volatile LAS f32x4*)(q10+q+80);
            const f32x2 u0=*(volatile LAS f32x2*)(q01+q), u1=*(volatile LAS f32x2*)(q11+q);
            if(k==0){ s[0][0]=x0; s[0][1]=x1; s[1][0]=y0; s[1][1]=y1; u[0]=u0; u[1]=u1; }
            else { s[0][0]+=x0; s[0][1]+=x1; s[1][0]+=y0; s[1][1]+=y1; u[0]+=u0; u[1]+=u1; } }
          if(__builtin_amdgcn_readfirstlane(f.x+f.y+f.z+f.w)==4u) break; }
        #pragma unroll
        for(int i=0;i<2;++i){ a0[i]=pack2(s[i][0],s[i][1]); a1[i]=pk_bf16(u[i].x,u[i].y); } }
      ST(14);
      { f32x4 n[8][2];
        #pragma unroll
        for(int t=0;t<8;++t){ const u32* f=pq[t>>1]+4*(t&1);
          #pragma unroll
          for(int i=0;i<2;++i) n[t][i]=mfma16(a0[i],u32x4{f[0],f[1],f[2],f[3]},Z4); }
        #pragma unroll
        for(int t=0;t<8;++t)
          #pragma unroll
          for(int i=0;i<2;++i) n[t][i]=mfma16(u32x4{a1[i],0u,0u,0u},u32x4{pq[t>>1][8+(t&1)],0u,0u,0u},n[t][i]);
        SB();
        float den[8];
        #pragma unroll
        for(int t=0;t<8;++t) den[t]=__builtin_bit_cast(float,__builtin_amdgcn_ds_bpermute(((lane&15)+16)*4,__builtin_bit_cast(int,n[t][1][0])));
        SB();
        #pragma unroll
        for(int t=0;t<8;++t){ const float r=__builtin_amdgcn_rcpf(rbf(rbf(den[t])+1e-6f));
          const f32x4 m=n[t][0]*r, e=n[t][1]*r;
          __builtin_amdgcn_raw_buffer_store_b64(u32x2{pk_bf16(m[0],m[1]),pk_bf16(m[2],m[3])},rs,om+t*20480,0,16);
          __builtin_amdgcn_raw_buffer_store_b64(u32x2{pk_bf16(e[0],e[1]),pk_bf16(e[2],e[3])},rs,ox+t*20480,0,16); } }
      signal();
      if(rt==15&&grp==7&&w==3){
        for(int it=0;it<SPIN_MAX;++it){ if(__all(flag_get(ws,lane)==E&&flag_get(ws,64+lane)==E&&flag_get(ws,128+lane)==E&&flag_get(ws,192+lane)==E)) break; __builtin_amdgcn_s_sleep(2); }
        if(lane==0) __hip_atomic_store((GAS u64*)ws,E,__ATOMIC_RELAXED,__HIP_MEMORY_SCOPE_AGENT); }
      ST(6);
    }
    if(!doout){ STDUMP(); return; }
    for(int it=0;it<SPIN_MAX;++it){ const u32x2 c=*(volatile LAS u32x2*)(L+B_F+24); if(__builtin_amdgcn_readfirstlane(c.x+c.y)>=(ISQ?8u:12u)) break; __builtin_amdgcn_s_sleep(1); }
    { const u32 ob=WS_O+ot*20480;
      const u32 sm=ob+((lane<32)?((5*(lane>>3)+((lane>>1)&3))*1024+(lane&1)*512+508):((5*((lane-32)>>3)+4)*1024+((lane&7)>>1)*256+244+8*(lane&1)));
      for(int it=0;it<SPIN_MAX;++it){ asm volatile("":::"memory");
        if(__all(__builtin_amdgcn_raw_buffer_load_b32(rs,sm,0,16)!=SENT)) break;
        __builtin_amdgcn_s_sleep(1); }
      ST(15);
      const u32 vo=lane*16;
      for(u32 it=1;it<SPIN_MAX;++it){
        #pragma unroll
        for(int i=0;i<5;++i) dma16x4_sc1((const char*)ws+ob+i*4096,vo,vo,vo,vo,L+OIM+w*20480+i*4096);
        waitv<0>();
        ST(7);
        rbar();
        rbar();
        if(__builtin_amdgcn_readfirstlane(*bad)!=it) break; } }
  }
  #pragma unroll
  for(int i=0;i<2;++i){ const int r=32*i+(tid>>4);
    *(u32x4*)((char*)out+(size_t)(64*tt64+r)*2560+1280+ct*256+(tid&15)*16)=lds_ld<u32x4>(L+r*272+(tid&15)*16); }
  ST(8); STDUMP();
}

constexpr int CODE_LINES=440;
__global__ __launch_bounds__(512,2) void fused(const u16* u,const u16* are,const u16* aim,const u16* bre,const u16* bim,const u16* cre,const u16* cim,
    const u16* logdt,const u16* dskip,const u16* x,const u16* wqkv,const u16* wphi,const u16* wo,u16* out,void* ws){
  extern __shared__ __attribute__((aligned(16))) char smem[];
  lptr L=(lptr)smem;
  const int b=blockIdx.x, X=b&7, idx=b>>3;
  u64 pc; asm volatile("s_getpc_b64 %0":"=s"(pc));
  STINIT(); ST(0);
  const int w=__builtin_amdgcn_readfirstlane(threadIdx.x>>6);
  const bool isa=(idx>=16&&idx<24), isq=idx>=24;
  if(__builtin_expect(!isa,1)){
    u32 warm=0;
    if(idx<16&&threadIdx.x<32&&idx*32+threadIdx.x<CODE_LINES) warm=*(const u32*)((const char*)(pc&~255ull)+(idx*32+threadIdx.x)*128);
    if(idx<16&&w==1&&(threadIdx.x&63)<50) warm=*(const u32*)((const char*)wo+((X*16+idx)*50+(threadIdx.x&63))*128);
    role_b(isq,X,isq?idx-24:idx,L,warm,x,wqkv,wphi,wo,out,ws);
  } else { const SsmIn in={are,aim,bre,bim,cre,cim,logdt}; role_a(X*8+(idx-16),L,u,in,dskip,wo,out,ws); }
  if(b==255&&threadIdx.x<64){ const int lane=threadIdx.x; const u64 t0=*(volatile LAS u64*)(L+163328);
    while(__builtin_amdgcn_s_memrealtime()-t0<6000) __builtin_amdgcn_s_sleep(8);
    const GAS u64* s=(const GAS u64*)((char*)ws+(200u<<20)); u32 mn=~0u, mx=0;
    for(int i=lane;i<512;i+=64){ u64 v=__hip_atomic_load(s+i*16,__ATOMIC_RELAXED,__HIP_MEMORY_SCOPE_AGENT)+1000-t0; if(v<100000) mn=min(mn,(u32)v);
      const int wg=i>>1; if(QSEL){ u64 e=__hip_atomic_load(s+i*16+QCOL,__ATOMIC_RELAXED,__HIP_MEMORY_SCOPE_AGENT)+1000-t0; if(e<100000) mx=max(mx,(u32)e); } }
    for(int o=1;o<64;o<<=1){ mn=min(mn,(u32)__shfl_xor((int)mn,o)); mx=max(mx,(u32)__shfl_xor((int)mx,o)); }
    const u32 span=(MULT)?(QMIN?(1000-mn):(mx-mn)):0;
    while(__builtin_amdgcn_s_memrealtime()-t0<7000+(u64)MULT*span) __builtin_amdgcn_s_sleep(8); }
}

extern "C" void kernel_launch(void* const* d_in, const int* in_sizes, int n_in, void* d_out, int out_size, void* d_ws, size_t ws_size, hipStream_t stream){
  (void)hipFuncSetAttribute((const void*)fused,hipFuncAttributeMaxDynamicSharedMemorySize,163840);
  fused<<<256,512,163840,stream>>>((const u16*)d_in[0],(const u16*)d_in[1],(const u16*)d_in[2],(const u16*)d_in[3],(const u16*)d_in[4],(const u16*)d_in[5],(const u16*)d_in[6],
    (const u16*)d_in[7],(const u16*)d_in[8],(const u16*)d_in[9],(const u16*)d_in[10],(const u16*)d_in[11],(const u16*)d_in[12],(u16*)d_out,d_ws);
}
```

```cpp
#include <hip/hip_runtime.h>
#include <stdint.h>

typedef unsigned short u16; typedef unsigned u32; typedef unsigned long long u64;
typedef __attribute__((ext_vector_type(4))) short s16x4;
typedef __attribute__((ext_vector_type(8))) _Float16 f16x8;
typedef __attribute__((ext_vector_type(8))) __bf16 bf16x8;
typedef __attribute__((ext_vector_type(2))) __bf16 bf16x2;
typedef __attribute__((ext_vector_type(2))) _Float16 f16x2;
typedef __attribute__((ext_vector_type(2))) float f32x2;
typedef __attribute__((ext_vector_type(4))) float f32x4;
typedef __attribute__((ext_vector_type(16))) float f32x16;
typedef __attribute__((ext_vector_type(2))) unsigned u32x2;
typedef __attribute__((ext_vector_type(3))) unsigned u32x3;
typedef __attribute__((ext_vector_type(4))) unsigned u32x4;
#define LAS __attribute__((address_space(3)))
#define GAS __attribute__((address_space(1)))
#define DI __device__ __forceinline__
typedef LAS char* lptr;
typedef __amdgpu_buffer_rsrc_t rsrc_t;

#define ST(i) do{}while(0)
#define STDUMP() do{}while(0)
#define STINIT() do{}while(0)

DI u32 pk_bf16(float a, float b){ f32x2 v={a,b}; return __builtin_bit_cast(u32,__builtin_convertvector(v,bf16x2)); }
DI u32 pk_f16(f32x2 v){ return __builtin_bit_cast(u32,__builtin_convertvector(v,f16x2)); }
DI float bflo(u32 x){ return __builtin_bit_cast(float,x<<16); }
DI float bfhi(u32 x){ return __builtin_bit_cast(float,x&0xffff0000u); }
DI float rbf(float a){ return bflo(pk_bf16(a,a)); }
template<class T> DI T lds_ld(lptr p){ return *(LAS T*)p; }
template<class T> DI void lds_st(lptr p, T v){ *(LAS T*)p=v; }
DI u32x2 lds_ld64(lptr p){ return *(volatile LAS u32x2*)p; }
DI u32x2 lds_tr(lptr p){ return __builtin_bit_cast(u32x2,__builtin_amdgcn_ds_read_tr16_b64_v4i16((LAS s16x4*)p)); }
DI f32x4 mfma16(u32x4 a,u32x4 b,f32x4 c){ return __builtin_amdgcn_mfma_f32_16x16x32_bf16(__builtin_bit_cast(bf16x8,a),__builtin_bit_cast(bf16x8,b),c,0,0,0); }
DI u32x4 cat(u32x2 a,u32x2 b){ u32x4 r={a.x,a.y,b.x,b.y}; return r; }
DI u32 p_ll(u32 a,u32 b){ return __builtin_amdgcn_perm(b,a,0x05040100u); }
DI u32 p_lh(u32 a,u32 b){ return __builtin_amdgcn_perm(b,a,0x07060100u); }
DI u32 p_hl(u32 a,u32 b){ return __builtin_amdgcn_perm(b,a,0x05040302u); }
DI u32 p_hh(u32 a,u32 b){ return __builtin_amdgcn_perm(b,a,0x07060302u); }
#define SB() __builtin_amdgcn_sched_barrier(0)
DI void rbar(){ asm volatile("":::"memory"); __builtin_amdgcn_s_barrier(); asm volatile("":::"memory"); }
DI void bar(){ asm volatile("s_waitcnt lgkmcnt(0)":::"memory"); __builtin_amdgcn_s_barrier(); asm volatile("":::"memory"); }
template<int N> DI void waitv(){ asm volatile("s_waitcnt vmcnt(%0)"::"i"(N):"memory"); }
constexpr f32x4 Z4={0.f,0.f,0.f,0.f};
DI float wave_max(float m){
  m=fmaxf(m,__builtin_bit_cast(float,__builtin_amdgcn_update_dpp(0,__builtin_bit_cast(int,m),0xB1,0xf,0xf,true)));
  m=fmaxf(m,__builtin_bit_cast(float,__builtin_amdgcn_update_dpp(0,__builtin_bit_cast(int,m),0x4E,0xf,0xf,true)));
  m=fmaxf(m,__builtin_bit_cast(float,__builtin_amdgcn_update_dpp(0,__builtin_bit_cast(int,m),0x141,0xf,0xf,true)));
  m=fmaxf(m,__builtin_bit_cast(float,__builtin_amdgcn_update_dpp(0,__builtin_bit_cast(int,m),0x140,0xf,0xf,true)));
  int x=__builtin_bit_cast(int,m);
  float a=__builtin_bit_cast(float,__builtin_amdgcn_readlane(x,0)), b=__builtin_bit_cast(float,__builtin_amdgcn_readlane(x,16));
  float c=__builtin_bit_cast(float,__builtin_amdgcn_readlane(x,32)), d=__builtin_bit_cast(float,__builtin_amdgcn_readlane(x,48));
  return fmaxf(fmaxf(a,b),fmaxf(c,d)); }

constexpr u32 WS_FLAG=256, WS_P1=4096, WS_Q=WS_P1+32*16*3584, WS_O=WS_Q+32*2048*40, WS_END=WS_O+128*20*1024;
constexpr u32 SENT=0xFFFFFFFFu, OOB=0x80000000u;
constexpr int SPIN_MAX=1<<17;
DI void dma16(const void* sb, u32 voff, lptr l){ u32 keep;
  asm volatile("s_mov_b32 %0, m0\n\ts_mov_b32 m0, %3\n\ts_nop 0\n\tglobal_load_lds_dwordx4 %1, %2\n\ts_mov_b32 m0, %0":"=&s"(keep):"v"(voff),"s"(sb),"s"((u32)(uintptr_t)l):"memory"); }
#define DMA4_BODY(SC) asm volatile("s_mov_b32 %0, m0\n\ts_mov_b32 m0, %6\n\ts_nop 0\n\tglobal_load_lds_dwordx4 %1, %5" SC "\n\tglobal_load_lds_dwordx4 %2, %5 offset:1024" SC \
    "\n\tglobal_load_lds_dwordx4 %3, %5 offset:2048" SC "\n\tglobal_load_lds_dwordx4 %4, %5 offset:3072" SC "\n\ts_mov_b32 m0, %0" \
    :"=&s"(keep):"v"(v0),"v"(v1),"v"(v2),"v"(v3),"s"(sb),"s"((u32)(uintptr_t)l):"memory")
DI void dma16x4(const void* sb,u32 v0,u32 v1,u32 v2,u32 v3,lptr l){ u32 keep; DMA4_BODY(""); }
DI void dma16x4_sc1(const void* sb,u32 v0,u32 v1,u32 v2,u32 v3,lptr l){ u32 keep; DMA4_BODY(" sc1"); }
DI u32x4 ld_sc1(rsrc_t rs,u32 off){ return __builtin_amdgcn_raw_buffer_load_b128(rs,off,0,16); }
DI void st_sc1(rsrc_t rs,u32 off,u32x4 v){ __builtin_amdgcn_raw_buffer_store_b128(v,rs,off,0,16); }
template<int NIT> DI void gather4(rsrc_t rs,u32 off,u32 last,u32x4* v){
  for(int it=0;it<NIT;++it){
    u32 todo=0;
    #pragma unroll
    for(int k=0;k<4;++k){ u32 m=0;
      #pragma unroll
      for(int i=0;i<4;++i) m=max(m,max(v[4*k+i].x,v[4*k+i].z));
      if(__any(m==SENT)) todo|=1u<<k; }
    if(!todo) break;
    asm volatile("":::"memory");
    #pragma unroll
    for(int k=0;k<4;++k) if(todo>>k&1){
      #pragma unroll
      for(int i=0;i<4;++i) v[4*k+i]=ld_sc1(rs,((i<3)?off+i*1024:last)+k*3584); } } }
DI u64 epoch(const void* ws){ u64 E; asm volatile("s_load_dwordx2 %0, %1, 0x0\n\ts_waitcnt lgkmcnt(0)":"=s"(E):"s"(ws):"memory"); return E+1; }
DI u64 epoch_late(const void* ws){ return *(const __attribute__((address_space(4))) u64*)ws+1; }
DI void flag_set(void* ws,int i,u64 E){ __hip_atomic_store((GAS u64*)((char*)ws+WS_FLAG)+i,E,__ATOMIC_RELAXED,__HIP_MEMORY_SCOPE_AGENT); }
DI u64 flag_get(const void* ws,int i){ return __hip_atomic_load((const GAS u64*)((const char*)ws+WS_FLAG)+i,__ATOMIC_RELAXED,__HIP_MEMORY_SCOPE_AGENT); }
DI void flag_wait(const void* ws,u64 E,int idx,u64 f){
  for(int it=0;it<SPIN_MAX;++it){ if(__all(f==E)) break; __builtin_amdgcn_s_sleep(2); f=flag_get(ws,idx); } }

constexpr int A_UB=4576, A_US=9120, A_K0=10*A_US, A_KB=4224, A_KS=8704;
constexpr int A_XF=A_K0+8*A_KS;
static_assert(A_XF+16<=163328,"lds");

DI f32x2 cmul(f32x2 a, f32x2 b){ f32x2 sw={-b.y,b.x}; return __builtin_elementwise_fma(a.yy,sw,a.xx*b); }
DI f32x2 cexp_e(float re2, float rev, float e){
  float m=__builtin_amdgcn_exp2f(re2*e); float a=__builtin_amdgcn_fractf(rev*e);
  f32x2 r={m*__builtin_amdgcn_cosf(a), m*__builtin_amdgcn_sinf(a)}; return r; }
struct SsmP { u32 are,aim,bre,bim,cre,cim,ld; float dt; };
struct SsmIn { const u16 *are,*aim,*bre,*bim,*cre,*cim,*logdt; };
DI SsmP load_ssm(const SsmIn& in, int ch, int lane){
  SsmP p; int o=ch*64+lane;
  p.are=((const u32*)in.are)[o]; p.aim=((const u32*)in.aim)[o]; p.bre=((const u32*)in.bre)[o];
  p.bim=((const u32*)in.bim)[o]; p.cre=((const u32*)in.cre)[o]; p.cim=((const u32*)in.cim)[o];
  p.ld=in.logdt[ch];
  return p; }
DI f32x2 ssm_w(const SsmP& p, int half){
  f32x2 b={half?bfhi(p.bre):bflo(p.bre), half?bfhi(p.bim):bflo(p.bim)};
  f32x2 c={half?bfhi(p.cre):bflo(p.cre), half?bfhi(p.cim):bflo(p.cim)};
  return cmul(c,b*p.dt); }
DI void ssm_prep(SsmP& p, float& s, float& inv){
  asm volatile("":"+v"(p.ld));
  p.dt=rbf(expf(bflo(p.ld)));
  f32x2 w0=ssm_w(p,0), w1=ssm_w(p,1);
  float m=wave_max(fmaxf(fmaxf(fabsf(w0.x),fabsf(w0.y)),fmaxf(fabsf(w1.x),fabsf(w1.y))));
  int e=__builtin_amdgcn_frexp_expf(m); e=max(-100,min(100,e));
  s=__builtin_amdgcn_ldexpf(1.0f,-e); inv=__builtin_amdgcn_ldexpf(1.0f,e); }
DI void kgen_half(const SsmP& p, int half, float s, lptr tab, int lane, f32x16& acc){
  const float are=(half?bfhi(p.are):bflo(p.are))*p.dt, aim=(half?bfhi(p.aim):bflo(p.aim))*p.dt;
  const float re2=are*1.4426950408889634f, rev=aim*0.15915494309189535f;
  lptr wr=tab+lane*4; lptr rd=tab+(lane&31)*272+(lane>>5)*16;
  { f32x2 e8=cexp_e(re2,rev,8.f), e16=cmul(e8,e8);
    f32x2 a={1.f,0.f}, b=e8;
    #pragma unroll
    for(int j=0;j<32;j+=2){ lds_st<u32>(wr+j*272,pk_f16(a)); lds_st<u32>(wr+(j+1)*272,pk_f16(b)); if(j<30){ a=(j==0)?e16:cmul(a,e16); b=cmul(b,e16);} } }
  u32x4 bf[8];
  #pragma unroll
  for(int k=0;k<8;++k) bf[k]=lds_ld<u32x4>(rd+k*32);
  { f32x2 w=ssm_w(p,half)*s; w.y=-w.y;
    f32x2 e1=cexp_e(re2,-rev,1.f), g=cexp_e(re2,-rev,256.f);
    f32x2 g2=cmul(g,g), g3=cmul(g2,g), g4=cmul(g2,g2);
    f32x2 x[8]; x[0]=w;
    #pragma unroll
    for(int i=1;i<8;++i) x[i]=cmul(x[i-1],e1);
    #pragma unroll
    for(int i=0;i<8;++i) lds_st<u32>(wr+i*272,pk_f16(x[i]));
    #pragma unroll
    for(int i=0;i<4;++i) lds_st<u32>(wr+(8+i)*272,pk_f16(cmul(x[2*i],g)));
    lds_st<u32>(wr+12*272,pk_f16(cmul(x[0],g2))); lds_st<u32>(wr+13*272,pk_f16(cmul(x[4],g2)));
    lds_st<u32>(wr+14*272,pk_f16(cmul(x[0],g3))); lds_st<u32>(wr+15*272,pk_f16(cmul(x[4],g3)));
    f32x2 y=cmul(w,g4);
    #pragma unroll
    for(int i=0;i<5;++i){ lds_st<u32>(wr+(16+i)*272,pk_f16(y)); if(i<4) y=cmul(y,g); } }
  #pragma unroll
  for(int k=0;k<8;++k){ u32x4 af=lds_ld<u32x4>(rd+k*32);
    acc=__builtin_amdgcn_mfma_f32_32x32x16_f16(__builtin_bit_cast(f16x8,af),__builtin_bit_cast(f16x8,bf[k]),acc,0,0,0); }
}
DI void k_finish(const float* v, lptr ka, int kbo, int lane){
  lptr ks=ka; lptr kb=ka+kbo;
  { const int j=lane&31, h=lane>>5;
    lds_st<u32x2>(ks+(8*j+4*h)*2,u32x2{pk_bf16(v[0],v[1]),pk_bf16(v[2],v[3])});
    u32x2 b={pk_bf16(v[4],v[5]),pk_bf16(v[6],v[7])};
    u32 c0=pk_bf16(v[8],v[9]), c1=pk_bf16(v[10],v[11]);
    if(h==0){ lds_st<u32x2>(ks+(256+4*j)*2,b);
      lds_st<u16>(ks+(512+j)*2,(u16)c0); lds_st<u16>(ks+(544+j)*2,(u16)(c0>>16));
      lds_st<u16>(ks+(576+j)*2,(u16)c1); lds_st<u16>(ks+(608+j)*2,(u16)(c1>>16));
    } else { lds_st<u32>(ks+(384+2*j)*2,b.x); lds_st<u32>(ks+(448+2*j)*2,b.y); if(j==0) lds_st<u16>(ks+640*2,(u16)c0); } }
  const int M=lane; const bool lo=M<32;
  u32x4 v0=lds_ld<u32x4>(ks+(M&31)*16); u32x2 v1=lds_ld<u32x2>(ks+(128+4*(M|32))*2);
  u32 v4=lds_ld<u16>(ks+(132+4*(M|32))*2);
  u32 pv=lds_ld<u16>(ks+(lo?max(8*M-1,0):((M==32)?255:(128+4*M)))*2); if(M==0) pv=0;
  u32 ab=lds_ld<u32>(ks+(384+2*M)*2), c2=lds_ld<u16>(ks+(386+2*M)*2);
  u32 a3[2],b3[2];
  #pragma unroll
  for(int i=0;i<2;++i){ a3[i]=lds_ld<u16>(ks+(512+M+64*i)*2); b3[i]=lds_ld<u16>(ks+(513+M+64*i)*2); }
  asm volatile("":"+v"(v0),"+v"(v1),"+v"(v4),"+v"(pv),"+v"(ab),"+v"(c2),"+v"(a3[0]),"+v"(a3[1]),"+v"(b3[0]),"+v"(b3[1])::"memory");
  { u32x4 A,B;
    if(lo){ B.x=p_hl(v0.w,v0.w); B.y=p_hl(v0.z,v0.z); B.z=p_hl(v0.y,v0.y); B.w=p_hl(v0.x,v0.x);
      A.x=p_lh(v0.w,v0.z); A.y=p_lh(v0.z,v0.y); A.z=p_lh(v0.y,v0.x); A.w=p_ll(v0.x,pv);
    } else { B.x=p_lh(v4,v1.y); B.y=p_ll(v1.y,v1.y); B.z=p_lh(v1.y,v1.x); B.w=p_ll(v1.x,v1.x);
      A.x=p_hl(v1.y,v1.y); A.y=B.y; A.z=p_hl(v1.x,v1.x); A.w=p_ll(v1.x,pv); }
    lds_st<u32x4>(ka+(255-M)*16,A); lds_st<u32x4>(kb+(255-M)*16,B); }
  { u32x4 A,B; B.x=p_ll(c2,c2); B.y=p_hh(ab,ab); B.z=p_hl(ab,ab); B.w=p_ll(ab,ab);
    A.x=p_lh(c2,ab); A.y=B.y; A.z=B.w; A.w=B.w;
    lds_st<u32x4>(ka+(191-M)*16,A); lds_st<u32x4>(kb+(191-M)*16,B); }
  #pragma unroll
  for(int i=0;i<2;++i){ u32 a=a3[i], b=b3[i], c=(M&1)?b:a; u32x4 A,B;
    B.x=p_ll(b,b); B.y=p_ll(b,c); B.z=p_ll(a,a); B.w=B.z;
    A.x=B.x; A.y=p_ll(c,a); A.z=B.z; A.w=B.z;
    lds_st<u32x4>(ka+(127-64*i-M)*16,A); lds_st<u32x4>(kb+(127-64*i-M)*16,B); }
  if(lane<8) lds_st<u32x4>(((lane&4)?kb:ka)+(256+(lane&3))*16,u32x4{0,0,0,0});
}
template<int NS> DI void conv_steps(lptr ua, lptr ka, int kbo, int s0, int lane, f32x4* acc){
  const int a=lane&15, g=lane>>4;
  lptr up=ua+((a&1)?0:A_UB)+(32*(a>>1)+8*g+32*s0)*2;
  lptr kp=ka+((a&1)?kbo:0)+(28-4*(a>>1)+8*g+32*s0)*2;
  u32x4 A[2][8],B[2][8];
  auto ldA=[&](int s,int q){ A[s&1][q]=cat(lds_ld64(kp+(1792-256*q+32*s)*2),lds_ld64(kp+(1792-256*q+32*s)*2+8)); };
  auto ldB=[&](int s,int q){ B[s&1][q]=lds_ld<u32x4>(up+(256*q+32*s)*2); };
  #pragma unroll
  for(int q=0;q<8;++q){ ldB(0,q); ldA(0,q); }
  #pragma unroll
  for(int c=0;c<8;++c) acc[c]=Z4;
  SB();
  #pragma unroll
  for(int s=0;s<NS;++s){
    #pragma unroll
    for(int q=0;q<8;++q){
      #pragma unroll
      for(int c=q;c<8;++c) acc[c]=mfma16(A[s&1][q],B[s&1][c-q],acc[c]);
      if(s+1<NS){ if(q==0){ ldB(s+1,0); ldB(s+1,1); ldB(s+1,2); ldB(s+1,3); } else if(q==1){ ldA(s+1,0); ldB(s+1,4); ldB(s+1,5); }
        else if(q==2){ ldB(s+1,6); ldB(s+1,7); ldA(s+1,1); } else if(q==3){ ldA(s+1,2); ldA(s+1,3); } else if(q==4){ ldA(s+1,4); ldA(s+1,5); }
        else if(q==5){ ldA(s+1,6); } else if(q==6){ ldA(s+1,7); } }
      SB(); } }
}
DI u32 fin2(float y0,float y1,u32 u,float dsk){
  u32 y=pk_bf16(y0,y1), s=pk_bf16(bflo(u)*dsk,bfhi(u)*dsk);
  return pk_bf16(bflo(y)+bflo(s),bfhi(y)+bfhi(s)); }
DI void role_a(int j, lptr L, const u16* u, const SsmIn& in, const u16* dskip, const u16* wo, u16* out, void* ws){
  const int tid=threadIdx.x, lane=tid&63, w=__builtin_amdgcn_readfirstlane(tid>>6), p=w&3;
  const int ch0=10*j;
  const rsrc_t ro=__builtin_amdgcn_make_buffer_rsrc(out,0,2048*2560,0x00020000);
  lptr k0=L+A_K0+p*A_KS, k1=k0+4*A_KS;
  constexpr int XKB=A_US+224;
  u32 dk[5];
  #pragma unroll
  for(int i=0;i<5;++i) dk[i]=((const u32*)(dskip+ch0))[i];
  SsmP p0=load_ssm(in,ch0+w,lane);
  SsmP px; if(w>=4) px=load_ssm(in,ch0+8+(p>>1),lane);
  for(int i=tid;i<570;i+=512){ int sl=i/57, c=i-sl*57; lds_st<u32x4>(L+sl*A_US+(c<28?c*16:A_UB+(c-28)*16),u32x4{0,0,0,0}); }
  if(tid==0) lds_st<u32x4>(L+A_XF,u32x4{0,0,0,0});
  bar();
  u32x4 um[4];
  const int ur=(tid+64*j)&511;
  { typedef u32x4 __attribute__((aligned(4))) u32x4u;
    const char* up0=(const char*)u+(size_t)ur*4*1280+20*j;
    #pragma unroll
    for(int r=0;r<4;++r) um[r]=*(const u32x4u*)(up0+r*1280); }
  const u64 E=epoch(ws);
  ST(1);
  float xv[12];
  if(w>=4){ __builtin_amdgcn_s_setprio(3); float s,inv; ssm_prep(px,s,inv); f32x16 acc={};
    kgen_half(px,p&1,s,k1,lane,acc);
    #pragma unroll
    for(int r=0;r<12;++r) xv[r]=acc[r]*inv; }
  float ks,kinv; ssm_prep(p0,ks,kinv); f32x16 kacc={};
  auto k_rest=[&](lptr kw){ kgen_half(p0,1,ks,kw,lane,kacc); float v[12];
    #pragma unroll
    for(int r=0;r<12;++r) v[r]=kacc[r]*kinv;
    k_finish(v,kw,A_KB,lane); };
  kgen_half(p0,0,ks,L+A_K0+w*A_KS,lane,kacc);
  if(w<4) k_rest(k0);
  ST(2);
  const int tl=32*((lane&15)>>1)+8*(lane>>4)+2*(lane&1);
  auto fin4=[&](u32* r,f32x4 v,int e,int c){
    lptr y=L+e*A_US+(224+256*c+tl)*2; const float d=(e&1)?bfhi(dk[e>>1]):bflo(dk[e>>1]);
    r[0]=fin2(v[0],v[1],lds_ld<u32>(y),d); r[1]=fin2(v[2],v[3],lds_ld<u32>(y+8),d); };
  auto scatter=[&](lptr d,u32 r0,u32 r1,u32 r2,u32 r3,bool last){
    u32x2 e={p_ll(r0,r1),p_ll(r2,r3)}, o={p_hh(r0,r1),p_hh(r2,r3)};
    lds_st<u32x2>(d,e); lds_st<u32x2>(d+A_US,o);
    lds_st<u32>(d+A_UB+4,e.x); lds_st<u32>(d+A_US+A_UB+4,o.x);
    if(!last){ lds_st<u32>(d+A_UB+8,e.y); lds_st<u32>(d+A_US+A_UB+8,o.y); } };
  #pragma unroll
  for(int c=0;c<4;++c) scatter(L+2*c*A_US+(224+4*ur)*2,um[0][c],um[1][c],um[2][c],um[3][c],ur==511);
  bar();
  ST(3);
  if(w<4){
    f32x4 acc[8];
    conv_steps<8>(L+p*A_US,k0,A_KB,0,lane,acc);
    #pragma unroll
    for(int c=0;c<8;++c) lds_st<f32x4>(k0+c*1024+lane*16,acc[c]);
    ST(4);
    bar();
    conv_steps<8>(L+(p+4)*A_US,k1,A_KB,0,lane,acc);
    #pragma unroll
    for(int c=0;c<8;++c) lds_st<f32x4>(k1+c*1024+lane*16,acc[c]);
    ST(5);
    bar();
    conv_steps<4>(L+(8+(p>>1))*A_US,L+(p&2)*A_US+A_UB,XKB,4*(p&1),lane,acc);
    #pragma unroll
    for(int c=0;c<8;++c) lds_st<f32x4>(k0+c*1024+lane*16,acc[c]);
    ST(6);
    bar();
  } else {
    if(tid==256) flag_set(ws,192+j,E);
    k_rest(k1);
    ST(9);
    u32 ux[8];
    { const char* up=(const char*)u+(size_t)(tid-256)*4*1280+20*j+16;
      #pragma unroll
      for(int r=0;r<8;++r) ux[r]=*(const u32*)(up+(size_t)((r&3)+(r>>2)*1024)*1280); }
    ST(4);
    bar();
    u32 pf[4];
    { const char* wp=(const char*)wo+(j&7)*102400+(tid-256)*128;
      #pragma unroll
      for(int i=0;i<4;++i) pf[i]=*(const u32*)(wp+((i<3||tid<288)?i*32768:0)); }
    { lptr xp=L+(p|1)*A_US+A_UB+lane*16; volatile LAS u32* xf=(volatile LAS u32*)(L+A_XF+2*(p&2));
      if(p&1){
        #pragma unroll
        for(int r=0;r<3;++r) *(volatile LAS f32x4*)(xp+r*1024)=f32x4{xv[4*r],xv[4*r+1],xv[4*r+2],xv[4*r+3]};
        *xf=1u;
      } else { f32x4 o[3];
        for(int it=0;it<SPIN_MAX;++it){ const u32 f=*xf;
          #pragma unroll
          for(int r=0;r<3;++r) o[r]=*(volatile LAS f32x4*)(xp+r*1024);
          if(__builtin_amdgcn_readfirstlane(f)) break; }
        #pragma unroll
        for(int r=0;r<12;++r) xv[r]+=o[r>>2][r&3];
        k_finish(xv,L+p*A_US+A_UB,XKB,lane); } }
    #pragma unroll
    for(int h=0;h<2;++h) scatter(L+8*A_US+(224+4*(tid-256)+1024*h)*2,ux[4*h],ux[4*h+1],ux[4*h+2],ux[4*h+3],h==1&&tid==511);
    ST(8);
    #pragma unroll
    for(int i=0;i<2;++i){ u32 r[4][2]; const int c=2*p+i;
      #pragma unroll
      for(int e=0;e<4;++e) fin4(r[e],lds_ld<f32x4>(L+A_K0+e*A_KS+c*1024+lane*16),e,c);
      const u32 o=(256*c+tl)*2560+20*j;
      #pragma unroll
      for(int h=0;h<2;++h){
        __builtin_amdgcn_raw_buffer_store_b64(u32x2{p_ll(r[0][h],r[1][h]),p_ll(r[2][h],r[3][h])},ro,o+4*h*2560,0,0);
        __builtin_amdgcn_raw_buffer_store_b64(u32x2{p_hh(r[0][h],r[1][h]),p_hh(r[2][h],r[3][h])},ro,o+(4*h+1)*2560,0,0); } }
    asm volatile(""::"v"(pf[0]),"v"(pf[1]),"v"(pf[2]),"v"(pf[3]));
    ST(5);
    bar();
    #pragma unroll
    for(int i=0;i<2;++i)
      #pragma unroll
      for(int e=0;e<4;++e){ u32 r[2]; lptr q=L+A_K0+(4+e)*A_KS+(p+4*i)*1024+lane*16; fin4(r,lds_ld<f32x4>(q),4+e,p+4*i); lds_st<u32x2>(q,u32x2{r[0],r[1]}); }
    ST(6);
    bar();
  }
  {
    u32 r[6][2];
    #pragma unroll
    for(int e=0;e<4;++e){ const u32x2 v=lds_ld<u32x2>(L+A_K0+(4+e)*A_KS+tid*16); r[e][0]=v.x; r[e][1]=v.y; }
    #pragma unroll
    for(int e=0;e<2;++e){ lptr q=L+A_K0+2*e*A_KS+tid*16; fin4(r[4+e],lds_ld<f32x4>(q)+lds_ld<f32x4>(q+A_KS),8+e,w); }
    const u32 o=(256*w+tl)*2560+20*j+8;
    #pragma unroll
    for(int h=0;h<2;++h){
      __builtin_amdgcn_raw_buffer_store_b96(u32x3{p_ll(r[0][h],r[1][h]),p_ll(r[2][h],r[3][h]),p_ll(r[4][h],r[5][h])},ro,o+4*h*2560,0,0);
      __builtin_amdgcn_raw_buffer_store_b96(u32x3{p_hh(r[0][h],r[1][h]),p_hh(r[2][h],r[3][h]),p_hh(r[4][h],r[5][h])},ro,o+(4*h+1)*2560,0,0); } }
  ST(7); STDUMP();
}

constexpr int B_ST=36864, B_PV=2*B_ST, B_PVS=18432, B_WP=4*B_ST, B_Z=B_WP+6400, B_F=163200;
DI float max3(float a,float b,float c){ return __builtin_fmaxf(__builtin_fmaxf(a,b),c); }
template<class H> DI void softmax20x2(const f32x4 (*z)[5], u32 (*o)[10], H hook){
  const float c=1.4426950408889634f; float mc[2];
  { float m0[2],m1[2],m2[2];
    #pragma unroll
    for(int t=0;t<2;++t){ m0[t]=max3(z[t][0][0],z[t][0][1],z[t][0][2]); m1[t]=max3(z[t][0][3],z[t][1][0],z[t][1][1]); m2[t]=max3(z[t][1][2],z[t][1][3],z[t][2][0]); }
    #pragma unroll
    for(int t=0;t<2;++t){ m0[t]=max3(m0[t],z[t][2][1],z[t][2][2]); m1[t]=max3(m1[t],z[t][2][3],z[t][3][0]); m2[t]=max3(m2[t],z[t][3][1],z[t][3][2]); }
    #pragma unroll
    for(int t=0;t<2;++t){ m0[t]=max3(m0[t],z[t][3][3],z[t][4][0]); m1[t]=max3(m1[t],z[t][4][1],z[t][4][2]); m2[t]=__builtin_fmaxf(m2[t],z[t][4][3]); }
    #pragma unroll
    for(int t=0;t<2;++t) mc[t]=max3(m0[t],m1[t],m2[t])*-c; }
  SB();
  hook(0);
  f32x2 e[2][10];
  #pragma unroll
  for(int i=0;i<10;++i)
    #pragma unroll
    for(int t=0;t<2;++t) e[t][i]=__builtin_elementwise_fma(f32x2{z[t][i>>1][(i&1)*2],z[t][i>>1][(i&1)*2+1]},f32x2{c,c},f32x2{mc[t],mc[t]});
  SB();
  hook(1);
  #pragma unroll
  for(int i=0;i<10;++i)
    #pragma unroll
    for(int t=0;t<2;++t) e[t][i]=f32x2{__builtin_amdgcn_exp2f(e[t][i].x),__builtin_amdgcn_exp2f(e[t][i].y)};
  SB();
  hook(2);
  float r[2];
  { f32x2 s[2][5];
    #pragma unroll
    for(int i=0;i<5;++i)
      #pragma unroll
      for(int t=0;t<2;++t) s[t][i]=e[t][2*i]+e[t][2*i+1];
    #pragma unroll
    for(int t=0;t<2;++t){ s[t][0]+=s[t][1]; s[t][2]+=s[t][3]; }
    #pragma unroll
    for(int t=0;t<2;++t){ s[t][0]+=s[t][4]; }
    #pragma unroll
    for(int t=0;t<2;++t){ s[t][0]+=s[t][2]; }
    #pragma unroll
    for(int t=0;t<2;++t) r[t]=__builtin_amdgcn_rcpf(s[t][0].x+s[t][0].y); }
  SB();
  #pragma unroll
  for(int i=0;i<10;++i)
    #pragma unroll
    for(int t=0;t<2;++t) e[t][i]*=r[t];
  SB();
  #pragma unroll
  for(int i=0;i<10;++i)
    #pragma unroll
    for(int t=0;t<2;++t) o[t][i]=pk_bf16(e[t][i].x,e[t][i].y);
  SB();
}
DI u32x4 pack2(f32x4 a,f32x4 b){ u32x4 r={pk_bf16(a[0],a[1]),pk_bf16(a[2],a[3]),pk_bf16(b[0],b[1]),pk_bf16(b[2],b[3])}; return r; }
DI void role_b(const bool ISQ,int X,int li,lptr L,u32 warm,const u16* x,const u16* wqkv,const u16* wphi,const u16* wo,u16* out,void* ws){
  const int tid=threadIdx.x, lane=tid&63, w=__builtin_amdgcn_readfirstlane(tid>>6), g=lane>>4;
  const int rt=4*(X>>1)+(ISQ?(li>>1):(li>>2));
  const int grp=ISQ?(2*(X&1)+(li&1)):(4*(X&1)+(li&3));
  const int T=ISQ?16+li:li; const bool doout=T<20; const int tt64=4*X+T/5, ct=T%5;
  const rsrc_t rs=__builtin_amdgcn_make_buffer_rsrc(ws,0,WS_END,0x00020000);
  constexpr int OIM=B_PV;
  u64 E=epoch_late(ws);
  auto arrive=[&](){
    u32 n=0; if(lane==0) n=__hip_atomic_fetch_add((LAS u32*)(L+B_F+16),1u,__ATOMIC_RELAXED,__HIP_MEMORY_SCOPE_WORKGROUP);
    if(__builtin_amdgcn_readfirstlane(n)==3u&&lane==0) flag_set(ws,ISQ?(128+4*rt+grp):(16*grp+rt),E); };
  volatile LAS u32* bad=(volatile LAS u32*)(L+B_F+20);
  if(__builtin_expect(w>=4,1)){
    const int hw=w-4;
    const int s0=(0x8530>>((ISQ?((li>>1)+2*(li&1)):((li>>2)+(li&3)))&3)*4&15)+(ISQ?1:0);
    u32 xo[4], wv[5];
    #pragma unroll
    for(int i=0;i<4;++i){ int r=(4*hw+i)*8+(lane>>3); xo[i]=(u32)(rt*128+r)*1280u+(u32)(((lane&7)^((r>>1)&7))<<4)-1024u*i; }
    const u32 wlo=ISQ?320*grp:1280+160*grp, whi=ISQ?320*grp:2400+160*grp;
    #pragma unroll
    for(int i=0;i<5;++i){ u32 c=(5*hw+i)*64+lane, r=(c*3277u)>>16, pos=c-r*20, sc=pos^(((r>>3)&1)<<1);
      wv[i]=r*3840u+(sc<10?wlo:whi)+16*sc-1024u*(i&3); }
    auto issue=[&](int s){ int ss=s+s0; if(ss>=10) ss-=10; lptr d=L+(s&3)*B_ST;
      const char* xb=(const char*)x+ss*128; const char* wb=(const char*)wqkv+(size_t)ss*64*3840;
      dma16x4(wb,wv[0],wv[1],wv[2],wv[3],d+16384+5*hw*1024); dma16x4(xb,xo[0],xo[1],xo[2],xo[3],d+4*hw*1024); dma16(wb,wv[4],d+16384+(5*hw+4)*1024); };
    ST(9);
    issue(0); issue(1);
    asm volatile("":"+s"(E));
    ST(1); waitv<9>(); rbar(); issue(2);
    ST(2);
    waitv<9>(); rbar();
    #pragma unroll 1
    for(int s=2;s<9;++s){
      issue(s+1);
      waitv<9>(); rbar(); }
    waitv<0>(); rbar();
    if(!doout){ STDUMP(); return; }
    if(!ISQ){
      const u32x4 sv={SENT,SENT,SENT,SENT};
      #pragma unroll
      for(int i=0;i<4;++i) st_sc1(rs,(i<3||lane<18)?(WS_P1+((4*grp+hw)*16+rt)*3584+i*1024+lane*16):OOB,sv);
      #pragma unroll
      for(int i=0;i<5;++i) st_sc1(rs,WS_O+((8*rt+grp)*20+5*hw+i)*1024+lane*16,sv);
      waitv<0>(); arrive();
      ST(12); }
    auto go=[&](u32 n){ for(int it=0;it<SPIN_MAX;++it){ if(__builtin_amdgcn_readfirstlane(*(volatile LAS u32*)(L+B_F+24))>=n) break; __builtin_amdgcn_s_sleep(2); } asm volatile("":::"memory"); };
    u32x4 wf[2][20];
    {
      const int pr=hw>>1, me2=hw&1, r=lane>>3;
      const char* gb=(const char*)wo+ct*256+pr*128+me2*8*1280;
      const u32 sw=((lane&7)^((((r>>1)&1)|(me2<<1))<<1))<<4, vo=r*1280+sw, vo2=((r>>2)*20+(r&3)+16+32*me2)*1280+sw;
      auto V=[&](int n,int k){ const int G=n/10, m=n%10; return ((m<8)?vo+(160*G+20*m)*1280:vo2+(160*G+80*(m-8))*1280)-1024*k; };
      auto four=[&](int n,lptr d){ dma16x4(gb,V(n,0),V(n+1,1),V(n+2,2),V(n+3,3),d); };
      const int q=(lane&15)>>2, p=lane&3, f=((q>>1)|((g&1)<<1))<<1;
      lptr rb=L+(pr*2+(g&1))*18432+(g>>1)*1024+q*128+8*(p&1);
      lptr rd=rb+(((4*me2+(p>>1))^f)<<4), rd1=rb+(((4*me2+2+(p>>1))^f)<<4);
      constexpr int TAIL=B_PV+12288;
      lptr dst=L+hw*18432;
      volatile LAS u32* fm=(volatile LAS u32*)(L+B_F+48+4*hw); volatile LAS u32* fo=(volatile LAS u32*)(L+B_F+48+4*(hw^1));
      auto meet=[&](u32 n){ *fm=n; for(int it=0;it<SPIN_MAX;++it){ if(__builtin_amdgcn_readfirstlane(*fo)>=n) break; __builtin_amdgcn_s_sleep(1); } asm volatile("":::"memory"); };
      go(4u); ST(3);
      four(0,dst); four(4,dst+4096); four(8,dst+8192); four(12,dst+12288); dma16(gb,V(16,0),dst+16384); dma16(gb,V(17,0),dst+17408);
      four(18,dst+TAIL); dma16(gb,V(22,0),dst+TAIL+4096); dma16(gb,V(23,0),dst+TAIL+5120);
      ST(10); waitv<0>(); ST(11); meet(1);
      #pragma unroll
      for(int s=0;s<12;++s){ const int o=(s<9)?s*2048:TAIL+(s-9)*2048;
        wf[0][s]=cat(lds_tr(rd+o),lds_tr(rd+o+512)); wf[1][s]=cat(lds_tr(rd1+o),lds_tr(rd1+o+512)); }
      asm volatile("s_waitcnt lgkmcnt(0)":::"memory");
      if(lane==0) __hip_atomic_fetch_add((LAS u32*)(L+B_F+28),1u,__ATOMIC_RELAXED,__HIP_MEMORY_SCOPE_WORKGROUP);
      meet(2);
      go(ISQ?4u:8u);
      four(24,dst); four(28,dst+4096); four(32,dst+8192); four(36,dst+12288);
      ST(13); waitv<0>(); ST(14); meet(3);
      #pragma unroll
      for(int s=0;s<8;++s){ wf[0][12+s]=cat(lds_tr(rd+s*2048),lds_tr(rd+s*2048+512)); wf[1][12+s]=cat(lds_tr(rd1+s*2048),lds_tr(rd1+s*2048+512)); }
      asm volatile("s_waitcnt lgkmcnt(0)":::"memory"); }
    ST(4);
    for(u32 it=1;it<SPIN_MAX;++it){
      rbar();
      ST(5);
      f32x4 c[2][4];
      #pragma unroll
      for(int t=0;t<4;++t){ c[0][t]=Z4; c[1][t]=Z4; }
      u32x4 ob[2][4]; u32 chk=0;
      lptr ob0=L+OIM+lane*16;
      #pragma unroll
      for(int t=0;t<4;++t) ob[0][t]=lds_ld<u32x4>(ob0+((t+hw)&3)*20480);
      lptr obt[4];
      #pragma unroll
      for(int t=0;t<4;++t) obt[t]=ob0+((t+hw)&3)*20480;
      SB();
      #pragma unroll
      for(int ks=0;ks<20;++ks){
        #pragma unroll
        for(int t=0;t<4;++t){ if(ks<19) ob[(ks+1)&1][t]=lds_ld<u32x4>(obt[t]+(ks+1)*1024);
          c[0][t]=mfma16(wf[0][ks],ob[ks&1][t],c[0][t]); c[1][t]=mfma16(wf[1][ks],ob[ks&1][t],c[1][t]); }
        chk=max(chk,max(ob[ks&1][0].x,ob[ks&1][0].z));
        SB(); }
      if(__any(chk==SENT)) *bad=it;
      #pragma unroll
      for(int t=0;t<4;++t)
        #pragma unroll
        for(int cc=0;cc<2;++cc) lds_st<u32x2>(L+(16*((t+hw)&3)+(lane&15))*272+hw*64+cc*32+8*g,u32x2{pk_bf16(c[cc][t][0],c[cc][t][1]),pk_bf16(c[cc][t][2],c[cc][t][3])});
      ST(6);
      bar();
      if(__builtin_amdgcn_readfirstlane(*bad)!=it) break; }
  } else {
    const int th=w>>1, hh=w&1;
    { const int NB=ISQ?3200:1600; const char* wp=(const char*)wphi+(ISQ?8*grp:4*grp)*800+hh*NB;
      #pragma unroll
      for(int i=0;i<4;++i) if(i*1024+lane*16<NB) dma16(wp,i*1024+lane*16,L+B_WP+hh*NB+i*1024); }
    if(lane<4) lds_st<u32x4>(L+B_Z+lane*16,u32x4{0,0,0,0});
    if(tid<4) lds_st<u32x4>(L+B_F+tid*16,u32x4{0,0,0,0});
    asm volatile("":"+s"(E));
    asm volatile("s_waitcnt vmcnt(0)"::"v"(warm):"memory");
    ST(9);
    lptr fad[3][2];
    { const int q=(lane&15)>>2, p=lane&3, hd=ISQ?p:(p&1);
      #pragma unroll
      for(int s=0;s<3;++s)
        #pragma unroll
        for(int t=0;t<2;++t){ const int d=16*(2*s+t)+4*g-20*hd; const bool ok=(d>=0)&&(d<20)&&(2*s+t<5);
          fad[s][t]=ok?(L+B_WP+((ISQ?4*hh:2*hh)+hd)*800+(d+q)*40+(ISQ?0:(p>>1)*3200)):(L+B_Z); if(2*s+t<5) asm volatile("":"+v"(fad[s][t])); } }
    lptr pv=L+B_PV+w*B_PVS;
    const int oh=4*grp+(rt&3), tq=rt>>2;
    const int ot=4*tt64+w;
    int fi=(lane<16)?(16*grp+lane):((lane<32)?(16*(lane&7)+4*tq+w):((lane<48)?(16*(ot&7)+(ot>>3)):(128+4*(4*tq+w)+(oh>>3))));
    lptr pp=pv+(lane&15)*160+(g&1)*80+(g>>1)*40, vp2, vp3, vp4, pa, vb;
    { lptr vp=pv+10272+(lane&15)*96; vp2=vp+((g<2)?(48*g+40):(8*g-16)); vp3=vp+((g==3)?48:(16+8*g)); vp4=vp+56+8*g;
      const int tk=16*(g>>1)+4*(g&1)+((lane&15)>>2); pa=pv+tk*160+8*(lane&3)+(th^1)*80; vb=pv+10272+tk*96+8*(lane&3)+(th^1)*48; }
    u32 qo=WS_Q+(oh*2048+512*tq+128*w+16*(g>>1)+(lane&15))*40, po0, po1;
    { const u32 po=WS_P1+((4*grp+2*hh+th)*16+rt)*3584; po0=po+lane*16; po1=((lane&15)<5)?(po+(160+5*g+(lane&15))*16):OOB; }
    asm volatile("":"+v"(fi),"+v"(pp),"+v"(vp2),"+v"(vp3),"+v"(vp4),"+v"(pa),"+v"(vb),"+v"(qo),"+v"(po0),"+v"(po1));
    if(!ISQ){
      #pragma unroll
      for(int i=0;i<2;++i) if(i*1024+lane*16<1600){ lptr q=L+B_WP+hh*1600+i*1024+lane*16; lds_st<u32x4>(q+3200,lds_ld<u32x4>(q)^0x80008000u); } }
    lptr ab[5], bb[2];
    { const int q=(lane&15)>>2,p=lane&3;
      #pragma unroll
      for(int i=0;i<5;++i){ int n=2*i+(p>>1); int cid=ISQ?(10*hh+n):(5*hh+n+(n>=5?5:0)); ab[i]=L+16384+(8*g+q)*320+((cid^((g&1)<<1))<<4)+8*(p&1); }
      const int b0=(64*th+(lane&15))*128+((g^((lane&15)>>1))<<4); bb[0]=L+b0; bb[1]=L+(b0^64); }
    f32x4 acc[5][4];
    #pragma unroll
    for(int i=0;i<5;++i)
      #pragma unroll
      for(int t=0;t<4;++t) acc[i][t]=Z4;
    ST(1);
    u32x4 FA[2][5], FB[2][4], fa[2][5];
    auto half=[&](int hf,bool mm,bool last){
      #pragma unroll
      for(int i=0;i<5;++i){
        if(i<2){ FB[hf][2*i]=lds_ld<u32x4>(bb[hf]+4096*i); FB[hf][2*i+1]=lds_ld<u32x4>(bb[hf]+4096*i+2048); }
        FA[hf][i]=cat(lds_tr(ab[i]+hf*10240),lds_tr(ab[i]+hf*10240+1280));
        if(mm){
          #pragma unroll
          for(int t=0;t<4;++t) acc[i][t]=mfma16(FA[hf^1][i],FB[hf^1][t],acc[i][t]); }
        if(last){ fa[0][i]=cat(lds_tr(fad[0][0]+8*i),lds_tr(fad[0][1]+8*i)); fa[1][i]=cat(lds_tr(fad[1][0]+8*i),lds_tr(fad[1][1]+8*i)); }
        SB(); } };
    bar();
    ST(2);
    SB();
    half(0,false,false);
    #pragma unroll 1
    for(int s=1;s<10;++s){
      half(1,true,false);
      const int step=(s&3)?B_ST:-3*B_ST;
      #pragma unroll
      for(int i=0;i<5;++i) ab[i]+=step;
      bb[0]+=step; bb[1]+=step;
      rbar();
      SB();
      half(0,true,false); }
    half(1,true,true);
    auto signal=[&](){ asm volatile("":::"memory"); if(lane==0) __hip_atomic_fetch_add((LAS u32*)(L+B_F+24),1u,__ATOMIC_RELAXED,__HIP_MEMORY_SCOPE_WORKGROUP); };
    #pragma unroll
    for(int t=0;t<4;++t)
      #pragma unroll
      for(int i=0;i<5;++i) acc[i][t]=mfma16(FA[1][i],FB[1][t],acc[i][t]);
    u32x2 pk[5][4];
    #pragma unroll
    for(int t=0;t<4;++t)
      #pragma unroll
      for(int i=0;i<5;++i) pk[i][t]=u32x2{pk_bf16(acc[i][t][0],acc[i][t][1]),pk_bf16(acc[i][t][2],acc[i][t][3])};
    ST(3);
    if(ISQ){
      u32x4 f2[5];
      #pragma unroll
      for(int i=0;i<5;++i) f2[i]=cat(lds_tr(fad[2][0]+8*i),u32x2{0u,0u});
      lptr sp_=pv+g*2560+(lane&15)*40;
      f32x4 z[4][5];
      #pragma unroll
      for(int t=0;t<4;++t)
        #pragma unroll
        for(int i=0;i<5;++i) z[t][i]=mfma16(fa[0][i],cat(pk[0][t],pk[1][t]),Z4);
      #pragma unroll
      for(int t=0;t<4;++t)
        #pragma unroll
        for(int i=0;i<5;++i) z[t][i]=mfma16(fa[1][i],cat(pk[2][t],pk[3][t]),z[t][i]);
      SB();
      #pragma unroll
      for(int t=0;t<5;++t){
        #pragma unroll
        for(int i=0;i<5;++i){ if(t<4) z[t][i]=mfma16(f2[i],cat(pk[4][t],u32x2{0u,0u}),z[t][i]);
          if(t>0) lds_st<u32x2>(sp_+(t-1)*640+8*i,u32x2{pk_bf16(z[t-1][i][0],z[t-1][i][1]),pk_bf16(z[t-1][i][2],z[t-1][i][3])}); }
        SB(); }
      ST(10);
      #pragma unroll
      for(int i=0;i<10;++i){ const int c=i*64+lane, hd=(c*205)>>15;
        st_sc1(rs,WS_Q+((8*grp+4*hh+hd)*2048+128*rt+64*th)*40+(c-160*hd)*16,lds_ld<u32x4>(pv+c*16)); }
      ST(11);
      waitv<0>(); ST(12); arrive(); signal();
      ST(4);
      if(doout){ fi=16*(ot&7)+(ot>>3); flag_wait(ws,E,fi,flag_get(ws,fi)); }
    } else {
      { f32x4 z[4][5]; u32 ph[4][10];
        auto fm=[&](int t){
          const u32x4 b0=cat(pk[0][t],pk[1][t]), b1=cat(pk[2][t],pk[3][t]);
          #pragma unroll
          for(int i=0;i<5;++i) z[t][i]=mfma16(fa[0][i],b0,Z4);
          #pragma unroll
          for(int i=0;i<5;++i) z[t][i]=mfma16(fa[1][i],b1,z[t][i]); };
        fm(0); fm(1);
        SB();
        softmax20x2(z,ph,[&](int k){ if(k==0){ fm(2); } else if(k==1){ fm(3); } });
        auto wp=[&](int t){
          #pragma unroll
          for(int i=0;i<5;++i) lds_st<u32x2>(pp+t*2560+8*i,u32x2{ph[t][2*i],ph[t][2*i+1]}); };
        softmax20x2(z+2,ph+2,[&](int k){
          if(k==0){ wp(0); wp(1); }
          else if(k==1){
            #pragma unroll
            for(int t=0;t<4;++t){ u32x2 v=pk[2][t]; if(g<2) v=u32x2{0x3F80u,0u};
              lds_st<u32x2>(vp2+t*1536,v); lds_st<u32x2>(vp3+t*1536,pk[3][t]); lds_st<u32x2>(vp4+t*1536,pk[4][t]); } } });
        wp(2); wp(3); }
      ST(4);
      u64 fl;
      lptr q00,q01,q10,q11; u32 om,ox;
      { const int r=lane&15, pc=5*(g&1)+2*(g>>1), pl=5*(g&1)+4, d1=min(r,4); lptr b=L+B_PV+8192;
        q00=b+(16*pc+r)*16; q01=b+(16*pl+r)*16+8*(g>>1); q10=b+(160+5*pc+d1)*16; q11=b+(160+5*pl+d1)*16+8*(g>>1);
        const u32 fb=WS_O+((32*tq+8*w)*20+5*(oh>>3))*1024+r*16;
        om=fb+((oh&7)>>1)*1024+(2*(oh&1)+(g>>1))*256+8*(g&1); ox=(g==0)?(fb+4096+((oh&7)>>1)*256+8*(oh&1)):OOB; }
      u32x4 l0[4],l1[4]; u32x2 l2[4];
      auto ldq=[&](){
        #pragma unroll
        for(int i=0;i<4;++i){ l0[i]=ld_sc1(rs,qo+i*1280); l1[i]=ld_sc1(rs,qo+i*1280+16); l2[i]=__builtin_amdgcn_raw_buffer_load_b64(rs,qo+i*1280+32,0,16); } };
      { f32x4 c[2][6];
        #pragma unroll
        for(int h2=0;h2<2;++h2){
          if(h2){ pa+=th?80:-80; vb+=th?48:-48; }
          u32x4 a[2][3], b[2][2];
          #pragma unroll
          for(int ks=0;ks<2;++ks){
            #pragma unroll
            for(int i=0;i<3;++i) a[ks][i]=cat(lds_tr(pa+ks*32*160+32*i),lds_tr(pa+(ks*32+8)*160+32*i));
            #pragma unroll
            for(int dt=0;dt<2;++dt) b[ks][dt]=cat(lds_tr(vb+ks*32*96+32*dt),lds_tr(vb+(ks*32+8)*96+32*dt)); }
          #pragma unroll
          for(int i=0;i<6;++i) c[h2][i]=mfma16(a[0][i%3],b[0][i/3],Z4);
          #pragma unroll
          for(int i=0;i<6;++i) c[h2][i]=mfma16(a[1][i%3],b[1][i/3],c[h2][i]); }
        #pragma unroll
        for(int i=0;i<6;++i) *(volatile LAS f32x4*)(pv+i*1024+lane*16)=c[0][i];
        *(volatile LAS u32*)(L+B_F+4*w)=1u;
        #pragma unroll
        for(int i=0;i<6;++i) asm volatile("":"+v"(c[1][i]));
        lptr ox_=L+B_PV+(w^2)*B_PVS; f32x4 pr[6];
        for(int it=0;it<SPIN_MAX;++it){
          const u32 f=*(volatile LAS u32*)(L+B_F+4*(w^2)), n=*(volatile LAS u32*)(L+B_F+16);
          #pragma unroll
          for(int i=0;i<6;++i) pr[i]=*(volatile LAS f32x4*)(ox_+i*1024+lane*16);
          if(__builtin_amdgcn_readfirstlane(f+n)==5u) break; }
        fl=flag_get(ws,fi);
        #pragma unroll
        for(int i=0;i<6;++i){ u32 o=(i<3)?(po0+i*1024):(po1+(i-3)*320); if(i%3==2) o=(g<2)?o:OOB;
          st_sc1(rs,o,__builtin_bit_cast(u32x4,c[1][i]+pr[i])); } }
      ST(5);
      if(!__all(fl==E)) flag_wait(ws,E,fi,fl);
      asm volatile("":::"memory"); ldq();
      u32 pq[4][10];
      {
        ST(11);
        const u32 sg=(g&1)?0x80008000u:0u;
        u32x4 v[16]; const u32 go0=WS_P1+(oh*16+4*w)*3584+lane*16, go1=(lane<18)?go0+3072:OOB;
        #pragma unroll
        for(int i2=0;i2<4;i2+=2){ f32x4 z[2][5];
          #pragma unroll
          for(int t=0;t<2;++t){ const u32x4 a=l0[i2+t]^sg, b=l1[i2+t]^sg; const u32x2 c=l2[i2+t]^sg;
            z[t][0]=f32x4{bflo(a.x),bfhi(a.x),bflo(a.y),bfhi(a.y)}; z[t][1]=f32x4{bflo(a.z),bfhi(a.z),bflo(a.w),bfhi(a.w)};
            z[t][2]=f32x4{bflo(b.x),bfhi(b.x),bflo(b.y),bfhi(b.y)}; z[t][3]=f32x4{bflo(b.z),bfhi(b.z),bflo(b.w),bfhi(b.w)};
            z[t][4]=f32x4{bflo(c.x),bfhi(c.x),bflo(c.y),bfhi(c.y)}; }
          if(i2==0){
            #pragma unroll
            for(int k=0;k<16;++k) v[k]=ld_sc1(rs,((k&3)<3?go0+(k&3)*1024:go1)+(k>>2)*3584);
            signal(); }
          SB();
          softmax20x2(z,pq+i2,[](int){});
          #pragma unroll
          for(int i=i2;i<i2+2;++i){
            #pragma unroll
            for(int k=0;k<5;++k){ const int a=(k<4)?k:8, b=(k<4)?k+4:9;
              auto r=__builtin_amdgcn_permlane32_swap(pq[i][a],pq[i][b],false,false); pq[i][a]=r[0]; pq[i][b]=r[1]; }
            #pragma unroll
            for(int k=0;k<10;++k) asm volatile("":"+v"(pq[i][k])); }
          if(i2==0){ gather4<1>(rs,go0,go1,v); SB(); } }
        asm volatile("":"+v"(q00),"+v"(q01),"+v"(q10),"+v"(q11),"+v"(om),"+v"(ox));
        ST(10);
        gather4<SPIN_MAX>(rs,go0,go1,v);
        ST(13);
        #pragma unroll
        for(int i=0;i<4;++i) *(volatile LAS f32x4*)(pv+8192+i*1024+lane*16)=(__builtin_bit_cast(f32x4,v[i])+__builtin_bit_cast(f32x4,v[4+i]))+(__builtin_bit_cast(f32x4,v[8+i])+__builtin_bit_cast(f32x4,v[12+i]));
        *(volatile LAS u32*)(L+B_F+32+4*w)=1u;
        SB();
      }
      ST(12);
      u32x4 a0[2]; u32 a1[2];
      {
        f32x4 s[2][2]; f32x2 u[2];
        for(int it=0;it<SPIN_MAX;++it){
          const u32x4 f=*(volatile LAS u32x4*)(L+B_F+32);
          #pragma unroll
          for(int k=0;k<4;++k){ const int q=k*B_PVS;
            const f32x4 x0=*(volatile LAS f32x4*)(q00+q), x1=*(volatile LAS f32x4*)(q00+q+256), y0=*(volatile LAS f32x4*)(q10+q), y1=*(volatile LAS f32x4*)(q10+q+80);
            const f32x2 u0=*(volatile LAS f32x2*)(q01+q), u1=*(volatile LAS f32x2*)(q11+q);
            if(k==0){ s[0][0]=x0; s[0][1]=x1; s[1][0]=y0; s[1][1]=y1; u[0]=u0; u[1]=u1; }
            else { s[0][0]+=x0; s[0][1]+=x1; s[1][0]+=y0; s[1][1]+=y1; u[0]+=u0; u[1]+=u1; } }
          if(__builtin_amdgcn_readfirstlane(f.x+f.y+f.z+f.w)==4u) break; }
        #pragma unroll
        for(int i=0;i<2;++i){ a0[i]=pack2(s[i][0],s[i][1]); a1[i]=pk_bf16(u[i].x,u[i].y); } }
      ST(14);
      { f32x4 n[8][2];
        #pragma unroll
        for(int t=0;t<8;++t){ const u32* f=pq[t>>1]+4*(t&1);
          #pragma unroll
          for(int i=0;i<2;++i) n[t][i]=mfma16(a0[i],u32x4{f[0],f[1],f[2],f[3]},Z4); }
        #pragma unroll
        for(int t=0;t<8;++t)
          #pragma unroll
          for(int i=0;i<2;++i) n[t][i]=mfma16(u32x4{a1[i],0u,0u,0u},u32x4{pq[t>>1][8+(t&1)],0u,0u,0u},n[t][i]);
        SB();
        float den[8];
        #pragma unroll
        for(int t=0;t<8;++t) den[t]=__builtin_bit_cast(float,__builtin_amdgcn_ds_bpermute(((lane&15)+16)*4,__builtin_bit_cast(int,n[t][1][0])));
        SB();
        #pragma unroll
        for(int t=0;t<8;++t){ const float r=__builtin_amdgcn_rcpf(rbf(rbf(den[t])+1e-6f));
          const f32x4 m=n[t][0]*r, e=n[t][1]*r;
          __builtin_amdgcn_raw_buffer_store_b64(u32x2{pk_bf16(m[0],m[1]),pk_bf16(m[2],m[3])},rs,om+t*20480,0,16);
          __builtin_amdgcn_raw_buffer_store_b64(u32x2{pk_bf16(e[0],e[1]),pk_bf16(e[2],e[3])},rs,ox+t*20480,0,16); } }
      signal();
      if(rt==15&&grp==7&&w==3){
        for(int it=0;it<SPIN_MAX;++it){ if(__all(flag_get(ws,lane)==E&&flag_get(ws,64+lane)==E&&flag_get(ws,128+lane)==E&&flag_get(ws,192+lane)==E)) break; __builtin_amdgcn_s_sleep(2); }
        if(lane==0) __hip_atomic_store((GAS u64*)ws,E,__ATOMIC_RELAXED,__HIP_MEMORY_SCOPE_AGENT); }
      ST(6);
    }
    if(!doout){ STDUMP(); return; }
    for(int it=0;it<SPIN_MAX;++it){ const u32x2 c=*(volatile LAS u32x2*)(L+B_F+24); if(__builtin_amdgcn_readfirstlane(c.x+c.y)>=(ISQ?8u:12u)) break; __builtin_amdgcn_s_sleep(1); }
    { const u32 ob=WS_O+ot*20480;
      const u32 sm=ob+((lane<32)?((5*(lane>>3)+((lane>>1)&3))*1024+(lane&1)*512+508):((5*((lane-32)>>3)+4)*1024+((lane&7)>>1)*256+244+8*(lane&1)));
      for(int it=0;it<SPIN_MAX;++it){ asm volatile("":::"memory");
        if(__all(__builtin_amdgcn_raw_buffer_load_b32(rs,sm,0,16)!=SENT)) break;
        __builtin_amdgcn_s_sleep(1); }
      ST(15);
      const u32 vo=lane*16;
      for(u32 it=1;it<SPIN_MAX;++it){
        #pragma unroll
        for(int i=0;i<5;++i) dma16x4_sc1((const char*)ws+ob+i*4096,vo,vo,vo,vo,L+OIM+w*20480+i*4096);
        waitv<0>();
        ST(7);
        rbar();
        rbar();
        if(__builtin_amdgcn_readfirstlane(*bad)!=it) break; } }
  }
  #pragma unroll
  for(int i=0;i<2;++i){ const int r=32*i+(tid>>4);
    *(u32x4*)((char*)out+(size_t)(64*tt64+r)*2560+1280+ct*256+(tid&15)*16)=lds_ld<u32x4>(L+r*272+(tid&15)*16); }
  ST(8); STDUMP();
}

constexpr int CODE_LINES=440;
__global__ __launch_bounds__(512,2) void fused(const u16* u,const u16* are,const u16* aim,const u16* bre,const u16* bim,const u16* cre,const u16* cim,
    const u16* logdt,const u16* dskip,const u16* x,const u16* wqkv,const u16* wphi,const u16* wo,u16* out,void* ws){
  extern __shared__ __attribute__((aligned(16))) char smem[];
  lptr L=(lptr)smem;
  const int b=blockIdx.x, X=b&7, idx=b>>3;
  u64 pc; asm volatile("s_getpc_b64 %0":"=s"(pc));
  STINIT(); ST(0);
  const int w=__builtin_amdgcn_readfirstlane(threadIdx.x>>6);
  const bool isa=(idx>=16&&idx<24), isq=idx>=24;
  if(__builtin_expect(!isa,1)){
    u32 warm=0;
    if(idx<16&&threadIdx.x<32&&idx*32+threadIdx.x<CODE_LINES) warm=*(const u32*)((const char*)(pc&~255ull)+(idx*32+threadIdx.x)*128);
    if(idx<16&&w==1&&(threadIdx.x&63)<50) warm=*(const u32*)((const char*)wo+((X*16+idx)*50+(threadIdx.x&63))*128);
    role_b(isq,X,isq?idx-24:idx,L,warm,x,wqkv,wphi,wo,out,ws);
  } else { const SsmIn in={are,aim,bre,bim,cre,cim,logdt}; role_a(X*8+(idx-16),L,u,in,dskip,wo,out,ws); }
}

__global__ void mark(void* ws){ if(threadIdx.x==0) __hip_atomic_store(((GAS u64*)((char*)ws+(100u<<20))),(u64)__builtin_amdgcn_s_memrealtime(),__ATOMIC_RELAXED,__HIP_MEMORY_SCOPE_AGENT); }
__global__ void spin_until(void* ws){ const u64 t0=__hip_atomic_load(((GAS u64*)((char*)ws+(100u<<20))),__ATOMIC_RELAXED,__HIP_MEMORY_SCOPE_AGENT); for(int i=0;i<1000000;++i){ if(__builtin_amdgcn_s_memrealtime()-t0>=7000) break; __builtin_amdgcn_s_sleep(8);} }
extern "C" void kernel_launch(void* const* d_in, const int* in_sizes, int n_in, void* d_out, int out_size, void* d_ws, size_t ws_size, hipStream_t stream){
  (void)hipFuncSetAttribute((const void*)fused,hipFuncAttributeMaxDynamicSharedMemorySize,163840);
  mark<<<1,64,0,stream>>>(d_ws);
  fused<<<256,512,163840,stream>>>((const u16*)d_in[0],(const u16*)d_in[1],(const u16*)d_in[2],(const u16*)d_in[3],(const u16*)d_in[4],(const u16*)d_in[5],(const u16*)d_in[6],
    (const u16*)d_in[7],(const u16*)d_in[8],(const u16*)d_in[9],(const u16*)d_in[10],(const u16*)d_in[11],(const u16*)d_in[12],(u16*)d_out,d_ws);
  spin_until<<<1,64,0,stream>>>(d_ws);
}
```

```cpp
#include <hip/hip_runtime.h>
#include <stdint.h>

typedef unsigned short u16; typedef unsigned u32; typedef unsigned long long u64;
typedef __attribute__((ext_vector_type(4))) short s16x4;
typedef __attribute__((ext_vector_type(8))) _Float16 f16x8;
typedef __attribute__((ext_vector_type(8))) __bf16 bf16x8;
typedef __attribute__((ext_vector_type(2))) __bf16 bf16x2;
typedef __attribute__((ext_vector_type(2))) _Float16 f16x2;
typedef __attribute__((ext_vector_type(2))) float f32x2;
typedef __attribute__((ext_vector_type(4))) float f32x4;
typedef __attribute__((ext_vector_type(16))) float f32x16;
typedef __attribute__((ext_vector_type(2))) unsigned u32x2;
typedef __attribute__((ext_vector_type(3))) unsigned u32x3;
typedef __attribute__((ext_vector_type(4))) unsigned u32x4;
#define LAS __attribute__((address_space(3)))
#define GAS __attribute__((address_space(1)))
#define DI __device__ __forceinline__
typedef LAS char* lptr;
typedef __amdgpu_buffer_rsrc_t rsrc_t;

#define ST(i) do{}while(0)
#define STDUMP() do{}while(0)
#define STINIT() do{}while(0)

DI u32 pk_bf16(float a, float b){ f32x2 v={a,b}; return __builtin_bit_cast(u32,__builtin_convertvector(v,bf16x2)); }
DI u32 pk_f16(f32x2 v){ return __builtin_bit_cast(u32,__builtin_convertvector(v,f16x2)); }
DI float bflo(u32 x){ return __builtin_bit_cast(float,x<<16); }
DI float bfhi(u32 x){ return __builtin_bit_cast(float,x&0xffff0000u); }
DI float rbf(float a){ return bflo(pk_bf16(a,a)); }
template<class T> DI T lds_ld(lptr p){ return *(LAS T*)p; }
template<class T> DI void lds_st(lptr p, T v){ *(LAS T*)p=v; }
DI u32x2 lds_ld64(lptr p){ return *(volatile LAS u32x2*)p; }
DI u32x2 lds_tr(lptr p){ return __builtin_bit_cast(u32x2,__builtin_amdgcn_ds_read_tr16_b64_v4i16((LAS s16x4*)p)); }
DI f32x4 mfma16(u32x4 a,u32x4 b,f32x4 c){ return __builtin_amdgcn_mfma_f32_16x16x32_bf16(__builtin_bit_cast(bf16x8,a),__builtin_bit_cast(bf16x8,b),c,0,0,0); }
DI u32x4 cat(u32x2 a,u32x2 b){ u32x4 r={a.x,a.y,b.x,b.y}; return r; }
DI u32 p_ll(u32 a,u32 b){ return __builtin_amdgcn_perm(b,a,0x05040100u); }
DI u32 p_lh(u32 a,u32 b){ return __builtin_amdgcn_perm(b,a,0x07060100u); }
DI u32 p_hl(u32 a,u32 b){ return __builtin_amdgcn_perm(b,a,0x05040302u); }
DI u32 p_hh(u32 a,u32 b){ return __builtin_amdgcn_perm(b,a,0x07060302u); }
#define SB() __builtin_amdgcn_sched_barrier(0)
DI void rbar(){ asm volatile("":::"memory"); __builtin_amdgcn_s_barrier(); asm volatile("":::"memory"); }
DI void bar(){ asm volatile("s_waitcnt lgkmcnt(0)":::"memory"); __builtin_amdgcn_s_barrier(); asm volatile("":::"memory"); }
template<int N> DI void waitv(){ asm volatile("s_waitcnt vmcnt(%0)"::"i"(N):"memory"); }
constexpr f32x4 Z4={0.f,0.f,0.f,0.f};
DI float wave_max(float m){
  m=fmaxf(m,__builtin_bit_cast(float,__builtin_amdgcn_update_dpp(0,__builtin_bit_cast(int,m),0xB1,0xf,0xf,true)));
  m=fmaxf(m,__builtin_bit_cast(float,__builtin_amdgcn_update_dpp(0,__builtin_bit_cast(int,m),0x4E,0xf,0xf,true)));
  m=fmaxf(m,__builtin_bit_cast(float,__builtin_amdgcn_update_dpp(0,__builtin_bit_cast(int,m),0x141,0xf,0xf,true)));
  m=fmaxf(m,__builtin_bit_cast(float,__builtin_amdgcn_update_dpp(0,__builtin_bit_cast(int,m),0x140,0xf,0xf,true)));
  int x=__builtin_bit_cast(int,m);
  float a=__builtin_bit_cast(float,__builtin_amdgcn_readlane(x,0)), b=__builtin_bit_cast(float,__builtin_amdgcn_readlane(x,16));
  float c=__builtin_bit_cast(float,__builtin_amdgcn_readlane(x,32)), d=__builtin_bit_cast(float,__builtin_amdgcn_readlane(x,48));
  return fmaxf(fmaxf(a,b),fmaxf(c,d)); }

constexpr u32 WS_FLAG=256, WS_P1=4096, WS_Q=WS_P1+32*16*3584, WS_O=WS_Q+32*2048*40, WS_END=WS_O+128*20*1024;
constexpr u32 SENT=0xFFFFFFFFu, OOB=0x80000000u;
constexpr int SPIN_MAX=1<<17;
DI void dma16(const void* sb, u32 voff, lptr l){ u32 keep;
  asm volatile("s_mov_b32 %0, m0\n\ts_mov_b32 m0, %3\n\ts_nop 0\n\tglobal_load_lds_dwordx4 %1, %2\n\ts_mov_b32 m0, %0":"=&s"(keep):"v"(voff),"s"(sb),"s"((u32)(uintptr_t)l):"memory"); }
#define DMA4_BODY(SC) asm volatile("s_mov_b32 %0, m0\n\ts_mov_b32 m0, %6\n\ts_nop 0\n\tglobal_load_lds_dwordx4 %1, %5" SC "\n\tglobal_load_lds_dwordx4 %2, %5 offset:1024" SC \
    "\n\tglobal_load_lds_dwordx4 %3, %5 offset:2048" SC "\n\tglobal_load_lds_dwordx4 %4, %5 offset:3072" SC "\n\ts_mov_b32 m0, %0" \
    :"=&s"(keep):"v"(v0),"v"(v1),"v"(v2),"v"(v3),"s"(sb),"s"((u32)(uintptr_t)l):"memory")
DI void dma16x4(const void* sb,u32 v0,u32 v1,u32 v2,u32 v3,lptr l){ u32 keep; DMA4_BODY(""); }
DI void dma16x4_sc1(const void* sb,u32 v0,u32 v1,u32 v2,u32 v3,lptr l){ u32 keep; DMA4_BODY(" sc1"); }
template<int N> DI void dma16xn(const void* sb,u32 v0,u32 v1,u32 v2,lptr l){ u32 keep;
  if(N==2) asm volatile("s_mov_b32 %0, m0\n\ts_mov_b32 m0, %4\n\ts_nop 0\n\tglobal_load_lds_dwordx4 %1, %3\n\tglobal_load_lds_dwordx4 %2, %3 offset:1024\n\ts_mov_b32 m0, %0"
    :"=&s"(keep):"v"(v0),"v"(v1),"s"(sb),"s"((u32)(uintptr_t)l):"memory");
  else asm volatile("s_mov_b32 %0, m0\n\ts_mov_b32 m0, %5\n\ts_nop 0\n\tglobal_load_lds_dwordx4 %1, %4\n\tglobal_load_lds_dwordx4 %2, %4 offset:1024\n\tglobal_load_lds_dwordx4 %3, %4 offset:2048\n\ts_mov_b32 m0, %0"
    :"=&s"(keep):"v"(v0),"v"(v1),"v"(v2),"s"(sb),"s"((u32)(uintptr_t)l):"memory"); }
DI u32x4 ld_sc1(rsrc_t rs,u32 off){ return __builtin_amdgcn_raw_buffer_load_b128(rs,off,0,16); }
DI void st_sc1(rsrc_t rs,u32 off,u32x4 v){ __builtin_amdgcn_raw_buffer_store_b128(v,rs,off,0,16); }
DI void gather4(rsrc_t rs,u32 off,u32 last,u32x4* v){
  for(int it=0;it<SPIN_MAX;++it){
    u32 todo=0;
    #pragma unroll
    for(int k=0;k<4;++k){ u32 m=0;
      #pragma unroll
      for(int i=0;i<4;++i) m=max(m,max(v[4*k+i].x,v[4*k+i].z));
      if(__any(m==SENT)) todo|=1u<<k; }
    if(!todo) break;
    asm volatile("":::"memory");
    #pragma unroll
    for(int k=0;k<4;++k) if(todo>>k&1){
      #pragma unroll
      for(int i=0;i<4;++i) v[4*k+i]=ld_sc1(rs,((i<3)?off+i*1024:last)+k*3584); } } }
DI void flag_set(void* ws,int i,u64 E){ __hip_atomic_store((GAS u64*)((char*)ws+WS_FLAG)+i,E,__ATOMIC_RELAXED,__HIP_MEMORY_SCOPE_AGENT); }
DI u64 flag_get(const void* ws,int i){ return __hip_atomic_load((const GAS u64*)((const char*)ws+WS_FLAG)+i,__ATOMIC_RELAXED,__HIP_MEMORY_SCOPE_AGENT); }
DI void flag_wait(const void* ws,u64 E,int idx,u64 f){
  for(int it=0;it<SPIN_MAX;++it){ if(__all(f==E)) break; __builtin_amdgcn_s_sleep(2); f=flag_get(ws,idx); } }

constexpr int A_UB=4576, A_US=9120, A_KB=4224, A_KS=8704;

DI f32x2 cmul(f32x2 a, f32x2 b){ f32x2 sw={-b.y,b.x}; return __builtin_elementwise_fma(a.yy,sw,a.xx*b); }
DI f32x2 cexp_e(float re2, float rev, float e){
  float m=__builtin_amdgcn_exp2f(re2*e); float a=__builtin_amdgcn_fractf(rev*e);
  f32x2 r={m*__builtin_amdgcn_cosf(a), m*__builtin_amdgcn_sinf(a)}; return r; }
struct SsmP { u32 are,aim,bre,bim,cre,cim,ld; float dt; };
struct SsmIn { const u16 *are,*aim,*bre,*bim,*cre,*cim,*logdt; };
DI SsmP load_ssm(const SsmIn& in, int ch, int lane){
  SsmP p; int o=ch*64+lane;
  p.are=((const u32*)in.are)[o]; p.aim=((const u32*)in.aim)[o]; p.bre=((const u32*)in.bre)[o];
  p.bim=((const u32*)in.bim)[o]; p.cre=((const u32*)in.cre)[o]; p.cim=((const u32*)in.cim)[o];
  p.ld=in.logdt[ch];
  return p; }
DI f32x2 ssm_w(const SsmP& p, int half){
  f32x2 b={half?bfhi(p.bre):bflo(p.bre), half?bfhi(p.bim):bflo(p.bim)};
  f32x2 c={half?bfhi(p.cre):bflo(p.cre), half?bfhi(p.cim):bflo(p.cim)};
  return cmul(c,b*p.dt); }
DI void ssm_prep(SsmP& p, float& s, float& inv){
  asm volatile("":"+v"(p.ld));
  p.dt=rbf(expf(bflo(p.ld)));
  f32x2 w0=ssm_w(p,0), w1=ssm_w(p,1);
  float m=wave_max(fmaxf(fmaxf(fabsf(w0.x),fabsf(w0.y)),fmaxf(fabsf(w1.x),fabsf(w1.y))));
  int e=__builtin_amdgcn_frexp_expf(m); e=max(-100,min(100,e));
  s=__builtin_amdgcn_ldexpf(1.0f,-e); inv=__builtin_amdgcn_ldexpf(1.0f,e); }
DI void kgen_half(const SsmP& p, int half, float s, lptr tab, int lane, f32x16& acc){
  const float are=(half?bfhi(p.are):bflo(p.are))*p.dt, aim=(half?bfhi(p.aim):bflo(p.aim))*p.dt;
  const float re2=are*1.4426950408889634f, rev=aim*0.15915494309189535f;
  lptr wr=tab+lane*4; lptr rd=tab+(lane&31)*272+(lane>>5)*16;
  { f32x2 e8=cexp_e(re2,rev,8.f), e16=cmul(e8,e8);
    f32x2 a={1.f,0.f}, b=e8;
    #pragma unroll
    for(int j=0;j<32;j+=2){ lds_st<u32>(wr+j*272,pk_f16(a)); lds_st<u32>(wr+(j+1)*272,pk_f16(b)); if(j<30){ a=(j==0)?e16:cmul(a,e16); b=cmul(b,e16);} } }
  u32x4 bf[8];
  #pragma unroll
  for(int k=0;k<8;++k) bf[k]=lds_ld<u32x4>(rd+k*32);
  { f32x2 w=ssm_w(p,half)*s; w.y=-w.y;
    f32x2 e1=cexp_e(re2,-rev,1.f), g=cexp_e(re2,-rev,256.f);
    f32x2 g2=cmul(g,g), g3=cmul(g2,g), g4=cmul(g2,g2);
    f32x2 x[8]; x[0]=w;
    #pragma unroll
    for(int i=1;i<8;++i) x[i]=cmul(x[i-1],e1);
    #pragma unroll
    for(int i=0;i<8;++i) lds_st<u32>(wr+i*272,pk_f16(x[i]));
    #pragma unroll
    for(int i=0;i<4;++i) lds_st<u32>(wr+(8+i)*272,pk_f16(cmul(x[2*i],g)));
    lds_st<u32>(wr+12*272,pk_f16(cmul(x[0],g2))); lds_st<u32>(wr+13*272,pk_f16(cmul(x[4],g2)));
    lds_st<u32>(wr+14*272,pk_f16(cmul(x[0],g3))); lds_st<u32>(wr+15*272,pk_f16(cmul(x[4],g3)));
    f32x2 y=cmul(w,g4);
    #pragma unroll
    for(int i=0;i<5;++i){ lds_st<u32>(wr+(16+i)*272,pk_f16(y)); if(i<4) y=cmul(y,g); } }
  #pragma unroll
  for(int k=0;k<8;++k){ u32x4 af=lds_ld<u32x4>(rd+k*32);
    acc=__builtin_amdgcn_mfma_f32_32x32x16_f16(__builtin_bit_cast(f16x8,af),__builtin_bit_cast(f16x8,bf[k]),acc,0,0,0); }
}
DI void k_finish(const float* v, lptr ka, int lane){
  lptr ks=ka; lptr kb=ka+A_KB;
  { const int j=lane&31, h=lane>>5;
    lds_st<u32x2>(ks+(8*j+4*h)*2,u32x2{pk_bf16(v[0],v[1]),pk_bf16(v[2],v[3])});
    u32x2 b={pk_bf16(v[4],v[5]),pk_bf16(v[6],v[7])};
    u32 c0=pk_bf16(v[8],v[9]), c1=pk_bf16(v[10],v[11]);
    if(h==0){ lds_st<u32x2>(ks+(256+4*j)*2,b);
      lds_st<u16>(ks+(512+j)*2,(u16)c0); lds_st<u16>(ks+(544+j)*2,(u16)(c0>>16));
      lds_st<u16>(ks+(576+j)*2,(u16)c1); lds_st<u16>(ks+(608+j)*2,(u16)(c1>>16));
    } else { lds_st<u32>(ks+(384+2*j)*2,b.x); lds_st<u32>(ks+(448+2*j)*2,b.y); if(j==0) lds_st<u16>(ks+640*2,(u16)c0); } }
  const int M=lane; const bool lo=M<32;
  u32x4 v0=lds_ld<u32x4>(ks+(M&31)*16); u32x2 v1=lds_ld<u32x2>(ks+(128+4*(M|32))*2);
  u32 v4=lds_ld<u16>(ks+(132+4*(M|32))*2);
  u32 pv=lds_ld<u16>(ks+(lo?max(8*M-1,0):((M==32)?255:(128+4*M)))*2); if(M==0) pv=0;
  u32 ab=lds_ld<u32>(ks+(384+2*M)*2), c2=lds_ld<u16>(ks+(386+2*M)*2);
  u32 a3[2],b3[2];
  #pragma unroll
  for(int i=0;i<2;++i){ a3[i]=lds_ld<u16>(ks+(512+M+64*i)*2); b3[i]=lds_ld<u16>(ks+(513+M+64*i)*2); }
  asm volatile("":"+v"(v0),"+v"(v1),"+v"(v4),"+v"(pv),"+v"(ab),"+v"(c2),"+v"(a3[0]),"+v"(a3[1]),"+v"(b3[0]),"+v"(b3[1])::"memory");
  { u32x4 A,B;
    if(lo){ B.x=p_hl(v0.w,v0.w); B.y=p_hl(v0.z,v0.z); B.z=p_hl(v0.y,v0.y); B.w=p_hl(v0.x,v0.x);
      A.x=p_lh(v0.w,v0.z); A.y=p_lh(v0.z,v0.y); A.z=p_lh(v0.y,v0.x); A.w=p_ll(v0.x,pv);
    } else { B.x=p_lh(v4,v1.y); B.y=p_ll(v1.y,v1.y); B.z=p_lh(v1.y,v1.x); B.w=p_ll(v1.x,v1.x);
      A.x=p_hl(v1.y,v1.y); A.y=B.y; A.z=p_hl(v1.x,v1.x); A.w=p_ll(v1.x,pv); }
    lds_st<u32x4>(ka+(255-M)*16,A); lds_st<u32x4>(kb+(255-M)*16,B); }
  { u32x4 A,B; B.x=p_ll(c2,c2); B.y=p_hh(ab,ab); B.z=p_hl(ab,ab); B.w=p_ll(ab,ab);
    A.x=p_lh(c2,ab); A.y=B.y; A.z=B.w; A.w=B.w;
    lds_st<u32x4>(ka+(191-M)*16,A); lds_st<u32x4>(kb+(191-M)*16,B); }
  #pragma unroll
  for(int i=0;i<2;++i){ u32 a=a3[i], b=b3[i], c=(M&1)?b:a; u32x4 A,B;
    B.x=p_ll(b,b); B.y=p_ll(b,c); B.z=p_ll(a,a); B.w=B.z;
    A.x=B.x; A.y=p_ll(c,a); A.z=B.z; A.w=B.z;
    lds_st<u32x4>(ka+(127-64*i-M)*16,A); lds_st<u32x4>(kb+(127-64*i-M)*16,B); }
  if(lane<8) lds_st<u32x4>(((lane&4)?kb:ka)+(256+(lane&3))*16,u32x4{0,0,0,0});
}
DI void conv_steps(lptr ua, lptr ka, int lane, f32x4* acc){
  constexpr int NS=8, s0=0, kbo=A_KB;
  const int a=lane&15, g=lane>>4;
  lptr up=ua+((a&1)?0:A_UB)+(32*(a>>1)+8*g+32*s0)*2;
  lptr kp=ka+((a&1)?kbo:0)+(28-4*(a>>1)+8*g+32*s0)*2;
  u32x4 A[2][8],B[2][8];
  auto ldA=[&](int s,int q){ A[s&1][q]=cat(lds_ld64(kp+(1792-256*q+32*s)*2),lds_ld64(kp+(1792-256*q+32*s)*2+8)); };
  auto ldB=[&](int s,int q){ B[s&1][q]=lds_ld<u32x4>(up+(256*q+32*s)*2); };
  #pragma unroll
  for(int q=0;q<8;++q){ ldB(0,q); ldA(0,q); }
  #pragma unroll
  for(int c=0;c<8;++c) acc[c]=Z4;
  SB();
  #pragma unroll
  for(int s=0;s<NS;++s){ int n=0; u32x2 lo;
    #pragma unroll
    for(int q=0;q<8;++q){
      #pragma unroll
      for(int c=q;c<8;++c){
        acc[c]=mfma16(A[s&1][q],B[s&1][c-q],acc[c]);
        SB();
        if(s+1<NS){
          if(n==0) ldB(s+1,0);
          else if(n<3){ if(n==1) lo=lds_ld64(kp+(1792+32*(s+1))*2); else A[(s+1)&1][0]=cat(lo,lds_ld64(kp+(1792+32*(s+1))*2+8)); }
          else if(n<10) ldB(s+1,n-2);
          else if(n<24){ const int j=1+((n-10)>>1); if(!(n&1)) lo=lds_ld64(kp+(1792-256*j+32*(s+1))*2); else A[(s+1)&1][j]=cat(lo,lds_ld64(kp+(1792-256*j+32*(s+1))*2+8)); } }
        SB();
        ++n; } } }
}
DI u32 fin2(float y0,float y1,u32 u,float dsk){
  u32 y=pk_bf16(y0,y1), s=pk_bf16(bflo(u)*dsk,bfhi(u)*dsk);
  return pk_bf16(bflo(y)+bflo(s),bfhi(y)+bfhi(s)); }
DI void conv_job(const bool BIG, int wi, int ch0, int rot, lptr L, SsmP& p0, u32 dsk, const u16* u, const u16* wo, u16* out, void* ws){
  const int lane=threadIdx.x&63, t=64*wi+lane;
  lptr us=L+wi*A_US, kw=L+(BIG?8:4)*A_US+wi*A_KS;
  if(lane<56) lds_st<u32x4>(us+(lane<28?lane*16:A_UB+(lane-28)*16),u32x4{0,0,0,0});
  if(lane==56) lds_st<u32>(us+A_UB+448,0u);
  u32x4 um[4];
  const int ur=BIG?((t+64*rot)&511):t;
  const char* up=(const char*)u+(size_t)ur*4*1280+2*ch0;
  if(BIG){
    #pragma unroll
    for(int r=0;r<4;++r) um[r]=*(const u32x4*)(up+r*1280); }
  ST(1);
  { float s,inv; ssm_prep(p0,s,inv); f32x16 acc={};
    #pragma unroll 1
    for(int h=0;h<2;++h){
      if(!BIG&&h==1){
        for(int it=0;it<SPIN_MAX;++it){ if(__builtin_amdgcn_readfirstlane(*(volatile LAS u32*)(L+163168+84))>=4u) break; __builtin_amdgcn_s_sleep(2); }
        asm volatile("":::"memory");
        #pragma unroll
        for(int r=0;r<4;++r) um[r]=cat(*(const u32x2*)(up+r*1280),*(const u32x2*)(up+(r+1024)*1280)); }
      kgen_half(p0,h,s,kw,lane,acc); }
    float v[12];
    #pragma unroll
    for(int r=0;r<12;++r) v[r]=acc[r]*inv;
    k_finish(v,kw,lane); }
  ST(2);
  u32 pf[4]={0,0,0,0};
  if(BIG&&wi<4){
    const char* wp=(const char*)wo+rot*102400+t*128;
    #pragma unroll
    for(int i=0;i<4;++i) pf[i]=*(const u32*)(wp+((i<3||t<32)?i*32768:0)); }
  #pragma unroll
  for(int c=0;c<4;++c){
    lptr d=L+(224+4*ur)*2+(BIG?2*c*A_US:(2*(c&1)*A_US+(c>>1)*2048)); const bool last=BIG?(ur==511):(c>=2&&ur==255);
    const u32x2 e={p_ll(um[0][c],um[1][c]),p_ll(um[2][c],um[3][c])}, o={p_hh(um[0][c],um[1][c]),p_hh(um[2][c],um[3][c])};
    lds_st<u32x2>(d,e); lds_st<u32x2>(d+A_US,o);
    lds_st<u32>(d+A_UB+4,e.x); lds_st<u32>(d+A_US+A_UB+4,o.x);
    if(!last){ lds_st<u32>(d+A_UB+8,e.y); lds_st<u32>(d+A_US+A_UB+8,o.y); } }
  bar();
  ST(3);
  f32x4 acc[8];
  conv_steps(us,kw,lane,acc);
  ST(4);
  { const int tl=32*((lane&15)>>1)+8*(lane>>4)+2*(lane&1); asm volatile("":"+v"(dsk)); const float d=bflo(dsk);
    u32 uu[16];
    #pragma unroll
    for(int c=0;c<8;++c){ uu[2*c]=lds_ld<u32>(us+(224+256*c+tl)*2); uu[2*c+1]=lds_ld<u32>(us+(228+256*c+tl)*2); }
    #pragma unroll
    for(int c=0;c<8;++c){ lds_st<u32>(kw+(256*c+tl)*2,fin2(acc[c][0],acc[c][1],uu[2*c],d)); lds_st<u32>(kw+(256*c+tl+4)*2,fin2(acc[c][2],acc[c][3],uu[2*c+1],d)); } }
  asm volatile(""::"v"(pf[0]),"v"(pf[1]),"v"(pf[2]),"v"(pf[3]));
  bar();
  ST(5);
  { lptr yp=L+(BIG?8:4)*A_US+((lane&15)>>2)*A_KS+(256*wi+16*(lane>>4)+4*(lane&3))*2;
    char* op=(char*)out+(size_t)(256*wi+lane)*2560+2*ch0;
    #pragma unroll
    for(int i=0;i<4;++i){
      if(BIG) *(u32x4*)(op+(size_t)i*64*2560)=cat(lds_tr(yp+i*128),lds_tr(yp+4*A_KS+i*128));
      else { *(u32x2*)(op+(size_t)i*64*2560)=lds_tr(yp+i*128); *(u32x2*)(op+(size_t)(i*64+1024)*2560)=lds_tr(yp+2048+i*128); } } }
  ST(7);
}

constexpr int B_ST=36864, B_PV=2*B_ST, B_PVS=18432, B_WP=4*B_ST, B_Z=B_WP+6400, B_F=163168;
DI float max3(float a,float b,float c){ return __builtin_fmaxf(__builtin_fmaxf(a,b),c); }
template<class H> DI void softmax20x2(const f32x4 (*z)[5], u32 (*o)[10], H hook){
  const float c=1.4426950408889634f; float mc[2];
  { float m0[2],m1[2],m2[2];
    #pragma unroll
    for(int t=0;t<2;++t){ m0[t]=max3(z[t][0][0],z[t][0][1],z[t][0][2]); m1[t]=max3(z[t][0][3],z[t][1][0],z[t][1][1]); m2[t]=max3(z[t][1][2],z[t][1][3],z[t][2][0]); }
    #pragma unroll
    for(int t=0;t<2;++t){ m0[t]=max3(m0[t],z[t][2][1],z[t][2][2]); m1[t]=max3(m1[t],z[t][2][3],z[t][3][0]); m2[t]=max3(m2[t],z[t][3][1],z[t][3][2]); }
    #pragma unroll
    for(int t=0;t<2;++t){ m0[t]=max3(m0[t],z[t][3][3],z[t][4][0]); m1[t]=max3(m1[t],z[t][4][1],z[t][4][2]); m2[t]=__builtin_fmaxf(m2[t],z[t][4][3]); }
    #pragma unroll
    for(int t=0;t<2;++t) mc[t]=max3(m0[t],m1[t],m2[t])*-c; }
  SB();
  hook(0);
  f32x2 e[2][10];
  #pragma unroll
  for(int i=0;i<10;++i)
    #pragma unroll
    for(int t=0;t<2;++t) e[t][i]=__builtin_elementwise_fma(f32x2{z[t][i>>1][(i&1)*2],z[t][i>>1][(i&1)*2+1]},f32x2{c,c},f32x2{mc[t],mc[t]});
  SB();
  hook(1);
  #pragma unroll
  for(int i=0;i<10;++i)
    #pragma unroll
    for(int t=0;t<2;++t) e[t][i]=f32x2{__builtin_amdgcn_exp2f(e[t][i].x),__builtin_amdgcn_exp2f(e[t][i].y)};
  SB();
  hook(2);
  float r[2];
  { f32x2 s[2][5];
    #pragma unroll
    for(int i=0;i<5;++i)
      #pragma unroll
      for(int t=0;t<2;++t) s[t][i]=e[t][2*i]+e[t][2*i+1];
    #pragma unroll
    for(int t=0;t<2;++t){ s[t][0]+=s[t][1]; s[t][2]+=s[t][3]; }
    #pragma unroll
    for(int t=0;t<2;++t){ s[t][0]+=s[t][4]; }
    #pragma unroll
    for(int t=0;t<2;++t){ s[t][0]+=s[t][2]; }
    #pragma unroll
    for(int t=0;t<2;++t) r[t]=__builtin_amdgcn_rcpf(s[t][0].x+s[t][0].y); }
  SB();
  #pragma unroll
  for(int i=0;i<10;++i)
    #pragma unroll
    for(int t=0;t<2;++t) e[t][i]*=r[t];
  SB();
  #pragma unroll
  for(int i=0;i<10;++i)
    #pragma unroll
    for(int t=0;t<2;++t) o[t][i]=pk_bf16(e[t][i].x,e[t][i].y);
  SB();
}
DI u32x4 pack2(f32x4 a,f32x4 b){ u32x4 r={pk_bf16(a[0],a[1]),pk_bf16(a[2],a[3]),pk_bf16(b[0],b[1]),pk_bf16(b[2],b[3])}; return r; }
struct Stream { u32 xo[4], wv[5]; int s0, qw; const u16 *x, *w; lptr L;
  DI void init(int q,int li,int rt,int grp,int qw_,int lane,const u16* x_,const u16* w_,lptr L_){
    qw=qw_; x=x_; w=w_; L=L_;
    s0=((0x8530>>((((li>>(2-q))+((li&(3>>q))<<q))&3)*4))&15)+q;
    #pragma unroll
    for(int i=0;i<4;++i){ const u32 r=(4*qw+i)*8+(lane>>3); xo[i]=(rt*128+r)*1280u+(((lane&7)^((r>>1)&7))<<4)-1024u*i; }
    const u32 wlo=(160<<q)*grp+1280*(1-q), dh=1120*(1-q);
    #pragma unroll
    for(int i=0;i<5;++i){ const u32 c=(5*qw+i)*64+lane, r=(c*3277u)>>16, pos=c-r*20, sc=pos^(((r>>3)&1)<<1);
      wv[i]=r*3840u+wlo+(sc<10?0u:dh)+16*sc-1024u*(i&3); } }
  DI void issue(int s) const { int ss=s+s0; if(ss>=10) ss-=10; lptr d=L+(s&3)*B_ST;
    const char* xb=(const char*)x+ss*128; const char* wb=(const char*)w+(size_t)ss*64*3840;
    dma16x4(xb,xo[0],xo[1],xo[2],xo[3],d+4*qw*1024); dma16x4(wb,wv[0],wv[1],wv[2],wv[3],d+16384+5*qw*1024); dma16(wb,wv[4],d+16384+(5*qw+4)*1024); } };

template<bool GATHER> DI void o_phase(lptr L,rsrc_t rs,void* ws,u64 E,int w,int oh,int tq,int fi,u64 fl){
  const int lane=threadIdx.x&63, g=lane>>4, r=lane&15;
  if(GATHER){ if(!__all(fl==E)) flag_wait(ws,E,fi,fl); *(volatile LAS u32*)(L+B_F+128+4*w)=1u; }
  else for(int it=0;it<SPIN_MAX;++it){ if(__builtin_amdgcn_readfirstlane(*(volatile LAS u32*)(L+B_F+128+4*(w&3)))) break; __builtin_amdgcn_s_sleep(2); }
  asm volatile("":::"memory");
  u32x4 l0[2],l1[2]; u32x2 l2[2];
  { const u32 qo=WS_Q+(oh*2048+512*tq+128*(w&3)+64*(w>>2)+16*(g>>1)+r)*40;
    #pragma unroll
    for(int i=0;i<2;++i){ l0[i]=ld_sc1(rs,qo+i*1280); l1[i]=ld_sc1(rs,qo+i*1280+16); l2[i]=__builtin_amdgcn_raw_buffer_load_b64(rs,qo+i*1280+32,0,16); } }
  lptr q00,q01,q10,q11; u32 om,ox;
  { const int pc=5*(g&1)+2*(g>>1), pl=5*(g&1)+4, d1=min(r,4); lptr b=L+B_PV+8192;
    q00=b+(16*pc+r)*16; q01=b+(16*pl+r)*16+8*(g>>1); q10=b+(160+5*pc+d1)*16; q11=b+(160+5*pl+d1)*16+8*(g>>1);
    const u32 fb=WS_O+((32*tq+8*(w&3)+4*(w>>2))*20+5*(oh>>3))*1024+r*16;
    om=fb+((oh&7)>>1)*1024+(2*(oh&1)+(g>>1))*256+8*(g&1); ox=(g==0)?(fb+4096+((oh&7)>>1)*256+8*(oh&1)):OOB; }
  if(GATHER) asm volatile("":"+v"(q00),"+v"(q01),"+v"(q10),"+v"(q11));
  asm volatile("":"+v"(om),"+v"(ox));
  ST(11);
  u32x4 v[16]; const u32 go0=WS_P1+(oh*16+4*w)*3584+lane*16, go1=(lane<18)?go0+3072:OOB;
  u32 pq[2][10];
  { const u32 sg=(g&1)?0x80008000u:0u; f32x4 z[2][5];
    #pragma unroll
    for(int t=0;t<2;++t){ const u32x4 a=l0[t]^sg, b=l1[t]^sg; const u32x2 c=l2[t]^sg;
      z[t][0]=f32x4{bflo(a.x),bfhi(a.x),bflo(a.y),bfhi(a.y)}; z[t][1]=f32x4{bflo(a.z),bfhi(a.z),bflo(a.w),bfhi(a.w)};
      z[t][2]=f32x4{bflo(b.x),bfhi(b.x),bflo(b.y),bfhi(b.y)}; z[t][3]=f32x4{bflo(b.z),bfhi(b.z),bflo(b.w),bfhi(b.w)};
      z[t][4]=f32x4{bflo(c.x),bfhi(c.x),bflo(c.y),bfhi(c.y)}; }
    SB();
    softmax20x2(z,pq,[&](int k){ if(GATHER&&k==1){
      #pragma unroll
      for(int i=0;i<16;++i) v[i]=ld_sc1(rs,((i&3)<3?go0+(i&3)*1024:go1)+(i>>2)*3584); } });
    #pragma unroll
    for(int i=0;i<2;++i){
      #pragma unroll
      for(int k=0;k<5;++k){ const int a=(k<4)?k:8, b=(k<4)?k+4:9;
        auto s=__builtin_amdgcn_permlane32_swap(pq[i][a],pq[i][b],false,false); pq[i][a]=s[0]; pq[i][b]=s[1]; }
      #pragma unroll
      for(int k=0;k<10;++k) asm volatile("":"+v"(pq[i][k])); } }
  ST(12);
  if(GATHER){
    gather4(rs,go0,go1,v);
    ST(13);
    #pragma unroll
    for(int i=0;i<4;++i) *(volatile LAS f32x4*)(L+B_PV+w*B_PVS+8192+i*1024+lane*16)=(__builtin_bit_cast(f32x4,v[i])+__builtin_bit_cast(f32x4,v[4+i]))+(__builtin_bit_cast(f32x4,v[8+i])+__builtin_bit_cast(f32x4,v[12+i]));
    *(volatile LAS u32*)(L+B_F+32+4*w)=1u;
  }
  u32x4 a0[2]; u32 a1[2];
  lptr mb=L+B_PV+(w&3)*B_PVS+12288+lane*16;
  if(GATHER){ f32x4 s[2][2]; f32x2 u[2];
    for(int it=0;it<SPIN_MAX;++it){
      const u32x4 f=*(volatile LAS u32x4*)(L+B_F+32);
      #pragma unroll
      for(int k=0;k<4;++k){ const int q=k*B_PVS;
        const f32x4 x0=*(volatile LAS f32x4*)(q00+q), x1=*(volatile LAS f32x4*)(q00+q+256), y0=*(volatile LAS f32x4*)(q10+q), y1=*(volatile LAS f32x4*)(q10+q+80);
        const f32x2 u0=*(volatile LAS f32x2*)(q01+q), u1=*(volatile LAS f32x2*)(q11+q);
        if(k==0){ s[0][0]=x0; s[0][1]=x1; s[1][0]=y0; s[1][1]=y1; u[0]=u0; u[1]=u1; }
        else { s[0][0]+=x0; s[0][1]+=x1; s[1][0]+=y0; s[1][1]+=y1; u[0]+=u0; u[1]+=u1; } }
      if(__builtin_amdgcn_readfirstlane(f.x+f.y+f.z+f.w)==4u) break; }
    #pragma unroll
    for(int i=0;i<2;++i){ a0[i]=pack2(s[i][0],s[i][1]); a1[i]=pk_bf16(u[i].x,u[i].y); }
    *(volatile LAS u32x4*)mb=a0[0]; *(volatile LAS u32x4*)(mb+1024)=a0[1]; *(volatile LAS u32x2*)(mb+2048)=u32x2{a1[0],a1[1]};
    *(volatile LAS u32*)(L+B_F+64+4*w)=1u;
  } else for(int it=0;it<SPIN_MAX;++it){
    const u32 f=*(volatile LAS u32*)(L+B_F+64+4*(w&3));
    a0[0]=*(volatile LAS u32x4*)mb; a0[1]=*(volatile LAS u32x4*)(mb+1024); const u32x2 t=*(volatile LAS u32x2*)(mb+2048); a1[0]=t.x; a1[1]=t.y;
    if(__builtin_amdgcn_readfirstlane(f)) break;
    __builtin_amdgcn_s_sleep(1); }
  ST(14);
  f32x4 n[4][2];
  #pragma unroll
  for(int i=1;i>=0;--i){
    #pragma unroll
    for(int t=0;t<4;++t){ const u32* f=pq[t>>1]+4*(t&1); n[t][i]=mfma16(a0[i],u32x4{f[0],f[1],f[2],f[3]},Z4); }
    #pragma unroll
    for(int t=0;t<4;++t) n[t][i]=mfma16(u32x4{a1[i],0u,0u,0u},u32x4{pq[t>>1][8+(t&1)],0u,0u,0u},n[t][i]); }
  SB();
  float den[4];
  #pragma unroll
  for(int t=0;t<4;++t) den[t]=__builtin_bit_cast(float,__builtin_amdgcn_ds_bpermute((r+16)*4,__builtin_bit_cast(int,n[t][1][0])));
  #pragma unroll
  for(int t=0;t<4;++t) den[t]=__builtin_amdgcn_rcpf(rbf(rbf(den[t])+1e-6f));
  SB();
  #pragma unroll
  for(int t=0;t<4;++t){ const f32x4 m=n[t][0]*den[t], e=n[t][1]*den[t];
    __builtin_amdgcn_raw_buffer_store_b64(u32x2{pk_bf16(m[0],m[1]),pk_bf16(m[2],m[3])},rs,om+t*20480,0,16);
    __builtin_amdgcn_raw_buffer_store_b64(u32x2{pk_bf16(e[0],e[1]),pk_bf16(e[2],e[3])},rs,ox+t*20480,0,16); }
}

template<class F> DI bool role_b(const bool ISQ,int X,int li,int rt,int grp,int w,const Stream& st,lptr L,F params,const u16* wo,u16* out,void* ws){
  const int tid=threadIdx.x, lane=tid&63, g=lane>>4;
  const int T=ISQ?16+li:li; const bool doout=T<20; const int tt64=4*X+T/5, ct=T%5;
  const rsrc_t rs=__builtin_amdgcn_make_buffer_rsrc(ws,0,WS_END,0x00020000);
  constexpr int OIM=B_PV;
  u64 E;
  auto count=[&](int word){ asm volatile("":::"memory"); u32 n=0; if(lane==0) n=__hip_atomic_fetch_add((LAS u32*)(L+B_F+4*word),1u,__ATOMIC_RELAXED,__HIP_MEMORY_SCOPE_WORKGROUP); return n; };
  auto until=[&](int word,u32 n){ for(int it=0;it<SPIN_MAX;++it){ if(__builtin_amdgcn_readfirstlane(*(volatile LAS u32*)(L+B_F+4*word))>=n) break; __builtin_amdgcn_s_sleep(2); } asm volatile("":::"memory"); };
  auto arrive=[&](){
    if(__builtin_amdgcn_readfirstlane(count(4))==3u&&lane==0){ flag_set(ws,ISQ?(128+4*rt+grp):(16*grp+rt),E); asm volatile("":::"memory"); *(volatile LAS u32*)(L+B_F+84)=4u; } };
  volatile LAS u32* bad=(volatile LAS u32*)(L+B_F+20);
  const int oh=4*grp+(rt&3), tq=rt>>2;
  const int ot=4*tt64+(w&3);
  int fi=(lane<16)?(16*grp+lane):((lane<32)?(16*(lane&7)+4*tq+(w&3)):((lane<48)?(16*(ot&7)+(ot>>3)):(128+4*(4*tq+(w&3))+(oh>>3))));
  if(w<4){
    const int th=w>>1, hh=w&1;
    if(lane<4) lds_st<u32x4>(L+B_Z+lane*16,u32x4{0,0,0,0});
    if(tid<9) lds_st<u32x4>(L+B_F+tid*16,u32x4{0,0,0,0});
    lptr fad[3][2];
    { const int q=(lane&15)>>2, p=lane&3, hd=ISQ?p:(p&1);
      #pragma unroll
      for(int s=0;s<3;++s)
        #pragma unroll
        for(int t=0;t<2;++t){ const int d=16*(2*s+t)+4*g-20*hd; const bool ok=(d>=0)&&(d<20)&&(2*s+t<5);
          fad[s][t]=ok?(L+B_WP+((ISQ?4*hh:2*hh)+hd)*800+(d+q)*40+(ISQ?0:(p>>1)*3200)):(L+B_Z); if(2*s+t<5) asm volatile("":"+v"(fad[s][t])); } }
    lptr pv=L+B_PV+w*B_PVS;
    lptr pp=pv+(lane&15)*160+(g&1)*80+(g>>1)*40, vp2, vp3, vp4, pa, vb;
    { lptr vp=pv+10272+(lane&15)*96; vp2=vp+((g<2)?(48*g+40):(8*g-16)); vp3=vp+((g==3)?48:(16+8*g)); vp4=vp+56+8*g;
      const int tk=16*(g>>1)+4*(g&1)+((lane&15)>>2); pa=pv+tk*160+8*(lane&3)+(th^1)*80; vb=pv+10272+tk*96+8*(lane&3)+(th^1)*48; }
    u32 po0, po1;
    { const u32 po=WS_P1+((4*grp+2*hh+th)*16+rt)*3584; po0=po+lane*16; po1=((lane&15)<5)?(po+(160+5*g+(lane&15))*16):OOB; }
    asm volatile("":"+v"(fi),"+v"(pp),"+v"(vp2),"+v"(vp3),"+v"(vp4),"+v"(pa),"+v"(vb),"+v"(po0),"+v"(po1));
    lptr ab[5], bb[2];
    { const int q=(lane&15)>>2,p=lane&3;
      #pragma unroll
      for(int i=0;i<5;++i){ int n=2*i+(p>>1); int cid=ISQ?(10*hh+n):(5*hh+n+(n>=5?5:0)); ab[i]=L+16384+(8*g+q)*320+((cid^((g&1)<<1))<<4)+8*(p&1); }
      const int b0=(64*th+(lane&15))*128+((g^((lane&15)>>1))<<4); bb[0]=L+b0; bb[1]=L+(b0^64); }
    f32x4 acc[5][4];
    #pragma unroll
    for(int i=0;i<5;++i)
      #pragma unroll
      for(int t=0;t<4;++t){ acc[i][t]=Z4; asm volatile("":"+v"(acc[i][t])); }
    ST(1);
    u32x4 FA[2][5], FB[2][4], fa[2][5];
    u32x2 lo2;
    auto half=[&](int hf,bool mm,bool last,int step){
      u32x2 lo;
      #pragma unroll
      for(int n=0;n<20;++n){ const int i=n>>2, t=n&3;
        if(mm) acc[i][t]=mfma16(FA[hf^1][i],FB[hf^1][t],acc[i][t]);
        SB();
        if(n==0) FB[hf][0]=lds_ld<u32x4>(bb[hf]);
        else if(n==1) lo=lds_tr(ab[0]+hf*10240);
        else if(n==2) FA[hf][0]=cat(lo,lds_tr(ab[0]+hf*10240+1280));
        else if(n<6) FB[hf][n-2]=lds_ld<u32x4>(bb[hf]+2048*(n-2));
        else if(n<14){ const int j=1+((n-6)>>1); if(!(n&1)) lo=lds_tr(ab[j]+hf*10240); else FA[hf][j]=cat(lo,lds_tr(ab[j]+hf*10240+1280)); }
        else if(hf==1&&!last){ if(n<19){ ab[n-14]+=step; asm volatile("":"+v"(ab[n-14])); } if(n>=18){ bb[n-18]+=step; asm volatile("":"+v"(bb[n-18])); } }
        if(last){ const int j=n>>2, s=(n>>1)&1; if(!(n&1)) lo2=lds_tr(fad[s][0]+8*j); else fa[s][j]=cat(lo2,lds_tr(fad[s][1]+8*j)); }
        SB(); } };
    volatile LAS u32x4* landed=(volatile LAS u32x4*)(L+B_F+96); volatile LAS u32* readw=(volatile LAS u32*)(L+B_F+112+4*w);
    auto min4=[](u32x4 v){ return (u32)__builtin_amdgcn_readfirstlane(min(min(v.x,v.y),min(v.z,v.w))); };
    bar();
    ST(2);
    __builtin_amdgcn_s_waitcnt(0xC07F);
    SB();
    half(0,false,false,0);
    #pragma unroll 1
    for(u32 s=1;s<10;++s){
      const u32x4 ld=*landed;
      half(1,true,false,(s&3)?B_ST:-3*B_ST);
      if(__builtin_expect(min4(ld)<=s,0)) for(int it=0;it<SPIN_MAX;++it){ if(min4(*landed)>s) break; }
      asm volatile("":::"memory");
      SB();
      half(0,true,false,0);
      *readw=s; }
    const u32x2 ev=*(volatile LAS u32x2*)(L+B_F+88);
    half(1,true,true,0);
    E=(u64)(u32)__builtin_amdgcn_readfirstlane(ev.x)|((u64)(u32)__builtin_amdgcn_readfirstlane(ev.y)<<32);
    asm volatile("s_waitcnt lgkmcnt(0)":::"memory");
    waitv<0>();
    count(7);
    #pragma unroll
    for(int t=0;t<4;++t)
      #pragma unroll
      for(int i=0;i<5;++i) acc[i][t]=mfma16(FA[1][i],FB[1][t],acc[i][t]);
    u32x2 pk[5][4];
    #pragma unroll
    for(int t=0;t<4;++t)
      #pragma unroll
      for(int i=0;i<5;++i) pk[i][t]=u32x2{pk_bf16(acc[i][t][0],acc[i][t][1]),pk_bf16(acc[i][t][2],acc[i][t][3])};
    ST(3);
    if(ISQ){
      u32x4 f2[5];
      #pragma unroll
      for(int i=0;i<5;++i) f2[i]=cat(lds_tr(fad[2][0]+8*i),u32x2{0u,0u});
      lptr sp_=pv+g*2560+(lane&15)*40;
      f32x4 z[4][5];
      #pragma unroll
      for(int t=0;t<4;++t)
        #pragma unroll
        for(int i=0;i<5;++i) z[t][i]=mfma16(fa[0][i],cat(pk[0][t],pk[1][t]),Z4);
      #pragma unroll
      for(int t=0;t<4;++t)
        #pragma unroll
        for(int i=0;i<5;++i) z[t][i]=mfma16(fa[1][i],cat(pk[2][t],pk[3][t]),z[t][i]);
      SB();
      #pragma unroll
      for(int t=0;t<5;++t){
        #pragma unroll
        for(int i=0;i<5;++i){ if(t<4) z[t][i]=mfma16(f2[i],cat(pk[4][t],u32x2{0u,0u}),z[t][i]);
          if(t>0) lds_st<u32x2>(sp_+(t-1)*640+8*i,u32x2{pk_bf16(z[t-1][i][0],z[t-1][i][1]),pk_bf16(z[t-1][i][2],z[t-1][i][3])}); }
        SB(); }
      ST(10);
      #pragma unroll
      for(int i=0;i<10;++i){ const int c=i*64+lane, hd=(c*205)>>15;
        st_sc1(rs,WS_Q+((8*grp+4*hh+hd)*2048+128*rt+64*th)*40+(c-160*hd)*16,lds_ld<u32x4>(pv+c*16)); }
      ST(11);
      waitv<0>(); ST(12); arrive();
      ST(4);
      if(doout){ fi=16*(ot&7)+(ot>>3); flag_wait(ws,E,fi,flag_get(ws,fi)); }
    } else {
      { f32x4 z[4][5]; u32 ph[4][10];
        auto fm=[&](int t){
          const u32x4 b0=cat(pk[0][t],pk[1][t]), b1=cat(pk[2][t],pk[3][t]);
          #pragma unroll
          for(int i=0;i<5;++i) z[t][i]=mfma16(fa[0][i],b0,Z4);
          #pragma unroll
          for(int i=0;i<5;++i) z[t][i]=mfma16(fa[1][i],b1,z[t][i]); };
        fm(0); fm(1);
        SB();
        softmax20x2(z,ph,[&](int k){ if(k==0){ fm(2); } else if(k==1){ fm(3); } });
        auto wp=[&](int t){
          #pragma unroll
          for(int i=0;i<5;++i) lds_st<u32x2>(pp+t*2560+8*i,u32x2{ph[t][2*i],ph[t][2*i+1]}); };
        softmax20x2(z+2,ph+2,[&](int k){
          if(k==0){ wp(0); wp(1); }
          else if(k==1){
            #pragma unroll
            for(int t=0;t<4;++t){ u32x2 v=pk[2][t]; if(g<2) v=u32x2{0x3F80u,0u};
              lds_st<u32x2>(vp2+t*1536,v); lds_st<u32x2>(vp3+t*1536,pk[3][t]); lds_st<u32x2>(vp4+t*1536,pk[4][t]); } } });
        wp(2); wp(3); }
      ST(4);
      u64 fl;
      { f32x4 c[2][6];
        #pragma unroll
        for(int h2=0;h2<2;++h2){
          if(h2){ pa+=th?80:-80; vb+=th?48:-48; }
          u32x4 a[2][3], b[2][2];
          #pragma unroll
          for(int ks=0;ks<2;++ks){
            #pragma unroll
            for(int i=0;i<3;++i) a[ks][i]=cat(lds_tr(pa+ks*32*160+32*i),lds_tr(pa+(ks*32+8)*160+32*i));
            #pragma unroll
            for(int dt=0;dt<2;++dt) b[ks][dt]=cat(lds_tr(vb+ks*32*96+32*dt),lds_tr(vb+(ks*32+8)*96+32*dt)); }
          #pragma unroll
          for(int i=0;i<6;++i) c[h2][i]=mfma16(a[0][i%3],b[0][i/3],Z4);
          #pragma unroll
          for(int i=0;i<6;++i) c[h2][i]=mfma16(a[1][i%3],b[1][i/3],c[h2][i]); }
        #pragma unroll
        for(int i=0;i<6;++i) *(volatile LAS f32x4*)(pv+i*1024+lane*16)=c[0][i];
        *(volatile LAS u32*)(L+B_F+4*w)=1u;
        #pragma unroll
        for(int i=0;i<6;++i) asm volatile("":"+v"(c[1][i]));
        lptr ox_=L+B_PV+(w^2)*B_PVS; f32x4 pr[6];
        for(int it=0;it<SPIN_MAX;++it){
          const u32 f=*(volatile LAS u32*)(L+B_F+4*(w^2)), n=*(volatile LAS u32*)(L+B_F+16);
          #pragma unroll
          for(int i=0;i<6;++i) pr[i]=*(volatile LAS f32x4*)(ox_+i*1024+lane*16);
          if(__builtin_amdgcn_readfirstlane(f+n)==5u) break; }
        fl=flag_get(ws,fi);
        #pragma unroll
        for(int i=0;i<6;++i){ u32 o=(i<3)?(po0+i*1024):(po1+(i-3)*320); if(i%3==2) o=(g<2)?o:OOB;
          st_sc1(rs,o,__builtin_bit_cast(u32x4,c[1][i]+pr[i])); } }
      ST(5);
      o_phase<true>(L,rs,ws,E,w,oh,tq,fi,fl);
      count(6);
      if(rt==15&&grp==7&&w==3){
        for(int it=0;it<SPIN_MAX;++it){ if(__all(flag_get(ws,lane)==E&&flag_get(ws,64+lane)==E&&flag_get(ws,128+lane)==E)) break; __builtin_amdgcn_s_sleep(2); }
        if(lane==0) __hip_atomic_store((GAS u64*)ws,E,__ATOMIC_RELAXED,__HIP_MEMORY_SCOPE_AGENT); }
      ST(6);
    }
    if(!doout){ __builtin_amdgcn_s_waitcnt(0x0F70); return false; }
    until(20,4u); until(ISQ?4:6,ISQ?4u:8u);
    { const u32 ob=WS_O+ot*20480;
      const u32 sm=ob+((lane<32)?((5*(lane>>3)+((lane>>1)&3))*1024+(lane&1)*512+508):((5*((lane-32)>>3)+4)*1024+((lane&7)>>1)*256+244+8*(lane&1)));
      for(int it=0;it<SPIN_MAX;++it){ asm volatile("":::"memory");
        if(__all(__builtin_amdgcn_raw_buffer_load_b32(rs,sm,0,16)!=SENT)) break;
        __builtin_amdgcn_s_sleep(1); }
      ST(15);
      const u32 vo=lane*16;
      for(u32 it=1;it<SPIN_MAX;++it){
        #pragma unroll
        for(int i=0;i<5;++i) dma16x4_sc1((const char*)ws+ob+i*4096,vo,vo,vo,vo,L+OIM+w*20480+i*4096);
        waitv<0>();
        ST(7);
        rbar();
        rbar();
        rbar();
        if(__builtin_amdgcn_readfirstlane(*bad)!=it) break; } }
    __builtin_amdgcn_s_waitcnt(0x0F70);
  } else {
    const int hw=w-4;
    if(!doout) params();
    asm volatile("s_load_dwordx2 %0, %1, 0x0":"=s"(E):"s"(ws):"memory");
    const int pr=hw>>1, me2=hw&1;
    u32 wq[40];
    { const int r=lane>>3; const u32 sw=((lane&7)^((((r>>1)&1)|(me2<<1))<<1))<<4, vo=r*1280+sw, vo2=((r>>2)*20+(r&3)+16+32*me2)*1280+sw;
      #pragma unroll
      for(int n=0;n<40;++n){ const int G=n/10, m=n%10, i=(n<22)?n:n-22, k=(i<16)?(i&3):((n>=18&&n<21)?n-18:((i<18)?i-16:0));
        wq[n]=((m<8)?vo+(160*G+20*m)*1280:vo2+(160*G+80*(m-8))*1280)-1024*k; asm volatile("":"+v"(wq[n])); } }
    lptr dst=L+hw*18432, rd, rd1; int x9, x10;
    { const int q=(lane&15)>>2, p=lane&3, f=((q>>1)|((g&1)<<1))<<1, ow=pr*2+(g&1);
      lptr rb=L+ow*18432+(g>>1)*1024+q*128+8*(p&1); rd=rb+(((4*me2+(p>>1))^f)<<4); rd1=rb+(((4*me2+2+(p>>1))^f)<<4);
      x9=B_WP+ow*3072-ow*18432; x10=(g>>1)?(B_PV+17408-1024):(x9+2048); }
    lptr ra9=rd+x9, rb9=rd1+x9, ra10=rd+x10, rb10=rd1+x10;
    asm volatile("":"+v"(rd),"+v"(rd1),"+v"(ra9),"+v"(rb9),"+v"(ra10),"+v"(rb10));
    ST(9);
    waitv<9>(); rbar();
    ST(8);
    { volatile LAS u32* lw=(volatile LAS u32*)(L+B_F+96+4*hw); volatile LAS u32x4* rdv=(volatile LAS u32x4*)(L+B_F+112);
      st.issue(2); waitv<9>(); *lw=2u;
      st.issue(3);
      asm volatile("s_waitcnt lgkmcnt(0)":"+s"(E)::"memory"); E+=1;
      if(tid==256) *(volatile LAS u32x2*)(L+B_F+88)=u32x2{(u32)E,(u32)(E>>32)};
      if(!ISQ&&lane<50){ lptr p=L+B_WP+hw*800+lane*16; lds_st<u32x4>(p+3200,lds_ld<u32x4>(p)^0x80008000u); }
      waitv<9>(); *lw=3u;
      #pragma unroll 1
      for(u32 s=4;s<10;++s){
        for(int it=0;it<SPIN_MAX;++it){ const u32x4 v=*rdv; if((u32)__builtin_amdgcn_readfirstlane(min(min(v.x,v.y),min(v.z,v.w)))>=s-3) break; __builtin_amdgcn_s_sleep(2); }
        asm volatile("":::"memory");
        st.issue(s);
        waitv<9>(); *lw=s; }
      waitv<0>(); *lw=10u; }
    if(!doout){ until(7,4u); return true; }
    if(!ISQ){
      const u32x4 sv={SENT,SENT,SENT,SENT};
      #pragma unroll
      for(int i=0;i<4;++i) st_sc1(rs,(i<3||lane<18)?(WS_P1+((4*grp+hw)*16+rt)*3584+i*1024+lane*16):OOB,sv);
      #pragma unroll
      for(int i=0;i<5;++i) st_sc1(rs,WS_O+((8*rt+grp)*20+5*hw+i)*1024+lane*16,sv);
      waitv<0>(); arrive(); }
    u32x4 wf[2][11];
    const char* gb=(const char*)wo+ct*256+pr*128+me2*8*1280;
    auto meet=[&](u32 n){ *(volatile LAS u32*)(L+B_F+48+4*hw)=n; until(12+(hw^1),n); };
    auto fetch18=[&](int n0){
      #pragma unroll
      for(int i=0;i<16;i+=4) dma16x4(gb,wq[n0+i],wq[n0+i+1],wq[n0+i+2],wq[n0+i+3],dst+i*1024);
      dma16xn<2>(gb,wq[n0+16],wq[n0+17],0,dst+16384); };
    until(ISQ?21:7,4u);
    ST(3);
    fetch18(0);
    dma16xn<3>(gb,wq[18],wq[19],wq[20],L+B_WP+hw*3072);
    dma16(gb,wq[21],dst+B_PV+17408);
    waitv<0>(); meet(1u);
    #pragma unroll
    for(int s=0;s<9;++s){ wf[0][s]=cat(lds_tr(rd+s*2048),lds_tr(rd+s*2048+512)); wf[1][s]=cat(lds_tr(rd1+s*2048),lds_tr(rd1+s*2048+512)); }
    wf[0][9]=cat(lds_tr(ra9),lds_tr(ra9+512)); wf[1][9]=cat(lds_tr(rb9),lds_tr(rb9+512));
    wf[0][10]=cat(lds_tr(ra10),lds_tr(ra10+512)); wf[1][10]=cat(lds_tr(rb10),lds_tr(rb10+512));
    asm volatile("s_waitcnt lgkmcnt(0)":::"memory");
    count(20); meet(2u);
    ST(10);
    fetch18(22);
    ST(4);
    if(!ISQ){ o_phase<false>(L,rs,ws,E,w,oh,tq,0,0); count(6); ST(6); }
    lptr obt[4];
    #pragma unroll
    for(int t=0;t<4;++t){ obt[t]=L+OIM+lane*16+((t+hw)&3)*20480; asm volatile("":"+v"(obt[t])); }
    waitv<0>();
    for(u32 it=1;it<SPIN_MAX;++it){
      rbar();
      ST(5);
      f32x4 c[2][4];
      #pragma unroll
      for(int t=0;t<4;++t){ c[0][t]=Z4; c[1][t]=Z4; }
      u32x4 ob[3][4], wl[3][2]; u32 chk=0;
      auto fetch=[&](int ks){
        #pragma unroll
        for(int t=0;t<4;++t) ob[ks%3][t]=lds_ld<u32x4>(obt[t]+ks*1024);
        if(ks>=11){ wl[ks%3][0]=cat(lds_tr(rd+(ks-11)*2048),lds_tr(rd+(ks-11)*2048+512)); wl[ks%3][1]=cat(lds_tr(rd1+(ks-11)*2048),lds_tr(rd1+(ks-11)*2048+512)); } };
      fetch(0); fetch(1);
      SB();
      #pragma unroll
      for(int ks=0;ks<20;++ks){ u32x2 lo;
        #pragma unroll
        for(int n=0;n<8;++n){ const int t=n>>1, cc=n&1, k2=ks+2;
          c[cc][t]=mfma16((ks<11)?wf[cc][ks<11?ks:0]:wl[ks%3][cc],ob[ks%3][t],c[cc][t]);
          SB();
          if(k2<20){
            if(n<4) ob[k2%3][n]=lds_ld<u32x4>(obt[n]+k2*1024);
            else if(k2>=11){ lptr p=((n<6)?rd:rd1)+(k2-11)*2048; if(!(n&1)) lo=lds_tr(p); else wl[k2%3][(n-4)>>1]=cat(lo,lds_tr(p+512)); } }
          if(n==7) chk=max(chk,max(ob[ks%3][0].x,ob[ks%3][0].z));
          SB(); } }
      if(__any(chk==SENT)) *bad=it;
      bar();
      #pragma unroll
      for(int t=0;t<4;++t)
        #pragma unroll
        for(int cc=0;cc<2;++cc) lds_st<u32x2>(L+OIM+(16*((t+hw)&3)+(lane&15))*272+hw*64+cc*32+8*g,u32x2{pk_bf16(c[cc][t][0],c[cc][t][1]),pk_bf16(c[cc][t][2],c[cc][t][3])});
      ST(7);
      bar();
      if(__builtin_amdgcn_readfirstlane(*bad)!=it) break; }
  }
  #pragma unroll
  for(int i=0;i<2;++i){ const int r=32*i+(tid>>4);
    __builtin_amdgcn_raw_buffer_store_b128(lds_ld<u32x4>(L+OIM+r*272+(tid&15)*16),__builtin_amdgcn_make_buffer_rsrc(out,0,2048*2560,0x00020000),(64*tt64+r)*2560+1280+ct*256+(tid&15)*16,0,16); }
  if(w<4) ST(8);
  return false;
}

constexpr int CODE_LINES=312;
__global__ __launch_bounds__(512,2) void fused(const u16* u,const u16* are,const u16* aim,const u16* bre,const u16* bim,const u16* cre,const u16* cim,
    const u16* logdt,const u16* dskip,const u16* x,const u16* wqkv,const u16* wphi,const u16* wo,u16* out,void* ws){
  extern __shared__ __attribute__((aligned(16))) char smem[];
  lptr L=(lptr)smem;
  const int b=blockIdx.x, X=b&7, idx=b>>3;
  u64 pc; asm volatile("s_getpc_b64 %0":"=s"(pc));
  const int w=__builtin_amdgcn_readfirstlane(threadIdx.x>>6);
  const bool isa=(idx>>3)==2;
  const int q=idx>=24, li=idx-24*q, rt=4*(X>>1)+(li>>(2-q)), grp=((X&1)<<(2-q))+(li&(3>>q));
  if(idx<16&&w<2){
    const u32 n=(w?(X*16+idx)*50:idx*32)+(threadIdx.x&63); u32 keep;
    if(w?((threadIdx.x&63)<50):(threadIdx.x<32&&n<CODE_LINES))
      asm volatile("s_mov_b32 %0, m0\n\ts_mov_b32 m0, %3\n\ts_nop 0\n\tglobal_load_lds_dword %1, %2\n\ts_mov_b32 m0, %0":"=&s"(keep):"v"(n*128),"s"(w?(u64)wo:(pc&~255ull)),"s"(162560+w*256):"memory"); }
  Stream st;
  if(__builtin_expect(!isa&&w>=4,1)){ st.init(q,li,rt,grp,w&3,threadIdx.x&63,x,wqkv,L); st.issue(0);
    { const u32 nb=800<<q, o=(threadIdx.x&63)*16; const char* wp=(const char*)wphi+(4*grp+(w&3))*nb;
      if(o<nb) dma16(wp,o,L+B_WP+(w&3)*nb);
      if(o+1024<nb) dma16(wp,o+1024,L+B_WP+(w&3)*nb+1024); }
    st.issue(1); }
  SB();
  STINIT();
  const int ch0=isa?(80*X+8*(idx-16)):(80*X+64+4*(idx-28)), wi=isa?w:w-4;
  SsmP sp; u32 dsk;
  auto params=[&](){ const SsmIn in={are,aim,bre,bim,cre,cim,logdt}; sp=load_ssm(in,ch0+wi,threadIdx.x&63); dsk=dskip[ch0+wi]; };
  bool job=true;
  if(__builtin_expect(!isa,1)) job=role_b(q,X,li,rt,grp,w,st,L,params,wo,out,ws);
  else params();
  if(job){
    if(isa) rbar();
    conv_job(isa,wi,ch0,idx&7,L,sp,dsk,u,wo,out,ws); }
  STDUMP();
}

extern "C" void kernel_launch(void* const* d_in, const int* in_sizes, int n_in, void* d_out, int out_size, void* d_ws, size_t ws_size, hipStream_t stream){
  (void)hipFuncSetAttribute((const void*)fused,hipFuncAttributeMaxDynamicSharedMemorySize,163840);
  fused<<<256,512,163840,stream>>>((const u16*)d_in[0],(const u16*)d_in[1],(const u16*)d_in[2],(const u16*)d_in[3],(const u16*)d_in[4],(const u16*)d_in[5],(const u16*)d_in[6],
    (const u16*)d_in[7],(const u16*)d_in[8],(const u16*)d_in[9],(const u16*)d_in[10],(const u16*)d_in[11],(const u16*)d_in[12],(u16*)d_out,d_ws);
}
```

```cpp
#include <hip/hip_runtime.h>
#include <stdint.h>

typedef unsigned short u16; typedef unsigned u32; typedef unsigned long long u64;
typedef __attribute__((ext_vector_type(4))) short s16x4;
typedef __attribute__((ext_vector_type(8))) _Float16 f16x8;
typedef __attribute__((ext_vector_type(8))) __bf16 bf16x8;
typedef __attribute__((ext_vector_type(2))) __bf16 bf16x2;
typedef __attribute__((ext_vector_type(2))) _Float16 f16x2;
typedef __attribute__((ext_vector_type(2))) float f32x2;
typedef __attribute__((ext_vector_type(4))) float f32x4;
typedef __attribute__((ext_vector_type(16))) float f32x16;
typedef __attribute__((ext_vector_type(2))) unsigned u32x2;
typedef __attribute__((ext_vector_type(3))) unsigned u32x3;
typedef __attribute__((ext_vector_type(4))) unsigned u32x4;
#define LAS __attribute__((address_space(3)))
#define GAS __attribute__((address_space(1)))
#define DI __device__ __forceinline__
typedef LAS char* lptr;
typedef __amdgpu_buffer_rsrc_t rsrc_t;

#define ST(i) do{}while(0)
#define STDUMP() do{}while(0)
#define STINIT() do{}while(0)

DI u32 pk_bf16(float a, float b){ f32x2 v={a,b}; return __builtin_bit_cast(u32,__builtin_convertvector(v,bf16x2)); }
DI u32 pk_f16(f32x2 v){ return __builtin_bit_cast(u32,__builtin_convertvector(v,f16x2)); }
DI float bflo(u32 x){ return __builtin_bit_cast(float,x<<16); }
DI float bfhi(u32 x){ return __builtin_bit_cast(float,x&0xffff0000u); }
DI float rbf(float a){ return bflo(pk_bf16(a,a)); }
template<class T> DI T lds_ld(lptr p){ return *(LAS T*)p; }
template<class T> DI void lds_st(lptr p, T v){ *(LAS T*)p=v; }
DI u32x2 lds_ld64(lptr p){ return *(volatile LAS u32x2*)p; }
DI u32x2 lds_tr(lptr p){ return __builtin_bit_cast(u32x2,__builtin_amdgcn_ds_read_tr16_b64_v4i16((LAS s16x4*)p)); }
DI f32x4 mfma16(u32x4 a,u32x4 b,f32x4 c){ return __builtin_amdgcn_mfma_f32_16x16x32_bf16(__builtin_bit_cast(bf16x8,a),__builtin_bit_cast(bf16x8,b),c,0,0,0); }
DI u32x4 cat(u32x2 a,u32x2 b){ u32x4 r={a.x,a.y,b.x,b.y}; return r; }
DI u32 p_ll(u32 a,u32 b){ return __builtin_amdgcn_perm(b,a,0x05040100u); }
DI u32 p_lh(u32 a,u32 b){ return __builtin_amdgcn_perm(b,a,0x07060100u); }
DI u32 p_hl(u32 a,u32 b){ return __builtin_amdgcn_perm(b,a,0x05040302u); }
DI u32 p_hh(u32 a,u32 b){ return __builtin_amdgcn_perm(b,a,0x07060302u); }
#define SB() __builtin_amdgcn_sched_barrier(0)
DI void rbar(){ asm volatile("":::"memory"); __builtin_amdgcn_s_barrier(); asm volatile("":::"memory"); }
DI void bar(){ asm volatile("s_waitcnt lgkmcnt(0)":::"memory"); __builtin_amdgcn_s_barrier(); asm volatile("":::"memory"); }
template<int N> DI void waitv(){ asm volatile("s_waitcnt vmcnt(%0)"::"i"(N):"memory"); }
constexpr f32x4 Z4={0.f,0.f,0.f,0.f};
DI float wave_max(float m){
  m=fmaxf(m,__builtin_bit_cast(float,__builtin_amdgcn_update_dpp(0,__builtin_bit_cast(int,m),0xB1,0xf,0xf,true)));
  m=fmaxf(m,__builtin_bit_cast(float,__builtin_amdgcn_update_dpp(0,__builtin_bit_cast(int,m),0x4E,0xf,0xf,true)));
  m=fmaxf(m,__builtin_bit_cast(float,__builtin_amdgcn_update_dpp(0,__builtin_bit_cast(int,m),0x141,0xf,0xf,true)));
  m=fmaxf(m,__builtin_bit_cast(float,__builtin_amdgcn_update_dpp(0,__builtin_bit_cast(int,m),0x140,0xf,0xf,true)));
  int x=__builtin_bit_cast(int,m);
  float a=__builtin_bit_cast(float,__builtin_amdgcn_readlane(x,0)), b=__builtin_bit_cast(float,__builtin_amdgcn_readlane(x,16));
  float c=__builtin_bit_cast(float,__builtin_amdgcn_readlane(x,32)), d=__builtin_bit_cast(float,__builtin_amdgcn_readlane(x,48));
  return fmaxf(fmaxf(a,b),fmaxf(c,d)); }

constexpr u32 WS_FLAG=256, WS_P1=4096, WS_Q=WS_P1+32*16*3584, WS_O=WS_Q+32*2048*40, WS_END=WS_O+128*20*1024;
constexpr u32 SENT=0xFFFFFFFFu, OOB=0x80000000u;
constexpr int SPIN_MAX=1<<17;
DI void dma16(const void* sb, u32 voff, lptr l){ u32 keep;
  asm volatile("s_mov_b32 %0, m0\n\ts_mov_b32 m0, %3\n\ts_nop 0\n\tglobal_load_lds_dwordx4 %1, %2\n\ts_mov_b32 m0, %0":"=&s"(keep):"v"(voff),"s"(sb),"s"((u32)(uintptr_t)l):"memory"); }
#define DMA4_BODY(SC) asm volatile("s_mov_b32 %0, m0\n\ts_mov_b32 m0, %6\n\ts_nop 0\n\tglobal_load_lds_dwordx4 %1, %5" SC "\n\tglobal_load_lds_dwordx4 %2, %5 offset:1024" SC \
    "\n\tglobal_load_lds_dwordx4 %3, %5 offset:2048" SC "\n\tglobal_load_lds_dwordx4 %4, %5 offset:3072" SC "\n\ts_mov_b32 m0, %0" \
    :"=&s"(keep):"v"(v0),"v"(v1),"v"(v2),"v"(v3),"s"(sb),"s"((u32)(uintptr_t)l):"memory")
DI void dma16x4(const void* sb,u32 v0,u32 v1,u32 v2,u32 v3,lptr l){ u32 keep; DMA4_BODY(""); }
DI void dma16x4_sc1(const void* sb,u32 v0,u32 v1,u32 v2,u32 v3,lptr l){ u32 keep; DMA4_BODY(" sc1"); }
template<int N> DI void dma16xn(const void* sb,u32 v0,u32 v1,u32 v2,lptr l){ u32 keep;
  if(N==2) asm volatile("s_mov_b32 %0, m0\n\ts_mov_b32 m0, %4\n\ts_nop 0\n\tglobal_load_lds_dwordx4 %1, %3\n\tglobal_load_lds_dwordx4 %2, %3 offset:1024\n\ts_mov_b32 m0, %0"
    :"=&s"(keep):"v"(v0),"v"(v1),"s"(sb),"s"((u32)(uintptr_t)l):"memory");
  else asm volatile("s_mov_b32 %0, m0\n\ts_mov_b32 m0, %5\n\ts_nop 0\n\tglobal_load_lds_dwordx4 %1, %4\n\tglobal_load_lds_dwordx4 %2, %4 offset:1024\n\tglobal_load_lds_dwordx4 %3, %4 offset:2048\n\ts_mov_b32 m0, %0"
    :"=&s"(keep):"v"(v0),"v"(v1),"v"(v2),"s"(sb),"s"((u32)(uintptr_t)l):"memory"); }
DI u32x4 ld_sc1(rsrc_t rs,u32 off){ return __builtin_amdgcn_raw_buffer_load_b128(rs,off,0,16); }
DI void st_sc1(rsrc_t rs,u32 off,u32x4 v){ __builtin_amdgcn_raw_buffer_store_b128(v,rs,off,0,16); }
DI void gather16(rsrc_t rs,u32 off,u32x4* v){
  for(int it=0;it<SPIN_MAX;++it){
    u32 todo=0;
    #pragma unroll
    for(int k=0;k<4;++k){ u32 m=0;
      #pragma unroll
      for(int i=0;i<4;++i) m=max(m,max(v[4*k+i].x,v[4*k+i].z));
      if(__any(m==SENT)) todo|=1u<<k; }
    if(!todo) break;
    asm volatile("":::"memory");
    #pragma unroll
    for(int k=0;k<4;++k) if(todo>>k&1){
      #pragma unroll
      for(int i=0;i<4;++i) v[4*k+i]=ld_sc1(rs,off+(4*k+i)*3584); } } }
DI void flag_set(void* ws,int i,u64 E){ __hip_atomic_store((GAS u64*)((char*)ws+WS_FLAG)+i,E,__ATOMIC_RELAXED,__HIP_MEMORY_SCOPE_AGENT); }
DI u64 flag_get(const void* ws,int i){ return __hip_atomic_load((const GAS u64*)((const char*)ws+WS_FLAG)+i,__ATOMIC_RELAXED,__HIP_MEMORY_SCOPE_AGENT); }
DI void flag_wait(const void* ws,u64 E,int idx,u64 f){
  for(int it=0;it<SPIN_MAX;++it){ if(__all(f==E)) break; __builtin_amdgcn_s_sleep(2); f=flag_get(ws,idx); } }

constexpr int A_UB=4576, A_US=9120, A_KB=4224, A_KS=8704;

DI f32x2 cmul(f32x2 a, f32x2 b){ f32x2 sw={-b.y,b.x}; return __builtin_elementwise_fma(a.yy,sw,a.xx*b); }
DI f32x2 cexp_e(float re2, float rev, float e){
  float m=__builtin_amdgcn_exp2f(re2*e); float a=__builtin_amdgcn_fractf(rev*e);
  f32x2 r={m*__builtin_amdgcn_cosf(a), m*__builtin_amdgcn_sinf(a)}; return r; }
struct SsmP { u32 are,aim,bre,bim,cre,cim,ld; float dt; };
struct SsmIn { const u16 *are,*aim,*bre,*bim,*cre,*cim,*logdt; };
DI SsmP load_ssm(const SsmIn& in, int ch, int lane){
  SsmP p; int o=ch*64+lane;
  p.are=((const u32*)in.are)[o]; p.aim=((const u32*)in.aim)[o]; p.bre=((const u32*)in.bre)[o];
  p.bim=((const u32*)in.bim)[o]; p.cre=((const u32*)in.cre)[o]; p.cim=((const u32*)in.cim)[o];
  p.ld=in.logdt[ch];
  return p; }
DI f32x2 ssm_w(const SsmP& p, int half){
  f32x2 b={half?bfhi(p.bre):bflo(p.bre), half?bfhi(p.bim):bflo(p.bim)};
  f32x2 c={half?bfhi(p.cre):bflo(p.cre), half?bfhi(p.cim):bflo(p.cim)};
  return cmul(c,b*p.dt); }
DI void ssm_prep(SsmP& p, float& s, float& inv){
  asm volatile("":"+v"(p.ld));
  p.dt=rbf(expf(bflo(p.ld)));
  f32x2 w0=ssm_w(p,0), w1=ssm_w(p,1);
  float m=wave_max(fmaxf(fmaxf(fabsf(w0.x),fabsf(w0.y)),fmaxf(fabsf(w1.x),fabsf(w1.y))));
  int e=__builtin_amdgcn_frexp_expf(m); e=max(-100,min(100,e));
  s=__builtin_amdgcn_ldexpf(1.0f,-e); inv=__builtin_amdgcn_ldexpf(1.0f,e); }
DI void kgen_half(const SsmP& p, int half, float s, lptr tab, int lane, f32x16& acc){
  const float are=(half?bfhi(p.are):bflo(p.are))*p.dt, aim=(half?bfhi(p.aim):bflo(p.aim))*p.dt;
  const float re2=are*1.4426950408889634f, rev=aim*0.15915494309189535f;
  lptr wr=tab+lane*4; lptr rd=tab+(lane&31)*272+(lane>>5)*16;
  { f32x2 e8=cexp_e(re2,rev,8.f), e16=cmul(e8,e8);
    f32x2 a={1.f,0.f}, b=e8;
    #pragma unroll
    for(int j=0;j<32;j+=2){ lds_st<u32>(wr+j*272,pk_f16(a)); lds_st<u32>(wr+(j+1)*272,pk_f16(b)); if(j<30){ a=(j==0)?e16:cmul(a,e16); b=cmul(b,e16);} } }
  u32x4 bf[8];
  #pragma unroll
  for(int k=0;k<8;++k) bf[k]=lds_ld<u32x4>(rd+k*32);
  { f32x2 w=ssm_w(p,half)*s; w.y=-w.y;
    f32x2 e1=cexp_e(re2,-rev,1.f), g=cexp_e(re2,-rev,256.f);
    f32x2 g2=cmul(g,g), g3=cmul(g2,g), g4=cmul(g2,g2);
    f32x2 x[8]; x[0]=w;
    #pragma unroll
    for(int i=1;i<8;++i) x[i]=cmul(x[i-1],e1);
    #pragma unroll
    for(int i=0;i<8;++i) lds_st<u32>(wr+i*272,pk_f16(x[i]));
    #pragma unroll
    for(int i=0;i<4;++i) lds_st<u32>(wr+(8+i)*272,pk_f16(cmul(x[2*i],g)));
    lds_st<u32>(wr+12*272,pk_f16(cmul(x[0],g2))); lds_st<u32>(wr+13*272,pk_f16(cmul(x[4],g2)));
    lds_st<u32>(wr+14*272,pk_f16(cmul(x[0],g3))); lds_st<u32>(wr+15*272,pk_f16(cmul(x[4],g3)));
    f32x2 y=cmul(w,g4);
    #pragma unroll
    for(int i=0;i<5;++i){ lds_st<u32>(wr+(16+i)*272,pk_f16(y)); if(i<4) y=cmul(y,g); } }
  #pragma unroll
  for(int k=0;k<8;++k){ u32x4 af=lds_ld<u32x4>(rd+k*32);
    acc=__builtin_amdgcn_mfma_f32_32x32x16_f16(__builtin_bit_cast(f16x8,af),__builtin_bit_cast(f16x8,bf[k]),acc,0,0,0); }
}
DI void k_finish(const float* v, lptr ka, int lane){
  lptr ks=ka; lptr kb=ka+A_KB;
  { const int j=lane&31, h=lane>>5;
    lds_st<u32x2>(ks+(8*j+4*h)*2,u32x2{pk_bf16(v[0],v[1]),pk_bf16(v[2],v[3])});
    u32x2 b={pk_bf16(v[4],v[5]),pk_bf16(v[6],v[7])};
    u32 c0=pk_bf16(v[8],v[9]), c1=pk_bf16(v[10],v[11]);
    if(h==0){ lds_st<u32x2>(ks+(256+4*j)*2,b);
      lds_st<u16>(ks+(512+j)*2,(u16)c0); lds_st<u16>(ks+(544+j)*2,(u16)(c0>>16));
      lds_st<u16>(ks+(576+j)*2,(u16)c1); lds_st<u16>(ks+(608+j)*2,(u16)(c1>>16));
    } else { lds_st<u32>(ks+(384+2*j)*2,b.x); lds_st<u32>(ks+(448+2*j)*2,b.y); if(j==0) lds_st<u16>(ks+640*2,(u16)c0); } }
  const int M=lane; const bool lo=M<32;
  u32x4 v0=lds_ld<u32x4>(ks+(M&31)*16); u32x2 v1=lds_ld<u32x2>(ks+(128+4*(M|32))*2);
  u32 v4=lds_ld<u16>(ks+(132+4*(M|32))*2);
  u32 pv=lds_ld<u16>(ks+(lo?max(8*M-1,0):((M==32)?255:(128+4*M)))*2); if(M==0) pv=0;
  u32 ab=lds_ld<u32>(ks+(384+2*M)*2), c2=lds_ld<u16>(ks+(386+2*M)*2);
  u32 a3[2],b3[2];
  #pragma unroll
  for(int i=0;i<2;++i){ a3[i]=lds_ld<u16>(ks+(512+M+64*i)*2); b3[i]=lds_ld<u16>(ks+(513+M+64*i)*2); }
  asm volatile("":"+v"(v0),"+v"(v1),"+v"(v4),"+v"(pv),"+v"(ab),"+v"(c2),"+v"(a3[0]),"+v"(a3[1]),"+v"(b3[0]),"+v"(b3[1])::"memory");
  { u32x4 A,B;
    if(lo){ B.x=p_hl(v0.w,v0.w); B.y=p_hl(v0.z,v0.z); B.z=p_hl(v0.y,v0.y); B.w=p_hl(v0.x,v0.x);
      A.x=p_lh(v0.w,v0.z); A.y=p_lh(v0.z,v0.y); A.z=p_lh(v0.y,v0.x); A.w=p_ll(v0.x,pv);
    } else { B.x=p_lh(v4,v1.y); B.y=p_ll(v1.y,v1.y); B.z=p_lh(v1.y,v1.x); B.w=p_ll(v1.x,v1.x);
      A.x=p_hl(v1.y,v1.y); A.y=B.y; A.z=p_hl(v1.x,v1.x); A.w=p_ll(v1.x,pv); }
    lds_st<u32x4>(ka+(255-M)*16,A); lds_st<u32x4>(kb+(255-M)*16,B); }
  { u32x4 A,B; B.x=p_ll(c2,c2); B.y=p_hh(ab,ab); B.z=p_hl(ab,ab); B.w=p_ll(ab,ab);
    A.x=p_lh(c2,ab); A.y=B.y; A.z=B.w; A.w=B.w;
    lds_st<u32x4>(ka+(191-M)*16,A); lds_st<u32x4>(kb+(191-M)*16,B); }
  #pragma unroll
  for(int i=0;i<2;++i){ u32 a=a3[i], b=b3[i], c=(M&1)?b:a; u32x4 A,B;
    B.x=p_ll(b,b); B.y=p_ll(b,c); B.z=p_ll(a,a); B.w=B.z;
    A.x=B.x; A.y=p_ll(c,a); A.z=B.z; A.w=B.z;
    lds_st<u32x4>(ka+(127-64*i-M)*16,A); lds_st<u32x4>(kb+(127-64*i-M)*16,B); }
  if(lane<8) lds_st<u32x4>(((lane&4)?kb:ka)+(256+(lane&3))*16,u32x4{0,0,0,0});
}
DI void conv_steps(lptr ua, lptr ka, int lane, f32x4* acc){
  constexpr int NS=8, s0=0, kbo=A_KB;
  const int a=lane&15, g=lane>>4;
  lptr up=ua+((a&1)?0:A_UB)+(32*(a>>1)+8*g+32*s0)*2;
  lptr kp=ka+((a&1)?kbo:0)+(28-4*(a>>1)+8*g+32*s0)*2;
  u32x4 A[2][8],B[2][8];
  auto ldA=[&](int s,int q){ A[s&1][q]=cat(lds_ld64(kp+(1792-256*q+32*s)*2),lds_ld64(kp+(1792-256*q+32*s)*2+8)); };
  auto ldB=[&](int s,int q){ B[s&1][q]=lds_ld<u32x4>(up+(256*q+32*s)*2); };
  #pragma unroll
  for(int q=0;q<8;++q){ ldB(0,q); ldA(0,q); }
  #pragma unroll
  for(int c=0;c<8;++c) acc[c]=Z4;
  SB();
  #pragma unroll
  for(int s=0;s<NS;++s){ int n=0; u32x2 lo;
    #pragma unroll
    for(int q=0;q<8;++q){
      #pragma unroll
      for(int c=q;c<8;++c){
        acc[c]=mfma16(A[s&1][q],B[s&1][c-q],acc[c]);
        SB();
        if(s+1<NS){
          if(n==0) ldB(s+1,0);
          else if(n<3){ if(n==1) lo=lds_ld64(kp+(1792+32*(s+1))*2); else A[(s+1)&1][0]=cat(lo,lds_ld64(kp+(1792+32*(s+1))*2+8)); }
          else if(n<10) ldB(s+1,n-2);
          else if(n<24){ const int j=1+((n-10)>>1); if(!(n&1)) lo=lds_ld64(kp+(1792-256*j+32*(s+1))*2); else A[(s+1)&1][j]=cat(lo,lds_ld64(kp+(1792-256*j+32*(s+1))*2+8)); } }
        SB();
        ++n; } } }
}
DI u32 fin2(float y0,float y1,u32 u,float dsk){
  u32 y=pk_bf16(y0,y1), s=pk_bf16(bflo(u)*dsk,bfhi(u)*dsk);
  return pk_bf16(bflo(y)+bflo(s),bfhi(y)+bfhi(s)); }
DI void conv_job(const bool BIG, int wi, int ch0, int rot, lptr L, SsmP& p0, u32 dsk, const u16* u, u16* out, void* ws){
  const int lane=threadIdx.x&63, t=64*wi+lane;
  lptr us=L+wi*A_US, kw=L+(BIG?8:4)*A_US+wi*A_KS;
  if(lane<56) lds_st<u32x4>(us+(lane<28?lane*16:A_UB+(lane-28)*16),u32x4{0,0,0,0});
  if(lane==56) lds_st<u32>(us+A_UB+448,0u);
  u32x4 um[4];
  const int ur=BIG?((t+64*rot)&511):t;
  const char* up=(const char*)u+(size_t)ur*4*1280+2*ch0;
  ST(1);
  { float s,inv; ssm_prep(p0,s,inv); f32x16 acc={};
    #pragma unroll 1
    for(int h=0;h<2;++h){
      if(!BIG&&h==1){
        for(int it=0;it<SPIN_MAX;++it){ if(__builtin_amdgcn_readfirstlane(*(volatile LAS u32*)(L+163168+84))>=4u) break; __builtin_amdgcn_s_sleep(2); }
        asm volatile("":::"memory");
        #pragma unroll
        for(int r=0;r<4;++r) um[r]=cat(*(const u32x2*)(up+r*1280),*(const u32x2*)(up+(r+1024)*1280)); }
      if(BIG&&h==1){
        #pragma unroll
        for(int r=0;r<4;++r) um[r]=*(const u32x4*)(up+r*1280); }
      kgen_half(p0,h,s,kw,lane,acc); }
    float v[12];
    #pragma unroll
    for(int r=0;r<12;++r) v[r]=acc[r]*inv;
    k_finish(v,kw,lane); }
  ST(2);
  #pragma unroll
  for(int c=0;c<4;++c){
    lptr d=L+(224+4*ur)*2+(BIG?2*c*A_US:(2*(c&1)*A_US+(c>>1)*2048)); const bool last=BIG?(ur==511):(c>=2&&ur==255);
    const u32x2 e={p_ll(um[0][c],um[1][c]),p_ll(um[2][c],um[3][c])}, o={p_hh(um[0][c],um[1][c]),p_hh(um[2][c],um[3][c])};
    lds_st<u32x2>(d,e); lds_st<u32x2>(d+A_US,o);
    lds_st<u32>(d+A_UB+4,e.x); lds_st<u32>(d+A_US+A_UB+4,o.x);
    if(!last){ lds_st<u32>(d+A_UB+8,e.y); lds_st<u32>(d+A_US+A_UB+8,o.y); } }
  bar();
  ST(3);
  f32x4 acc[8];
  conv_steps(us,kw,lane,acc);
  ST(4);
  { const int tl=32*((lane&15)>>1)+8*(lane>>4)+2*(lane&1); asm volatile("":"+v"(dsk)); const float d=bflo(dsk);
    u32 uu[16];
    #pragma unroll
    for(int c=0;c<8;++c){ uu[2*c]=lds_ld<u32>(us+(224+256*c+tl)*2); uu[2*c+1]=lds_ld<u32>(us+(228+256*c+tl)*2); }
    #pragma unroll
    for(int c=0;c<8;++c){ lds_st<u32>(kw+(256*c+tl)*2,fin2(acc[c][0],acc[c][1],uu[2*c],d)); lds_st<u32>(kw+(256*c+tl+4)*2,fin2(acc[c][2],acc[c][3],uu[2*c+1],d)); } }
  bar();
  ST(5);
  { lptr yp=L+(BIG?8:4)*A_US+((lane&15)>>2)*A_KS+(256*wi+16*(lane>>4)+4*(lane&3))*2;
    char* op=(char*)out+(size_t)(256*wi+lane)*2560+2*ch0;
    #pragma unroll
    for(int i=0;i<4;++i){
      if(BIG) *(u32x4*)(op+(size_t)i*64*2560)=cat(lds_tr(yp+i*128),lds_tr(yp+4*A_KS+i*128));
      else { *(u32x2*)(op+(size_t)i*64*2560)=lds_tr(yp+i*128); *(u32x2*)(op+(size_t)(i*64+1024)*2560)=lds_tr(yp+2048+i*128); } } }
  ST(7);
}

constexpr int B_ST=36864, B_PV=2*B_ST, B_PVS=18432, B_WP=4*B_ST, B_Z=B_WP+6400, B_KT=159744, B_F=163168;
DI float max3(float a,float b,float c){ return __builtin_fmaxf(__builtin_fmaxf(a,b),c); }
template<class H> DI void softmax20x2(const f32x4 (*z)[5], u32 (*o)[10], H hook){
  const float c=1.4426950408889634f; float mc[2];
  { float m0[2],m1[2],m2[2];
    #pragma unroll
    for(int t=0;t<2;++t){ m0[t]=max3(z[t][0][0],z[t][0][1],z[t][0][2]); m1[t]=max3(z[t][0][3],z[t][1][0],z[t][1][1]); m2[t]=max3(z[t][1][2],z[t][1][3],z[t][2][0]); }
    #pragma unroll
    for(int t=0;t<2;++t){ m0[t]=max3(m0[t],z[t][2][1],z[t][2][2]); m1[t]=max3(m1[t],z[t][2][3],z[t][3][0]); m2[t]=max3(m2[t],z[t][3][1],z[t][3][2]); }
    #pragma unroll
    for(int t=0;t<2;++t){ m0[t]=max3(m0[t],z[t][3][3],z[t][4][0]); m1[t]=max3(m1[t],z[t][4][1],z[t][4][2]); m2[t]=__builtin_fmaxf(m2[t],z[t][4][3]); }
    #pragma unroll
    for(int t=0;t<2;++t) mc[t]=max3(m0[t],m1[t],m2[t])*-c; }
  SB();
  f32x2 e[2][10];
  #pragma unroll
  for(int i=0;i<10;++i)
    #pragma unroll
    for(int t=0;t<2;++t) e[t][i]=__builtin_elementwise_fma(f32x2{z[t][i>>1][(i&1)*2],z[t][i>>1][(i&1)*2+1]},f32x2{c,c},f32x2{mc[t],mc[t]});
  SB();
  #pragma unroll
  for(int i=0;i<10;++i)
    #pragma unroll
    for(int t=0;t<2;++t){ hook(2*i+t); e[t][i]=f32x2{__builtin_amdgcn_exp2f(e[t][i].x),__builtin_amdgcn_exp2f(e[t][i].y)}; SB(); }
  float r[2];
  { f32x2 s[2][5];
    #pragma unroll
    for(int i=0;i<5;++i)
      #pragma unroll
      for(int t=0;t<2;++t) s[t][i]=e[t][2*i]+e[t][2*i+1];
    #pragma unroll
    for(int t=0;t<2;++t){ s[t][0]+=s[t][1]; s[t][2]+=s[t][3]; }
    #pragma unroll
    for(int t=0;t<2;++t){ s[t][0]+=s[t][4]; }
    #pragma unroll
    for(int t=0;t<2;++t){ s[t][0]+=s[t][2]; }
    #pragma unroll
    for(int t=0;t<2;++t) r[t]=__builtin_amdgcn_rcpf(s[t][0].x+s[t][0].y); }
  SB();
  #pragma unroll
  for(int i=0;i<10;++i)
    #pragma unroll
    for(int t=0;t<2;++t) e[t][i]*=r[t];
  SB();
  #pragma unroll
  for(int i=0;i<10;++i)
    #pragma unroll
    for(int t=0;t<2;++t) o[t][i]=pk_bf16(e[t][i].x,e[t][i].y);
  SB();
}
DI u32x4 pack2(f32x4 a,f32x4 b){ u32x4 r={pk_bf16(a[0],a[1]),pk_bf16(a[2],a[3]),pk_bf16(b[0],b[1]),pk_bf16(b[2],b[3])}; return r; }
struct Stream { u32 xo[4], wv[5]; int s0, qw; const u16 *x, *w; lptr L;
  DI void init(int q,int li,int rt,int grp,int qw_,int lane,const u16* x_,const u16* w_,lptr L_){
    qw=qw_; x=x_; w=w_; L=L_;
    s0=((0x8530>>((((li>>(2-q))+((li&(3>>q))<<q))&3)*4))&15)+q;
    #pragma unroll
    for(int i=0;i<4;++i){ const u32 r=(4*qw+i)*8+(lane>>3); xo[i]=(rt*128+r)*1280u+(((lane&7)^((r>>1)&7))<<4)-1024u*i; }
    dma16x4((const char*)x+(s0>=10?s0-10:s0)*128,xo[0],xo[1],xo[2],xo[3],L+4*qw*1024);
    const u32 wlo=(160<<q)*grp+1280*(1-q), dh=1120*(1-q);
    #pragma unroll
    for(int i=0;i<5;++i){ const u32 c=(5*qw+i)*64+lane, r=(c*3277u)>>16, pos=c-r*20, sc=pos^(((r>>3)&1)<<1);
      wv[i]=r*3840u+wlo+(sc<10?0u:dh)+16*sc-1024u*(i&3); } }
  DI void issue(int s,bool withx=true) const { int ss=s+s0; if(ss>=10) ss-=10; lptr d=L+(s&3)*B_ST;
    const char* xb=(const char*)x+ss*128; const char* wb=(const char*)w+(size_t)ss*64*3840;
    if(withx) dma16x4(xb,xo[0],xo[1],xo[2],xo[3],d+4*qw*1024);
    dma16x4(wb,wv[0],wv[1],wv[2],wv[3],d+16384+5*qw*1024); dma16(wb,wv[4],d+16384+(5*qw+4)*1024); } };

template<bool GATHER> DI void o_phase(lptr L,rsrc_t rs,void* ws,u64 E,int w,int oh,int tq,int fi,u64 fl){
  const int lane=threadIdx.x&63, g=lane>>4, r=lane&15;
  if(GATHER){ if(!__all(fl==E)) flag_wait(ws,E,fi,fl); *(volatile LAS u32*)(L+B_F+128+4*w)=1u; }
  else for(int it=0;it<SPIN_MAX;++it){ if(__builtin_amdgcn_readfirstlane(*(volatile LAS u32*)(L+B_F+128+4*(w&3)))) break; __builtin_amdgcn_s_sleep(2); }
  asm volatile("":::"memory");
  u32x4 l0[2],l1[2]; u32x2 l2[2];
  { const u32 qo=WS_Q+(oh*2048+512*tq+128*(w&3)+64*(w>>2)+16*(g>>1)+r)*40;
    #pragma unroll
    for(int i=0;i<2;++i){ l0[i]=ld_sc1(rs,qo+i*1280); l1[i]=ld_sc1(rs,qo+i*1280+16); l2[i]=__builtin_amdgcn_raw_buffer_load_b64(rs,qo+i*1280+32,0,16); } }
  lptr kt=L+B_KT, q00,q01,q10,q11; u32 om,ox;
  { const int pc=5*(g&1)+2*(g>>1), pl=5*(g&1)+4, d1=min(r,4); lptr b=kt;
    q00=b+(16*pc+r)*8; q01=b+(16*pl+r)*8+4*(g>>1); q10=b+(160+5*pc+d1)*8; q11=b+(160+5*pl+d1)*8+4*(g>>1);
    const u32 fb=WS_O+((32*tq+8*(w&3)+4*(w>>2))*20+5*(oh>>3))*1024+r*16;
    om=fb+((oh&7)>>1)*1024+(2*(oh&1)+(g>>1))*256+8*(g&1); ox=(g==0)?(fb+4096+((oh&7)>>1)*256+8*(oh&1)):OOB; }
  asm volatile("":"+v"(q00),"+v"(q01),"+v"(q10),"+v"(q11),"+v"(om),"+v"(ox));
  ST(11);
  u32x4 v[16]; const u32 go=(lane<53&&53*w+lane<210)?(WS_P1+oh*16*3584+(53*w+lane)*16):OOB;
  u32 pq[2][10];
  { const u32 sg=(g&1)?0x80008000u:0u; f32x4 z[2][5];
    #pragma unroll
    for(int t=0;t<2;++t){ const u32x4 a=l0[t]^sg, b=l1[t]^sg; const u32x2 c=l2[t]^sg;
      z[t][0]=f32x4{bflo(a.x),bfhi(a.x),bflo(a.y),bfhi(a.y)}; z[t][1]=f32x4{bflo(a.z),bfhi(a.z),bflo(a.w),bfhi(a.w)};
      z[t][2]=f32x4{bflo(b.x),bfhi(b.x),bflo(b.y),bfhi(b.y)}; z[t][3]=f32x4{bflo(b.z),bfhi(b.z),bflo(b.w),bfhi(b.w)};
      z[t][4]=f32x4{bflo(c.x),bfhi(c.x),bflo(c.y),bfhi(c.y)}; }
    SB();
    softmax20x2(z,pq,[&](int k){ if(GATHER&&k<16) v[k]=ld_sc1(rs,go+k*3584); });
    #pragma unroll
    for(int i=0;i<2;++i){
      #pragma unroll
      for(int k=0;k<5;++k){ const int a=(k<4)?k:8, b=(k<4)?k+4:9;
        auto s=__builtin_amdgcn_permlane32_swap(pq[i][a],pq[i][b],false,false); pq[i][a]=s[0]; pq[i][b]=s[1]; }
      #pragma unroll
      for(int k=0;k<10;++k) asm volatile("":"+v"(pq[i][k])); } }
  ST(12);
  if(GATHER){
    gather16(rs,go,v);
    ST(13);
    f32x4 s[4];
    #pragma unroll
    for(int i=0;i<4;++i) s[i]=(__builtin_bit_cast(f32x4,v[4*i])+__builtin_bit_cast(f32x4,v[4*i+1]))+(__builtin_bit_cast(f32x4,v[4*i+2])+__builtin_bit_cast(f32x4,v[4*i+3]));
    s[0]=(s[0]+s[1])+(s[2]+s[3]);
    if(lane<53) *(volatile LAS u32x2*)(kt+(53*w+lane)*8)=u32x2{pk_bf16(s[0][0],s[0][1]),pk_bf16(s[0][2],s[0][3])};
    *(volatile LAS u32*)(L+B_F+32+4*w)=1u;
  }
  u32x4 a0[2]; u32 a1[2];
  for(int it=0;it<SPIN_MAX;++it){
    const u32x4 f=*(volatile LAS u32x4*)(L+B_F+32);
    a0[0]=cat(*(volatile LAS u32x2*)q00,*(volatile LAS u32x2*)(q00+128)); a0[1]=cat(*(volatile LAS u32x2*)q10,*(volatile LAS u32x2*)(q10+40));
    a1[0]=*(volatile LAS u32*)q01; a1[1]=*(volatile LAS u32*)q11;
    if(__builtin_amdgcn_readfirstlane(f.x+f.y+f.z+f.w)==4u) break;
    if(!GATHER) __builtin_amdgcn_s_sleep(1); }
  ST(14);
  f32x4 n[4][2];
  #pragma unroll
  for(int i=1;i>=0;--i){
    #pragma unroll
    for(int t=0;t<4;++t){ const u32* f=pq[t>>1]+4*(t&1); n[t][i]=mfma16(a0[i],u32x4{f[0],f[1],f[2],f[3]},Z4); }
    #pragma unroll
    for(int t=0;t<4;++t) n[t][i]=mfma16(u32x4{a1[i],0u,0u,0u},u32x4{pq[t>>1][8+(t&1)],0u,0u,0u},n[t][i]); }
  SB();
  float den[4];
  #pragma unroll
  for(int t=0;t<4;++t) den[t]=__builtin_bit_cast(float,__builtin_amdgcn_ds_bpermute((r+16)*4,__builtin_bit_cast(int,n[t][1][0])));
  #pragma unroll
  for(int t=0;t<4;++t) den[t]=__builtin_amdgcn_rcpf(rbf(rbf(den[t])+1e-6f));
  SB();
  #pragma unroll
  for(int t=0;t<4;++t){ const f32x4 m=n[t][0]*den[t], e=n[t][1]*den[t];
    __builtin_amdgcn_raw_buffer_store_b64(u32x2{pk_bf16(m[0],m[1]),pk_bf16(m[2],m[3])},rs,om+t*20480,0,16);
    __builtin_amdgcn_raw_buffer_store_b64(u32x2{pk_bf16(e[0],e[1]),pk_bf16(e[2],e[3])},rs,ox+t*20480,0,16); }
}

template<class F> DI bool role_b(const bool ISQ,int X,int li,int rt,int grp,int w,const Stream& st,lptr L,F params,const u16* wo,u16* out,void* ws){
  const int tid=threadIdx.x, lane=tid&63, g=lane>>4;
  const int T=ISQ?16+li:li; const bool doout=T<20; const int tt64=4*X+T/5, ct=T%5;
  const rsrc_t rs=__builtin_amdgcn_make_buffer_rsrc(ws,0,WS_END,0x00020000);
  constexpr int OIM=B_PV;
  u64 E;
  auto count=[&](int word){ asm volatile("":::"memory"); u32 n=0; if(lane==0) n=__hip_atomic_fetch_add((LAS u32*)(L+B_F+4*word),1u,__ATOMIC_RELAXED,__HIP_MEMORY_SCOPE_WORKGROUP); return n; };
  auto until=[&](int word,u32 n){ for(int it=0;it<SPIN_MAX;++it){ if(__builtin_amdgcn_readfirstlane(*(volatile LAS u32*)(L+B_F+4*word))>=n) break; __builtin_amdgcn_s_sleep(2); } asm volatile("":::"memory"); };
  auto arrive=[&](){
    if(__builtin_amdgcn_readfirstlane(count(4))==3u&&lane==0){ flag_set(ws,ISQ?(128+4*rt+grp):(16*grp+rt),E); asm volatile("":::"memory"); *(volatile LAS u32*)(L+B_F+84)=4u; } };
  volatile LAS u32* bad=(volatile LAS u32*)(L+B_F+20);
  const int oh=4*grp+(rt&3), tq=rt>>2;
  const int ot=4*tt64+(w&3);
  int fi=(lane<16)?(16*grp+lane):((lane<32)?(16*(lane&7)+4*tq+(w&3)):((lane<48)?(16*(ot&7)+(ot>>3)):(128+4*(4*tq+(w&3))+(oh>>3))));
  if(w<4){
    const int th=w>>1, hh=w&1;
    if(lane<4) lds_st<u32x4>(L+B_Z+lane*16,u32x4{0,0,0,0});
    if(tid<9) lds_st<u32x4>(L+B_F+tid*16,u32x4{0,0,0,0});
    lptr fad[3][2];
    { const int q=(lane&15)>>2, p=lane&3, hd=ISQ?p:(p&1);
      #pragma unroll
      for(int s=0;s<3;++s)
        #pragma unroll
        for(int t=0;t<2;++t){ const int d=16*(2*s+t)+4*g-20*hd; const bool ok=(d>=0)&&(d<20)&&(2*s+t<5);
          fad[s][t]=ok?(L+B_WP+((ISQ?4*hh:2*hh)+hd)*800+(d+q)*40+(ISQ?0:(p>>1)*3200)):(L+B_Z); if(2*s+t<5) asm volatile("":"+v"(fad[s][t])); } }
    lptr pv=L+B_PV+w*B_PVS;
    lptr pp=pv+(lane&15)*168+(g&1)*80+(g>>1)*40, vp2, vp3, vp4, pa, vb;
    { lptr vp=pv+10752+(lane&15)*104; vp2=vp+((g<2)?(48*g+40):(8*g-16)); vp3=vp+((g==3)?48:(16+8*g)); vp4=vp+56+8*g;
      const int tk=16*(g&1)+4*((lane&15)>>2)+2*(g>>1); pa=pv+tk*168+8*(lane&3)+(th^1)*80; vb=pv+10752+tk*104+8*(lane&3)+(th^1)*48; }
    u32 po0, po1;
    { const u32 po=WS_P1+((4*grp+2*hh+th)*16+rt)*3584; po0=po+lane*16; po1=((lane&15)<5)?(po+(160+5*g+(lane&15))*16):OOB; }
    asm volatile("":"+v"(fi),"+v"(pp),"+v"(vp2),"+v"(vp3),"+v"(vp4),"+v"(pa),"+v"(vb),"+v"(po0),"+v"(po1));
    lptr ab[5], bb[2];
    { const int q=(lane&15)>>2,p=lane&3;
      #pragma unroll
      for(int i=0;i<5;++i){ int n=2*i+(p>>1); int cid=ISQ?(10*hh+n):(5*hh+n+(n>=5?5:0)); ab[i]=L+16384+(8*g+q)*320+((cid^((g&1)<<1))<<4)+8*(p&1); }
      const int b0=(64*th+(lane&15))*128+((g^((lane&15)>>1))<<4); bb[0]=L+b0; bb[1]=L+(b0^64); }
    f32x4 acc[5][4];
    #pragma unroll
    for(int i=0;i<5;++i)
      #pragma unroll
      for(int t=0;t<4;++t){ acc[i][t]=Z4; asm volatile("":"+v"(acc[i][t])); }
    ST(1);
    u32x4 FA[2][5], FB[2][4], fa[2][5];
    u32x2 lo2;
    auto half=[&](int hf,bool mm,bool last,int step){
      u32x2 lo;
      #pragma unroll
      for(int n=0;n<20;++n){ const int i=n>>2, t=n&3;
        if(mm) acc[i][t]=mfma16(FA[hf^1][i],FB[hf^1][t],acc[i][t]);
        SB();
        if(n==0) FB[hf][0]=lds_ld<u32x4>(bb[hf]);
        else if(n==1) lo=lds_tr(ab[0]+hf*10240);
        else if(n==2) FA[hf][0]=cat(lo,lds_tr(ab[0]+hf*10240+1280));
        else if(n<6) FB[hf][n-2]=lds_ld<u32x4>(bb[hf]+2048*(n-2));
        else if(n<14){ const int j=1+((n-6)>>1); if(!(n&1)) lo=lds_tr(ab[j]+hf*10240); else FA[hf][j]=cat(lo,lds_tr(ab[j]+hf*10240+1280)); }
        else if(hf==1&&!last){ if(n<19){ ab[n-14]+=step; asm volatile("":"+v"(ab[n-14])); } if(n>=18){ bb[n-18]+=step; asm volatile("":"+v"(bb[n-18])); } }
        if(last){ const int j=n>>2, s=(n>>1)&1; if(!(n&1)) lo2=lds_tr(fad[s][0]+8*j); else fa[s][j]=cat(lo2,lds_tr(fad[s][1]+8*j)); }
        SB(); } };
    bar();
    ST(2);
    __builtin_amdgcn_s_waitcnt(0xC07F);
    SB();
    half(0,false,false,0);
    #pragma unroll 1
    for(u32 s=1;s<10;++s){
      half(1,true,false,(s&3)?B_ST:-3*B_ST);
      rbar();
      SB();
      half(0,true,false,0); }
    const u32x2 ev=*(volatile LAS u32x2*)(L+B_F+88);
    half(1,true,true,0);
    E=(u64)(u32)__builtin_amdgcn_readfirstlane(ev.x)|((u64)(u32)__builtin_amdgcn_readfirstlane(ev.y)<<32);
    asm volatile("s_waitcnt lgkmcnt(0)":::"memory");
    waitv<0>();
    count(7);
    #pragma unroll
    for(int t=0;t<4;++t)
      #pragma unroll
      for(int i=0;i<5;++i) acc[i][t]=mfma16(FA[1][i],FB[1][t],acc[i][t]);
    u32x2 pk[5][4];
    #pragma unroll
    for(int t=0;t<4;++t)
      #pragma unroll
      for(int i=0;i<5;++i) pk[i][t]=u32x2{pk_bf16(acc[i][t][0],acc[i][t][1]),pk_bf16(acc[i][t][2],acc[i][t][3])};
    ST(3);
    if(ISQ){
      u32x4 f2[5];
      #pragma unroll
      for(int i=0;i<5;++i) f2[i]=cat(lds_tr(fad[2][0]+8*i),u32x2{0u,0u});
      lptr sp_=pv+g*2560+(lane&15)*40;
      f32x4 z[4][5];
      #pragma unroll
      for(int t=0;t<4;++t)
        #pragma unroll
        for(int i=0;i<5;++i) z[t][i]=mfma16(fa[0][i],cat(pk[0][t],pk[1][t]),Z4);
      #pragma unroll
      for(int t=0;t<4;++t)
        #pragma unroll
        for(int i=0;i<5;++i) z[t][i]=mfma16(fa[1][i],cat(pk[2][t],pk[3][t]),z[t][i]);
      SB();
      #pragma unroll
      for(int t=0;t<5;++t){
        #pragma unroll
        for(int i=0;i<5;++i){ if(t<4) z[t][i]=mfma16(f2[i],cat(pk[4][t],u32x2{0u,0u}),z[t][i]);
          if(t>0) lds_st<u32x2>(sp_+(t-1)*640+8*i,u32x2{pk_bf16(z[t-1][i][0],z[t-1][i][1]),pk_bf16(z[t-1][i][2],z[t-1][i][3])}); }
        SB(); }
      ST(10);
      #pragma unroll
      for(int i=0;i<10;++i){ const int c=i*64+lane, hd=(c*205)>>15;
        st_sc1(rs,WS_Q+((8*grp+4*hh+hd)*2048+128*rt+64*th)*40+(c-160*hd)*16,lds_ld<u32x4>(pv+c*16)); }
      ST(11);
      waitv<0>(); ST(12); arrive();
      ST(4);
      if(doout){ fi=16*(ot&7)+(ot>>3); flag_wait(ws,E,fi,flag_get(ws,fi)); }
    } else {
      { f32x4 z[4][5]; u32 ph[4][10];
        auto fm=[&](int t){
          const u32x4 b0=cat(pk[0][t],pk[1][t]), b1=cat(pk[2][t],pk[3][t]);
          #pragma unroll
          for(int i=0;i<5;++i) z[t][i]=mfma16(fa[0][i],b0,Z4);
          #pragma unroll
          for(int i=0;i<5;++i) z[t][i]=mfma16(fa[1][i],b1,z[t][i]); };
        fm(0); fm(1);
        SB();
        softmax20x2(z,ph,[&](int k){ const int t=2+k/10, i=k%5;
          z[t][i]=(k%10<5)?mfma16(fa[0][i],cat(pk[0][t],pk[1][t]),Z4):mfma16(fa[1][i],cat(pk[2][t],pk[3][t]),z[t][i]); });
        auto wp=[&](int t){
          #pragma unroll
          for(int i=0;i<5;++i) lds_st<u32x2>(pp+t*2688+8*i,u32x2{ph[t][2*i],ph[t][2*i+1]}); };
        u32x2 v2[4];
        #pragma unroll
        for(int t=0;t<4;++t) v2[t]=(g<2)?u32x2{0x3F80u,0u}:pk[2][t];
        softmax20x2(z+2,ph+2,[&](int k){
          if(k<10) *(volatile LAS u32x2*)(pp+(k/5)*2688+8*(k%5))=u32x2{ph[k/5][2*(k%5)],ph[k/5][2*(k%5)+1]};
          else if(k<14) *(volatile LAS u32x2*)(vp2+(k-10)*1664)=v2[k-10];
          else if(k<18) *(volatile LAS u32x2*)(vp3+(k-14)*1664)=pk[3][k-14];
          else { *(volatile LAS u32x2*)(vp4+(2*k-36)*1664)=pk[4][2*k-36]; *(volatile LAS u32x2*)(vp4+(2*k-35)*1664)=pk[4][2*k-35]; } });
        wp(2); wp(3); }
      ST(4);
      u64 fl;
      { f32x4 c[2][6];
        #pragma unroll
        for(int h2=0;h2<2;++h2){
          if(h2){ pa+=th?80:-80; vb+=th?48:-48; }
          u32x4 a[2][3], b[2][2];
          #pragma unroll
          for(int ks=0;ks<2;++ks){
            #pragma unroll
            for(int i=0;i<3;++i) a[ks][i]=cat(lds_tr(pa+ks*32*168+32*i),lds_tr(pa+(ks*32+1)*168+32*i));
            #pragma unroll
            for(int dt=0;dt<2;++dt) b[ks][dt]=cat(lds_tr(vb+ks*32*104+32*dt),lds_tr(vb+(ks*32+1)*104+32*dt)); }
          #pragma unroll
          for(int i=0;i<6;++i) c[h2][i]=mfma16(a[0][i%3],b[0][i/3],Z4);
          #pragma unroll
          for(int i=0;i<6;++i) c[h2][i]=mfma16(a[1][i%3],b[1][i/3],c[h2][i]); }
        #pragma unroll
        for(int i=0;i<6;++i) *(volatile LAS f32x4*)(pv+i*1024+lane*16)=c[0][i];
        *(volatile LAS u32*)(L+B_F+4*w)=1u;
        #pragma unroll
        for(int i=0;i<6;++i) asm volatile("":"+v"(c[1][i]));
        lptr ox_=L+B_PV+(w^2)*B_PVS; f32x4 pr[6];
        for(int it=0;it<SPIN_MAX;++it){
          const u32 f=*(volatile LAS u32*)(L+B_F+4*(w^2)), n=*(volatile LAS u32*)(L+B_F+16);
          #pragma unroll
          for(int i=0;i<6;++i) pr[i]=*(volatile LAS f32x4*)(ox_+i*1024+lane*16);
          if(__builtin_amdgcn_readfirstlane(f+n)==5u) break; }
        fl=flag_get(ws,fi);
        #pragma unroll
        for(int i=0;i<6;++i){ u32 o=(i<3)?(po0+i*1024):(po1+(i-3)*320); if(i%3==2) o=(g<2)?o:OOB;
          st_sc1(rs,o,__builtin_bit_cast(u32x4,c[1][i]+pr[i])); } }
      ST(5);
      o_phase<true>(L,rs,ws,E,w,oh,tq,fi,fl);
      count(6);
      if(rt==15&&grp==7&&w==3){
        for(int it=0;it<SPIN_MAX;++it){ if(__all(flag_get(ws,lane)==E&&flag_get(ws,64+lane)==E&&flag_get(ws,128+lane)==E)) break; __builtin_amdgcn_s_sleep(2); }
        if(lane==0) __hip_atomic_store((GAS u64*)ws,E,__ATOMIC_RELAXED,__HIP_MEMORY_SCOPE_AGENT); }
      ST(6);
    }
    if(!doout){ __builtin_amdgcn_s_waitcnt(0x0F70); return false; }
    until(20,4u);
    { const u32 ob=WS_O+ot*20480;
      const u32 sm=ob+((lane<32)?((5*(lane>>3)+((lane>>1)&3))*1024+(lane&1)*512+508):((5*((lane-32)>>3)+4)*1024+((lane&7)>>1)*256+244+8*(lane&1)));
      for(int it=0;it<SPIN_MAX;++it){ asm volatile("":::"memory");
        if(__all(__builtin_amdgcn_raw_buffer_load_b32(rs,sm,0,16)!=SENT)) break;
        __builtin_amdgcn_s_sleep(1); }
      ST(15);
      const u32 vo=lane*16;
      for(u32 it=1;it<SPIN_MAX;++it){
        #pragma unroll
        for(int i=0;i<5;++i) dma16x4_sc1((const char*)ws+ob+i*4096,vo,vo,vo,vo,L+OIM+w*20480+i*4096);
        waitv<0>();
        ST(7);
        rbar();
        rbar();
        rbar();
        if(__builtin_amdgcn_readfirstlane(*bad)!=it) break; } }
    __builtin_amdgcn_s_waitcnt(0x0F70);
  } else {
    const int hw=w-4;
    asm volatile("s_load_dwordx2 %0, %1, 0x0":"=s"(E):"s"(ws):"memory");
    ST(9);
    waitv<9>(); rbar(); st.issue(2);
    ST(8);
    waitv<9>(); rbar();
    st.issue(3);
    asm volatile("s_waitcnt lgkmcnt(0)":"+s"(E)::"memory"); E+=1;
    if(tid==256) *(volatile LAS u32x2*)(L+B_F+88)=u32x2{(u32)E,(u32)(E>>32)};
    if(!ISQ&&lane<50){ lptr p=L+B_WP+hw*800+lane*16; lds_st<u32x4>(p+3200,lds_ld<u32x4>(p)^0x80008000u); }
    waitv<9>(); bar();
    if(!doout) params();
    st.issue(4);
    const int pr=hw>>1, me2=hw&1;
    u32 wq[40];
    { const int r=lane>>3; const u32 sw=((lane&7)^((((r>>1)&1)|(me2<<1))<<1))<<4, vo=r*1280+sw, vo2=((r>>2)*20+(r&3)+16+32*me2)*1280+sw;
      #pragma unroll
      for(int n=0;n<40;++n){ const int G=n/10, m=n%10, i=(n<22)?n:n-22, k=(i<16)?(i&3):((n>=18&&n<21)?n-18:((i<18)?i-16:0));
        wq[n]=((m<8)?vo+(160*G+20*m)*1280:vo2+(160*G+80*(m-8))*1280)-1024*k; asm volatile("":"+v"(wq[n])); } }
    lptr dst=L+hw*18432, rd, rd1; int x9, x10;
    { const int q=(lane&15)>>2, p=lane&3, f=((q>>1)|((g&1)<<1))<<1, ow=pr*2+(g&1);
      lptr rb=L+ow*18432+(g>>1)*1024+q*128+8*(p&1); rd=rb+(((4*me2+(p>>1))^f)<<4); rd1=rb+(((4*me2+2+(p>>1))^f)<<4);
      x9=B_WP+ow*3072-ow*18432; x10=(g>>1)?(B_PV+17408-1024):(x9+2048); }
    lptr ra9=rd+x9, rb9=rd1+x9, ra10=rd+x10, rb10=rd1+x10;
    asm volatile("":"+v"(rd),"+v"(rd1),"+v"(ra9),"+v"(rb9),"+v"(ra10),"+v"(rb10));
    waitv<9>(); rbar();
    #pragma unroll 1
    for(int s=4;s<9;++s){
      st.issue(s+1);
      waitv<9>(); rbar(); }
    waitv<0>(); rbar();
    if(!doout){ until(7,4u); return true; }
    if(!ISQ){
      const u32x4 sv={SENT,SENT,SENT,SENT};
      #pragma unroll
      for(int i=0;i<4;++i) st_sc1(rs,(i<3||lane<18)?(WS_P1+((4*grp+hw)*16+rt)*3584+i*1024+lane*16):OOB,sv);
      #pragma unroll
      for(int i=0;i<5;++i) st_sc1(rs,WS_O+((8*rt+grp)*20+5*hw+i)*1024+lane*16,sv);
      waitv<0>(); arrive(); }
    u32x4 wf[2][11];
    const char* gb=(const char*)wo+ct*256+pr*128+me2*8*1280;
    auto meet=[&](u32 n){ *(volatile LAS u32*)(L+B_F+48+4*hw)=n; until(12+(hw^1),n); };
    auto fetch18=[&](int n0){
      #pragma unroll
      for(int i=0;i<16;i+=4) dma16x4(gb,wq[n0+i],wq[n0+i+1],wq[n0+i+2],wq[n0+i+3],dst+i*1024);
      dma16xn<2>(gb,wq[n0+16],wq[n0+17],0,dst+16384); };
    until(ISQ?21:7,4u);
    ST(3);
    fetch18(0);
    dma16xn<3>(gb,wq[18],wq[19],wq[20],L+B_WP+hw*3072);
    dma16(gb,wq[21],dst+B_PV+17408);
    waitv<0>(); meet(1u);
    #pragma unroll
    for(int s=0;s<9;++s){ wf[0][s]=cat(lds_tr(rd+s*2048),lds_tr(rd+s*2048+512)); wf[1][s]=cat(lds_tr(rd1+s*2048),lds_tr(rd1+s*2048+512)); }
    wf[0][9]=cat(lds_tr(ra9),lds_tr(ra9+512)); wf[1][9]=cat(lds_tr(rb9),lds_tr(rb9+512));
    wf[0][10]=cat(lds_tr(ra10),lds_tr(ra10+512)); wf[1][10]=cat(lds_tr(rb10),lds_tr(rb10+512));
    asm volatile("s_waitcnt lgkmcnt(0)":::"memory");
    meet(2u);
    ST(10);
#define R2A 8
    #pragma unroll
    for(int i=0;i<R2A;i+=4) dma16x4(gb,wq[22+i],wq[23+i],wq[24+i],wq[25+i],dst+i*1024);
    if(!ISQ){ o_phase<false>(L,rs,ws,E,w,oh,tq,0,0); count(6); ST(6); until(6,8u); }
    #pragma unroll
    for(int i=R2A;i<16;i+=4) dma16x4(gb,wq[22+i],wq[23+i],wq[24+i],wq[25+i],dst+i*1024);
    dma16xn<2>(gb,wq[38],wq[39],0,dst+16384);
    count(20);
    ST(4);
    lptr obt[4];
    #pragma unroll
    for(int t=0;t<4;++t){ obt[t]=L+OIM+lane*16+((t+hw)&3)*20480; asm volatile("":"+v"(obt[t])); }
    waitv<0>();
    for(u32 it=1;it<SPIN_MAX;++it){
      rbar();
      ST(5);
      f32x4 c[2][4];
      #pragma unroll
      for(int t=0;t<4;++t){ c[0][t]=Z4; c[1][t]=Z4; }
      u32x4 ob[3][4], wl[3][2]; u32 chk=0;
      auto fetch=[&](int ks){
        #pragma unroll
        for(int t=0;t<4;++t) ob[ks%3][t]=lds_ld<u32x4>(obt[t]+ks*1024);
        if(ks>=11){ wl[ks%3][0]=cat(lds_tr(rd+(ks-11)*2048),lds_tr(rd+(ks-11)*2048+512)); wl[ks%3][1]=cat(lds_tr(rd1+(ks-11)*2048),lds_tr(rd1+(ks-11)*2048+512)); } };
      fetch(0); fetch(1);
      SB();
      #pragma unroll
      for(int ks=0;ks<20;++ks){ u32x2 lo;
        #pragma unroll
        for(int n=0;n<8;++n){ const int t=n>>1, cc=n&1, k2=ks+2;
          c[cc][t]=mfma16((ks<11)?wf[cc][ks<11?ks:0]:wl[ks%3][cc],ob[ks%3][t],c[cc][t]);
          SB();
          if(k2<20){
            if(n<4) ob[k2%3][n]=lds_ld<u32x4>(obt[n]+k2*1024);
            else if(k2>=11){ lptr p=((n<6)?rd:rd1)+(k2-11)*2048; if(!(n&1)) lo=lds_tr(p); else wl[k2%3][(n-4)>>1]=cat(lo,lds_tr(p+512)); } }
          if(n==7) chk=max(chk,max(ob[ks%3][0].x,ob[ks%3][0].z));
          SB(); } }
      if(__any(chk==SENT)) *bad=it;
      bar();
      #pragma unroll
      for(int t=0;t<4;++t)
        #pragma unroll
        for(int cc=0;cc<2;++cc) lds_st<u32x2>(L+OIM+(16*((t+hw)&3)+(lane&15))*272+hw*64+cc*32+8*g,u32x2{pk_bf16(c[cc][t][0],c[cc][t][1]),pk_bf16(c[cc][t][2],c[cc][t][3])});
      ST(7);
      bar();
      if(__builtin_amdgcn_readfirstlane(*bad)!=it) break; }
  }
  #pragma unroll
  for(int i=0;i<2;++i){ const int r=32*i+(tid>>4);
    __builtin_amdgcn_raw_buffer_store_b128(lds_ld<u32x4>(L+OIM+r*272+(tid&15)*16),__builtin_amdgcn_make_buffer_rsrc(out,0,2048*2560,0x00020000),(64*tt64+r)*2560+1280+ct*256+(tid&15)*16,0,16); }
  if(w<4) ST(8);
  return false;
}

constexpr int CODE_LINES=314;
__global__ __launch_bounds__(512,2) void fused(const u16* u,const u16* are,const u16* aim,const u16* bre,const u16* bim,const u16* cre,const u16* cim,
    const u16* logdt,const u16* dskip,const u16* x,const u16* wqkv,const u16* wphi,const u16* wo,u16* out,void* ws){
  extern __shared__ __attribute__((aligned(16))) char smem[];
  lptr L=(lptr)smem;
  const int b=blockIdx.x, X=b&7, idx=b>>3;
  u64 pc; asm volatile("s_getpc_b64 %0":"=s"(pc));
  const int w=__builtin_amdgcn_readfirstlane(threadIdx.x>>6);
  const bool isa=(idx>>3)==2;
  const int q=idx>=24, li=idx-24*q, rt=4*(X>>1)+(li>>(2-q)), grp=((X&1)<<(2-q))+(li&(3>>q));
  if(idx<16&&w<2){
    const u32 n=(w?(X*16+idx)*50:idx*32)+(threadIdx.x&63); u32 keep;
    if(w?((threadIdx.x&63)<50):(threadIdx.x<32&&n<CODE_LINES))
      asm volatile("s_mov_b32 %0, m0\n\ts_mov_b32 m0, %3\n\ts_nop 0\n\tglobal_load_lds_dword %1, %2\n\ts_mov_b32 m0, %0":"=&s"(keep):"v"(n*128),"s"(w?(u64)wo:(pc&~255ull)),"s"(162560+w*256):"memory"); }
  Stream st;
  if(__builtin_expect(!isa&&w>=4,1)){ st.init(q,li,rt,grp,w&3,threadIdx.x&63,x,wqkv,L); st.issue(0,false);
    { const u32 nb=800<<q, o=(threadIdx.x&63)*16; const char* wp=(const char*)wphi+(4*grp+(w&3))*nb;
      if(o<nb) dma16(wp,o,L+B_WP+(w&3)*nb);
      if(o+1024<nb) dma16(wp,o+1024,L+B_WP+(w&3)*nb+1024); }
    st.issue(1); }
  SB();
  STINIT();
  const int ch0=isa?(80*X+8*(idx-16)):(80*X+64+4*(idx-28)), wi=isa?w:w-4;
  SsmP sp; u32 dsk;
  auto params=[&](){ const SsmIn in={are,aim,bre,bim,cre,cim,logdt}; sp=load_ssm(in,ch0+wi,threadIdx.x&63); dsk=dskip[ch0+wi]; };
  bool job=true;
  if(__builtin_expect(!isa,1)) job=role_b(q,X,li,rt,grp,w,st,L,params,wo,out,ws);
  else params();
  if(job){
    conv_job(isa,wi,ch0,idx&7,L,sp,dsk,u,out,ws); }
  STDUMP();
}

extern "C" void kernel_launch(void* const* d_in, const int* in_sizes, int n_in, void* d_out, int out_size, void* d_ws, size_t ws_size, hipStream_t stream){
  (void)hipFuncSetAttribute((const void*)fused,hipFuncAttributeMaxDynamicSharedMemorySize,163840);
  fused<<<256,512,163840,stream>>>((const u16*)d_in[0],(const u16*)d_in[1],(const u16*)d_in[2],(const u16*)d_in[3],(const u16*)d_in[4],(const u16*)d_in[5],(const u16*)d_in[6],
    (const u16*)d_in[7],(const u16*)d_in[8],(const u16*)d_in[9],(const u16*)d_in[10],(const u16*)d_in[11],(const u16*)d_in[12],(u16*)d_out,d_ws);
}
```

```cpp
#define HB 2
#include <hip/hip_runtime.h>
#include <stdint.h>

typedef unsigned short u16; typedef unsigned u32; typedef unsigned long long u64;
typedef __attribute__((ext_vector_type(4))) short s16x4;
typedef __attribute__((ext_vector_type(8))) _Float16 f16x8;
typedef __attribute__((ext_vector_type(8))) __bf16 bf16x8;
typedef __attribute__((ext_vector_type(2))) __bf16 bf16x2;
typedef __attribute__((ext_vector_type(2))) _Float16 f16x2;
typedef __attribute__((ext_vector_type(2))) float f32x2;
typedef __attribute__((ext_vector_type(4))) float f32x4;
typedef __attribute__((ext_vector_type(16))) float f32x16;
typedef __attribute__((ext_vector_type(2))) unsigned u32x2;
typedef __attribute__((ext_vector_type(3))) unsigned u32x3;
typedef __attribute__((ext_vector_type(4))) unsigned u32x4;
#define LAS __attribute__((address_space(3)))
#define GAS __attribute__((address_space(1)))
#define DI __device__ __forceinline__
typedef LAS char* lptr;
typedef __amdgpu_buffer_rsrc_t rsrc_t;

#define ST(i) do{}while(0)
#define STDUMP() do{}while(0)
#define STINIT() do{}while(0)

DI u32 pk_bf16(float a, float b){ f32x2 v={a,b}; return __builtin_bit_cast(u32,__builtin_convertvector(v,bf16x2)); }
DI u32 pk_f16(f32x2 v){ return __builtin_bit_cast(u32,__builtin_convertvector(v,f16x2)); }
DI float bflo(u32 x){ return __builtin_bit_cast(float,x<<16); }
DI float bfhi(u32 x){ return __builtin_bit_cast(float,x&0xffff0000u); }
DI float rbf(float a){ return bflo(pk_bf16(a,a)); }
template<class T> DI T lds_ld(lptr p){ return *(LAS T*)p; }
template<class T> DI void lds_st(lptr p, T v){ *(LAS T*)p=v; }
DI u32x2 lds_ld64(lptr p){ return *(volatile LAS u32x2*)p; }
DI u32x2 lds_tr(lptr p){ return __builtin_bit_cast(u32x2,__builtin_amdgcn_ds_read_tr16_b64_v4i16((LAS s16x4*)p)); }
DI f32x4 mfma16(u32x4 a,u32x4 b,f32x4 c){ return __builtin_amdgcn_mfma_f32_16x16x32_bf16(__builtin_bit_cast(bf16x8,a),__builtin_bit_cast(bf16x8,b),c,0,0,0); }
DI u32x4 cat(u32x2 a,u32x2 b){ u32x4 r={a.x,a.y,b.x,b.y}; return r; }
DI u32 p_ll(u32 a,u32 b){ return __builtin_amdgcn_perm(b,a,0x05040100u); }
DI u32 p_lh(u32 a,u32 b){ return __builtin_amdgcn_perm(b,a,0x07060100u); }
DI u32 p_hl(u32 a,u32 b){ return __builtin_amdgcn_perm(b,a,0x05040302u); }
DI u32 p_hh(u32 a,u32 b){ return __builtin_amdgcn_perm(b,a,0x07060302u); }
#define SB() __builtin_amdgcn_sched_barrier(0)
DI void rbar(){ asm volatile("":::"memory"); __builtin_amdgcn_s_barrier(); asm volatile("":::"memory"); }
DI void bar(){ asm volatile("s_waitcnt lgkmcnt(0)":::"memory"); __builtin_amdgcn_s_barrier(); asm volatile("":::"memory"); }
template<int N> DI void waitv(){ asm volatile("s_waitcnt vmcnt(%0)"::"i"(N):"memory"); }
constexpr f32x4 Z4={0.f,0.f,0.f,0.f};
DI float wave_max(float m){
  m=fmaxf(m,__builtin_bit_cast(float,__builtin_amdgcn_update_dpp(0,__builtin_bit_cast(int,m),0xB1,0xf,0xf,true)));
  m=fmaxf(m,__builtin_bit_cast(float,__builtin_amdgcn_update_dpp(0,__builtin_bit_cast(int,m),0x4E,0xf,0xf,true)));
  m=fmaxf(m,__builtin_bit_cast(float,__builtin_amdgcn_update_dpp(0,__builtin_bit_cast(int,m),0x141,0xf,0xf,true)));
  m=fmaxf(m,__builtin_bit_cast(float,__builtin_amdgcn_update_dpp(0,__builtin_bit_cast(int,m),0x140,0xf,0xf,true)));
  int x=__builtin_bit_cast(int,m);
  float a=__builtin_bit_cast(float,__builtin_amdgcn_readlane(x,0)), b=__builtin_bit_cast(float,__builtin_amdgcn_readlane(x,16));
  float c=__builtin_bit_cast(float,__builtin_amdgcn_readlane(x,32)), d=__builtin_bit_cast(float,__builtin_amdgcn_readlane(x,48));
  return fmaxf(fmaxf(a,b),fmaxf(c,d)); }

constexpr u32 WS_FLAG=256, WS_P1=4096, WS_Q=WS_P1+32*16*3584, WS_O=WS_Q+32*2048*40, WS_END=WS_O+128*20*1024;
constexpr u32 SENT=0xFFFFFFFFu, OOB=0x80000000u;
constexpr int SPIN_MAX=1<<17;
DI void dma16(const void* sb, u32 voff, lptr l){ u32 keep;
  asm volatile("s_mov_b32 %0, m0\n\ts_mov_b32 m0, %3\n\ts_nop 0\n\tglobal_load_lds_dwordx4 %1, %2\n\ts_mov_b32 m0, %0":"=&s"(keep):"v"(voff),"s"(sb),"s"((u32)(uintptr_t)l):"memory"); }
#define DMA4_BODY(SC) asm volatile("s_mov_b32 %0, m0\n\ts_mov_b32 m0, %6\n\ts_nop 0\n\tglobal_load_lds_dwordx4 %1, %5" SC "\n\tglobal_load_lds_dwordx4 %2, %5 offset:1024" SC \
    "\n\tglobal_load_lds_dwordx4 %3, %5 offset:2048" SC "\n\tglobal_load_lds_dwordx4 %4, %5 offset:3072" SC "\n\ts_mov_b32 m0, %0" \
    :"=&s"(keep):"v"(v0),"v"(v1),"v"(v2),"v"(v3),"s"(sb),"s"((u32)(uintptr_t)l):"memory")
DI void dma16x4(const void* sb,u32 v0,u32 v1,u32 v2,u32 v3,lptr l){ u32 keep; DMA4_BODY(""); }
DI void dma16x4_sc1(const void* sb,u32 v0,u32 v1,u32 v2,u32 v3,lptr l){ u32 keep; DMA4_BODY(" sc1"); }
template<int N> DI void dma16xn(const void* sb,u32 v0,u32 v1,u32 v2,lptr l){ u32 keep;
  if(N==2) asm volatile("s_mov_b32 %0, m0\n\ts_mov_b32 m0, %4\n\ts_nop 0\n\tglobal_load_lds_dwordx4 %1, %3\n\tglobal_load_lds_dwordx4 %2, %3 offset:1024\n\ts_mov_b32 m0, %0"
    :"=&s"(keep):"v"(v0),"v"(v1),"s"(sb),"s"((u32)(uintptr_t)l):"memory");
  else asm volatile("s_mov_b32 %0, m0\n\ts_mov_b32 m0, %5\n\ts_nop 0\n\tglobal_load_lds_dwordx4 %1, %4\n\tglobal_load_lds_dwordx4 %2, %4 offset:1024\n\tglobal_load_lds_dwordx4 %3, %4 offset:2048\n\ts_mov_b32 m0, %0"
    :"=&s"(keep):"v"(v0),"v"(v1),"v"(v2),"s"(sb),"s"((u32)(uintptr_t)l):"memory"); }
DI u32x4 ld_sc1(rsrc_t rs,u32 off){ return __builtin_amdgcn_raw_buffer_load_b128(rs,off,0,16); }
DI void st_sc1(rsrc_t rs,u32 off,u32x4 v){ __builtin_amdgcn_raw_buffer_store_b128(v,rs,off,0,16); }
DI void gather16(rsrc_t rs,u32 off,u32x4* v){
  for(int it=0;it<SPIN_MAX;++it){
    u32 todo=0;
    #pragma unroll
    for(int k=0;k<4;++k){ u32 m=0;
      #pragma unroll
      for(int i=0;i<4;++i) m=max(m,max(v[4*k+i].x,v[4*k+i].z));
      if(__any(m==SENT)) todo|=1u<<k; }
    if(!todo) break;
    asm volatile("":::"memory");
    #pragma unroll
    for(int k=0;k<4;++k) if(todo>>k&1){
      #pragma unroll
      for(int i=0;i<4;++i) v[4*k+i]=ld_sc1(rs,off+(4*k+i)*3584); } } }
DI void flag_set(void* ws,int i,u64 E){ __hip_atomic_store((GAS u64*)((char*)ws+WS_FLAG)+i,E,__ATOMIC_RELAXED,__HIP_MEMORY_SCOPE_AGENT); }
DI u64 flag_get(const void* ws,int i){ return __hip_atomic_load((const GAS u64*)((const char*)ws+WS_FLAG)+i,__ATOMIC_RELAXED,__HIP_MEMORY_SCOPE_AGENT); }
DI void flag_wait(const void* ws,u64 E,int idx,u64 f){
  for(int it=0;it<SPIN_MAX;++it){ if(__all(f==E)) break; __builtin_amdgcn_s_sleep(2); f=flag_get(ws,idx); } }

constexpr int A_UB=4576, A_US=9120, A_KB=4224, A_KS=8704;

DI f32x2 cmul(f32x2 a, f32x2 b){ f32x2 sw={-b.y,b.x}; return __builtin_elementwise_fma(a.yy,sw,a.xx*b); }
DI f32x2 cexp_e(float re2, float rev, float e){
  float m=__builtin_amdgcn_exp2f(re2*e); float a=__builtin_amdgcn_fractf(rev*e);
  f32x2 r={m*__builtin_amdgcn_cosf(a), m*__builtin_amdgcn_sinf(a)}; return r; }
struct SsmP { u32 are,aim,bre,bim,cre,cim,ld; float dt; };
struct SsmIn { const u16 *are,*aim,*bre,*bim,*cre,*cim,*logdt; };
DI SsmP load_ssm(const SsmIn& in, int ch, int lane){
  SsmP p; int o=ch*64+lane;
  p.are=((const u32*)in.are)[o]; p.aim=((const u32*)in.aim)[o]; p.bre=((const u32*)in.bre)[o];
  p.bim=((const u32*)in.bim)[o]; p.cre=((const u32*)in.cre)[o]; p.cim=((const u32*)in.cim)[o];
  p.ld=in.logdt[ch];
  return p; }
DI f32x2 ssm_w(const SsmP& p, int half){
  f32x2 b={half?bfhi(p.bre):bflo(p.bre), half?bfhi(p.bim):bflo(p.bim)};
  f32x2 c={half?bfhi(p.cre):bflo(p.cre), half?bfhi(p.cim):bflo(p.cim)};
  return cmul(c,b*p.dt); }
DI void ssm_prep(SsmP& p, float& s, float& inv){
  asm volatile("":"+v"(p.ld));
  p.dt=rbf(expf(bflo(p.ld)));
  f32x2 w0=ssm_w(p,0), w1=ssm_w(p,1);
  float m=wave_max(fmaxf(fmaxf(fabsf(w0.x),fabsf(w0.y)),fmaxf(fabsf(w1.x),fabsf(w1.y))));
  int e=__builtin_amdgcn_frexp_expf(m); e=max(-100,min(100,e));
  s=__builtin_amdgcn_ldexpf(1.0f,-e); inv=__builtin_amdgcn_ldexpf(1.0f,e); }
DI void kgen_half(const SsmP& p, int half, float s, lptr tab, int lane, f32x16& acc){
  const float are=(half?bfhi(p.are):bflo(p.are))*p.dt, aim=(half?bfhi(p.aim):bflo(p.aim))*p.dt;
  const float re2=are*1.4426950408889634f, rev=aim*0.15915494309189535f;
  lptr wr=tab+lane*4; lptr rd=tab+(lane&31)*272+(lane>>5)*16;
  { f32x2 e8=cexp_e(re2,rev,8.f), e16=cmul(e8,e8);
    f32x2 a={1.f,0.f}, b=e8;
    #pragma unroll
    for(int j=0;j<32;j+=2){ lds_st<u32>(wr+j*272,pk_f16(a)); lds_st<u32>(wr+(j+1)*272,pk_f16(b)); if(j<30){ a=(j==0)?e16:cmul(a,e16); b=cmul(b,e16);} } }
  u32x4 bf[8];
  #pragma unroll
  for(int k=0;k<8;++k) bf[k]=lds_ld<u32x4>(rd+k*32);
  { f32x2 w=ssm_w(p,half)*s; w.y=-w.y;
    f32x2 e1=cexp_e(re2,-rev,1.f), g=cexp_e(re2,-rev,256.f);
    f32x2 g2=cmul(g,g), g3=cmul(g2,g), g4=cmul(g2,g2);
    f32x2 x[8]; x[0]=w;
    #pragma unroll
    for(int i=1;i<8;++i) x[i]=cmul(x[i-1],e1);
    #pragma unroll
    for(int i=0;i<8;++i) lds_st<u32>(wr+i*272,pk_f16(x[i]));
    #pragma unroll
    for(int i=0;i<4;++i) lds_st<u32>(wr+(8+i)*272,pk_f16(cmul(x[2*i],g)));
    lds_st<u32>(wr+12*272,pk_f16(cmul(x[0],g2))); lds_st<u32>(wr+13*272,pk_f16(cmul(x[4],g2)));
    lds_st<u32>(wr+14*272,pk_f16(cmul(x[0],g3))); lds_st<u32>(wr+15*272,pk_f16(cmul(x[4],g3)));
    f32x2 y=cmul(w,g4);
    #pragma unroll
    for(int i=0;i<5;++i){ lds_st<u32>(wr+(16+i)*272,pk_f16(y)); if(i<4) y=cmul(y,g); } }
  #pragma unroll
  for(int k=0;k<8;++k){ u32x4 af=lds_ld<u32x4>(rd+k*32);
    acc=__builtin_amdgcn_mfma_f32_32x32x16_f16(__builtin_bit_cast(f16x8,af),__builtin_bit_cast(f16x8,bf[k]),acc,0,0,0); }
}
DI void k_finish(const float* v, lptr ka, int lane){
  lptr ks=ka; lptr kb=ka+A_KB;
  { const int j=lane&31, h=lane>>5;
    lds_st<u32x2>(ks+(8*j+4*h)*2,u32x2{pk_bf16(v[0],v[1]),pk_bf16(v[2],v[3])});
    u32x2 b={pk_bf16(v[4],v[5]),pk_bf16(v[6],v[7])};
    u32 c0=pk_bf16(v[8],v[9]), c1=pk_bf16(v[10],v[11]);
    if(h==0){ lds_st<u32x2>(ks+(256+4*j)*2,b);
      lds_st<u16>(ks+(512+j)*2,(u16)c0); lds_st<u16>(ks+(544+j)*2,(u16)(c0>>16));
      lds_st<u16>(ks+(576+j)*2,(u16)c1); lds_st<u16>(ks+(608+j)*2,(u16)(c1>>16));
    } else { lds_st<u32>(ks+(384+2*j)*2,b.x); lds_st<u32>(ks+(448+2*j)*2,b.y); if(j==0) lds_st<u16>(ks+640*2,(u16)c0); } }
  const int M=lane; const bool lo=M<32;
  u32x4 v0=lds_ld<u32x4>(ks+(M&31)*16); u32x2 v1=lds_ld<u32x2>(ks+(128+4*(M|32))*2);
  u32 v4=lds_ld<u16>(ks+(132+4*(M|32))*2);
  u32 pv=lds_ld<u16>(ks+(lo?max(8*M-1,0):((M==32)?255:(128+4*M)))*2); if(M==0) pv=0;
  u32 ab=lds_ld<u32>(ks+(384+2*M)*2), c2=lds_ld<u16>(ks+(386+2*M)*2);
  u32 a3[2],b3[2];
  #pragma unroll
  for(int i=0;i<2;++i){ a3[i]=lds_ld<u16>(ks+(512+M+64*i)*2); b3[i]=lds_ld<u16>(ks+(513+M+64*i)*2); }
  asm volatile("":"+v"(v0),"+v"(v1),"+v"(v4),"+v"(pv),"+v"(ab),"+v"(c2),"+v"(a3[0]),"+v"(a3[1]),"+v"(b3[0]),"+v"(b3[1])::"memory");
  { u32x4 A,B;
    if(lo){ B.x=p_hl(v0.w,v0.w); B.y=p_hl(v0.z,v0.z); B.z=p_hl(v0.y,v0.y); B.w=p_hl(v0.x,v0.x);
      A.x=p_lh(v0.w,v0.z); A.y=p_lh(v0.z,v0.y); A.z=p_lh(v0.y,v0.x); A.w=p_ll(v0.x,pv);
    } else { B.x=p_lh(v4,v1.y); B.y=p_ll(v1.y,v1.y); B.z=p_lh(v1.y,v1.x); B.w=p_ll(v1.x,v1.x);
      A.x=p_hl(v1.y,v1.y); A.y=B.y; A.z=p_hl(v1.x,v1.x); A.w=p_ll(v1.x,pv); }
    lds_st<u32x4>(ka+(255-M)*16,A); lds_st<u32x4>(kb+(255-M)*16,B); }
  { u32x4 A,B; B.x=p_ll(c2,c2); B.y=p_hh(ab,ab); B.z=p_hl(ab,ab); B.w=p_ll(ab,ab);
    A.x=p_lh(c2,ab); A.y=B.y; A.z=B.w; A.w=B.w;
    lds_st<u32x4>(ka+(191-M)*16,A); lds_st<u32x4>(kb+(191-M)*16,B); }
  #pragma unroll
  for(int i=0;i<2;++i){ u32 a=a3[i], b=b3[i], c=(M&1)?b:a; u32x4 A,B;
    B.x=p_ll(b,b); B.y=p_ll(b,c); B.z=p_ll(a,a); B.w=B.z;
    A.x=B.x; A.y=p_ll(c,a); A.z=B.z; A.w=B.z;
    lds_st<u32x4>(ka+(127-64*i-M)*16,A); lds_st<u32x4>(kb+(127-64*i-M)*16,B); }
  if(lane<8) lds_st<u32x4>(((lane&4)?kb:ka)+(256+(lane&3))*16,u32x4{0,0,0,0});
}
DI void conv_steps(lptr ua, lptr ka, int lane, f32x4* acc){
  constexpr int NS=8, s0=0, kbo=A_KB;
  const int a=lane&15, g=lane>>4;
  lptr up=ua+((a&1)?0:A_UB)+(32*(a>>1)+8*g+32*s0)*2;
  lptr kp=ka+((a&1)?kbo:0)+(28-4*(a>>1)+8*g+32*s0)*2;
  u32x4 A[2][8],B[2][8];
  auto ldA=[&](int s,int q){ A[s&1][q]=cat(lds_ld64(kp+(1792-256*q+32*s)*2),lds_ld64(kp+(1792-256*q+32*s)*2+8)); };
  auto ldB=[&](int s,int q){ B[s&1][q]=lds_ld<u32x4>(up+(256*q+32*s)*2); };
  #pragma unroll
  for(int q=0;q<8;++q){ ldB(0,q); ldA(0,q); }
  #pragma unroll
  for(int c=0;c<8;++c) acc[c]=Z4;
  SB();
  #pragma unroll
  for(int s=0;s<NS;++s){ int n=0; u32x2 lo;
    #pragma unroll
    for(int q=0;q<8;++q){
      #pragma unroll
      for(int c=q;c<8;++c){
        acc[c]=mfma16(A[s&1][q],B[s&1][c-q],acc[c]);
        SB();
        if(s+1<NS){
          if(n==0) ldB(s+1,0);
          else if(n<3){ if(n==1) lo=lds_ld64(kp+(1792+32*(s+1))*2); else A[(s+1)&1][0]=cat(lo,lds_ld64(kp+(1792+32*(s+1))*2+8)); }
          else if(n<10) ldB(s+1,n-2);
          else if(n<24){ const int j=1+((n-10)>>1); if(!(n&1)) lo=lds_ld64(kp+(1792-256*j+32*(s+1))*2); else A[(s+1)&1][j]=cat(lo,lds_ld64(kp+(1792-256*j+32*(s+1))*2+8)); } }
        SB();
        ++n; } } }
}
DI u32 fin2(float y0,float y1,u32 u,float dsk){
  u32 y=pk_bf16(y0,y1), s=pk_bf16(bflo(u)*dsk,bfhi(u)*dsk);
  return pk_bf16(bflo(y)+bflo(s),bfhi(y)+bfhi(s)); }
DI void u_scatter(const bool BIG, int ur, lptr L, const u32x4* um){
  #pragma unroll
  for(int c=0;c<4;++c){
    lptr d=L+(224+4*ur)*2+(BIG?2*c*A_US:(2*(c&1)*A_US+(c>>1)*2048)); const bool last=BIG?(ur==511):(c>=2&&ur==255);
    const u32x2 e={p_ll(um[0][c],um[1][c]),p_ll(um[2][c],um[3][c])}, o={p_hh(um[0][c],um[1][c]),p_hh(um[2][c],um[3][c])};
    lds_st<u32x2>(d,e); lds_st<u32x2>(d+A_US,o);
    lds_st<u32>(d+A_UB+4,e.x); lds_st<u32>(d+A_US+A_UB+4,o.x);
    if(!last){ lds_st<u32>(d+A_UB+8,e.y); lds_st<u32>(d+A_US+A_UB+8,o.y); } } }
DI void conv_job(const bool BIG, int wi, int ch0, int rot, lptr L, SsmP& p0, u32 dsk, const u16* u, u16* out, void* ws){
  const int lane=threadIdx.x&63;
  lptr us=L+wi*A_US, kw=L+(BIG?8:4)*A_US+wi*A_KS;
  if(lane<56) lds_st<u32x4>(us+(lane<28?lane*16:A_UB+(lane-28)*16),u32x4{0,0,0,0});
  if(lane==56) lds_st<u32>(us+A_UB+448,0u);
  u32x4 um[4];
  const int ur=(64*(wi+rot)+lane)&511;
  ST(1);
  { float s,inv; ssm_prep(p0,s,inv); f32x16 acc={};
    #pragma unroll 1
    for(int h=0;h<2;++h){
      if(BIG&&h==1){ const char* up=(const char*)u+(size_t)ur*4*1280+2*ch0;
        #pragma unroll
        for(int r=0;r<4;++r) um[r]=*(const u32x4*)(up+r*1280); }
      kgen_half(p0,h,s,kw,lane,acc); }
    float v[12];
    #pragma unroll
    for(int r=0;r<12;++r) v[r]=acc[r]*inv;
    k_finish(v,kw,lane); }
  ST(2);
  if(BIG) u_scatter(true,ur,L,um);
  bar();
  ST(3);
  f32x4 acc[8];
  conv_steps(us,kw,lane,acc);
  ST(4);
  { const int tl=32*((lane&15)>>1)+8*(lane>>4)+2*(lane&1); asm volatile("":"+v"(dsk)); const float d=bflo(dsk);
    u32 uu[16];
    #pragma unroll
    for(int c=0;c<8;++c){ uu[2*c]=lds_ld<u32>(us+(224+256*c+tl)*2); uu[2*c+1]=lds_ld<u32>(us+(228+256*c+tl)*2); }
    #pragma unroll
    for(int c=0;c<8;++c){ lds_st<u32>(kw+(256*c+tl)*2,fin2(acc[c][0],acc[c][1],uu[2*c],d)); lds_st<u32>(kw+(256*c+tl+4)*2,fin2(acc[c][2],acc[c][3],uu[2*c+1],d)); } }
  bar();
  ST(5);
  { lptr yp=L+(BIG?8:4)*A_US+((lane&15)>>2)*A_KS+(256*wi+16*(lane>>4)+4*(lane&3))*2;
    char* op=(char*)out+(size_t)(256*wi+lane)*2560+2*ch0;
    #pragma unroll
    for(int i=0;i<4;++i){
      if(BIG) *(u32x4*)(op+(size_t)i*64*2560)=cat(lds_tr(yp+i*128),lds_tr(yp+4*A_KS+i*128));
      else { *(u32x2*)(op+(size_t)i*64*2560)=lds_tr(yp+i*128); *(u32x2*)(op+(size_t)(i*64+1024)*2560)=lds_tr(yp+2048+i*128); } } }
  ST(7);
}

constexpr int B_ST=36864, B_PV=2*B_ST, B_PVS=18432, B_WP=4*B_ST, B_Z=B_WP+6400, B_KT=159744, B_F=163168;
DI float max3(float a,float b,float c){ return __builtin_fmaxf(__builtin_fmaxf(a,b),c); }
template<class H> DI void softmax20x2(const f32x4 (*z)[5], u32 (*o)[10], H hook){
  const float c=1.4426950408889634f; float mc[2];
  { float m0[2],m1[2],m2[2];
    #pragma unroll
    for(int t=0;t<2;++t){ m0[t]=max3(z[t][0][0],z[t][0][1],z[t][0][2]); m1[t]=max3(z[t][0][3],z[t][1][0],z[t][1][1]); m2[t]=max3(z[t][1][2],z[t][1][3],z[t][2][0]); }
    #pragma unroll
    for(int t=0;t<2;++t){ m0[t]=max3(m0[t],z[t][2][1],z[t][2][2]); m1[t]=max3(m1[t],z[t][2][3],z[t][3][0]); m2[t]=max3(m2[t],z[t][3][1],z[t][3][2]); }
    #pragma unroll
    for(int t=0;t<2;++t){ m0[t]=max3(m0[t],z[t][3][3],z[t][4][0]); m1[t]=max3(m1[t],z[t][4][1],z[t][4][2]); m2[t]=__builtin_fmaxf(m2[t],z[t][4][3]); }
    #pragma unroll
    for(int t=0;t<2;++t) mc[t]=max3(m0[t],m1[t],m2[t])*-c; }
  SB();
  f32x2 e[2][10];
  #pragma unroll
  for(int i=0;i<10;++i)
    #pragma unroll
    for(int t=0;t<2;++t) e[t][i]=__builtin_elementwise_fma(f32x2{z[t][i>>1][(i&1)*2],z[t][i>>1][(i&1)*2+1]},f32x2{c,c},f32x2{mc[t],mc[t]});
  SB();
  #pragma unroll
  for(int i=0;i<10;++i)
    #pragma unroll
    for(int t=0;t<2;++t){ hook(2*i+t); e[t][i]=f32x2{__builtin_amdgcn_exp2f(e[t][i].x),__builtin_amdgcn_exp2f(e[t][i].y)}; SB(); }
  float r[2];
  { f32x2 s[2][5];
    #pragma unroll
    for(int i=0;i<5;++i)
      #pragma unroll
      for(int t=0;t<2;++t) s[t][i]=e[t][2*i]+e[t][2*i+1];
    #pragma unroll
    for(int t=0;t<2;++t){ s[t][0]+=s[t][1]; s[t][2]+=s[t][3]; }
    #pragma unroll
    for(int t=0;t<2;++t){ s[t][0]+=s[t][4]; }
    #pragma unroll
    for(int t=0;t<2;++t){ s[t][0]+=s[t][2]; }
    #pragma unroll
    for(int t=0;t<2;++t) r[t]=__builtin_amdgcn_rcpf(s[t][0].x+s[t][0].y); }
  SB();
  #pragma unroll
  for(int i=0;i<10;++i)
    #pragma unroll
    for(int t=0;t<2;++t) e[t][i]*=r[t];
  SB();
  #pragma unroll
  for(int i=0;i<10;++i)
    #pragma unroll
    for(int t=0;t<2;++t) o[t][i]=pk_bf16(e[t][i].x,e[t][i].y);
  SB();
}
DI u32x4 pack2(f32x4 a,f32x4 b){ u32x4 r={pk_bf16(a[0],a[1]),pk_bf16(a[2],a[3]),pk_bf16(b[0],b[1]),pk_bf16(b[2],b[3])}; return r; }
struct Stream { u32 xo[4], wv[5]; int s0, qw; const u16 *x, *w; lptr L;
  DI void init(int q,int li,int rt,int grp,int qw_,int lane,const u16* x_,const u16* w_,lptr L_){
    qw=qw_; x=x_; w=w_; L=L_;
    s0=((0x8530>>((((li>>(2-q))+((li&(3>>q))<<q))&3)*4))&15)+q;
    #pragma unroll
    for(int i=0;i<4;++i){ const u32 r=(4*qw+i)*8+(lane>>3); xo[i]=(rt*128+r)*1280u+(((lane&7)^((r>>1)&7))<<4)-1024u*i; }
    dma16x4((const char*)x+(s0>=10?s0-10:s0)*128,xo[0],xo[1],xo[2],xo[3],L+4*qw*1024);
    const u32 wlo=(160<<q)*grp+1280*(1-q), dh=1120*(1-q);
    #pragma unroll
    for(int i=0;i<5;++i){ const u32 c=(5*qw+i)*64+lane, r=(c*3277u)>>16, pos=c-r*20, sc=pos^(((r>>3)&1)<<1);
      wv[i]=r*3840u+wlo+(sc<10?0u:dh)+16*sc-1024u*(i&3); } }
  DI void issue(int s,bool withx=true) const { int ss=s+s0; if(ss>=10) ss-=10; lptr d=L+(s&3)*B_ST;
    const char* xb=(const char*)x+ss*128; const char* wb=(const char*)w+(size_t)ss*64*3840;
    if(withx) dma16x4(xb,xo[0],xo[1],xo[2],xo[3],d+4*qw*1024);
    dma16x4(wb,wv[0],wv[1],wv[2],wv[3],d+16384+5*qw*1024); dma16(wb,wv[4],d+16384+(5*qw+4)*1024); } };

template<bool GATHER> DI void o_phase(lptr L,rsrc_t rs,void* ws,u64 E,int w,int oh,int tq,int fi,u64 fl){
  const int lane=threadIdx.x&63, g=lane>>4, r=lane&15;
  if(GATHER){ if(!__all(fl==E)) flag_wait(ws,E,fi,fl); *(volatile LAS u32*)(L+B_F+128+4*w)=1u; }
  else for(int it=0;it<SPIN_MAX;++it){ if(__builtin_amdgcn_readfirstlane(*(volatile LAS u32*)(L+B_F+128+4*(w&3)))) break; __builtin_amdgcn_s_sleep(2); }
  asm volatile("":::"memory");
  u32x4 l0[2],l1[2]; u32x2 l2[2];
  { const u32 qo=WS_Q+(oh*2048+512*tq+128*(w&3)+64*(w>>2)+16*(g>>1)+r)*40;
    #pragma unroll
    for(int i=0;i<2;++i){ l0[i]=ld_sc1(rs,qo+i*1280); l1[i]=ld_sc1(rs,qo+i*1280+16); l2[i]=__builtin_amdgcn_raw_buffer_load_b64(rs,qo+i*1280+32,0,16); } }
  lptr kt=L+B_KT, q00,q01,q10,q11; u32 om,ox;
  { const int pc=5*(g&1)+2*(g>>1), pl=5*(g&1)+4, d1=min(r,4); lptr b=kt;
    q00=b+(16*pc+r)*8; q01=b+(16*pl+r)*8+4*(g>>1); q10=b+(160+5*pc+d1)*8; q11=b+(160+5*pl+d1)*8+4*(g>>1);
    const u32 fb=WS_O+((32*tq+8*(w&3)+4*(w>>2))*20+5*(oh>>3))*1024+r*16;
    om=fb+((oh&7)>>1)*1024+(2*(oh&1)+(g>>1))*256+8*(g&1); ox=(g==0)?(fb+4096+((oh&7)>>1)*256+8*(oh&1)):OOB; }
  asm volatile("":"+v"(q00),"+v"(q01),"+v"(q10),"+v"(q11),"+v"(om),"+v"(ox));
  ST(11);
  u32x4 v[16]; const u32 go=(lane<53&&53*w+lane<210)?(WS_P1+oh*16*3584+(53*w+lane)*16):OOB;
  u32 pq[2][10];
  { const u32 sg=(g&1)?0x80008000u:0u; f32x4 z[2][5];
    #pragma unroll
    for(int t=0;t<2;++t){ const u32x4 a=l0[t]^sg, b=l1[t]^sg; const u32x2 c=l2[t]^sg;
      z[t][0]=f32x4{bflo(a.x),bfhi(a.x),bflo(a.y),bfhi(a.y)}; z[t][1]=f32x4{bflo(a.z),bfhi(a.z),bflo(a.w),bfhi(a.w)};
      z[t][2]=f32x4{bflo(b.x),bfhi(b.x),bflo(b.y),bfhi(b.y)}; z[t][3]=f32x4{bflo(b.z),bfhi(b.z),bflo(b.w),bfhi(b.w)};
      z[t][4]=f32x4{bflo(c.x),bfhi(c.x),bflo(c.y),bfhi(c.y)}; }
    SB();
    softmax20x2(z,pq,[&](int k){ if(GATHER&&k<16) v[k]=ld_sc1(rs,go+k*3584); });
    #pragma unroll
    for(int i=0;i<2;++i){
      #pragma unroll
      for(int k=0;k<5;++k){ const int a=(k<4)?k:8, b=(k<4)?k+4:9;
        auto s=__builtin_amdgcn_permlane32_swap(pq[i][a],pq[i][b],false,false); pq[i][a]=s[0]; pq[i][b]=s[1]; }
      #pragma unroll
      for(int k=0;k<10;++k) asm volatile("":"+v"(pq[i][k])); } }
  ST(12);
  if(GATHER){
    gather16(rs,go,v);
    ST(13);
    f32x4 s[4];
    #pragma unroll
    for(int i=0;i<4;++i) s[i]=(__builtin_bit_cast(f32x4,v[4*i])+__builtin_bit_cast(f32x4,v[4*i+1]))+(__builtin_bit_cast(f32x4,v[4*i+2])+__builtin_bit_cast(f32x4,v[4*i+3]));
    s[0]=(s[0]+s[1])+(s[2]+s[3]);
    if(lane<53) *(volatile LAS u32x2*)(kt+(53*w+lane)*8)=u32x2{pk_bf16(s[0][0],s[0][1]),pk_bf16(s[0][2],s[0][3])};
    *(volatile LAS u32*)(L+B_F+32+4*w)=1u;
  }
  u32x4 a0[2]; u32 a1[2];
  for(int it=0;it<SPIN_MAX;++it){
    const u32x4 f=*(volatile LAS u32x4*)(L+B_F+32);
    a0[0]=cat(*(volatile LAS u32x2*)q00,*(volatile LAS u32x2*)(q00+128)); a0[1]=cat(*(volatile LAS u32x2*)q10,*(volatile LAS u32x2*)(q10+40));
    a1[0]=*(volatile LAS u32*)q01; a1[1]=*(volatile LAS u32*)q11;
    if(__builtin_amdgcn_readfirstlane(f.x+f.y+f.z+f.w)==4u) break;
    if(!GATHER) __builtin_amdgcn_s_sleep(1); }
  ST(14);
  f32x4 n[4][2];
  #pragma unroll
  for(int i=1;i>=0;--i){
    #pragma unroll
    for(int t=0;t<4;++t){ const u32* f=pq[t>>1]+4*(t&1); n[t][i]=mfma16(a0[i],u32x4{f[0],f[1],f[2],f[3]},Z4); }
    #pragma unroll
    for(int t=0;t<4;++t) n[t][i]=mfma16(u32x4{a1[i],0u,0u,0u},u32x4{pq[t>>1][8+(t&1)],0u,0u,0u},n[t][i]); }
  SB();
  float den[4];
  #pragma unroll
  for(int t=0;t<4;++t) den[t]=__builtin_bit_cast(float,__builtin_amdgcn_ds_bpermute((r+16)*4,__builtin_bit_cast(int,n[t][1][0])));
  #pragma unroll
  for(int t=0;t<4;++t) den[t]=__builtin_amdgcn_rcpf(rbf(rbf(den[t])+1e-6f));
  SB();
  #pragma unroll
  for(int t=0;t<4;++t){ const f32x4 m=n[t][0]*den[t], e=n[t][1]*den[t];
    __builtin_amdgcn_raw_buffer_store_b64(u32x2{pk_bf16(m[0],m[1]),pk_bf16(m[2],m[3])},rs,om+t*20480,0,16);
    __builtin_amdgcn_raw_buffer_store_b64(u32x2{pk_bf16(e[0],e[1]),pk_bf16(e[2],e[3])},rs,ox+t*20480,0,16); }
}

template<class F> DI bool role_b(const bool ISQ,int X,int li,int rt,int grp,int w,const Stream& st,lptr L,F params,const u16* u,const u16* wo,u16* out,void* ws){
  const int tid=threadIdx.x, lane=tid&63, g=lane>>4;
  const int T=ISQ?16+li:li; const bool doout=T<20; const int tt64=4*X+T/5, ct=T%5;
  const rsrc_t rs=__builtin_amdgcn_make_buffer_rsrc(ws,0,WS_END,0x00020000);
  constexpr int OIM=B_PV;
  u64 E;
  auto count=[&](int word){ asm volatile("":::"memory"); u32 n=0; if(lane==0) n=__hip_atomic_fetch_add((LAS u32*)(L+B_F+4*word),1u,__ATOMIC_RELAXED,__HIP_MEMORY_SCOPE_WORKGROUP); return n; };
  auto until=[&](int word,u32 n){ for(int it=0;it<SPIN_MAX;++it){ if(__builtin_amdgcn_readfirstlane(*(volatile LAS u32*)(L+B_F+4*word))>=n) break; __builtin_amdgcn_s_sleep(2); } asm volatile("":::"memory"); };
  auto arrive=[&](){
    if(__builtin_amdgcn_readfirstlane(count(4))==3u&&lane==0){ flag_set(ws,ISQ?(128+4*rt+grp):(16*grp+rt),E); asm volatile("":::"memory"); *(volatile LAS u32*)(L+B_F+84)=4u; } };
  volatile LAS u32* bad=(volatile LAS u32*)(L+B_F+20);
  const int oh=4*grp+(rt&3), tq=rt>>2;
  const int ot=4*tt64+(w&3);
  int fi=(lane<16)?(16*grp+lane):((lane<32)?(16*(lane&7)+4*tq+(w&3)):((lane<48)?(16*(ot&7)+(ot>>3)):(128+4*(4*tq+(w&3))+(oh>>3))));
  if(w<4){
    const int th=w>>1, hh=w&1;
    if(lane<4) lds_st<u32x4>(L+B_Z+lane*16,u32x4{0,0,0,0});
    if(tid<9) lds_st<u32x4>(L+B_F+tid*16,u32x4{0,0,0,0});
    lptr fad[3][2];
    { const int q=(lane&15)>>2, p=lane&3, hd=ISQ?p:(p&1);
      #pragma unroll
      for(int s=0;s<3;++s)
        #pragma unroll
        for(int t=0;t<2;++t){ const int d=16*(2*s+t)+4*g-20*hd; const bool ok=(d>=0)&&(d<20)&&(2*s+t<5);
          fad[s][t]=ok?(L+B_WP+((ISQ?4*hh:2*hh)+hd)*800+(d+q)*40+(ISQ?0:(p>>1)*3200)):(L+B_Z); if(2*s+t<5) asm volatile("":"+v"(fad[s][t])); } }
    lptr pv=L+B_PV+w*B_PVS;
    lptr pp=pv+(lane&15)*168+(g&1)*80+(g>>1)*40, vp2, vp3, vp4, pa, vb;
    { lptr vp=pv+10752+(lane&15)*104; vp2=vp+((g<2)?(48*g+40):(8*g-16)); vp3=vp+((g==3)?48:(16+8*g)); vp4=vp+56+8*g;
      const int tk=16*(g&1)+4*((lane&15)>>2)+2*(g>>1); pa=pv+tk*168+8*(lane&3)+(th^1)*80; vb=pv+10752+tk*104+8*(lane&3)+(th^1)*48; }
    u32 po0, po1;
    { const u32 po=WS_P1+((4*grp+2*hh+th)*16+rt)*3584; po0=po+lane*16; po1=((lane&15)<5)?(po+(160+5*g+(lane&15))*16):OOB; }
    asm volatile("":"+v"(fi),"+v"(pp),"+v"(vp2),"+v"(vp3),"+v"(vp4),"+v"(pa),"+v"(vb),"+v"(po0),"+v"(po1));
    lptr ab[5], bb[2];
    { const int q=(lane&15)>>2,p=lane&3;
      #pragma unroll
      for(int i=0;i<5;++i){ int n=2*i+(p>>1); int cid=ISQ?(10*hh+n):(5*hh+n+(n>=5?5:0)); ab[i]=L+16384+(8*g+q)*320+((cid^((g&1)<<1))<<4)+8*(p&1); }
      const int b0=(64*th+(lane&15))*128+((g^((lane&15)>>1))<<4); bb[0]=L+b0; bb[1]=L+(b0^64); }
    f32x4 acc[5][4];
    #pragma unroll
    for(int i=0;i<5;++i)
      #pragma unroll
      for(int t=0;t<4;++t){ acc[i][t]=Z4; asm volatile("":"+v"(acc[i][t])); }
    ST(1);
    u32x4 FA[2][5], FB[2][4], fa[2][5];
    u32x2 lo2;
    auto half=[&](int hf,bool mm,bool last,int step){
      u32x2 lo;
      #pragma unroll
      for(int n=0;n<20;++n){ const int i=n>>2, t=n&3;
        if(mm) acc[i][t]=mfma16(FA[hf^1][i],FB[hf^1][t],acc[i][t]);
        SB();
        if(n==0) FB[hf][0]=lds_ld<u32x4>(bb[hf]);
        else if(n==1) lo=lds_tr(ab[0]+hf*10240);
        else if(n==2) FA[hf][0]=cat(lo,lds_tr(ab[0]+hf*10240+1280));
        else if(n<6) FB[hf][n-2]=lds_ld<u32x4>(bb[hf]+2048*(n-2));
        else if(n<14){ const int j=1+((n-6)>>1); if(!(n&1)) lo=lds_tr(ab[j]+hf*10240); else FA[hf][j]=cat(lo,lds_tr(ab[j]+hf*10240+1280)); }
        else if(hf==1&&!last){ if(n<19){ ab[n-14]+=step; asm volatile("":"+v"(ab[n-14])); } if(n>=18){ bb[n-18]+=step; asm volatile("":"+v"(bb[n-18])); } }
        if(last){ const int j=n>>2, s=(n>>1)&1; if(!(n&1)) lo2=lds_tr(fad[s][0]+8*j); else fa[s][j]=cat(lo2,lds_tr(fad[s][1]+8*j)); }
        SB(); } };
    bar();
    ST(2);
    __builtin_amdgcn_s_waitcnt(0xC07F);
    SB();
    half(0,false,false,0);
    #pragma unroll 1
    for(u32 s=1;s<10;++s){
      half(1,true,false,(s&3)?B_ST:-3*B_ST);
      rbar();
      SB();
      half(0,true,false,0); }
    const u32x2 ev=*(volatile LAS u32x2*)(L+B_F+88);
    half(1,true,true,0);
    E=(u64)(u32)__builtin_amdgcn_readfirstlane(ev.x)|((u64)(u32)__builtin_amdgcn_readfirstlane(ev.y)<<32);
    asm volatile("s_waitcnt lgkmcnt(0)":::"memory");
    waitv<0>();
    count(7);
    #pragma unroll
    for(int t=0;t<4;++t)
      #pragma unroll
      for(int i=0;i<5;++i) acc[i][t]=mfma16(FA[1][i],FB[1][t],acc[i][t]);
    u32x2 pk[5][4];
    #pragma unroll
    for(int t=0;t<4;++t)
      #pragma unroll
      for(int i=0;i<5;++i) pk[i][t]=u32x2{pk_bf16(acc[i][t][0],acc[i][t][1]),pk_bf16(acc[i][t][2],acc[i][t][3])};
    ST(3);
    if(ISQ){
      u32x4 f2[5];
      #pragma unroll
      for(int i=0;i<5;++i) f2[i]=cat(lds_tr(fad[2][0]+8*i),u32x2{0u,0u});
      lptr sp_=pv+g*2560+(lane&15)*40;
      f32x4 z[4][5];
      #pragma unroll
      for(int t=0;t<4;++t)
        #pragma unroll
        for(int i=0;i<5;++i) z[t][i]=mfma16(fa[0][i],cat(pk[0][t],pk[1][t]),Z4);
      #pragma unroll
      for(int t=0;t<4;++t)
        #pragma unroll
        for(int i=0;i<5;++i) z[t][i]=mfma16(fa[1][i],cat(pk[2][t],pk[3][t]),z[t][i]);
      SB();
      #pragma unroll
      for(int t=0;t<5;++t){
        #pragma unroll
        for(int i=0;i<5;++i){ if(t<4) z[t][i]=mfma16(f2[i],cat(pk[4][t],u32x2{0u,0u}),z[t][i]);
          if(t>0) lds_st<u32x2>(sp_+(t-1)*640+8*i,u32x2{pk_bf16(z[t-1][i][0],z[t-1][i][1]),pk_bf16(z[t-1][i][2],z[t-1][i][3])}); }
        SB(); }
      ST(10);
      #pragma unroll
      for(int i=0;i<10;++i){ const int c=i*64+lane, hd=(c*205)>>15;
        st_sc1(rs,WS_Q+((8*grp+4*hh+hd)*2048+128*rt+64*th)*40+(c-160*hd)*16,lds_ld<u32x4>(pv+c*16)); }
      ST(11);
      waitv<0>(); ST(12); arrive();
      ST(4);
      if(doout){ fi=16*(ot&7)+(ot>>3); flag_wait(ws,E,fi,flag_get(ws,fi)); }
    } else {
      { f32x4 z[4][5]; u32 ph[4][10];
        auto fm=[&](int t){
          const u32x4 b0=cat(pk[0][t],pk[1][t]), b1=cat(pk[2][t],pk[3][t]);
          #pragma unroll
          for(int i=0;i<5;++i) z[t][i]=mfma16(fa[0][i],b0,Z4);
          #pragma unroll
          for(int i=0;i<5;++i) z[t][i]=mfma16(fa[1][i],b1,z[t][i]); };
        fm(0); fm(1);
        SB();
        softmax20x2(z,ph,[&](int k){ const int t=2+k/10, i=k%5;
          z[t][i]=(k%10<5)?mfma16(fa[0][i],cat(pk[0][t],pk[1][t]),Z4):mfma16(fa[1][i],cat(pk[2][t],pk[3][t]),z[t][i]); });
        auto wp=[&](int t){
          #pragma unroll
          for(int i=0;i<5;++i) lds_st<u32x2>(pp+t*2688+8*i,u32x2{ph[t][2*i],ph[t][2*i+1]}); };
        u32x2 v2[4];
        #pragma unroll
        for(int t=0;t<4;++t) v2[t]=(g<2)?u32x2{0x3F80u,0u}:pk[2][t];
        softmax20x2(z+2,ph+2,[&](int k){
          if(k<10) *(volatile LAS u32x2*)(pp+(k/5)*2688+8*(k%5))=u32x2{ph[k/5][2*(k%5)],ph[k/5][2*(k%5)+1]};
          else if(k<14) *(volatile LAS u32x2*)(vp2+(k-10)*1664)=v2[k-10];
          else if(k<18) *(volatile LAS u32x2*)(vp3+(k-14)*1664)=pk[3][k-14];
          else { *(volatile LAS u32x2*)(vp4+(2*k-36)*1664)=pk[4][2*k-36]; *(volatile LAS u32x2*)(vp4+(2*k-35)*1664)=pk[4][2*k-35]; } });
        wp(2); wp(3); }
      ST(4);
      u64 fl;
      { f32x4 c[2][6];
        #pragma unroll
        for(int h2=0;h2<2;++h2){
          if(h2){ pa+=th?80:-80; vb+=th?48:-48; }
          u32x4 a[2][3], b[2][2];
          #pragma unroll
          for(int ks=0;ks<2;++ks){
            #pragma unroll
            for(int i=0;i<3;++i) a[ks][i]=cat(lds_tr(pa+ks*32*168+32*i),lds_tr(pa+(ks*32+1)*168+32*i));
            #pragma unroll
            for(int dt=0;dt<2;++dt) b[ks][dt]=cat(lds_tr(vb+ks*32*104+32*dt),lds_tr(vb+(ks*32+1)*104+32*dt)); }
          #pragma unroll
          for(int i=0;i<6;++i) c[h2][i]=mfma16(a[0][i%3],b[0][i/3],Z4);
          #pragma unroll
          for(int i=0;i<6;++i) c[h2][i]=mfma16(a[1][i%3],b[1][i/3],c[h2][i]); }
        #pragma unroll
        for(int i=0;i<6;++i) *(volatile LAS f32x4*)(pv+i*1024+lane*16)=c[0][i];
        *(volatile LAS u32*)(L+B_F+4*w)=1u;
        #pragma unroll
        for(int i=0;i<6;++i) asm volatile("":"+v"(c[1][i]));
        lptr ox_=L+B_PV+(w^2)*B_PVS; f32x4 pr[6];
        for(int it=0;it<SPIN_MAX;++it){
          const u32 f=*(volatile LAS u32*)(L+B_F+4*(w^2)), n=*(volatile LAS u32*)(L+B_F+16);
          #pragma unroll
          for(int i=0;i<6;++i) pr[i]=*(volatile LAS f32x4*)(ox_+i*1024+lane*16);
          if(__builtin_amdgcn_readfirstlane(f+n)==5u) break; }
        fl=flag_get(ws,fi);
        #pragma unroll
        for(int i=0;i<6;++i){ u32 o=(i<3)?(po0+i*1024):(po1+(i-3)*320); if(i%3==2) o=(g<2)?o:OOB;
          st_sc1(rs,o,__builtin_bit_cast(u32x4,c[1][i]+pr[i])); } }
      ST(5);
      o_phase<true>(L,rs,ws,E,w,oh,tq,fi,fl);
      count(6);
      if(rt==15&&grp==7&&w==3){
        for(int it=0;it<SPIN_MAX;++it){ if(__all(flag_get(ws,lane)==E&&flag_get(ws,64+lane)==E&&flag_get(ws,128+lane)==E)) break; __builtin_amdgcn_s_sleep(2); }
        if(lane==0) __hip_atomic_store((GAS u64*)ws,E,__ATOMIC_RELAXED,__HIP_MEMORY_SCOPE_AGENT); }
      ST(6);
    }
    if(!doout){
      until(21,4u);
      const char* up=(const char*)u+(size_t)tid*4*1280+2*(80*X+64+4*(li-4)); u32x4 um[4];
      #pragma unroll
      for(int r=0;r<4;++r) um[r]=cat(*(const u32x2*)(up+r*1280),*(const u32x2*)(up+(r+1024)*1280));
      u_scatter(false,tid,L,um);
      asm volatile("s_waitcnt lgkmcnt(0)":::"memory");
      __builtin_amdgcn_s_waitcnt(0x0F70); return false; }
    until(20,4u);
    { const u32 ob=WS_O+ot*20480;
      const u32 sm=ob+((lane<32)?((5*(lane>>3)+((lane>>1)&3))*1024+(lane&1)*512+508):((5*((lane-32)>>3)+4)*1024+((lane&7)>>1)*256+244+8*(lane&1)));
      for(int it=0;it<SPIN_MAX;++it){ asm volatile("":::"memory");
        if(__all(__builtin_amdgcn_raw_buffer_load_b32(rs,sm,0,16)!=SENT)) break;
        __builtin_amdgcn_s_sleep(1); }
      ST(15);
      const u32 vo=lane*16;
      for(u32 it=1;it<SPIN_MAX;++it){
        #pragma unroll
        for(int i=0;i<5;++i) dma16x4_sc1((const char*)ws+ob+i*4096,vo,vo,vo,vo,L+OIM+w*20480+i*4096);
        waitv<20-4*HB>();
        rbar();
        waitv<0>();
        ST(7);
        rbar();
        rbar();
        rbar();
        if(__builtin_amdgcn_readfirstlane(*bad)!=it) break; } }
    __builtin_amdgcn_s_waitcnt(0x0F70);
  } else {
    const int hw=w-4;
    asm volatile("s_load_dwordx2 %0, %1, 0x0":"=s"(E):"s"(ws):"memory");
    ST(9);
    waitv<9>(); rbar(); st.issue(2);
    ST(8);
    waitv<9>(); rbar();
    st.issue(3);
    asm volatile("s_waitcnt lgkmcnt(0)":"+s"(E)::"memory"); E+=1;
    if(tid==256) *(volatile LAS u32x2*)(L+B_F+88)=u32x2{(u32)E,(u32)(E>>32)};
    if(!ISQ&&lane<50){ lptr p=L+B_WP+hw*800+lane*16; lds_st<u32x4>(p+3200,lds_ld<u32x4>(p)^0x80008000u); }
    waitv<9>(); bar();
    if(!doout) params();
    st.issue(4);
    const int pr=hw>>1, me2=hw&1;
    u32 wq[40];
    { const int r=lane>>3; const u32 sw=((lane&7)^((((r>>1)&1)|(me2<<1))<<1))<<4, vo=r*1280+sw, vo2=((r>>2)*20+(r&3)+16+32*me2)*1280+sw;
      #pragma unroll
      for(int n=0;n<40;++n){ const int G=n/10, m=n%10, i=(n<22)?n:n-22, k=(i<16)?(i&3):((n>=18&&n<21)?n-18:((i<18)?i-16:0));
        wq[n]=((m<8)?vo+(160*G+20*m)*1280:vo2+(160*G+80*(m-8))*1280)-1024*k; asm volatile("":"+v"(wq[n])); } }
    lptr dst=L+hw*18432, rd, rd1; int x9, x10;
    { const int q=(lane&15)>>2, p=lane&3, f=((q>>1)|((g&1)<<1))<<1, ow=pr*2+(g&1);
      lptr rb=L+ow*18432+(g>>1)*1024+q*128+8*(p&1); rd=rb+(((4*me2+(p>>1))^f)<<4); rd1=rb+(((4*me2+2+(p>>1))^f)<<4);
      x9=B_WP+ow*3072-ow*18432; x10=(g>>1)?(B_PV+17408-1024):(x9+2048); }
    lptr ra9=rd+x9, rb9=rd1+x9, ra10=rd+x10, rb10=rd1+x10;
    asm volatile("":"+v"(rd),"+v"(rd1),"+v"(ra9),"+v"(rb9),"+v"(ra10),"+v"(rb10));
    waitv<9>(); rbar();
    #pragma unroll 1
    for(int s=4;s<9;++s){
      st.issue(s+1);
      waitv<9>(); rbar(); }
    waitv<0>(); rbar();
    if(!doout){ until(7,4u); return true; }
    if(!ISQ){
      const u32x4 sv={SENT,SENT,SENT,SENT};
      #pragma unroll
      for(int i=0;i<4;++i) st_sc1(rs,(i<3||lane<18)?(WS_P1+((4*grp+hw)*16+rt)*3584+i*1024+lane*16):OOB,sv);
      #pragma unroll
      for(int i=0;i<5;++i) st_sc1(rs,WS_O+((8*rt+grp)*20+5*hw+i)*1024+lane*16,sv);
      waitv<0>(); arrive(); }
    u32x4 wf[2][11];
    const char* gb=(const char*)wo+ct*256+pr*128+me2*8*1280;
    auto meet=[&](u32 n){ *(volatile LAS u32*)(L+B_F+48+4*hw)=n; until(12+(hw^1),n); };
    auto fetch18=[&](int n0){
      #pragma unroll
      for(int i=0;i<16;i+=4) dma16x4(gb,wq[n0+i],wq[n0+i+1],wq[n0+i+2],wq[n0+i+3],dst+i*1024);
      dma16xn<2>(gb,wq[n0+16],wq[n0+17],0,dst+16384); };
    until(ISQ?21:7,4u);
    ST(3);
    fetch18(0);
    dma16xn<3>(gb,wq[18],wq[19],wq[20],L+B_WP+hw*3072);
    dma16(gb,wq[21],dst+B_PV+17408);
    waitv<0>(); meet(1u);
    #pragma unroll
    for(int s=0;s<9;++s){ wf[0][s]=cat(lds_tr(rd+s*2048),lds_tr(rd+s*2048+512)); wf[1][s]=cat(lds_tr(rd1+s*2048),lds_tr(rd1+s*2048+512)); }
    wf[0][9]=cat(lds_tr(ra9),lds_tr(ra9+512)); wf[1][9]=cat(lds_tr(rb9),lds_tr(rb9+512));
    wf[0][10]=cat(lds_tr(ra10),lds_tr(ra10+512)); wf[1][10]=cat(lds_tr(rb10),lds_tr(rb10+512));
    asm volatile("s_waitcnt lgkmcnt(0)":::"memory");
    meet(2u);
    ST(10);
#define R2A 8
    #pragma unroll
    for(int i=0;i<R2A;i+=4) dma16x4(gb,wq[22+i],wq[23+i],wq[24+i],wq[25+i],dst+i*1024);
    if(!ISQ){ o_phase<false>(L,rs,ws,E,w,oh,tq,0,0); count(6); ST(6); until(6,8u); }
    #pragma unroll
    for(int i=R2A;i<16;i+=4) dma16x4(gb,wq[22+i],wq[23+i],wq[24+i],wq[25+i],dst+i*1024);
    dma16xn<2>(gb,wq[38],wq[39],0,dst+16384);
    count(20);
    ST(4);
    lptr obt[4];
    #pragma unroll
    for(int t=0;t<4;++t){ obt[t]=L+OIM+lane*16+((t+hw)&3)*20480; asm volatile("":"+v"(obt[t])); }
    waitv<0>();
    for(u32 it=1;it<SPIN_MAX;++it){
      rbar();
      ST(5);
      f32x4 c[2][4];
      #pragma unroll
      for(int t=0;t<4;++t){ c[0][t]=Z4; c[1][t]=Z4; }
      u32x4 ob[3][4], wl[3][2]; u32 chk=0;
      auto fetch=[&](int ks){
        #pragma unroll
        for(int t=0;t<4;++t) ob[ks%3][t]=lds_ld<u32x4>(obt[t]+ks*1024);
        if(ks>=11){ wl[ks%3][0]=cat(lds_tr(rd+(ks-11)*2048),lds_tr(rd+(ks-11)*2048+512)); wl[ks%3][1]=cat(lds_tr(rd1+(ks-11)*2048),lds_tr(rd1+(ks-11)*2048+512)); } };
      fetch(0); fetch(1);
      SB();
      #pragma unroll
      for(int ks=0;ks<20;++ks){ u32x2 lo;
        if(ks==4*HB-2){ rbar(); SB(); }
        #pragma unroll
        for(int n=0;n<8;++n){ const int t=n>>1, cc=n&1, k2=ks+2;
          c[cc][t]=mfma16((ks<11)?wf[cc][ks<11?ks:0]:wl[ks%3][cc],ob[ks%3][t],c[cc][t]);
          SB();
          if(k2<20){
            if(n<4) ob[k2%3][n]=lds_ld<u32x4>(obt[n]+k2*1024);
            else if(k2>=11){ lptr p=((n<6)?rd:rd1)+(k2-11)*2048; if(!(n&1)) lo=lds_tr(p); else wl[k2%3][(n-4)>>1]=cat(lo,lds_tr(p+512)); } }
          if(n==7) chk=max(chk,max(ob[ks%3][0].x,ob[ks%3][0].z));
          SB(); } }
      if(__any(chk==SENT)) *bad=it;
      bar();
      #pragma unroll
      for(int t=0;t<4;++t)
        #pragma unroll
        for(int cc=0;cc<2;++cc) lds_st<u32x2>(L+OIM+(16*((t+hw)&3)+(lane&15))*272+hw*64+cc*32+8*g,u32x2{pk_bf16(c[cc][t][0],c[cc][t][1]),pk_bf16(c[cc][t][2],c[cc][t][3])});
      ST(7);
      bar();
      if(__builtin_amdgcn_readfirstlane(*bad)!=it) break; }
  }
  #pragma unroll
  for(int i=0;i<2;++i){ const int r=32*i+(tid>>4);
    __builtin_amdgcn_raw_buffer_store_b128(lds_ld<u32x4>(L+OIM+r*272+(tid&15)*16),__builtin_amdgcn_make_buffer_rsrc(out,0,2048*2560,0x00020000),(64*tt64+r)*2560+1280+ct*256+(tid&15)*16,0,16); }
  if(w<4) ST(8);
  return false;
}

constexpr int CODE_LINES=316;
__global__ __launch_bounds__(512,2) void fused(const u16* u,const u16* are,const u16* aim,const u16* bre,const u16* bim,const u16* cre,const u16* cim,
    const u16* logdt,const u16* dskip,const u16* x,const u16* wqkv,const u16* wphi,const u16* wo,u16* out,void* ws){
  extern __shared__ __attribute__((aligned(16))) char smem[];
  lptr L=(lptr)smem;
  const int b=blockIdx.x, X=b&7, idx=b>>3;
  u64 pc; asm volatile("s_getpc_b64 %0":"=s"(pc));
  const int w=__builtin_amdgcn_readfirstlane(threadIdx.x>>6);
  const bool isa=(idx>>3)==2;
  const int q=idx>=24, li=idx-24*q, rt=4*(X>>1)+(li>>(2-q)), grp=((X&1)<<(2-q))+(li&(3>>q));
  if(idx<16&&w<2){
    const u32 n=(w?(X*16+idx)*50:idx*32)+(threadIdx.x&63); u32 keep;
    if(w?((threadIdx.x&63)<50):(threadIdx.x<32&&n<CODE_LINES))
      asm volatile("s_mov_b32 %0, m0\n\ts_mov_b32 m0, %3\n\ts_nop 0\n\tglobal_load_lds_dword %1, %2\n\ts_mov_b32 m0, %0":"=&s"(keep):"v"(n*128),"s"(w?(u64)wo:(pc&~255ull)),"s"(162560+w*256):"memory"); }
  Stream st;
  if(__builtin_expect(!isa&&w>=4,1)){ st.init(q,li,rt,grp,w&3,threadIdx.x&63,x,wqkv,L); st.issue(0,false);
    { const u32 nb=800<<q, o=(threadIdx.x&63)*16; const char* wp=(const char*)wphi+(4*grp+(w&3))*nb;
      if(o<nb) dma16(wp,o,L+B_WP+(w&3)*nb);
      if(o+1024<nb) dma16(wp,o+1024,L+B_WP+(w&3)*nb+1024); }
    st.issue(1); }
  SB();
  STINIT();
  const int ch0=isa?(80*X+8*(idx-16)):(80*X+64+4*(idx-28)), wi=isa?w:w-4;
  SsmP sp; u32 dsk;
  auto params=[&](){ const SsmIn in={are,aim,bre,bim,cre,cim,logdt}; sp=load_ssm(in,ch0+wi,threadIdx.x&63); dsk=dskip[ch0+wi]; };
  bool job=true;
  if(__builtin_expect(!isa,1)) job=role_b(q,X,li,rt,grp,w,st,L,params,u,wo,out,ws);
  else params();
  if(job){
    conv_job(isa,wi,ch0,idx&7,L,sp,dsk,u,out,ws); }
  STDUMP();
}

extern "C" void kernel_launch(void* const* d_in, const int* in_sizes, int n_in, void* d_out, int out_size, void* d_ws, size_t ws_size, hipStream_t stream){
  (void)hipFuncSetAttribute((const void*)fused,hipFuncAttributeMaxDynamicSharedMemorySize,163840);
  fused<<<256,512,163840,stream>>>((const u16*)d_in[0],(const u16*)d_in[1],(const u16*)d_in[2],(const u16*)d_in[3],(const u16*)d_in[4],(const u16*)d_in[5],(const u16*)d_in[6],
    (const u16*)d_in[7],(const u16*)d_in[8],(const u16*)d_in[9],(const u16*)d_in[10],(const u16*)d_in[11],(const u16*)d_in[12],(u16*)d_out,d_ws);
}
```

```cpp
#define AAUX 0
#define HB 2
#include <hip/hip_runtime.h>
#include <stdint.h>

typedef unsigned short u16; typedef unsigned u32; typedef unsigned long long u64;
typedef __attribute__((ext_vector_type(4))) short s16x4;
typedef __attribute__((ext_vector_type(8))) _Float16 f16x8;
typedef __attribute__((ext_vector_type(8))) __bf16 bf16x8;
typedef __attribute__((ext_vector_type(2))) __bf16 bf16x2;
typedef __attribute__((ext_vector_type(2))) _Float16 f16x2;
typedef __attribute__((ext_vector_type(2))) float f32x2;
typedef __attribute__((ext_vector_type(4))) float f32x4;
typedef __attribute__((ext_vector_type(16))) float f32x16;
typedef __attribute__((ext_vector_type(2))) unsigned u32x2;
typedef __attribute__((ext_vector_type(3))) unsigned u32x3;
typedef __attribute__((ext_vector_type(4))) unsigned u32x4;
#define LAS __attribute__((address_space(3)))
#define GAS __attribute__((address_space(1)))
#define DI __device__ __forceinline__
typedef LAS char* lptr;
typedef __amdgpu_buffer_rsrc_t rsrc_t;

#define ST(i) do{}while(0)
#define STDUMP() do{}while(0)
#define STINIT() do{}while(0)

DI u32 pk_bf16(float a, float b){ f32x2 v={a,b}; return __builtin_bit_cast(u32,__builtin_convertvector(v,bf16x2)); }
DI u32 pk_f16(f32x2 v){ return __builtin_bit_cast(u32,__builtin_convertvector(v,f16x2)); }
DI float bflo(u32 x){ return __builtin_bit_cast(float,x<<16); }
DI float bfhi(u32 x){ return __builtin_bit_cast(float,x&0xffff0000u); }
DI float rbf(float a){ return bflo(pk_bf16(a,a)); }
template<class T> DI T lds_ld(lptr p){ return *(LAS T*)p; }
template<class T> DI void lds_st(lptr p, T v){ *(LAS T*)p=v; }
DI u32x2 lds_ld64(lptr p){ return *(volatile LAS u32x2*)p; }
DI u32x2 lds_tr(lptr p){ return __builtin_bit_cast(u32x2,__builtin_amdgcn_ds_read_tr16_b64_v4i16((LAS s16x4*)p)); }
DI f32x4 mfma16(u32x4 a,u32x4 b,f32x4 c){ return __builtin_amdgcn_mfma_f32_16x16x32_bf16(__builtin_bit_cast(bf16x8,a),__builtin_bit_cast(bf16x8,b),c,0,0,0); }
DI u32x4 cat(u32x2 a,u32x2 b){ u32x4 r={a.x,a.y,b.x,b.y}; return r; }
DI u32 p_ll(u32 a,u32 b){ return __builtin_amdgcn_perm(b,a,0x05040100u); }
DI u32 p_lh(u32 a,u32 b){ return __builtin_amdgcn_perm(b,a,0x07060100u); }
DI u32 p_hl(u32 a,u32 b){ return __builtin_amdgcn_perm(b,a,0x05040302u); }
DI u32 p_hh(u32 a,u32 b){ return __builtin_amdgcn_perm(b,a,0x07060302u); }
#define SB() __builtin_amdgcn_sched_barrier(0)
DI void rbar(){ asm volatile("":::"memory"); __builtin_amdgcn_s_barrier(); asm volatile("":::"memory"); }
DI void bar(){ asm volatile("s_waitcnt lgkmcnt(0)":::"memory"); __builtin_amdgcn_s_barrier(); asm volatile("":::"memory"); }
template<int N> DI void waitv(){ asm volatile("s_waitcnt vmcnt(%0)"::"i"(N):"memory"); }
constexpr f32x4 Z4={0.f,0.f,0.f,0.f};
DI float wave_max(float m){
  m=fmaxf(m,__builtin_bit_cast(float,__builtin_amdgcn_update_dpp(0,__builtin_bit_cast(int,m),0xB1,0xf,0xf,true)));
  m=fmaxf(m,__builtin_bit_cast(float,__builtin_amdgcn_update_dpp(0,__builtin_bit_cast(int,m),0x4E,0xf,0xf,true)));
  m=fmaxf(m,__builtin_bit_cast(float,__builtin_amdgcn_update_dpp(0,__builtin_bit_cast(int,m),0x141,0xf,0xf,true)));
  m=fmaxf(m,__builtin_bit_cast(float,__builtin_amdgcn_update_dpp(0,__builtin_bit_cast(int,m),0x140,0xf,0xf,true)));
  int x=__builtin_bit_cast(int,m);
  float a=__builtin_bit_cast(float,__builtin_amdgcn_readlane(x,0)), b=__builtin_bit_cast(float,__builtin_amdgcn_readlane(x,16));
  float c=__builtin_bit_cast(float,__builtin_amdgcn_readlane(x,32)), d=__builtin_bit_cast(float,__builtin_amdgcn_readlane(x,48));
  return fmaxf(fmaxf(a,b),fmaxf(c,d)); }

constexpr u32 WS_FLAG=256, WS_P1=4096, WS_Q=WS_P1+32*16*3584, WS_O=WS_Q+32*2048*40, WS_END=WS_O+128*20*1024;
constexpr u32 SENT=0xFFFFFFFFu, OOB=0x80000000u;
constexpr int SPIN_MAX=1<<17;
DI void dma16(const void* sb, u32 voff, lptr l){ u32 keep;
  asm volatile("s_mov_b32 %0, m0\n\ts_mov_b32 m0, %3\n\ts_nop 0\n\tglobal_load_lds_dwordx4 %1, %2\n\ts_mov_b32 m0, %0":"=&s"(keep):"v"(voff),"s"(sb),"s"((u32)(uintptr_t)l):"memory"); }
#define DMA4_BODY(SC) asm volatile("s_mov_b32 %0, m0\n\ts_mov_b32 m0, %6\n\ts_nop 0\n\tglobal_load_lds_dwordx4 %1, %5" SC "\n\tglobal_load_lds_dwordx4 %2, %5 offset:1024" SC \
    "\n\tglobal_load_lds_dwordx4 %3, %5 offset:2048" SC "\n\tglobal_load_lds_dwordx4 %4, %5 offset:3072" SC "\n\ts_mov_b32 m0, %0" \
    :"=&s"(keep):"v"(v0),"v"(v1),"v"(v2),"v"(v3),"s"(sb),"s"((u32)(uintptr_t)l):"memory")
DI void dma16x4(const void* sb,u32 v0,u32 v1,u32 v2,u32 v3,lptr l){ u32 keep; DMA4_BODY(""); }
DI void dma16x4_sc1(const void* sb,u32 v0,u32 v1,u32 v2,u32 v3,lptr l){ u32 keep; DMA4_BODY(" sc1"); }
template<int N> DI void dma16xn(const void* sb,u32 v0,u32 v1,u32 v2,lptr l){ u32 keep;
  if(N==2) asm volatile("s_mov_b32 %0, m0\n\ts_mov_b32 m0, %4\n\ts_nop 0\n\tglobal_load_lds_dwordx4 %1, %3\n\tglobal_load_lds_dwordx4 %2, %3 offset:1024\n\ts_mov_b32 m0, %0"
    :"=&s"(keep):"v"(v0),"v"(v1),"s"(sb),"s"((u32)(uintptr_t)l):"memory");
  else asm volatile("s_mov_b32 %0, m0\n\ts_mov_b32 m0, %5\n\ts_nop 0\n\tglobal_load_lds_dwordx4 %1, %4\n\tglobal_load_lds_dwordx4 %2, %4 offset:1024\n\tglobal_load_lds_dwordx4 %3, %4 offset:2048\n\ts_mov_b32 m0, %0"
    :"=&s"(keep):"v"(v0),"v"(v1),"v"(v2),"s"(sb),"s"((u32)(uintptr_t)l):"memory"); }
DI u32x4 ld_sc1(rsrc_t rs,u32 off){ return __builtin_amdgcn_raw_buffer_load_b128(rs,off,0,16); }
DI void st_sc1(rsrc_t rs,u32 off,u32x4 v){ __builtin_amdgcn_raw_buffer_store_b128(v,rs,off,0,16); }
DI void gather16(rsrc_t rs,u32 off,u32x4* v){
  for(int it=0;it<SPIN_MAX;++it){
    u32 todo=0;
    #pragma unroll
    for(int k=0;k<4;++k){ u32 m=0;
      #pragma unroll
      for(int i=0;i<4;++i) m=max(m,max(v[4*k+i].x,v[4*k+i].z));
      if(__any(m==SENT)) todo|=1u<<k; }
    if(!todo) break;
    asm volatile("":::"memory");
    #pragma unroll
    for(int k=0;k<4;++k) if(todo>>k&1){
      #pragma unroll
      for(int i=0;i<4;++i) v[4*k+i]=ld_sc1(rs,off+(4*k+i)*3584); } } }
DI void flag_set(void* ws,int i,u64 E){ __hip_atomic_store((GAS u64*)((char*)ws+WS_FLAG)+i,E,__ATOMIC_RELAXED,__HIP_MEMORY_SCOPE_AGENT); }
DI u64 flag_get(const void* ws,int i){ return __hip_atomic_load((const GAS u64*)((const char*)ws+WS_FLAG)+i,__ATOMIC_RELAXED,__HIP_MEMORY_SCOPE_AGENT); }
DI void flag_wait(const void* ws,u64 E,int idx,u64 f){
  for(int it=0;it<SPIN_MAX;++it){ if(__all(f==E)) break; __builtin_amdgcn_s_sleep(2); f=flag_get(ws,idx); } }

constexpr int A_UB=4576, A_US=9120, A_KB=4224, A_KS=8704;

DI f32x2 cmul(f32x2 a, f32x2 b){ f32x2 sw={-b.y,b.x}; return __builtin_elementwise_fma(a.yy,sw,a.xx*b); }
DI f32x2 cexp_e(float re2, float rev, float e){
  float m=__builtin_amdgcn_exp2f(re2*e); float a=__builtin_amdgcn_fractf(rev*e);
  f32x2 r={m*__builtin_amdgcn_cosf(a), m*__builtin_amdgcn_sinf(a)}; return r; }
struct SsmP { u32 are,aim,bre,bim,cre,cim,ld; float dt; };
struct SsmIn { const u16 *are,*aim,*bre,*bim,*cre,*cim,*logdt; };
DI SsmP load_ssm(const SsmIn& in, int ch, int lane){
  SsmP p; int o=ch*64+lane;
  p.are=((const u32*)in.are)[o]; p.aim=((const u32*)in.aim)[o]; p.bre=((const u32*)in.bre)[o];
  p.bim=((const u32*)in.bim)[o]; p.cre=((const u32*)in.cre)[o]; p.cim=((const u32*)in.cim)[o];
  p.ld=in.logdt[ch];
  return p; }
DI f32x2 ssm_w(const SsmP& p, int half){
  f32x2 b={half?bfhi(p.bre):bflo(p.bre), half?bfhi(p.bim):bflo(p.bim)};
  f32x2 c={half?bfhi(p.cre):bflo(p.cre), half?bfhi(p.cim):bflo(p.cim)};
  return cmul(c,b*p.dt); }
DI void ssm_prep(SsmP& p, float& s, float& inv){
  asm volatile("":"+v"(p.ld));
  p.dt=rbf(expf(bflo(p.ld)));
  f32x2 w0=ssm_w(p,0), w1=ssm_w(p,1);
  float m=wave_max(fmaxf(fmaxf(fabsf(w0.x),fabsf(w0.y)),fmaxf(fabsf(w1.x),fabsf(w1.y))));
  int e=__builtin_amdgcn_frexp_expf(m); e=max(-100,min(100,e));
  s=__builtin_amdgcn_ldexpf(1.0f,-e); inv=__builtin_amdgcn_ldexpf(1.0f,e); }
DI void kgen_half(const SsmP& p, int half, float s, lptr tab, int lane, f32x16& acc){
  const float are=(half?bfhi(p.are):bflo(p.are))*p.dt, aim=(half?bfhi(p.aim):bflo(p.aim))*p.dt;
  const float re2=are*1.4426950408889634f, rev=aim*0.15915494309189535f;
  lptr wr=tab+lane*4; lptr rd=tab+(lane&31)*272+(lane>>5)*16;
  { f32x2 e8=cexp_e(re2,rev,8.f), e16=cmul(e8,e8);
    f32x2 a={1.f,0.f}, b=e8;
    #pragma unroll
    for(int j=0;j<32;j+=2){ lds_st<u32>(wr+j*272,pk_f16(a)); lds_st<u32>(wr+(j+1)*272,pk_f16(b)); if(j<30){ a=(j==0)?e16:cmul(a,e16); b=cmul(b,e16);} } }
  u32x4 bf[8];
  #pragma unroll
  for(int k=0;k<8;++k) bf[k]=lds_ld<u32x4>(rd+k*32);
  { f32x2 w=ssm_w(p,half)*s; w.y=-w.y;
    f32x2 e1=cexp_e(re2,-rev,1.f), g=cexp_e(re2,-rev,256.f);
    f32x2 g2=cmul(g,g), g3=cmul(g2,g), g4=cmul(g2,g2);
    f32x2 x[8]; x[0]=w;
    #pragma unroll
    for(int i=1;i<8;++i) x[i]=cmul(x[i-1],e1);
    #pragma unroll
    for(int i=0;i<8;++i) lds_st<u32>(wr+i*272,pk_f16(x[i]));
    #pragma unroll
    for(int i=0;i<4;++i) lds_st<u32>(wr+(8+i)*272,pk_f16(cmul(x[2*i],g)));
    lds_st<u32>(wr+12*272,pk_f16(cmul(x[0],g2))); lds_st<u32>(wr+13*272,pk_f16(cmul(x[4],g2)));
    lds_st<u32>(wr+14*272,pk_f16(cmul(x[0],g3))); lds_st<u32>(wr+15*272,pk_f16(cmul(x[4],g3)));
    f32x2 y=cmul(w,g4);
    #pragma unroll
    for(int i=0;i<5;++i){ lds_st<u32>(wr+(16+i)*272,pk_f16(y)); if(i<4) y=cmul(y,g); } }
  #pragma unroll
  for(int k=0;k<8;++k){ u32x4 af=lds_ld<u32x4>(rd+k*32);
    acc=__builtin_amdgcn_mfma_f32_32x32x16_f16(__builtin_bit_cast(f16x8,af),__builtin_bit_cast(f16x8,bf[k]),acc,0,0,0); }
}
DI void k_finish(const float* v, lptr ka, int lane){
  lptr ks=ka; lptr kb=ka+A_KB;
  { const int j=lane&31, h=lane>>5;
    lds_st<u32x2>(ks+(8*j+4*h)*2,u32x2{pk_bf16(v[0],v[1]),pk_bf16(v[2],v[3])});
    u32x2 b={pk_bf16(v[4],v[5]),pk_bf16(v[6],v[7])};
    u32 c0=pk_bf16(v[8],v[9]), c1=pk_bf16(v[10],v[11]);
    if(h==0){ lds_st<u32x2>(ks+(256+4*j)*2,b);
      lds_st<u16>(ks+(512+j)*2,(u16)c0); lds_st<u16>(ks+(544+j)*2,(u16)(c0>>16));
      lds_st<u16>(ks+(576+j)*2,(u16)c1); lds_st<u16>(ks+(608+j)*2,(u16)(c1>>16));
    } else { lds_st<u32>(ks+(384+2*j)*2,b.x); lds_st<u32>(ks+(448+2*j)*2,b.y); if(j==0) lds_st<u16>(ks+640*2,(u16)c0); } }
  const int M=lane; const bool lo=M<32;
  u32x4 v0=lds_ld<u32x4>(ks+(M&31)*16); u32x2 v1=lds_ld<u32x2>(ks+(128+4*(M|32))*2);
  u32 v4=lds_ld<u16>(ks+(132+4*(M|32))*2);
  u32 pv=lds_ld<u16>(ks+(lo?max(8*M-1,0):((M==32)?255:(128+4*M)))*2); if(M==0) pv=0;
  u32 ab=lds_ld<u32>(ks+(384+2*M)*2), c2=lds_ld<u16>(ks+(386+2*M)*2);
  u32 a3[2],b3[2];
  #pragma unroll
  for(int i=0;i<2;++i){ a3[i]=lds_ld<u16>(ks+(512+M+64*i)*2); b3[i]=lds_ld<u16>(ks+(513+M+64*i)*2); }
  asm volatile("":"+v"(v0),"+v"(v1),"+v"(v4),"+v"(pv),"+v"(ab),"+v"(c2),"+v"(a3[0]),"+v"(a3[1]),"+v"(b3[0]),"+v"(b3[1])::"memory");
  { u32x4 A,B;
    if(lo){ B.x=p_hl(v0.w,v0.w); B.y=p_hl(v0.z,v0.z); B.z=p_hl(v0.y,v0.y); B.w=p_hl(v0.x,v0.x);
      A.x=p_lh(v0.w,v0.z); A.y=p_lh(v0.z,v0.y); A.z=p_lh(v0.y,v0.x); A.w=p_ll(v0.x,pv);
    } else { B.x=p_lh(v4,v1.y); B.y=p_ll(v1.y,v1.y); B.z=p_lh(v1.y,v1.x); B.w=p_ll(v1.x,v1.x);
      A.x=p_hl(v1.y,v1.y); A.y=B.y; A.z=p_hl(v1.x,v1.x); A.w=p_ll(v1.x,pv); }
    lds_st<u32x4>(ka+(255-M)*16,A); lds_st<u32x4>(kb+(255-M)*16,B); }
  { u32x4 A,B; B.x=p_ll(c2,c2); B.y=p_hh(ab,ab); B.z=p_hl(ab,ab); B.w=p_ll(ab,ab);
    A.x=p_lh(c2,ab); A.y=B.y; A.z=B.w; A.w=B.w;
    lds_st<u32x4>(ka+(191-M)*16,A); lds_st<u32x4>(kb+(191-M)*16,B); }
  #pragma unroll
  for(int i=0;i<2;++i){ u32 a=a3[i], b=b3[i], c=(M&1)?b:a; u32x4 A,B;
    B.x=p_ll(b,b); B.y=p_ll(b,c); B.z=p_ll(a,a); B.w=B.z;
    A.x=B.x; A.y=p_ll(c,a); A.z=B.z; A.w=B.z;
    lds_st<u32x4>(ka+(127-64*i-M)*16,A); lds_st<u32x4>(kb+(127-64*i-M)*16,B); }
  if(lane<8) lds_st<u32x4>(((lane&4)?kb:ka)+(256+(lane&3))*16,u32x4{0,0,0,0});
}
DI void conv_steps(lptr ua, lptr ka, int lane, f32x4* acc){
  constexpr int NS=8, s0=0, kbo=A_KB;
  const int a=lane&15, g=lane>>4;
  lptr up=ua+((a&1)?0:A_UB)+(32*(a>>1)+8*g+32*s0)*2;
  lptr kp=ka+((a&1)?kbo:0)+(28-4*(a>>1)+8*g+32*s0)*2;
  u32x4 A[2][8],B[2][8];
  auto ldA=[&](int s,int q){ A[s&1][q]=cat(lds_ld64(kp+(1792-256*q+32*s)*2),lds_ld64(kp+(1792-256*q+32*s)*2+8)); };
  auto ldB=[&](int s,int q){ B[s&1][q]=lds_ld<u32x4>(up+(256*q+32*s)*2); };
  #pragma unroll
  for(int q=0;q<8;++q){ ldB(0,q); ldA(0,q); }
  #pragma unroll
  for(int c=0;c<8;++c) acc[c]=Z4;
  SB();
  #pragma unroll
  for(int s=0;s<NS;++s){ int n=0; u32x2 lo;
    #pragma unroll
    for(int q=0;q<8;++q){
      #pragma unroll
      for(int c=q;c<8;++c){
        acc[c]=mfma16(A[s&1][q],B[s&1][c-q],acc[c]);
        SB();
        if(s+1<NS){
          if(n==0) ldB(s+1,0);
          else if(n<3){ if(n==1) lo=lds_ld64(kp+(1792+32*(s+1))*2); else A[(s+1)&1][0]=cat(lo,lds_ld64(kp+(1792+32*(s+1))*2+8)); }
          else if(n<10) ldB(s+1,n-2);
          else if(n<24){ const int j=1+((n-10)>>1); if(!(n&1)) lo=lds_ld64(kp+(1792-256*j+32*(s+1))*2); else A[(s+1)&1][j]=cat(lo,lds_ld64(kp+(1792-256*j+32*(s+1))*2+8)); } }
        SB();
        ++n; } } }
}
DI u32 fin2(float y0,float y1,u32 u,float dsk){
  u32 y=pk_bf16(y0,y1), s=pk_bf16(bflo(u)*dsk,bfhi(u)*dsk);
  return pk_bf16(bflo(y)+bflo(s),bfhi(y)+bfhi(s)); }
DI void u_scatter(const bool BIG, int ur, lptr L, const u32x4* um){
  #pragma unroll
  for(int c=0;c<4;++c){
    lptr d=L+(224+4*ur)*2+(BIG?2*c*A_US:(2*(c&1)*A_US+(c>>1)*2048)); const bool last=BIG?(ur==511):(c>=2&&ur==255);
    const u32x2 e={p_ll(um[0][c],um[1][c]),p_ll(um[2][c],um[3][c])}, o={p_hh(um[0][c],um[1][c]),p_hh(um[2][c],um[3][c])};
    lds_st<u32x2>(d,e); lds_st<u32x2>(d+A_US,o);
    lds_st<u32>(d+A_UB+4,e.x); lds_st<u32>(d+A_US+A_UB+4,o.x);
    if(!last){ lds_st<u32>(d+A_UB+8,e.y); lds_st<u32>(d+A_US+A_UB+8,o.y); } } }
DI void conv_job(const bool BIG, int wi, int ch0, int rot, lptr L, SsmP& p0, u32 dsk, const u16* u, u16* out, void* ws){
  const int lane=threadIdx.x&63;
  lptr us=L+wi*A_US, kw=L+(BIG?8:4)*A_US+wi*A_KS;
  if(lane<56) lds_st<u32x4>(us+(lane<28?lane*16:A_UB+(lane-28)*16),u32x4{0,0,0,0});
  if(lane==56) lds_st<u32>(us+A_UB+448,0u);
  u32x4 um[4];
  const int ur=(64*(wi+rot)+lane)&511;
  ST(1);
  { float s,inv; ssm_prep(p0,s,inv); f32x16 acc={};
    #pragma unroll 1
    for(int h=0;h<2;++h){
      if(BIG&&h==1){ const char* up=(const char*)u+(size_t)ur*4*1280+2*ch0;
        #pragma unroll
        for(int r=0;r<4;++r) um[r]=*(const u32x4*)(up+r*1280); }
      kgen_half(p0,h,s,kw,lane,acc); }
    float v[12];
    #pragma unroll
    for(int r=0;r<12;++r) v[r]=acc[r]*inv;
    k_finish(v,kw,lane); }
  ST(2);
  if(BIG) u_scatter(true,ur,L,um);
  bar();
  ST(3);
  f32x4 acc[8];
  conv_steps(us,kw,lane,acc);
  ST(4);
  { const int tl=32*((lane&15)>>1)+8*(lane>>4)+2*(lane&1); asm volatile("":"+v"(dsk)); const float d=bflo(dsk);
    u32 uu[16];
    #pragma unroll
    for(int c=0;c<8;++c){ uu[2*c]=lds_ld<u32>(us+(224+256*c+tl)*2); uu[2*c+1]=lds_ld<u32>(us+(228+256*c+tl)*2); }
    #pragma unroll
    for(int c=0;c<8;++c){ lds_st<u32>(kw+(256*c+tl)*2,fin2(acc[c][0],acc[c][1],uu[2*c],d)); lds_st<u32>(kw+(256*c+tl+4)*2,fin2(acc[c][2],acc[c][3],uu[2*c+1],d)); } }
  bar();
  ST(5);
  { lptr yp=L+(BIG?8:4)*A_US+((lane&15)>>2)*A_KS+(256*wi+16*(lane>>4)+4*(lane&3))*2;
    const u32 op=(256*wi+lane)*2560+2*ch0; const rsrc_t ro=__builtin_amdgcn_make_buffer_rsrc(out,0,2048*2560,0x00020000);
    #pragma unroll
    for(int i=0;i<4;++i){
      if(BIG) __builtin_amdgcn_raw_buffer_store_b128(cat(lds_tr(yp+i*128),lds_tr(yp+4*A_KS+i*128)),ro,op+i*64*2560,0,AAUX);
      else { __builtin_amdgcn_raw_buffer_store_b64(lds_tr(yp+i*128),ro,op+i*64*2560,0,AAUX); __builtin_amdgcn_raw_buffer_store_b64(lds_tr(yp+2048+i*128),ro,op+(i*64+1024)*2560,0,AAUX); } } }
  ST(7);
}

constexpr int B_ST=36864, B_PV=2*B_ST, B_PVS=18432, B_WP=4*B_ST, B_Z=B_WP+6400, B_KT=159744, B_F=163168;
DI float max3(float a,float b,float c){ return __builtin_fmaxf(__builtin_fmaxf(a,b),c); }
template<class H> DI void softmax20x2(const f32x4 (*z)[5], u32 (*o)[10], H hook){
  const float c=1.4426950408889634f; float mc[2];
  { float m0[2],m1[2],m2[2];
    #pragma unroll
    for(int t=0;t<2;++t){ m0[t]=max3(z[t][0][0],z[t][0][1],z[t][0][2]); m1[t]=max3(z[t][0][3],z[t][1][0],z[t][1][1]); m2[t]=max3(z[t][1][2],z[t][1][3],z[t][2][0]); }
    #pragma unroll
    for(int t=0;t<2;++t){ m0[t]=max3(m0[t],z[t][2][1],z[t][2][2]); m1[t]=max3(m1[t],z[t][2][3],z[t][3][0]); m2[t]=max3(m2[t],z[t][3][1],z[t][3][2]); }
    #pragma unroll
    for(int t=0;t<2;++t){ m0[t]=max3(m0[t],z[t][3][3],z[t][4][0]); m1[t]=max3(m1[t],z[t][4][1],z[t][4][2]); m2[t]=__builtin_fmaxf(m2[t],z[t][4][3]); }
    #pragma unroll
    for(int t=0;t<2;++t) mc[t]=max3(m0[t],m1[t],m2[t])*-c; }
  SB();
  f32x2 e[2][10];
  #pragma unroll
  for(int i=0;i<10;++i)
    #pragma unroll
    for(int t=0;t<2;++t) e[t][i]=__builtin_elementwise_fma(f32x2{z[t][i>>1][(i&1)*2],z[t][i>>1][(i&1)*2+1]},f32x2{c,c},f32x2{mc[t],mc[t]});
  SB();
  #pragma unroll
  for(int i=0;i<10;++i)
    #pragma unroll
    for(int t=0;t<2;++t){ hook(2*i+t); e[t][i]=f32x2{__builtin_amdgcn_exp2f(e[t][i].x),__builtin_amdgcn_exp2f(e[t][i].y)}; SB(); }
  float r[2];
  { f32x2 s[2][5];
    #pragma unroll
    for(int i=0;i<5;++i)
      #pragma unroll
      for(int t=0;t<2;++t) s[t][i]=e[t][2*i]+e[t][2*i+1];
    #pragma unroll
    for(int t=0;t<2;++t){ s[t][0]+=s[t][1]; s[t][2]+=s[t][3]; }
    #pragma unroll
    for(int t=0;t<2;++t){ s[t][0]+=s[t][4]; }
    #pragma unroll
    for(int t=0;t<2;++t){ s[t][0]+=s[t][2]; }
    #pragma unroll
    for(int t=0;t<2;++t) r[t]=__builtin_amdgcn_rcpf(s[t][0].x+s[t][0].y); }
  SB();
  #pragma unroll
  for(int i=0;i<10;++i)
    #pragma unroll
    for(int t=0;t<2;++t) e[t][i]*=r[t];
  SB();
  #pragma unroll
  for(int i=0;i<10;++i)
    #pragma unroll
    for(int t=0;t<2;++t) o[t][i]=pk_bf16(e[t][i].x,e[t][i].y);
  SB();
}
DI u32x4 pack2(f32x4 a,f32x4 b){ u32x4 r={pk_bf16(a[0],a[1]),pk_bf16(a[2],a[3]),pk_bf16(b[0],b[1]),pk_bf16(b[2],b[3])}; return r; }
struct Stream { u32 xo[4], wv[5]; int s0, qw; const u16 *x, *w; lptr L;
  DI void init(int q,int li,int rt,int grp,int qw_,int lane,const u16* x_,const u16* w_,lptr L_){
    qw=qw_; x=x_; w=w_; L=L_;
    s0=((0x8530>>((((li>>(2-q))+((li&(3>>q))<<q))&3)*4))&15)+q;
    #pragma unroll
    for(int i=0;i<4;++i){ const u32 r=(4*qw+i)*8+(lane>>3); xo[i]=(rt*128+r)*1280u+(((lane&7)^((r>>1)&7))<<4)-1024u*i; }
    dma16x4((const char*)x+(s0>=10?s0-10:s0)*128,xo[0],xo[1],xo[2],xo[3],L+4*qw*1024);
    const u32 wlo=(160<<q)*grp+1280*(1-q), dh=1120*(1-q);
    #pragma unroll
    for(int i=0;i<5;++i){ const u32 c=(5*qw+i)*64+lane, r=(c*3277u)>>16, pos=c-r*20, sc=pos^(((r>>3)&1)<<1);
      wv[i]=r*3840u+wlo+(sc<10?0u:dh)+16*sc-1024u*(i&3); } }
  DI void issue(int s,bool withx=true) const { int ss=s+s0; if(ss>=10) ss-=10; lptr d=L+(s&3)*B_ST;
    const char* xb=(const char*)x+ss*128; const char* wb=(const char*)w+(size_t)ss*64*3840;
    if(withx) dma16x4(xb,xo[0],xo[1],xo[2],xo[3],d+4*qw*1024);
    dma16x4(wb,wv[0],wv[1],wv[2],wv[3],d+16384+5*qw*1024); dma16(wb,wv[4],d+16384+(5*qw+4)*1024); } };

template<bool GATHER> DI void o_phase(lptr L,rsrc_t rs,void* ws,u64 E,int w,int oh,int tq,int fi,u64 fl){
  const int lane=threadIdx.x&63, g=lane>>4, r=lane&15;
  if(GATHER){ if(!__all(fl==E)) flag_wait(ws,E,fi,fl); *(volatile LAS u32*)(L+B_F+128+4*w)=1u; }
  else for(int it=0;it<SPIN_MAX;++it){ if(__builtin_amdgcn_readfirstlane(*(volatile LAS u32*)(L+B_F+128+4*(w&3)))) break; __builtin_amdgcn_s_sleep(2); }
  asm volatile("":::"memory");
  u32x4 l0[2],l1[2]; u32x2 l2[2];
  { const u32 qo=WS_Q+(oh*2048+512*tq+128*(w&3)+64*(w>>2)+16*(g>>1)+r)*40;
    #pragma unroll
    for(int i=0;i<2;++i){ l0[i]=ld_sc1(rs,qo+i*1280); l1[i]=ld_sc1(rs,qo+i*1280+16); l2[i]=__builtin_amdgcn_raw_buffer_load_b64(rs,qo+i*1280+32,0,16); } }
  lptr kt=L+B_KT, q00,q01,q10,q11; u32 om,ox;
  { const int pc=5*(g&1)+2*(g>>1), pl=5*(g&1)+4, d1=min(r,4); lptr b=kt;
    q00=b+(16*pc+r)*8; q01=b+(16*pl+r)*8+4*(g>>1); q10=b+(160+5*pc+d1)*8; q11=b+(160+5*pl+d1)*8+4*(g>>1);
    const u32 fb=WS_O+((32*tq+8*(w&3)+4*(w>>2))*20+5*(oh>>3))*1024+r*16;
    om=fb+((oh&7)>>1)*1024+(2*(oh&1)+(g>>1))*256+8*(g&1); ox=(g==0)?(fb+4096+((oh&7)>>1)*256+8*(oh&1)):OOB; }
  asm volatile("":"+v"(q00),"+v"(q01),"+v"(q10),"+v"(q11),"+v"(om),"+v"(ox));
  ST(11);
  u32x4 v[16]; const u32 go=(lane<53&&53*w+lane<210)?(WS_P1+oh*16*3584+(53*w+lane)*16):OOB;
  u32 pq[2][10];
  { const u32 sg=(g&1)?0x80008000u:0u; f32x4 z[2][5];
    #pragma unroll
    for(int t=0;t<2;++t){ const u32x4 a=l0[t]^sg, b=l1[t]^sg; const u32x2 c=l2[t]^sg;
      z[t][0]=f32x4{bflo(a.x),bfhi(a.x),bflo(a.y),bfhi(a.y)}; z[t][1]=f32x4{bflo(a.z),bfhi(a.z),bflo(a.w),bfhi(a.w)};
      z[t][2]=f32x4{bflo(b.x),bfhi(b.x),bflo(b.y),bfhi(b.y)}; z[t][3]=f32x4{bflo(b.z),bfhi(b.z),bflo(b.w),bfhi(b.w)};
      z[t][4]=f32x4{bflo(c.x),bfhi(c.x),bflo(c.y),bfhi(c.y)}; }
    SB();
    softmax20x2(z,pq,[&](int k){ if(GATHER&&k<16) v[k]=ld_sc1(rs,go+k*3584); });
    #pragma unroll
    for(int i=0;i<2;++i){
      #pragma unroll
      for(int k=0;k<5;++k){ const int a=(k<4)?k:8, b=(k<4)?k+4:9;
        auto s=__builtin_amdgcn_permlane32_swap(pq[i][a],pq[i][b],false,false); pq[i][a]=s[0]; pq[i][b]=s[1]; }
      #pragma unroll
      for(int k=0;k<10;++k) asm volatile("":"+v"(pq[i][k])); } }
  ST(12);
  if(GATHER){
    gather16(rs,go,v);
    ST(13);
    f32x4 s[4];
    #pragma unroll
    for(int i=0;i<4;++i) s[i]=(__builtin_bit_cast(f32x4,v[4*i])+__builtin_bit_cast(f32x4,v[4*i+1]))+(__builtin_bit_cast(f32x4,v[4*i+2])+__builtin_bit_cast(f32x4,v[4*i+3]));
    s[0]=(s[0]+s[1])+(s[2]+s[3]);
    if(lane<53) *(volatile LAS u32x2*)(kt+(53*w+lane)*8)=u32x2{pk_bf16(s[0][0],s[0][1]),pk_bf16(s[0][2],s[0][3])};
    *(volatile LAS u32*)(L+B_F+32+4*w)=1u;
  }
  u32x4 a0[2]; u32 a1[2];
  for(int it=0;it<SPIN_MAX;++it){
    const u32x4 f=*(volatile LAS u32x4*)(L+B_F+32);
    a0[0]=cat(*(volatile LAS u32x2*)q00,*(volatile LAS u32x2*)(q00+128)); a0[1]=cat(*(volatile LAS u32x2*)q10,*(volatile LAS u32x2*)(q10+40));
    a1[0]=*(volatile LAS u32*)q01; a1[1]=*(volatile LAS u32*)q11;
    if(__builtin_amdgcn_readfirstlane(f.x+f.y+f.z+f.w)==4u) break;
    if(!GATHER) __builtin_amdgcn_s_sleep(1); }
  ST(14);
  f32x4 n[4][2];
  #pragma unroll
  for(int i=1;i>=0;--i){
    #pragma unroll
    for(int t=0;t<4;++t){ const u32* f=pq[t>>1]+4*(t&1); n[t][i]=mfma16(a0[i],u32x4{f[0],f[1],f[2],f[3]},Z4); }
    #pragma unroll
    for(int t=0;t<4;++t) n[t][i]=mfma16(u32x4{a1[i],0u,0u,0u},u32x4{pq[t>>1][8+(t&1)],0u,0u,0u},n[t][i]); }
  SB();
  float den[4];
  #pragma unroll
  for(int t=0;t<4;++t) den[t]=__builtin_bit_cast(float,__builtin_amdgcn_ds_bpermute((r+16)*4,__builtin_bit_cast(int,n[t][1][0])));
  #pragma unroll
  for(int t=0;t<4;++t) den[t]=__builtin_amdgcn_rcpf(rbf(rbf(den[t])+1e-6f));
  SB();
  #pragma unroll
  for(int t=0;t<4;++t){ const f32x4 m=n[t][0]*den[t], e=n[t][1]*den[t];
    __builtin_amdgcn_raw_buffer_store_b64(u32x2{pk_bf16(m[0],m[1]),pk_bf16(m[2],m[3])},rs,om+t*20480,0,16);
    __builtin_amdgcn_raw_buffer_store_b64(u32x2{pk_bf16(e[0],e[1]),pk_bf16(e[2],e[3])},rs,ox+t*20480,0,16); }
}

template<class F> DI bool role_b(const bool ISQ,int X,int li,int rt,int grp,int w,const Stream& st,lptr L,F params,const u16* u,const u16* wo,u16* out,void* ws){
  const int tid=threadIdx.x, lane=tid&63, g=lane>>4;
  const int T=ISQ?16+li:li; const bool doout=T<20; const int tt64=4*X+T/5, ct=T%5;
  const rsrc_t rs=__builtin_amdgcn_make_buffer_rsrc(ws,0,WS_END,0x00020000);
  constexpr int OIM=B_PV;
  u64 E;
  auto count=[&](int word){ asm volatile("":::"memory"); u32 n=0; if(lane==0) n=__hip_atomic_fetch_add((LAS u32*)(L+B_F+4*word),1u,__ATOMIC_RELAXED,__HIP_MEMORY_SCOPE_WORKGROUP); return n; };
  auto until=[&](int word,u32 n){ for(int it=0;it<SPIN_MAX;++it){ if(__builtin_amdgcn_readfirstlane(*(volatile LAS u32*)(L+B_F+4*word))>=n) break; __builtin_amdgcn_s_sleep(2); } asm volatile("":::"memory"); };
  auto arrive=[&](){
    if(__builtin_amdgcn_readfirstlane(count(4))==3u&&lane==0){ flag_set(ws,ISQ?(128+4*rt+grp):(16*grp+rt),E); asm volatile("":::"memory"); *(volatile LAS u32*)(L+B_F+84)=4u; } };
  volatile LAS u32* bad=(volatile LAS u32*)(L+B_F+20);
  const int oh=4*grp+(rt&3), tq=rt>>2;
  const int ot=4*tt64+(w&3);
  int fi=(lane<16)?(16*grp+lane):((lane<32)?(16*(lane&7)+4*tq+(w&3)):((lane<48)?(16*(ot&7)+(ot>>3)):(128+4*(4*tq+(w&3))+(oh>>3))));
  if(w<4){
    const int th=w>>1, hh=w&1;
    if(lane<4) lds_st<u32x4>(L+B_Z+lane*16,u32x4{0,0,0,0});
    if(tid<9) lds_st<u32x4>(L+B_F+tid*16,u32x4{0,0,0,0});
    lptr fad[3][2];
    { const int q=(lane&15)>>2, p=lane&3, hd=ISQ?p:(p&1);
      #pragma unroll
      for(int s=0;s<3;++s)
        #pragma unroll
        for(int t=0;t<2;++t){ const int d=16*(2*s+t)+4*g-20*hd; const bool ok=(d>=0)&&(d<20)&&(2*s+t<5);
          fad[s][t]=ok?(L+B_WP+((ISQ?4*hh:2*hh)+hd)*800+(d+q)*40+(ISQ?0:(p>>1)*3200)):(L+B_Z); if(2*s+t<5) asm volatile("":"+v"(fad[s][t])); } }
    lptr pv=L+B_PV+w*B_PVS;
    lptr pp=pv+(lane&15)*168+(g&1)*80+(g>>1)*40, vp2, vp3, vp4, pa, vb;
    { lptr vp=pv+10752+(lane&15)*104; vp2=vp+((g<2)?(48*g+40):(8*g-16)); vp3=vp+((g==3)?48:(16+8*g)); vp4=vp+56+8*g;
      const int tk=16*(g&1)+4*((lane&15)>>2)+2*(g>>1); pa=pv+tk*168+8*(lane&3)+(th^1)*80; vb=pv+10752+tk*104+8*(lane&3)+(th^1)*48; }
    u32 po0, po1;
    { const u32 po=WS_P1+((4*grp+2*hh+th)*16+rt)*3584; po0=po+lane*16; po1=((lane&15)<5)?(po+(160+5*g+(lane&15))*16):OOB; }
    asm volatile("":"+v"(fi),"+v"(pp),"+v"(vp2),"+v"(vp3),"+v"(vp4),"+v"(pa),"+v"(vb),"+v"(po0),"+v"(po1));
    lptr ab[5], bb[2];
    { const int q=(lane&15)>>2,p=lane&3;
      #pragma unroll
      for(int i=0;i<5;++i){ int n=2*i+(p>>1); int cid=ISQ?(10*hh+n):(5*hh+n+(n>=5?5:0)); ab[i]=L+16384+(8*g+q)*320+((cid^((g&1)<<1))<<4)+8*(p&1); }
      const int b0=(64*th+(lane&15))*128+((g^((lane&15)>>1))<<4); bb[0]=L+b0; bb[1]=L+(b0^64); }
    f32x4 acc[5][4];
    #pragma unroll
    for(int i=0;i<5;++i)
      #pragma unroll
      for(int t=0;t<4;++t){ acc[i][t]=Z4; asm volatile("":"+v"(acc[i][t])); }
    ST(1);
    u32x4 FA[2][5], FB[2][4], fa[2][5];
    u32x2 lo2;
    auto half=[&](int hf,bool mm,bool last,int step){
      u32x2 lo;
      #pragma unroll
      for(int n=0;n<20;++n){ const int i=n>>2, t=n&3;
        if(mm) acc[i][t]=mfma16(FA[hf^1][i],FB[hf^1][t],acc[i][t]);
        SB();
        if(n==0) FB[hf][0]=lds_ld<u32x4>(bb[hf]);
        else if(n==1) lo=lds_tr(ab[0]+hf*10240);
        else if(n==2) FA[hf][0]=cat(lo,lds_tr(ab[0]+hf*10240+1280));
        else if(n<6) FB[hf][n-2]=lds_ld<u32x4>(bb[hf]+2048*(n-2));
        else if(n<14){ const int j=1+((n-6)>>1); if(!(n&1)) lo=lds_tr(ab[j]+hf*10240); else FA[hf][j]=cat(lo,lds_tr(ab[j]+hf*10240+1280)); }
        else if(hf==1&&!last){ if(n<19){ ab[n-14]+=step; asm volatile("":"+v"(ab[n-14])); } if(n>=18){ bb[n-18]+=step; asm volatile("":"+v"(bb[n-18])); } }
        if(last){ const int j=n>>2, s=(n>>1)&1; if(!(n&1)) lo2=lds_tr(fad[s][0]+8*j); else fa[s][j]=cat(lo2,lds_tr(fad[s][1]+8*j)); }
        SB(); } };
    bar();
    ST(2);
    __builtin_amdgcn_s_waitcnt(0xC07F);
    SB();
    half(0,false,false,0);
    #pragma unroll 1
    for(u32 s=1;s<10;++s){
      half(1,true,false,(s&3)?B_ST:-3*B_ST);
      rbar();
      SB();
      half(0,true,false,0); }
    const u32x2 ev=*(volatile LAS u32x2*)(L+B_F+88);
    half(1,true,true,0);
    E=(u64)(u32)__builtin_amdgcn_readfirstlane(ev.x)|((u64)(u32)__builtin_amdgcn_readfirstlane(ev.y)<<32);
    asm volatile("s_waitcnt lgkmcnt(0)":::"memory");
    waitv<0>();
    count(7);
    #pragma unroll
    for(int t=0;t<4;++t)
      #pragma unroll
      for(int i=0;i<5;++i) acc[i][t]=mfma16(FA[1][i],FB[1][t],acc[i][t]);
    u32x2 pk[5][4];
    #pragma unroll
    for(int t=0;t<4;++t)
      #pragma unroll
      for(int i=0;i<5;++i) pk[i][t]=u32x2{pk_bf16(acc[i][t][0],acc[i][t][1]),pk_bf16(acc[i][t][2],acc[i][t][3])};
    ST(3);
    if(ISQ){
      u32x4 f2[5];
      #pragma unroll
      for(int i=0;i<5;++i) f2[i]=cat(lds_tr(fad[2][0]+8*i),u32x2{0u,0u});
      lptr sp_=pv+g*2560+(lane&15)*40;
      f32x4 z[4][5];
      #pragma unroll
      for(int t=0;t<4;++t)
        #pragma unroll
        for(int i=0;i<5;++i) z[t][i]=mfma16(fa[0][i],cat(pk[0][t],pk[1][t]),Z4);
      #pragma unroll
      for(int t=0;t<4;++t)
        #pragma unroll
        for(int i=0;i<5;++i) z[t][i]=mfma16(fa[1][i],cat(pk[2][t],pk[3][t]),z[t][i]);
      SB();
      #pragma unroll
      for(int t=0;t<5;++t){
        #pragma unroll
        for(int i=0;i<5;++i){ if(t<4) z[t][i]=mfma16(f2[i],cat(pk[4][t],u32x2{0u,0u}),z[t][i]);
          if(t>0) lds_st<u32x2>(sp_+(t-1)*640+8*i,u32x2{pk_bf16(z[t-1][i][0],z[t-1][i][1]),pk_bf16(z[t-1][i][2],z[t-1][i][3])}); }
        SB(); }
      ST(10);
      #pragma unroll
      for(int i=0;i<10;++i){ const int c=i*64+lane, hd=(c*205)>>15;
        st_sc1(rs,WS_Q+((8*grp+4*hh+hd)*2048+128*rt+64*th)*40+(c-160*hd)*16,lds_ld<u32x4>(pv+c*16)); }
      ST(11);
      waitv<0>(); ST(12); arrive();
      ST(4);
      if(doout){ fi=16*(ot&7)+(ot>>3); flag_wait(ws,E,fi,flag_get(ws,fi)); }
    } else {
      { f32x4 z[4][5]; u32 ph[4][10];
        auto fm=[&](int t){
          const u32x4 b0=cat(pk[0][t],pk[1][t]), b1=cat(pk[2][t],pk[3][t]);
          #pragma unroll
          for(int i=0;i<5;++i) z[t][i]=mfma16(fa[0][i],b0,Z4);
          #pragma unroll
          for(int i=0;i<5;++i) z[t][i]=mfma16(fa[1][i],b1,z[t][i]); };
        fm(0); fm(1);
        SB();
        softmax20x2(z,ph,[&](int k){ const int t=2+k/10, i=k%5;
          z[t][i]=(k%10<5)?mfma16(fa[0][i],cat(pk[0][t],pk[1][t]),Z4):mfma16(fa[1][i],cat(pk[2][t],pk[3][t]),z[t][i]); });
        auto wp=[&](int t){
          #pragma unroll
          for(int i=0;i<5;++i) lds_st<u32x2>(pp+t*2688+8*i,u32x2{ph[t][2*i],ph[t][2*i+1]}); };
        u32x2 v2[4];
        #pragma unroll
        for(int t=0;t<4;++t) v2[t]=(g<2)?u32x2{0x3F80u,0u}:pk[2][t];
        softmax20x2(z+2,ph+2,[&](int k){
          if(k<10) *(volatile LAS u32x2*)(pp+(k/5)*2688+8*(k%5))=u32x2{ph[k/5][2*(k%5)],ph[k/5][2*(k%5)+1]};
          else if(k<14) *(volatile LAS u32x2*)(vp2+(k-10)*1664)=v2[k-10];
          else if(k<18) *(volatile LAS u32x2*)(vp3+(k-14)*1664)=pk[3][k-14];
          else { *(volatile LAS u32x2*)(vp4+(2*k-36)*1664)=pk[4][2*k-36]; *(volatile LAS u32x2*)(vp4+(2*k-35)*1664)=pk[4][2*k-35]; } });
        wp(2); wp(3); }
      ST(4);
      u64 fl;
      { f32x4 c[2][6];
        #pragma unroll
        for(int h2=0;h2<2;++h2){
          if(h2){ pa+=th?80:-80; vb+=th?48:-48; }
          u32x4 a[2][3], b[2][2];
          #pragma unroll
          for(int ks=0;ks<2;++ks){
            #pragma unroll
            for(int i=0;i<3;++i) a[ks][i]=cat(lds_tr(pa+ks*32*168+32*i),lds_tr(pa+(ks*32+1)*168+32*i));
            #pragma unroll
            for(int dt=0;dt<2;++dt) b[ks][dt]=cat(lds_tr(vb+ks*32*104+32*dt),lds_tr(vb+(ks*32+1)*104+32*dt)); }
          #pragma unroll
          for(int i=0;i<6;++i) c[h2][i]=mfma16(a[0][i%3],b[0][i/3],Z4);
          #pragma unroll
          for(int i=0;i<6;++i) c[h2][i]=mfma16(a[1][i%3],b[1][i/3],c[h2][i]); }
        #pragma unroll
        for(int i=0;i<6;++i) *(volatile LAS f32x4*)(pv+i*1024+lane*16)=c[0][i];
        *(volatile LAS u32*)(L+B_F+4*w)=1u;
        #pragma unroll
        for(int i=0;i<6;++i) asm volatile("":"+v"(c[1][i]));
        lptr ox_=L+B_PV+(w^2)*B_PVS; f32x4 pr[6];
        for(int it=0;it<SPIN_MAX;++it){
          const u32 f=*(volatile LAS u32*)(L+B_F+4*(w^2)), n=*(volatile LAS u32*)(L+B_F+16);
          #pragma unroll
          for(int i=0;i<6;++i) pr[i]=*(volatile LAS f32x4*)(ox_+i*1024+lane*16);
          if(__builtin_amdgcn_readfirstlane(f+n)==5u) break; }
        fl=flag_get(ws,fi);
        #pragma unroll
        for(int i=0;i<6;++i){ u32 o=(i<3)?(po0+i*1024):(po1+(i-3)*320); if(i%3==2) o=(g<2)?o:OOB;
          st_sc1(rs,o,__builtin_bit_cast(u32x4,c[1][i]+pr[i])); } }
      ST(5);
      o_phase<true>(L,rs,ws,E,w,oh,tq,fi,fl);
      count(6);
      if(rt==15&&grp==7&&w==3){
        for(int it=0;it<SPIN_MAX;++it){ if(__all(flag_get(ws,lane)==E&&flag_get(ws,64+lane)==E&&flag_get(ws,128+lane)==E)) break; __builtin_amdgcn_s_sleep(2); }
        if(lane==0) __hip_atomic_store((GAS u64*)ws,E,__ATOMIC_RELAXED,__HIP_MEMORY_SCOPE_AGENT); }
      ST(6);
    }
    if(!doout){
      until(21,4u);
      const char* up=(const char*)u+(size_t)tid*4*1280+2*(80*X+64+4*(li-4)); u32x4 um[4];
      #pragma unroll
      for(int r=0;r<4;++r) um[r]=cat(*(const u32x2*)(up+r*1280),*(const u32x2*)(up+(r+1024)*1280));
      u_scatter(false,tid,L,um);
      asm volatile("s_waitcnt lgkmcnt(0)":::"memory");
      __builtin_amdgcn_s_waitcnt(0x0F70); return false; }
    until(20,4u);
    { const u32 ob=WS_O+ot*20480;
      const u32 sm=ob+((lane<32)?((5*(lane>>3)+((lane>>1)&3))*1024+(lane&1)*512+508):((5*((lane-32)>>3)+4)*1024+((lane&7)>>1)*256+244+8*(lane&1)));
      for(int it=0;it<SPIN_MAX;++it){ asm volatile("":::"memory");
        if(__all(__builtin_amdgcn_raw_buffer_load_b32(rs,sm,0,16)!=SENT)) break;
        __builtin_amdgcn_s_sleep(1); }
      ST(15);
      const u32 vo=lane*16;
      for(u32 it=1;it<SPIN_MAX;++it){
        #pragma unroll
        for(int i=0;i<5;++i) dma16x4_sc1((const char*)ws+ob+i*4096,vo,vo,vo,vo,L+OIM+w*20480+i*4096);
        waitv<20-4*HB>();
        rbar();
        waitv<0>();
        ST(7);
        rbar();
        rbar();
        rbar();
        if(__builtin_amdgcn_readfirstlane(*bad)!=it) break; } }
    __builtin_amdgcn_s_waitcnt(0x0F70);
  } else {
    const int hw=w-4;
    asm volatile("s_load_dwordx2 %0, %1, 0x0":"=s"(E):"s"(ws):"memory");
    ST(9);
    waitv<9>(); rbar(); st.issue(2);
    ST(8);
    waitv<9>(); rbar();
    st.issue(3);
    asm volatile("s_waitcnt lgkmcnt(0)":"+s"(E)::"memory"); E+=1;
    if(tid==256) *(volatile LAS u32x2*)(L+B_F+88)=u32x2{(u32)E,(u32)(E>>32)};
    if(!ISQ&&lane<50){ lptr p=L+B_WP+hw*800+lane*16; lds_st<u32x4>(p+3200,lds_ld<u32x4>(p)^0x80008000u); }
    waitv<9>(); bar();
    if(!doout) params();
    st.issue(4);
    const int pr=hw>>1, me2=hw&1;
    u32 wq[40];
    { const int r=lane>>3; const u32 sw=((lane&7)^((((r>>1)&1)|(me2<<1))<<1))<<4, vo=r*1280+sw, vo2=((r>>2)*20+(r&3)+16+32*me2)*1280+sw;
      #pragma unroll
      for(int n=0;n<40;++n){ const int G=n/10, m=n%10, i=(n<22)?n:n-22, k=(i<16)?(i&3):((n>=18&&n<21)?n-18:((i<18)?i-16:0));
        wq[n]=((m<8)?vo+(160*G+20*m)*1280:vo2+(160*G+80*(m-8))*1280)-1024*k; asm volatile("":"+v"(wq[n])); } }
    lptr dst=L+hw*18432, rd, rd1; int x9, x10;
    { const int q=(lane&15)>>2, p=lane&3, f=((q>>1)|((g&1)<<1))<<1, ow=pr*2+(g&1);
      lptr rb=L+ow*18432+(g>>1)*1024+q*128+8*(p&1); rd=rb+(((4*me2+(p>>1))^f)<<4); rd1=rb+(((4*me2+2+(p>>1))^f)<<4);
      x9=B_WP+ow*3072-ow*18432; x10=(g>>1)?(B_PV+17408-1024):(x9+2048); }
    lptr ra9=rd+x9, rb9=rd1+x9, ra10=rd+x10, rb10=rd1+x10;
    asm volatile("":"+v"(rd),"+v"(rd1),"+v"(ra9),"+v"(rb9),"+v"(ra10),"+v"(rb10));
    waitv<9>(); rbar();
    #pragma unroll 1
    for(int s=4;s<9;++s){
      st.issue(s+1);
      waitv<9>(); rbar(); }
    waitv<0>(); rbar();
    if(!doout){ until(7,4u); return true; }
    if(!ISQ){
      const u32x4 sv={SENT,SENT,SENT,SENT};
      #pragma unroll
      for(int i=0;i<4;++i) st_sc1(rs,(i<3||lane<18)?(WS_P1+((4*grp+hw)*16+rt)*3584+i*1024+lane*16):OOB,sv);
      #pragma unroll
      for(int i=0;i<5;++i) st_sc1(rs,WS_O+((8*rt+grp)*20+5*hw+i)*1024+lane*16,sv);
      waitv<0>(); arrive(); }
    u32x4 wf[2][11];
    const char* gb=(const char*)wo+ct*256+pr*128+me2*8*1280;
    auto meet=[&](u32 n){ *(volatile LAS u32*)(L+B_F+48+4*hw)=n; until(12+(hw^1),n); };
    auto fetch18=[&](int n0){
      #pragma unroll
      for(int i=0;i<16;i+=4) dma16x4(gb,wq[n0+i],wq[n0+i+1],wq[n0+i+2],wq[n0+i+3],dst+i*1024);
      dma16xn<2>(gb,wq[n0+16],wq[n0+17],0,dst+16384); };
    until(ISQ?21:7,4u);
    ST(3);
    fetch18(0);
    dma16xn<3>(gb,wq[18],wq[19],wq[20],L+B_WP+hw*3072);
    dma16(gb,wq[21],dst+B_PV+17408);
    waitv<0>(); meet(1u);
    #pragma unroll
    for(int s=0;s<9;++s){ wf[0][s]=cat(lds_tr(rd+s*2048),lds_tr(rd+s*2048+512)); wf[1][s]=cat(lds_tr(rd1+s*2048),lds_tr(rd1+s*2048+512)); }
    wf[0][9]=cat(lds_tr(ra9),lds_tr(ra9+512)); wf[1][9]=cat(lds_tr(rb9),lds_tr(rb9+512));
    wf[0][10]=cat(lds_tr(ra10),lds_tr(ra10+512)); wf[1][10]=cat(lds_tr(rb10),lds_tr(rb10+512));
    asm volatile("s_waitcnt lgkmcnt(0)":::"memory");
    meet(2u);
    ST(10);
#define R2A 8
    #pragma unroll
    for(int i=0;i<R2A;i+=4) dma16x4(gb,wq[22+i],wq[23+i],wq[24+i],wq[25+i],dst+i*1024);
    if(!ISQ){ o_phase<false>(L,rs,ws,E,w,oh,tq,0,0); count(6); ST(6); until(6,8u); }
    #pragma unroll
    for(int i=R2A;i<16;i+=4) dma16x4(gb,wq[22+i],wq[23+i],wq[24+i],wq[25+i],dst+i*1024);
    dma16xn<2>(gb,wq[38],wq[39],0,dst+16384);
    count(20);
    ST(4);
    lptr obt[4];
    #pragma unroll
    for(int t=0;t<4;++t){ obt[t]=L+OIM+lane*16+((t+hw)&3)*20480; asm volatile("":"+v"(obt[t])); }
    waitv<0>();
    for(u32 it=1;it<SPIN_MAX;++it){
      rbar();
      ST(5);
      f32x4 c[2][4];
      #pragma unroll
      for(int t=0;t<4;++t){ c[0][t]=Z4; c[1][t]=Z4; }
      u32x4 ob[3][4], wl[3][2]; u32 chk=0;
      auto fetch=[&](int ks){
        #pragma unroll
        for(int t=0;t<4;++t) ob[ks%3][t]=lds_ld<u32x4>(obt[t]+ks*1024);
        if(ks>=11){ wl[ks%3][0]=cat(lds_tr(rd+(ks-11)*2048),lds_tr(rd+(ks-11)*2048+512)); wl[ks%3][1]=cat(lds_tr(rd1+(ks-11)*2048),lds_tr(rd1+(ks-11)*2048+512)); } };
      fetch(0); fetch(1);
      SB();
      #pragma unroll
      for(int ks=0;ks<20;++ks){ u32x2 lo;
        if(ks==4*HB-2){ rbar(); SB(); }
        #pragma unroll
        for(int n=0;n<8;++n){ const int t=n>>1, cc=n&1, k2=ks+2;
          c[cc][t]=mfma16((ks<11)?wf[cc][ks<11?ks:0]:wl[ks%3][cc],ob[ks%3][t],c[cc][t]);
          SB();
          if(k2<20){
            if(n<4) ob[k2%3][n]=lds_ld<u32x4>(obt[n]+k2*1024);
            else if(k2>=11){ lptr p=((n<6)?rd:rd1)+(k2-11)*2048; if(!(n&1)) lo=lds_tr(p); else wl[k2%3][(n-4)>>1]=cat(lo,lds_tr(p+512)); } }
          if(n==7) chk=max(chk,max(ob[ks%3][0].x,ob[ks%3][0].z));
          SB(); } }
      if(__any(chk==SENT)) *bad=it;
      bar();
      #pragma unroll
      for(int t=0;t<4;++t)
        #pragma unroll
        for(int cc=0;cc<2;++cc) lds_st<u32x2>(L+OIM+(16*((t+hw)&3)+(lane&15))*272+hw*64+cc*32+8*g,u32x2{pk_bf16(c[cc][t][0],c[cc][t][1]),pk_bf16(c[cc][t][2],c[cc][t][3])});
      ST(7);
      bar();
      if(__builtin_amdgcn_readfirstlane(*bad)!=it) break; }
  }
  #pragma unroll
  for(int i=0;i<2;++i){ const int r=32*i+(tid>>4);
    __builtin_amdgcn_raw_buffer_store_b128(lds_ld<u32x4>(L+OIM+r*272+(tid&15)*16),__builtin_amdgcn_make_buffer_rsrc(out,0,2048*2560,0x00020000),(64*tt64+r)*2560+1280+ct*256+(tid&15)*16,0,16); }
  if(w<4) ST(8);
  return false;
}

constexpr int CODE_LINES=312;
__global__ __launch_bounds__(512,2) void fused(const u16* u,const u16* are,const u16* aim,const u16* bre,const u16* bim,const u16* cre,const u16* cim,
    const u16* logdt,const u16* dskip,const u16* x,const u16* wqkv,const u16* wphi,const u16* wo,u16* out,void* ws){
  extern __shared__ __attribute__((aligned(16))) char smem[];
  lptr L=(lptr)smem;
  const int b=blockIdx.x, X=b&7, idx=b>>3;
  u64 pc; asm volatile("s_getpc_b64 %0":"=s"(pc));
  const int w=__builtin_amdgcn_readfirstlane(threadIdx.x>>6);
  const bool isa=(idx>>3)==2;
  const int q=idx>=24, li=idx-24*q, rt=4*(X>>1)+(li>>(2-q)), grp=((X&1)<<(2-q))+(li&(3>>q));
  if(idx<16&&w<2){
    const u32 n=(w?(X*16+idx)*50:idx*32)+(threadIdx.x&63); u32 keep;
    if(w?((threadIdx.x&63)<50):(threadIdx.x<32&&n<CODE_LINES))
      asm volatile("s_mov_b32 %0, m0\n\ts_mov_b32 m0, %3\n\ts_nop 0\n\tglobal_load_lds_dword %1, %2\n\ts_mov_b32 m0, %0":"=&s"(keep):"v"(n*128),"s"(w?(u64)wo:(pc&~255ull)),"s"(162560+w*256):"memory"); }
  Stream st;
  if(__builtin_expect(!isa&&w>=4,1)){ st.init(q,li,rt,grp,w&3,threadIdx.x&63,x,wqkv,L); st.issue(0,false);
    { const u32 nb=800<<q, o=(threadIdx.x&63)*16; const char* wp=(const char*)wphi+(4*grp+(w&3))*nb;
      if(o<nb) dma16(wp,o,L+B_WP+(w&3)*nb);
      if(o+1024<nb) dma16(wp,o+1024,L+B_WP+(w&3)*nb+1024); }
    st.issue(1); }
  SB();
  STINIT();
  const int ch0=isa?(80*X+8*(idx-16)):(80*X+64+4*(idx-28)), wi=isa?w:w-4;
  SsmP sp; u32 dsk;
  auto params=[&](){ const SsmIn in={are,aim,bre,bim,cre,cim,logdt}; sp=load_ssm(in,ch0+wi,threadIdx.x&63); dsk=dskip[ch0+wi]; };
  bool job=true;
  if(__builtin_expect(!isa,1)) job=role_b(q,X,li,rt,grp,w,st,L,params,u,wo,out,ws);
  else params();
  if(job){
    conv_job(isa,wi,ch0,idx&7,L,sp,dsk,u,out,ws); }
  STDUMP();
}

extern "C" void kernel_launch(void* const* d_in, const int* in_sizes, int n_in, void* d_out, int out_size, void* d_ws, size_t ws_size, hipStream_t stream){
  (void)hipFuncSetAttribute((const void*)fused,hipFuncAttributeMaxDynamicSharedMemorySize,163840);
  fused<<<256,512,163840,stream>>>((const u16*)d_in[0],(const u16*)d_in[1],(const u16*)d_in[2],(const u16*)d_in[3],(const u16*)d_in[4],(const u16*)d_in[5],(const u16*)d_in[6],
    (const u16*)d_in[7],(const u16*)d_in[8],(const u16*)d_in[9],(const u16*)d_in[10],(const u16*)d_in[11],(const u16*)d_in[12],(u16*)d_out,d_ws);
}
```

```cpp
#define HB 2
#include <hip/hip_runtime.h>
#include <stdint.h>

typedef unsigned short u16; typedef unsigned u32; typedef unsigned long long u64;
typedef __attribute__((ext_vector_type(4))) short s16x4;
typedef __attribute__((ext_vector_type(8))) _Float16 f16x8;
typedef __attribute__((ext_vector_type(8))) __bf16 bf16x8;
typedef __attribute__((ext_vector_type(2))) __bf16 bf16x2;
typedef __attribute__((ext_vector_type(2))) _Float16 f16x2;
typedef __attribute__((ext_vector_type(2))) float f32x2;
typedef __attribute__((ext_vector_type(4))) float f32x4;
typedef __attribute__((ext_vector_type(16))) float f32x16;
typedef __attribute__((ext_vector_type(2))) unsigned u32x2;
typedef __attribute__((ext_vector_type(3))) unsigned u32x3;
typedef __attribute__((ext_vector_type(4))) unsigned u32x4;
#define LAS __attribute__((address_space(3)))
#define GAS __attribute__((address_space(1)))
#define DI __device__ __forceinline__
typedef LAS char* lptr;
typedef __amdgpu_buffer_rsrc_t rsrc_t;

#define ST(i) do{}while(0)
#define STDUMP() do{}while(0)
#define STINIT() do{}while(0)

DI u32 pk_bf16(float a, float b){ f32x2 v={a,b}; return __builtin_bit_cast(u32,__builtin_convertvector(v,bf16x2)); }
DI u32 pk_f16(f32x2 v){ return __builtin_bit_cast(u32,__builtin_convertvector(v,f16x2)); }
DI float bflo(u32 x){ return __builtin_bit_cast(float,x<<16); }
DI float bfhi(u32 x){ return __builtin_bit_cast(float,x&0xffff0000u); }
DI float rbf(float a){ return bflo(pk_bf16(a,a)); }
template<class T> DI T lds_ld(lptr p){ return *(LAS T*)p; }
template<class T> DI void lds_st(lptr p, T v){ *(LAS T*)p=v; }
DI u32x2 lds_ld64(lptr p){ return *(volatile LAS u32x2*)p; }
DI u32x2 lds_tr(lptr p){ return __builtin_bit_cast(u32x2,__builtin_amdgcn_ds_read_tr16_b64_v4i16((LAS s16x4*)p)); }
DI f32x4 mfma16(u32x4 a,u32x4 b,f32x4 c){ return __builtin_amdgcn_mfma_f32_16x16x32_bf16(__builtin_bit_cast(bf16x8,a),__builtin_bit_cast(bf16x8,b),c,0,0,0); }
DI u32x4 cat(u32x2 a,u32x2 b){ u32x4 r={a.x,a.y,b.x,b.y}; return r; }
DI u32 p_ll(u32 a,u32 b){ return __builtin_amdgcn_perm(b,a,0x05040100u); }
DI u32 p_lh(u32 a,u32 b){ return __builtin_amdgcn_perm(b,a,0x07060100u); }
DI u32 p_hl(u32 a,u32 b){ return __builtin_amdgcn_perm(b,a,0x05040302u); }
DI u32 p_hh(u32 a,u32 b){ return __builtin_amdgcn_perm(b,a,0x07060302u); }
#define SB() __builtin_amdgcn_sched_barrier(0)
DI void rbar(){ asm volatile("":::"memory"); __builtin_amdgcn_s_barrier(); asm volatile("":::"memory"); }
DI void bar(){ asm volatile("s_waitcnt lgkmcnt(0)":::"memory"); __builtin_amdgcn_s_barrier(); asm volatile("":::"memory"); }
template<int N> DI void waitv(){ asm volatile("s_waitcnt vmcnt(%0)"::"i"(N):"memory"); }
constexpr f32x4 Z4={0.f,0.f,0.f,0.f};
DI float wave_max(float m){
  m=fmaxf(m,__builtin_bit_cast(float,__builtin_amdgcn_update_dpp(0,__builtin_bit_cast(int,m),0xB1,0xf,0xf,true)));
  m=fmaxf(m,__builtin_bit_cast(float,__builtin_amdgcn_update_dpp(0,__builtin_bit_cast(int,m),0x4E,0xf,0xf,true)));
  m=fmaxf(m,__builtin_bit_cast(float,__builtin_amdgcn_update_dpp(0,__builtin_bit_cast(int,m),0x141,0xf,0xf,true)));
  m=fmaxf(m,__builtin_bit_cast(float,__builtin_amdgcn_update_dpp(0,__builtin_bit_cast(int,m),0x140,0xf,0xf,true)));
  int x=__builtin_bit_cast(int,m);
  float a=__builtin_bit_cast(float,__builtin_amdgcn_readlane(x,0)), b=__builtin_bit_cast(float,__builtin_amdgcn_readlane(x,16));
  float c=__builtin_bit_cast(float,__builtin_amdgcn_readlane(x,32)), d=__builtin_bit_cast(float,__builtin_amdgcn_readlane(x,48));
  return fmaxf(fmaxf(a,b),fmaxf(c,d)); }

constexpr u32 WS_FLAG=256, WS_P1=8192, WS_Q=WS_P1+32*16*3584, WS_O=WS_Q+32*2048*40, WS_END=WS_O+128*20*1024;
constexpr u32 SENT=0xFFFFFFFFu, OOB=0x80000000u;
constexpr int SPIN_MAX=1<<17;
DI void dma16(const void* sb, u32 voff, lptr l){ u32 keep;
  asm volatile("s_mov_b32 %0, m0\n\ts_mov_b32 m0, %3\n\ts_nop 0\n\tglobal_load_lds_dwordx4 %1, %2\n\ts_mov_b32 m0, %0":"=&s"(keep):"v"(voff),"s"(sb),"s"((u32)(uintptr_t)l):"memory"); }
#define DMA4_BODY(SC) asm volatile("s_mov_b32 %0, m0\n\ts_mov_b32 m0, %6\n\ts_nop 0\n\tglobal_load_lds_dwordx4 %1, %5" SC "\n\tglobal_load_lds_dwordx4 %2, %5 offset:1024" SC \
    "\n\tglobal_load_lds_dwordx4 %3, %5 offset:2048" SC "\n\tglobal_load_lds_dwordx4 %4, %5 offset:3072" SC "\n\ts_mov_b32 m0, %0" \
    :"=&s"(keep):"v"(v0),"v"(v1),"v"(v2),"v"(v3),"s"(sb),"s"((u32)(uintptr_t)l):"memory")
DI void dma16x4(const void* sb,u32 v0,u32 v1,u32 v2,u32 v3,lptr l){ u32 keep; DMA4_BODY(""); }
DI void dma16x4_sc1(const void* sb,u32 v0,u32 v1,u32 v2,u32 v3,lptr l){ u32 keep; DMA4_BODY(" sc1"); }
template<int N> DI void dma16xn(const void* sb,u32 v0,u32 v1,u32 v2,lptr l){ u32 keep;
  if(N==2) asm volatile("s_mov_b32 %0, m0\n\ts_mov_b32 m0, %4\n\ts_nop 0\n\tglobal_load_lds_dwordx4 %1, %3\n\tglobal_load_lds_dwordx4 %2, %3 offset:1024\n\ts_mov_b32 m0, %0"
    :"=&s"(keep):"v"(v0),"v"(v1),"s"(sb),"s"((u32)(uintptr_t)l):"memory");
  else asm volatile("s_mov_b32 %0, m0\n\ts_mov_b32 m0, %5\n\ts_nop 0\n\tglobal_load_lds_dwordx4 %1, %4\n\tglobal_load_lds_dwordx4 %2, %4 offset:1024\n\tglobal_load_lds_dwordx4 %3, %4 offset:2048\n\ts_mov_b32 m0, %0"
    :"=&s"(keep):"v"(v0),"v"(v1),"v"(v2),"s"(sb),"s"((u32)(uintptr_t)l):"memory"); }
DI u32x4 ld_sc1(rsrc_t rs,u32 off){ return __builtin_amdgcn_raw_buffer_load_b128(rs,off,0,16); }
DI void st_sc1(rsrc_t rs,u32 off,u32x4 v){ __builtin_amdgcn_raw_buffer_store_b128(v,rs,off,0,16); }
DI void gather16(rsrc_t rs,u32 off,u32x4* v){
  for(int it=0;it<SPIN_MAX;++it){
    u32 todo=0;
    #pragma unroll
    for(int k=0;k<4;++k){ u32 m=0;
      #pragma unroll
      for(int i=0;i<4;++i) m=max(m,max(v[4*k+i].x,v[4*k+i].z));
      if(__any(m==SENT)) todo|=1u<<k; }
    if(!todo) break;
    asm volatile("":::"memory");
    #pragma unroll
    for(int k=0;k<4;++k) if(todo>>k&1){
      #pragma unroll
      for(int i=0;i<4;++i) v[4*k+i]=ld_sc1(rs,off+(4*k+i)*3584); } } }
DI void flag_set(void* ws,int i,u64 E){ __hip_atomic_store((GAS u64*)((char*)ws+WS_FLAG)+i,E,__ATOMIC_RELAXED,__HIP_MEMORY_SCOPE_AGENT); }
DI u64 flag_get(const void* ws,int i){ return __hip_atomic_load((const GAS u64*)((const char*)ws+WS_FLAG)+i,__ATOMIC_RELAXED,__HIP_MEMORY_SCOPE_AGENT); }
DI void flag_wait(const void* ws,u64 E,int idx,u64 f){
  for(int it=0;it<SPIN_MAX;++it){ if(__all(f==E)) break; __builtin_amdgcn_s_sleep(2); f=flag_get(ws,idx); } }

constexpr int A_UB=4576, A_US=9120, A_KB=4224, A_KS=8704;

DI f32x2 cmul(f32x2 a, f32x2 b){ f32x2 sw={-b.y,b.x}; return __builtin_elementwise_fma(a.yy,sw,a.xx*b); }
DI f32x2 cexp_e(float re2, float rev, float e){
  float m=__builtin_amdgcn_exp2f(re2*e); float a=__builtin_amdgcn_fractf(rev*e);
  f32x2 r={m*__builtin_amdgcn_cosf(a), m*__builtin_amdgcn_sinf(a)}; return r; }
struct SsmP { u32 are,aim,bre,bim,cre,cim,ld; float dt; };
struct SsmIn { const u16 *are,*aim,*bre,*bim,*cre,*cim,*logdt; };
DI SsmP load_ssm(const SsmIn& in, int ch, int lane){
  SsmP p; int o=ch*64+lane;
  p.are=((const u32*)in.are)[o]; p.aim=((const u32*)in.aim)[o]; p.bre=((const u32*)in.bre)[o];
  p.bim=((const u32*)in.bim)[o]; p.cre=((const u32*)in.cre)[o]; p.cim=((const u32*)in.cim)[o];
  p.ld=in.logdt[ch];
  return p; }
DI f32x2 ssm_w(const SsmP& p, int half){
  f32x2 b={half?bfhi(p.bre):bflo(p.bre), half?bfhi(p.bim):bflo(p.bim)};
  f32x2 c={half?bfhi(p.cre):bflo(p.cre), half?bfhi(p.cim):bflo(p.cim)};
  return cmul(c,b*p.dt); }
DI void ssm_prep(SsmP& p, float& s, float& inv){
  asm volatile("":"+v"(p.ld));
  p.dt=rbf(expf(bflo(p.ld)));
  f32x2 w0=ssm_w(p,0), w1=ssm_w(p,1);
  float m=wave_max(fmaxf(fmaxf(fabsf(w0.x),fabsf(w0.y)),fmaxf(fabsf(w1.x),fabsf(w1.y))));
  int e=__builtin_amdgcn_frexp_expf(m); e=max(-100,min(100,e));
  s=__builtin_amdgcn_ldexpf(1.0f,-e); inv=__builtin_amdgcn_ldexpf(1.0f,e); }
DI void kgen_half(const SsmP& p, int half, float s, lptr tab, int lane, f32x16& acc){
  const float are=(half?bfhi(p.are):bflo(p.are))*p.dt, aim=(half?bfhi(p.aim):bflo(p.aim))*p.dt;
  const float re2=are*1.4426950408889634f, rev=aim*0.15915494309189535f;
  lptr wr=tab+lane*4; lptr rd=tab+(lane&31)*272+(lane>>5)*16;
  { f32x2 e8=cexp_e(re2,rev,8.f), e16=cmul(e8,e8);
    f32x2 a={1.f,0.f}, b=e8;
    #pragma unroll
    for(int j=0;j<32;j+=2){ lds_st<u32>(wr+j*272,pk_f16(a)); lds_st<u32>(wr+(j+1)*272,pk_f16(b)); if(j<30){ a=(j==0)?e16:cmul(a,e16); b=cmul(b,e16);} } }
  u32x4 bf[8];
  #pragma unroll
  for(int k=0;k<8;++k) bf[k]=lds_ld<u32x4>(rd+k*32);
  { f32x2 w=ssm_w(p,half)*s; w.y=-w.y;
    f32x2 e1=cexp_e(re2,-rev,1.f), g=cexp_e(re2,-rev,256.f);
    f32x2 g2=cmul(g,g), g3=cmul(g2,g), g4=cmul(g2,g2);
    f32x2 x[8]; x[0]=w;
    #pragma unroll
    for(int i=1;i<8;++i) x[i]=cmul(x[i-1],e1);
    #pragma unroll
    for(int i=0;i<8;++i) lds_st<u32>(wr+i*272,pk_f16(x[i]));
    #pragma unroll
    for(int i=0;i<4;++i) lds_st<u32>(wr+(8+i)*272,pk_f16(cmul(x[2*i],g)));
    lds_st<u32>(wr+12*272,pk_f16(cmul(x[0],g2))); lds_st<u32>(wr+13*272,pk_f16(cmul(x[4],g2)));
    lds_st<u32>(wr+14*272,pk_f16(cmul(x[0],g3))); lds_st<u32>(wr+15*272,pk_f16(cmul(x[4],g3)));
    f32x2 y=cmul(w,g4);
    #pragma unroll
    for(int i=0;i<5;++i){ lds_st<u32>(wr+(16+i)*272,pk_f16(y)); if(i<4) y=cmul(y,g); } }
  #pragma unroll
  for(int k=0;k<8;++k){ u32x4 af=lds_ld<u32x4>(rd+k*32);
    acc=__builtin_amdgcn_mfma_f32_32x32x16_f16(__builtin_bit_cast(f16x8,af),__builtin_bit_cast(f16x8,bf[k]),acc,0,0,0); }
}
DI void k_finish(const float* v, lptr ka, int lane){
  lptr ks=ka; lptr kb=ka+A_KB;
  { const int j=lane&31, h=lane>>5;
    lds_st<u32x2>(ks+(8*j+4*h)*2,u32x2{pk_bf16(v[0],v[1]),pk_bf16(v[2],v[3])});
    u32x2 b={pk_bf16(v[4],v[5]),pk_bf16(v[6],v[7])};
    u32 c0=pk_bf16(v[8],v[9]), c1=pk_bf16(v[10],v[11]);
    if(h==0){ lds_st<u32x2>(ks+(256+4*j)*2,b);
      lds_st<u16>(ks+(512+j)*2,(u16)c0); lds_st<u16>(ks+(544+j)*2,(u16)(c0>>16));
      lds_st<u16>(ks+(576+j)*2,(u16)c1); lds_st<u16>(ks+(608+j)*2,(u16)(c1>>16));
    } else { lds_st<u32>(ks+(384+2*j)*2,b.x); lds_st<u32>(ks+(448+2*j)*2,b.y); if(j==0) lds_st<u16>(ks+640*2,(u16)c0); } }
  const int M=lane; const bool lo=M<32;
  u32x4 v0=lds_ld<u32x4>(ks+(M&31)*16); u32x2 v1=lds_ld<u32x2>(ks+(128+4*(M|32))*2);
  u32 v4=lds_ld<u16>(ks+(132+4*(M|32))*2);
  u32 pv=lds_ld<u16>(ks+(lo?max(8*M-1,0):((M==32)?255:(128+4*M)))*2); if(M==0) pv=0;
  u32 ab=lds_ld<u32>(ks+(384+2*M)*2), c2=lds_ld<u16>(ks+(386+2*M)*2);
  u32 a3[2],b3[2];
  #pragma unroll
  for(int i=0;i<2;++i){ a3[i]=lds_ld<u16>(ks+(512+M+64*i)*2); b3[i]=lds_ld<u16>(ks+(513+M+64*i)*2); }
  asm volatile("":"+v"(v0),"+v"(v1),"+v"(v4),"+v"(pv),"+v"(ab),"+v"(c2),"+v"(a3[0]),"+v"(a3[1]),"+v"(b3[0]),"+v"(b3[1])::"memory");
  { u32x4 A,B;
    if(lo){ B.x=p_hl(v0.w,v0.w); B.y=p_hl(v0.z,v0.z); B.z=p_hl(v0.y,v0.y); B.w=p_hl(v0.x,v0.x);
      A.x=p_lh(v0.w,v0.z); A.y=p_lh(v0.z,v0.y); A.z=p_lh(v0.y,v0.x); A.w=p_ll(v0.x,pv);
    } else { B.x=p_lh(v4,v1.y); B.y=p_ll(v1.y,v1.y); B.z=p_lh(v1.y,v1.x); B.w=p_ll(v1.x,v1.x);
      A.x=p_hl(v1.y,v1.y); A.y=B.y; A.z=p_hl(v1.x,v1.x); A.w=p_ll(v1.x,pv); }
    lds_st<u32x4>(ka+(255-M)*16,A); lds_st<u32x4>(kb+(255-M)*16,B); }
  { u32x4 A,B; B.x=p_ll(c2,c2); B.y=p_hh(ab,ab); B.z=p_hl(ab,ab); B.w=p_ll(ab,ab);
    A.x=p_lh(c2,ab); A.y=B.y; A.z=B.w; A.w=B.w;
    lds_st<u32x4>(ka+(191-M)*16,A); lds_st<u32x4>(kb+(191-M)*16,B); }
  #pragma unroll
  for(int i=0;i<2;++i){ u32 a=a3[i], b=b3[i], c=(M&1)?b:a; u32x4 A,B;
    B.x=p_ll(b,b); B.y=p_ll(b,c); B.z=p_ll(a,a); B.w=B.z;
    A.x=B.x; A.y=p_ll(c,a); A.z=B.z; A.w=B.z;
    lds_st<u32x4>(ka+(127-64*i-M)*16,A); lds_st<u32x4>(kb+(127-64*i-M)*16,B); }
  if(lane<8) lds_st<u32x4>(((lane&4)?kb:ka)+(256+(lane&3))*16,u32x4{0,0,0,0});
}
DI void conv_steps(lptr ua, lptr ka, int lane, f32x4* acc){
  constexpr int NS=8, s0=0, kbo=A_KB;
  const int a=lane&15, g=lane>>4;
  lptr up=ua+((a&1)?0:A_UB)+(32*(a>>1)+8*g+32*s0)*2;
  lptr kp=ka+((a&1)?kbo:0)+(28-4*(a>>1)+8*g+32*s0)*2;
  u32x4 A[2][8],B[2][8];
  auto ldA=[&](int s,int q){ A[s&1][q]=cat(lds_ld64(kp+(1792-256*q+32*s)*2),lds_ld64(kp+(1792-256*q+32*s)*2+8)); };
  auto ldB=[&](int s,int q){ B[s&1][q]=lds_ld<u32x4>(up+(256*q+32*s)*2); };
  #pragma unroll
  for(int q=0;q<8;++q){ ldB(0,q); ldA(0,q); }
  #pragma unroll
  for(int c=0;c<8;++c) acc[c]=Z4;
  SB();
  #pragma unroll
  for(int s=0;s<NS;++s){ int n=0; u32x2 lo;
    #pragma unroll
    for(int q=0;q<8;++q){
      #pragma unroll
      for(int c=q;c<8;++c){
        acc[c]=mfma16(A[s&1][q],B[s&1][c-q],acc[c]);
        SB();
        if(s+1<NS){
          if(n==0) ldB(s+1,0);
          else if(n<3){ if(n==1) lo=lds_ld64(kp+(1792+32*(s+1))*2); else A[(s+1)&1][0]=cat(lo,lds_ld64(kp+(1792+32*(s+1))*2+8)); }
          else if(n<10) ldB(s+1,n-2);
          else if(n<24){ const int j=1+((n-10)>>1); if(!(n&1)) lo=lds_ld64(kp+(1792-256*j+32*(s+1))*2); else A[(s+1)&1][j]=cat(lo,lds_ld64(kp+(1792-256*j+32*(s+1))*2+8)); } }
        SB();
        ++n; } } }
}
DI u32 fin2(float y0,float y1,u32 u,float dsk){
  u32 y=pk_bf16(y0,y1), s=pk_bf16(bflo(u)*dsk,bfhi(u)*dsk);
  return pk_bf16(bflo(y)+bflo(s),bfhi(y)+bfhi(s)); }
DI void u_scatter(const bool BIG, int ur, lptr L, const u32x4* um){
  #pragma unroll
  for(int c=0;c<4;++c){
    lptr d=L+(224+4*ur)*2+(BIG?2*c*A_US:(2*(c&1)*A_US+(c>>1)*2048)); const bool last=BIG?(ur==511):(c>=2&&ur==255);
    const u32x2 e={p_ll(um[0][c],um[1][c]),p_ll(um[2][c],um[3][c])}, o={p_hh(um[0][c],um[1][c]),p_hh(um[2][c],um[3][c])};
    lds_st<u32x2>(d,e); lds_st<u32x2>(d+A_US,o);
    lds_st<u32>(d+A_UB+4,e.x); lds_st<u32>(d+A_US+A_UB+4,o.x);
    if(!last){ lds_st<u32>(d+A_UB+8,e.y); lds_st<u32>(d+A_US+A_UB+8,o.y); } } }
DI void conv_job(const bool BIG, int wi, int ch0, int rot, lptr L, SsmP& p0, u32 dsk, const u16* u, u16* out, void* ws){
  const int lane=threadIdx.x&63;
  lptr us=L+wi*A_US, kw=L+(BIG?8:4)*A_US+wi*A_KS;
  if(lane<56) lds_st<u32x4>(us+(lane<28?lane*16:A_UB+(lane-28)*16),u32x4{0,0,0,0});
  if(lane==56) lds_st<u32>(us+A_UB+448,0u);
  u32x4 um[4];
  const int ur=(64*(wi+rot)+lane)&511;
  ST(1);
  { float s,inv; ssm_prep(p0,s,inv); f32x16 acc={};
    #pragma unroll 1
    for(int h=0;h<2;++h){
      if(BIG&&h==1){ const char* up=(const char*)u+(size_t)ur*4*1280+2*ch0;
        #pragma unroll
        for(int r=0;r<4;++r) um[r]=*(const u32x4*)(up+r*1280); }
      kgen_half(p0,h,s,kw,lane,acc); }
    float v[12];
    #pragma unroll
    for(int r=0;r<12;++r) v[r]=acc[r]*inv;
    k_finish(v,kw,lane); }
  ST(2);
  if(BIG) u_scatter(true,ur,L,um);
  bar();
  ST(3);
  f32x4 acc[8];
  conv_steps(us,kw,lane,acc);
  ST(4);
  { const int tl=32*((lane&15)>>1)+8*(lane>>4)+2*(lane&1); asm volatile("":"+v"(dsk)); const float d=bflo(dsk);
    u32 uu[16];
    #pragma unroll
    for(int c=0;c<8;++c){ uu[2*c]=lds_ld<u32>(us+(224+256*c+tl)*2); uu[2*c+1]=lds_ld<u32>(us+(228+256*c+tl)*2); }
    #pragma unroll
    for(int c=0;c<8;++c){ lds_st<u32>(kw+(256*c+tl)*2,fin2(acc[c][0],acc[c][1],uu[2*c],d)); lds_st<u32>(kw+(256*c+tl+4)*2,fin2(acc[c][2],acc[c][3],uu[2*c+1],d)); } }
  bar();
  ST(5);
  { lptr yp=L+(BIG?8:4)*A_US+((lane&15)>>2)*A_KS+(256*wi+16*(lane>>4)+4*(lane&3))*2;
    char* op=(char*)out+(size_t)(256*wi+lane)*2560+2*ch0;
    #pragma unroll
    for(int i=0;i<4;++i){
      if(BIG) *(u32x4*)(op+(size_t)i*64*2560)=cat(lds_tr(yp+i*128),lds_tr(yp+4*A_KS+i*128));
      else { *(u32x2*)(op+(size_t)i*64*2560)=lds_tr(yp+i*128); *(u32x2*)(op+(size_t)(i*64+1024)*2560)=lds_tr(yp+2048+i*128); } } }
  ST(7);
}

constexpr int B_ST=36864, B_PV=2*B_ST, B_PVS=18432, B_WP=4*B_ST, B_Z=B_WP+6400, B_KT=159744, B_F=163168;
DI float max3(float a,float b,float c){ return __builtin_fmaxf(__builtin_fmaxf(a,b),c); }
template<class H> DI void softmax20x2(const f32x4 (*z)[5], u32 (*o)[10], H hook){
  const float c=1.4426950408889634f; float mc[2];
  { float m0[2],m1[2],m2[2];
    #pragma unroll
    for(int t=0;t<2;++t){ m0[t]=max3(z[t][0][0],z[t][0][1],z[t][0][2]); m1[t]=max3(z[t][0][3],z[t][1][0],z[t][1][1]); m2[t]=max3(z[t][1][2],z[t][1][3],z[t][2][0]); }
    #pragma unroll
    for(int t=0;t<2;++t){ m0[t]=max3(m0[t],z[t][2][1],z[t][2][2]); m1[t]=max3(m1[t],z[t][2][3],z[t][3][0]); m2[t]=max3(m2[t],z[t][3][1],z[t][3][2]); }
    #pragma unroll
    for(int t=0;t<2;++t){ m0[t]=max3(m0[t],z[t][3][3],z[t][4][0]); m1[t]=max3(m1[t],z[t][4][1],z[t][4][2]); m2[t]=__builtin_fmaxf(m2[t],z[t][4][3]); }
    #pragma unroll
    for(int t=0;t<2;++t) mc[t]=max3(m0[t],m1[t],m2[t])*-c; }
  SB();
  f32x2 e[2][10];
  #pragma unroll
  for(int i=0;i<10;++i)
    #pragma unroll
    for(int t=0;t<2;++t) e[t][i]=__builtin_elementwise_fma(f32x2{z[t][i>>1][(i&1)*2],z[t][i>>1][(i&1)*2+1]},f32x2{c,c},f32x2{mc[t],mc[t]});
  SB();
  #pragma unroll
  for(int i=0;i<10;++i)
    #pragma unroll
    for(int t=0;t<2;++t){ hook(2*i+t); e[t][i]=f32x2{__builtin_amdgcn_exp2f(e[t][i].x),__builtin_amdgcn_exp2f(e[t][i].y)}; SB(); }
  float r[2];
  { f32x2 s[2][5];
    #pragma unroll
    for(int i=0;i<5;++i)
      #pragma unroll
      for(int t=0;t<2;++t) s[t][i]=e[t][2*i]+e[t][2*i+1];
    #pragma unroll
    for(int t=0;t<2;++t){ s[t][0]+=s[t][1]; s[t][2]+=s[t][3]; }
    #pragma unroll
    for(int t=0;t<2;++t){ s[t][0]+=s[t][4]; }
    #pragma unroll
    for(int t=0;t<2;++t){ s[t][0]+=s[t][2]; }
    #pragma unroll
    for(int t=0;t<2;++t) r[t]=__builtin_amdgcn_rcpf(s[t][0].x+s[t][0].y); }
  SB();
  #pragma unroll
  for(int i=0;i<10;++i)
    #pragma unroll
    for(int t=0;t<2;++t) e[t][i]*=r[t];
  SB();
  #pragma unroll
  for(int i=0;i<10;++i)
    #pragma unroll
    for(int t=0;t<2;++t) o[t][i]=pk_bf16(e[t][i].x,e[t][i].y);
  SB();
}
DI u32x4 pack2(f32x4 a,f32x4 b){ u32x4 r={pk_bf16(a[0],a[1]),pk_bf16(a[2],a[3]),pk_bf16(b[0],b[1]),pk_bf16(b[2],b[3])}; return r; }
struct Stream { u32 xo[4], wv[5]; int s0, qw; const u16 *x, *w; lptr L;
  DI void init(int q,int li,int rt,int grp,int qw_,int lane,const u16* x_,const u16* w_,lptr L_){
    qw=qw_; x=x_; w=w_; L=L_;
    s0=((0x8530>>((((li>>(2-q))+((li&(3>>q))<<q))&3)*4))&15)+q;
    #pragma unroll
    for(int i=0;i<4;++i){ const u32 r=(4*qw+i)*8+(lane>>3); xo[i]=(rt*128+r)*1280u+(((lane&7)^((r>>1)&7))<<4)-1024u*i; }
    dma16x4((const char*)x+(s0>=10?s0-10:s0)*128,xo[0],xo[1],xo[2],xo[3],L+4*qw*1024);
    const u32 wlo=(160<<q)*grp+1280*(1-q), dh=1120*(1-q);
    #pragma unroll
    for(int i=0;i<5;++i){ const u32 c=(5*qw+i)*64+lane, r=(c*3277u)>>16, pos=c-r*20, sc=pos^(((r>>3)&1)<<1);
      wv[i]=r*3840u+wlo+(sc<10?0u:dh)+16*sc-1024u*(i&3); } }
  DI void issue(int s,bool withx=true) const { int ss=s+s0; if(ss>=10) ss-=10; lptr d=L+(s&3)*B_ST;
    const char* xb=(const char*)x+ss*128; const char* wb=(const char*)w+(size_t)ss*64*3840;
    if(withx) dma16x4(xb,xo[0],xo[1],xo[2],xo[3],d+4*qw*1024);
    dma16x4(wb,wv[0],wv[1],wv[2],wv[3],d+16384+5*qw*1024); dma16(wb,wv[4],d+16384+(5*qw+4)*1024); } };

template<bool GATHER> DI void o_phase(lptr L,rsrc_t rs,void* ws,u64 E,int w,int oh,int tq,int fi,u64 fl){
  const int lane=threadIdx.x&63, g=lane>>4, r=lane&15;
  if(GATHER){ if(!__all(fl==E)) flag_wait(ws,E,fi,fl); *(volatile LAS u32*)(L+B_F+128+4*w)=1u; }
  else for(int it=0;it<SPIN_MAX;++it){ if(__builtin_amdgcn_readfirstlane(*(volatile LAS u32*)(L+B_F+128+4*(w&3)))) break; __builtin_amdgcn_s_sleep(2); }
  asm volatile("":::"memory");
  u32x4 l0[2],l1[2]; u32x2 l2[2];
  { const u32 qo=WS_Q+(oh*2048+512*tq+128*(w&3)+64*(w>>2)+16*(g>>1)+r)*40;
    #pragma unroll
    for(int i=0;i<2;++i){ l0[i]=ld_sc1(rs,qo+i*1280); l1[i]=ld_sc1(rs,qo+i*1280+16); l2[i]=__builtin_amdgcn_raw_buffer_load_b64(rs,qo+i*1280+32,0,16); } }
  lptr kt=L+B_KT, q00,q01,q10,q11; u32 om,ox;
  { const int pc=5*(g&1)+2*(g>>1), pl=5*(g&1)+4, d1=min(r,4); lptr b=kt;
    q00=b+(16*pc+r)*8; q01=b+(16*pl+r)*8+4*(g>>1); q10=b+(160+5*pc+d1)*8; q11=b+(160+5*pl+d1)*8+4*(g>>1);
    const u32 fb=WS_O+((32*tq+8*(w&3)+4*(w>>2))*20+5*(oh>>3))*1024+r*16;
    om=fb+((oh&7)>>1)*1024+(2*(oh&1)+(g>>1))*256+8*(g&1); ox=(g==0)?(fb+4096+((oh&7)>>1)*256+8*(oh&1)):OOB; }
  asm volatile("":"+v"(q00),"+v"(q01),"+v"(q10),"+v"(q11),"+v"(om),"+v"(ox));
  ST(11);
  u32x4 v[16]; const u32 go=(lane<53&&53*w+lane<210)?(WS_P1+oh*16*3584+(53*w+lane)*16):OOB;
  u32 pq[2][10];
  { const u32 sg=(g&1)?0x80008000u:0u; f32x4 z[2][5];
    #pragma unroll
    for(int t=0;t<2;++t){ const u32x4 a=l0[t]^sg, b=l1[t]^sg; const u32x2 c=l2[t]^sg;
      z[t][0]=f32x4{bflo(a.x),bfhi(a.x),bflo(a.y),bfhi(a.y)}; z[t][1]=f32x4{bflo(a.z),bfhi(a.z),bflo(a.w),bfhi(a.w)};
      z[t][2]=f32x4{bflo(b.x),bfhi(b.x),bflo(b.y),bfhi(b.y)}; z[t][3]=f32x4{bflo(b.z),bfhi(b.z),bflo(b.w),bfhi(b.w)};
      z[t][4]=f32x4{bflo(c.x),bfhi(c.x),bflo(c.y),bfhi(c.y)}; }
    SB();
#define P1K 0
    softmax20x2(z,pq,[&](int k){ if(GATHER&&P1K==0&&k<16) v[k]=ld_sc1(rs,go+k*3584);
      if(GATHER&&P1K==1&&k>=12){ v[2*k-24]=ld_sc1(rs,go+(2*k-24)*3584); v[2*k-23]=ld_sc1(rs,go+(2*k-23)*3584); } });
    if(GATHER&&P1K==2){
      #pragma unroll
      for(int k=0;k<16;++k) v[k]=ld_sc1(rs,go+k*3584); }
    #pragma unroll
    for(int i=0;i<2;++i){
      #pragma unroll
      for(int k=0;k<5;++k){ const int a=(k<4)?k:8, b=(k<4)?k+4:9;
        auto s=__builtin_amdgcn_permlane32_swap(pq[i][a],pq[i][b],false,false); pq[i][a]=s[0]; pq[i][b]=s[1]; }
      #pragma unroll
      for(int k=0;k<10;++k) asm volatile("":"+v"(pq[i][k])); } }
  ST(12);
  if(GATHER){
    gather16(rs,go,v);
    ST(13);
    f32x4 s[4];
    #pragma unroll
    for(int i=0;i<4;++i) s[i]=(__builtin_bit_cast(f32x4,v[4*i])+__builtin_bit_cast(f32x4,v[4*i+1]))+(__builtin_bit_cast(f32x4,v[4*i+2])+__builtin_bit_cast(f32x4,v[4*i+3]));
    s[0]=(s[0]+s[1])+(s[2]+s[3]);
    if(lane<53) *(volatile LAS u32x2*)(kt+(53*w+lane)*8)=u32x2{pk_bf16(s[0][0],s[0][1]),pk_bf16(s[0][2],s[0][3])};
    *(volatile LAS u32*)(L+B_F+32+4*w)=1u;
  }
  u32x4 a0[2]; u32 a1[2];
  for(int it=0;it<SPIN_MAX;++it){
    const u32x4 f=*(volatile LAS u32x4*)(L+B_F+32);
    a0[0]=cat(*(volatile LAS u32x2*)q00,*(volatile LAS u32x2*)(q00+128)); a0[1]=cat(*(volatile LAS u32x2*)q10,*(volatile LAS u32x2*)(q10+40));
    a1[0]=*(volatile LAS u32*)q01; a1[1]=*(volatile LAS u32*)q11;
    if(__builtin_amdgcn_readfirstlane(f.x+f.y+f.z+f.w)==4u) break;
    if(!GATHER) __builtin_amdgcn_s_sleep(1); }
  ST(14);
  f32x4 n[4][2];
  #pragma unroll
  for(int i=1;i>=0;--i){
    #pragma unroll
    for(int t=0;t<4;++t){ const u32* f=pq[t>>1]+4*(t&1); n[t][i]=mfma16(a0[i],u32x4{f[0],f[1],f[2],f[3]},Z4); }
    #pragma unroll
    for(int t=0;t<4;++t) n[t][i]=mfma16(u32x4{a1[i],0u,0u,0u},u32x4{pq[t>>1][8+(t&1)],0u,0u,0u},n[t][i]); }
  SB();
  float den[4];
  #pragma unroll
  for(int t=0;t<4;++t) den[t]=__builtin_bit_cast(float,__builtin_amdgcn_ds_bpermute((r+16)*4,__builtin_bit_cast(int,n[t][1][0])));
  #pragma unroll
  for(int t=0;t<4;++t) den[t]=__builtin_amdgcn_rcpf(rbf(rbf(den[t])+1e-6f));
  SB();
  #pragma unroll
  for(int t=0;t<4;++t){ const f32x4 m=n[t][0]*den[t], e=n[t][1]*den[t];
    __builtin_amdgcn_raw_buffer_store_b64(u32x2{pk_bf16(m[0],m[1]),pk_bf16(m[2],m[3])},rs,om+t*20480,0,16);
    __builtin_amdgcn_raw_buffer_store_b64(u32x2{pk_bf16(e[0],e[1]),pk_bf16(e[2],e[3])},rs,ox+t*20480,0,16); }
}

template<class F> DI bool role_b(const bool ISQ,int X,int li,int rt,int grp,int w,const Stream& st,lptr L,F params,const u16* u,const u16* wo,u16* out,void* ws){
  const int tid=threadIdx.x, lane=tid&63, g=lane>>4;
  const int T=ISQ?16+li:li; const bool doout=T<20; const int tt64=4*X+T/5, ct=T%5;
  const rsrc_t rs=__builtin_amdgcn_make_buffer_rsrc(ws,0,WS_END,0x00020000);
  constexpr int OIM=B_PV;
  u64 E;
  auto count=[&](int word){ asm volatile("":::"memory"); u32 n=0; if(lane==0) n=__hip_atomic_fetch_add((LAS u32*)(L+B_F+4*word),1u,__ATOMIC_RELAXED,__HIP_MEMORY_SCOPE_WORKGROUP); return n; };
  auto until=[&](int word,u32 n){ for(int it=0;it<SPIN_MAX;++it){ if(__builtin_amdgcn_readfirstlane(*(volatile LAS u32*)(L+B_F+4*word))>=n) break; __builtin_amdgcn_s_sleep(2); } asm volatile("":::"memory"); };
  auto arrive=[&](){
    if(__builtin_amdgcn_readfirstlane(count(4))==3u&&lane==0){ flag_set(ws,ISQ?(128+4*rt+grp):(16*grp+rt),E); asm volatile("":::"memory"); *(volatile LAS u32*)(L+B_F+84)=4u; } };
  volatile LAS u32* bad=(volatile LAS u32*)(L+B_F+20);
  const int oh=4*grp+(rt&3), tq=rt>>2;
  const int ot=4*tt64+(w&3);
  int fi=(lane<16)?(16*grp+lane):((lane<32)?(16*(lane&7)+4*tq+(w&3)):((lane<48)?(16*(ot&7)+(ot>>3)):(128+4*(4*tq+(w&3))+(oh>>3))));
  if(w<4){
    const int th=w>>1, hh=w&1;
    if(lane<4) lds_st<u32x4>(L+B_Z+lane*16,u32x4{0,0,0,0});
    if(tid<9) lds_st<u32x4>(L+B_F+tid*16,u32x4{0,0,0,0});
    lptr fad[3][2];
    { const int q=(lane&15)>>2, p=lane&3, hd=ISQ?p:(p&1);
      #pragma unroll
      for(int s=0;s<3;++s)
        #pragma unroll
        for(int t=0;t<2;++t){ const int d=16*(2*s+t)+4*g-20*hd; const bool ok=(d>=0)&&(d<20)&&(2*s+t<5);
          fad[s][t]=ok?(L+B_WP+((ISQ?4*hh:2*hh)+hd)*800+(d+q)*40+(ISQ?0:(p>>1)*3200)):(L+B_Z); if(2*s+t<5) asm volatile("":"+v"(fad[s][t])); } }
    lptr pv=L+B_PV+w*B_PVS;
    lptr pp=pv+(lane&15)*168+(g&1)*80+(g>>1)*40, vp2, vp3, vp4, pa, vb;
    { lptr vp=pv+10752+(lane&15)*104; vp2=vp+((g<2)?(48*g+40):(8*g-16)); vp3=vp+((g==3)?48:(16+8*g)); vp4=vp+56+8*g;
      const int tk=16*(g&1)+4*((lane&15)>>2)+2*(g>>1); pa=pv+tk*168+8*(lane&3)+(th^1)*80; vb=pv+10752+tk*104+8*(lane&3)+(th^1)*48; }
    u32 po0, po1;
    { const u32 po=WS_P1+((4*grp+2*hh+th)*16+rt)*3584; po0=po+lane*16; po1=((lane&15)<5)?(po+(160+5*g+(lane&15))*16):OOB; }
    asm volatile("":"+v"(fi),"+v"(pp),"+v"(vp2),"+v"(vp3),"+v"(vp4),"+v"(pa),"+v"(vb),"+v"(po0),"+v"(po1));
    lptr ab[5], bb[2];
    { const int q=(lane&15)>>2,p=lane&3;
      #pragma unroll
      for(int i=0;i<5;++i){ int n=2*i+(p>>1); int cid=ISQ?(10*hh+n):(5*hh+n+(n>=5?5:0)); ab[i]=L+16384+(8*g+q)*320+((cid^((g&1)<<1))<<4)+8*(p&1); }
      const int b0=(64*th+(lane&15))*128+((g^((lane&15)>>1))<<4); bb[0]=L+b0; bb[1]=L+(b0^64); }
    f32x4 acc[5][4];
    #pragma unroll
    for(int i=0;i<5;++i)
      #pragma unroll
      for(int t=0;t<4;++t){ acc[i][t]=Z4; asm volatile("":"+v"(acc[i][t])); }
    ST(1);
    u32x4 FA[2][5], FB[2][4], fa[2][5];
    u32x2 lo2;
    auto half=[&](int hf,bool mm,bool last,int step){
      u32x2 lo;
      #pragma unroll
      for(int n=0;n<20;++n){ const int i=n>>2, t=n&3;
        if(mm) acc[i][t]=mfma16(FA[hf^1][i],FB[hf^1][t],acc[i][t]);
        SB();
        if(n==0) FB[hf][0]=lds_ld<u32x4>(bb[hf]);
        else if(n==1) lo=lds_tr(ab[0]+hf*10240);
        else if(n==2) FA[hf][0]=cat(lo,lds_tr(ab[0]+hf*10240+1280));
        else if(n<6) FB[hf][n-2]=lds_ld<u32x4>(bb[hf]+2048*(n-2));
        else if(n<14){ const int j=1+((n-6)>>1); if(!(n&1)) lo=lds_tr(ab[j]+hf*10240); else FA[hf][j]=cat(lo,lds_tr(ab[j]+hf*10240+1280)); }
        else if(hf==1&&!last){ if(n<19){ ab[n-14]+=step; asm volatile("":"+v"(ab[n-14])); } if(n>=18){ bb[n-18]+=step; asm volatile("":"+v"(bb[n-18])); } }
        if(last){ const int j=n>>2, s=(n>>1)&1; if(!(n&1)) lo2=lds_tr(fad[s][0]+8*j); else fa[s][j]=cat(lo2,lds_tr(fad[s][1]+8*j)); }
        SB(); } };
    bar();
    ST(2);
    __builtin_amdgcn_s_waitcnt(0xC07F);
    SB();
    half(0,false,false,0);
    #pragma unroll 1
    for(u32 s=1;s<10;++s){
      half(1,true,false,(s&3)?B_ST:-3*B_ST);
      rbar();
      SB();
      half(0,true,false,0); }
    const u32x2 ev=*(volatile LAS u32x2*)(L+B_F+88);
    half(1,true,true,0);
    E=(u64)(u32)__builtin_amdgcn_readfirstlane(ev.x)|((u64)(u32)__builtin_amdgcn_readfirstlane(ev.y)<<32);
    asm volatile("s_waitcnt lgkmcnt(0)":::"memory");
    waitv<0>();
    count(7);
    #pragma unroll
    for(int t=0;t<4;++t)
      #pragma unroll
      for(int i=0;i<5;++i) acc[i][t]=mfma16(FA[1][i],FB[1][t],acc[i][t]);
    u32x2 pk[5][4];
    #pragma unroll
    for(int t=0;t<4;++t)
      #pragma unroll
      for(int i=0;i<5;++i) pk[i][t]=u32x2{pk_bf16(acc[i][t][0],acc[i][t][1]),pk_bf16(acc[i][t][2],acc[i][t][3])};
    ST(3);
    if(ISQ){
      u32x4 f2[5];
      #pragma unroll
      for(int i=0;i<5;++i) f2[i]=cat(lds_tr(fad[2][0]+8*i),u32x2{0u,0u});
      lptr sp_=pv+g*2560+(lane&15)*40;
      f32x4 z[4][5];
      #pragma unroll
      for(int t=0;t<4;++t)
        #pragma unroll
        for(int i=0;i<5;++i) z[t][i]=mfma16(fa[0][i],cat(pk[0][t],pk[1][t]),Z4);
      #pragma unroll
      for(int t=0;t<4;++t)
        #pragma unroll
        for(int i=0;i<5;++i) z[t][i]=mfma16(fa[1][i],cat(pk[2][t],pk[3][t]),z[t][i]);
      SB();
      #pragma unroll
      for(int t=0;t<5;++t){
        #pragma unroll
        for(int i=0;i<5;++i){ if(t<4) z[t][i]=mfma16(f2[i],cat(pk[4][t],u32x2{0u,0u}),z[t][i]);
          if(t>0) lds_st<u32x2>(sp_+(t-1)*640+8*i,u32x2{pk_bf16(z[t-1][i][0],z[t-1][i][1]),pk_bf16(z[t-1][i][2],z[t-1][i][3])}); }
        SB(); }
      ST(10);
      #pragma unroll
      for(int i=0;i<10;++i){ const int c=i*64+lane, hd=(c*205)>>15;
        st_sc1(rs,WS_Q+((8*grp+4*hh+hd)*2048+128*rt+64*th)*40+(c-160*hd)*16,lds_ld<u32x4>(pv+c*16)); }
      ST(11);
      waitv<0>(); ST(12); arrive();
      ST(4);
    } else {
      { f32x4 z[4][5]; u32 ph[4][10];
        auto fm=[&](int t){
          const u32x4 b0=cat(pk[0][t],pk[1][t]), b1=cat(pk[2][t],pk[3][t]);
          #pragma unroll
          for(int i=0;i<5;++i) z[t][i]=mfma16(fa[0][i],b0,Z4);
          #pragma unroll
          for(int i=0;i<5;++i) z[t][i]=mfma16(fa[1][i],b1,z[t][i]); };
        fm(0); fm(1);
        SB();
        softmax20x2(z,ph,[&](int k){ const int t=2+k/10, i=k%5;
          z[t][i]=(k%10<5)?mfma16(fa[0][i],cat(pk[0][t],pk[1][t]),Z4):mfma16(fa[1][i],cat(pk[2][t],pk[3][t]),z[t][i]); });
        auto wp=[&](int t){
          #pragma unroll
          for(int i=0;i<5;++i) lds_st<u32x2>(pp+t*2688+8*i,u32x2{ph[t][2*i],ph[t][2*i+1]}); };
        u32x2 v2[4];
        #pragma unroll
        for(int t=0;t<4;++t) v2[t]=(g<2)?u32x2{0x3F80u,0u}:pk[2][t];
        softmax20x2(z+2,ph+2,[&](int k){
          if(k<10) *(volatile LAS u32x2*)(pp+(k/5)*2688+8*(k%5))=u32x2{ph[k/5][2*(k%5)],ph[k/5][2*(k%5)+1]};
          else if(k<14) *(volatile LAS u32x2*)(vp2+(k-10)*1664)=v2[k-10];
          else if(k<18) *(volatile LAS u32x2*)(vp3+(k-14)*1664)=pk[3][k-14];
          else { *(volatile LAS u32x2*)(vp4+(2*k-36)*1664)=pk[4][2*k-36]; *(volatile LAS u32x2*)(vp4+(2*k-35)*1664)=pk[4][2*k-35]; } });
        wp(2); wp(3); }
      ST(4);
      u64 fl;
      { f32x4 c[2][6];
        #pragma unroll
        for(int h2=0;h2<2;++h2){
          if(h2){ pa+=th?80:-80; vb+=th?48:-48; }
          u32x4 a[2][3], b[2][2];
          #pragma unroll
          for(int ks=0;ks<2;++ks){
            #pragma unroll
            for(int i=0;i<3;++i) a[ks][i]=cat(lds_tr(pa+ks*32*168+32*i),lds_tr(pa+(ks*32+1)*168+32*i));
            #pragma unroll
            for(int dt=0;dt<2;++dt) b[ks][dt]=cat(lds_tr(vb+ks*32*104+32*dt),lds_tr(vb+(ks*32+1)*104+32*dt)); }
          #pragma unroll
          for(int i=0;i<6;++i) c[h2][i]=mfma16(a[0][i%3],b[0][i/3],Z4);
          #pragma unroll
          for(int i=0;i<6;++i) c[h2][i]=mfma16(a[1][i%3],b[1][i/3],c[h2][i]); }
        #pragma unroll
        for(int i=0;i<6;++i) *(volatile LAS f32x4*)(pv+i*1024+lane*16)=c[0][i];
        *(volatile LAS u32*)(L+B_F+4*w)=1u;
        #pragma unroll
        for(int i=0;i<6;++i) asm volatile("":"+v"(c[1][i]));
        lptr ox_=L+B_PV+(w^2)*B_PVS; f32x4 pr[6];
        for(int it=0;it<SPIN_MAX;++it){
          const u32 f=*(volatile LAS u32*)(L+B_F+4*(w^2)), n=*(volatile LAS u32*)(L+B_F+16);
          #pragma unroll
          for(int i=0;i<6;++i) pr[i]=*(volatile LAS f32x4*)(ox_+i*1024+lane*16);
          if(__builtin_amdgcn_readfirstlane(f+n)==5u) break; }
        fl=flag_get(ws,fi);
        #pragma unroll
        for(int i=0;i<6;++i){ u32 o=(i<3)?(po0+i*1024):(po1+(i-3)*320); if(i%3==2) o=(g<2)?o:OOB;
          st_sc1(rs,o,__builtin_bit_cast(u32x4,c[1][i]+pr[i])); } }
      ST(5);
      o_phase<true>(L,rs,ws,E,w,oh,tq,fi,fl);
      count(6);
      if(rt==15&&grp==7&&w==3){
        for(int it=0;it<SPIN_MAX;++it){ if(__all(flag_get(ws,lane)==E&&flag_get(ws,64+lane)==E&&flag_get(ws,128+lane)==E)) break; __builtin_amdgcn_s_sleep(2); }
        if(lane==0) __hip_atomic_store((GAS u64*)ws,E,__ATOMIC_RELAXED,__HIP_MEMORY_SCOPE_AGENT); }
      ST(6);
    }
    if(!doout){
      until(21,4u);
      const char* up=(const char*)u+(size_t)tid*4*1280+2*(80*X+64+4*(li-4)); u32x4 um[4];
      #pragma unroll
      for(int r=0;r<4;++r) um[r]=cat(*(const u32x2*)(up+r*1280),*(const u32x2*)(up+(r+1024)*1280));
      u_scatter(false,tid,L,um);
      asm volatile("s_waitcnt lgkmcnt(0)":::"memory");
      __builtin_amdgcn_s_waitcnt(0x0F70); return false; }
    until(20,4u);
    if(ISQ){ fi=16*(ot&7)+(ot>>3); flag_wait(ws,E,fi,flag_get(ws,fi)); }
    if(ISQ){
      const int hi=224+16*(4*X+li); flag_wait(ws,E,hi,flag_get(ws,hi)); }
    { const u32 ob=WS_O+ot*20480;
      const u32 sm=ob+((lane<32)?((5*(lane>>3)+((lane>>1)&3))*1024+(lane&1)*512+508):((5*((lane-32)>>3)+4)*1024+((lane&7)>>1)*256+244+8*(lane&1)));
      for(int it=0;it<SPIN_MAX;++it){ asm volatile("":::"memory");
        if(__all(__builtin_amdgcn_raw_buffer_load_b32(rs,sm,0,16)!=SENT)) break;
        __builtin_amdgcn_s_sleep(1); }
      ST(15);
      const u32 vo=lane*16;
      for(u32 it=1;it<SPIN_MAX;++it){
        #pragma unroll
        for(int i=0;i<5;++i) dma16x4_sc1((const char*)ws+ob+i*4096,vo,vo,vo,vo,L+OIM+w*20480+i*4096);
        waitv<20-4*HB>();
        rbar();
        waitv<0>();
        ST(7);
        rbar();
        rbar();
        rbar();
        if(__builtin_amdgcn_readfirstlane(*bad)!=it) break; } }
    __builtin_amdgcn_s_waitcnt(0x0F70);
  } else {
    const int hw=w-4;
    asm volatile("s_load_dwordx2 %0, %1, 0x0":"=s"(E):"s"(ws):"memory");
    ST(9);
    waitv<9>(); rbar(); st.issue(2);
    ST(8);
    waitv<9>(); rbar();
    st.issue(3);
    asm volatile("s_waitcnt lgkmcnt(0)":"+s"(E)::"memory"); E+=1;
    if(tid==256) *(volatile LAS u32x2*)(L+B_F+88)=u32x2{(u32)E,(u32)(E>>32)};
    if(!ISQ&&lane<50){ lptr p=L+B_WP+hw*800+lane*16; lds_st<u32x4>(p+3200,lds_ld<u32x4>(p)^0x80008000u); }
    waitv<9>(); bar();
    if(!doout) params();
    st.issue(4);
    const int pr=hw>>1, me2=hw&1;
    u32 wq[40];
    { const int r=lane>>3; const u32 sw=((lane&7)^((((r>>1)&1)|(me2<<1))<<1))<<4, vo=r*1280+sw, vo2=((r>>2)*20+(r&3)+16+32*me2)*1280+sw;
      #pragma unroll
      for(int n=0;n<40;++n){ const int G=n/10, m=n%10, i=(n<22)?n:n-22, k=(i<16)?(i&3):((n>=18&&n<21)?n-18:((i<18)?i-16:0));
        wq[n]=((m<8)?vo+(160*G+20*m)*1280:vo2+(160*G+80*(m-8))*1280)-1024*k; asm volatile("":"+v"(wq[n])); } }
    lptr dst=L+hw*18432, rd, rd1; int x9, x10;
    { const int q=(lane&15)>>2, p=lane&3, f=((q>>1)|((g&1)<<1))<<1, ow=pr*2+(g&1);
      lptr rb=L+ow*18432+(g>>1)*1024+q*128+8*(p&1); rd=rb+(((4*me2+(p>>1))^f)<<4); rd1=rb+(((4*me2+2+(p>>1))^f)<<4);
      x9=B_WP+ow*3072-ow*18432; x10=(g>>1)?(B_PV+17408-1024):(x9+2048); }
    lptr ra9=rd+x9, rb9=rd1+x9, ra10=rd+x10, rb10=rd1+x10;
    asm volatile("":"+v"(rd),"+v"(rd1),"+v"(ra9),"+v"(rb9),"+v"(ra10),"+v"(rb10));
    waitv<9>(); rbar();
    #pragma unroll 1
    for(int s=4;s<9;++s){
      st.issue(s+1);
      waitv<9>(); rbar(); }
    waitv<0>(); rbar();
    if(!doout){ until(7,4u); return true; }
    if(!ISQ){
      const u32x4 sv={SENT,SENT,SENT,SENT};
      #pragma unroll
      for(int i=0;i<4;++i) st_sc1(rs,(i<3||lane<18)?(WS_P1+((4*grp+hw)*16+rt)*3584+i*1024+lane*16):OOB,sv);
      #pragma unroll
      for(int i=0;i<5;++i) st_sc1(rs,WS_O+((8*rt+grp)*20+5*hw+i)*1024+lane*16,sv);
      waitv<0>(); arrive(); }
    u32x4 wf[2][11];
    const char* gb=(const char*)wo+ct*256+pr*128+me2*8*1280;
    auto meet=[&](u32 n){ *(volatile LAS u32*)(L+B_F+48+4*hw)=n; until(12+(hw^1),n); };
    auto fetch18=[&](int n0){
      #pragma unroll
      for(int i=0;i<16;i+=4) dma16x4(gb,wq[n0+i],wq[n0+i+1],wq[n0+i+2],wq[n0+i+3],dst+i*1024);
      dma16xn<2>(gb,wq[n0+16],wq[n0+17],0,dst+16384); };
    until(ISQ?21:7,4u);
    ST(3);
    fetch18(0);
    dma16xn<3>(gb,wq[18],wq[19],wq[20],L+B_WP+hw*3072);
    dma16(gb,wq[21],dst+B_PV+17408);
    waitv<0>(); meet(1u);
    #pragma unroll
    for(int s=0;s<9;++s){ wf[0][s]=cat(lds_tr(rd+s*2048),lds_tr(rd+s*2048+512)); wf[1][s]=cat(lds_tr(rd1+s*2048),lds_tr(rd1+s*2048+512)); }
    wf[0][9]=cat(lds_tr(ra9),lds_tr(ra9+512)); wf[1][9]=cat(lds_tr(rb9),lds_tr(rb9+512));
    wf[0][10]=cat(lds_tr(ra10),lds_tr(ra10+512)); wf[1][10]=cat(lds_tr(rb10),lds_tr(rb10+512));
    asm volatile("s_waitcnt lgkmcnt(0)":::"memory");
    meet(2u);
    ST(10);
#define R2A 8
    #pragma unroll
    for(int i=0;i<R2A;i+=4) dma16x4(gb,wq[22+i],wq[23+i],wq[24+i],wq[25+i],dst+i*1024);
    if(!ISQ){ o_phase<false>(L,rs,ws,E,w,oh,tq,0,0); count(6); ST(6); until(6,8u);
      if(li<4&&tid==256) flag_set(ws,224+16*(4*X+li),E); }
    #pragma unroll
    for(int i=R2A;i<16;i+=4) dma16x4(gb,wq[22+i],wq[23+i],wq[24+i],wq[25+i],dst+i*1024);
    dma16xn<2>(gb,wq[38],wq[39],0,dst+16384);
    count(20);
    ST(4);
    lptr obt[4];
    #pragma unroll
    for(int t=0;t<4;++t){ obt[t]=L+OIM+lane*16+((t+hw)&3)*20480; asm volatile("":"+v"(obt[t])); }
    waitv<0>();
    for(u32 it=1;it<SPIN_MAX;++it){
      rbar();
      ST(5);
      f32x4 c[2][4];
      #pragma unroll
      for(int t=0;t<4;++t){ c[0][t]=Z4; c[1][t]=Z4; }
      u32x4 ob[3][4], wl[3][2]; u32 chk=0;
      auto fetch=[&](int ks){
        #pragma unroll
        for(int t=0;t<4;++t) ob[ks%3][t]=lds_ld<u32x4>(obt[t]+ks*1024);
        if(ks>=11){ wl[ks%3][0]=cat(lds_tr(rd+(ks-11)*2048),lds_tr(rd+(ks-11)*2048+512)); wl[ks%3][1]=cat(lds_tr(rd1+(ks-11)*2048),lds_tr(rd1+(ks-11)*2048+512)); } };
      fetch(0); fetch(1);
      SB();
      #pragma unroll
      for(int ks=0;ks<20;++ks){ u32x2 lo;
        if(ks==4*HB-2){ rbar(); SB(); }
        #pragma unroll
        for(int n=0;n<8;++n){ const int t=n>>1, cc=n&1, k2=ks+2;
          c[cc][t]=mfma16((ks<11)?wf[cc][ks<11?ks:0]:wl[ks%3][cc],ob[ks%3][t],c[cc][t]);
          SB();
          if(k2<20){
            if(n<4) ob[k2%3][n]=lds_ld<u32x4>(obt[n]+k2*1024);
            else if(k2>=11){ lptr p=((n<6)?rd:rd1)+(k2-11)*2048; if(!(n&1)) lo=lds_tr(p); else wl[k2%3][(n-4)>>1]=cat(lo,lds_tr(p+512)); } }
          if(n==7) chk=max(chk,max(ob[ks%3][0].x,ob[ks%3][0].z));
          SB(); } }
      if(__any(chk==SENT)) *bad=it;
      bar();
      #pragma unroll
      for(int t=0;t<4;++t)
        #pragma unroll
        for(int cc=0;cc<2;++cc) lds_st<u32x2>(L+OIM+(16*((t+hw)&3)+(lane&15))*272+hw*64+cc*32+8*g,u32x2{pk_bf16(c[cc][t][0],c[cc][t][1]),pk_bf16(c[cc][t][2],c[cc][t][3])});
      ST(7);
      bar();
      if(__builtin_amdgcn_readfirstlane(*bad)!=it) break; }
  }
  #pragma unroll
  for(int i=0;i<2;++i){ const int r=32*i+(tid>>4);
    __builtin_amdgcn_raw_buffer_store_b128(lds_ld<u32x4>(L+OIM+r*272+(tid&15)*16),__builtin_amdgcn_make_buffer_rsrc(out,0,2048*2560,0x00020000),(64*tt64+r)*2560+1280+ct*256+(tid&15)*16,0,16); }
  if(w<4) ST(8);
  return false;
}

constexpr int CODE_LINES=318;
__global__ __launch_bounds__(512,2) void fused(const u16* u,const u16* are,const u16* aim,const u16* bre,const u16* bim,const u16* cre,const u16* cim,
    const u16* logdt,const u16* dskip,const u16* x,const u16* wqkv,const u16* wphi,const u16* wo,u16* out,void* ws){
  extern __shared__ __attribute__((aligned(16))) char smem[];
  lptr L=(lptr)smem;
  const int b=blockIdx.x, X=b&7, idx=b>>3;
  u64 pc; asm volatile("s_getpc_b64 %0":"=s"(pc));
  const int w=__builtin_amdgcn_readfirstlane(threadIdx.x>>6);
  const bool isa=(idx>>3)==2;
  const int q=idx>=24, li=idx-24*q, rt=4*(X>>1)+(li>>(2-q)), grp=((X&1)<<(2-q))+(li&(3>>q));
  if(idx<16&&w<2){
    const u32 n=(w?(X*16+idx)*50:idx*32)+(threadIdx.x&63); u32 keep;
    if(w?((threadIdx.x&63)<50):(threadIdx.x<32&&n<CODE_LINES))
      asm volatile("s_mov_b32 %0, m0\n\ts_mov_b32 m0, %3\n\ts_nop 0\n\tglobal_load_lds_dword %1, %2\n\ts_mov_b32 m0, %0":"=&s"(keep):"v"(n*128),"s"(w?(u64)wo:(pc&~255ull)),"s"(162560+w*256):"memory"); }
  Stream st;
  if(__builtin_expect(!isa&&w>=4,1)){ st.init(q,li,rt,grp,w&3,threadIdx.x&63,x,wqkv,L); st.issue(0,false);
    { const u32 nb=800<<q, o=(threadIdx.x&63)*16; const char* wp=(const char*)wphi+(4*grp+(w&3))*nb;
      if(o<nb) dma16(wp,o,L+B_WP+(w&3)*nb);
      if(o+1024<nb) dma16(wp,o+1024,L+B_WP+(w&3)*nb+1024); }
    st.issue(1); }
  SB();
  STINIT();
  const int ch0=isa?(80*X+8*(idx-16)):(80*X+64+4*(idx-28)), wi=isa?w:w-4;
  SsmP sp; u32 dsk;
  auto params=[&](){ const SsmIn in={are,aim,bre,bim,cre,cim,logdt}; sp=load_ssm(in,ch0+wi,threadIdx.x&63); dsk=dskip[ch0+wi]; };
  bool job=true;
  if(__builtin_expect(!isa,1)) job=role_b(q,X,li,rt,grp,w,st,L,params,u,wo,out,ws);
  else params();
  if(job){
    conv_job(isa,wi,ch0,idx&7,L,sp,dsk,u,out,ws); }
  STDUMP();
}

extern "C" void kernel_launch(void* const* d_in, const int* in_sizes, int n_in, void* d_out, int out_size, void* d_ws, size_t ws_size, hipStream_t stream){
  (void)hipFuncSetAttribute((const void*)fused,hipFuncAttributeMaxDynamicSharedMemorySize,163840);
  fused<<<256,512,163840,stream>>>((const u16*)d_in[0],(const u16*)d_in[1],(const u16*)d_in[2],(const u16*)d_in[3],(const u16*)d_in[4],(const u16*)d_in[5],(const u16*)d_in[6],
    (const u16*)d_in[7],(const u16*)d_in[8],(const u16*)d_in[9],(const u16*)d_in[10],(const u16*)d_in[11],(const u16*)d_in[12],(u16*)d_out,d_ws);
}
```

```cpp
#define MULT 0
#define QSEL (true)
#define QCOL 0
#define STAMP
#include <hip/hip_runtime.h>
#include <stdint.h>

typedef unsigned short u16; typedef unsigned u32; typedef unsigned long long u64;
typedef __attribute__((ext_vector_type(4))) short s16x4;
typedef __attribute__((ext_vector_type(8))) _Float16 f16x8;
typedef __attribute__((ext_vector_type(8))) __bf16 bf16x8;
typedef __attribute__((ext_vector_type(2))) __bf16 bf16x2;
typedef __attribute__((ext_vector_type(2))) _Float16 f16x2;
typedef __attribute__((ext_vector_type(2))) float f32x2;
typedef __attribute__((ext_vector_type(4))) float f32x4;
typedef __attribute__((ext_vector_type(16))) float f32x16;
typedef __attribute__((ext_vector_type(2))) unsigned u32x2;
typedef __attribute__((ext_vector_type(3))) unsigned u32x3;
typedef __attribute__((ext_vector_type(4))) unsigned u32x4;
#define LAS __attribute__((address_space(3)))
#define GAS __attribute__((address_space(1)))
#define DI __device__ __forceinline__
typedef LAS char* lptr;
typedef __amdgpu_buffer_rsrc_t rsrc_t;

#define ST(i) do{ if((threadIdx.x&255)==0){ *(LAS u64*)(L+163328+(threadIdx.x>>8)*128+(i)*8)=__builtin_amdgcn_s_memrealtime(); } }while(0)
#define STDUMP() do{ if((threadIdx.x&255)<16){ asm volatile("s_waitcnt lgkmcnt(0)":::"memory"); __hip_atomic_store((GAS u64*)((char*)ws+(200u<<20))+(blockIdx.x*2+(threadIdx.x>>8))*16+(threadIdx.x&15),*(volatile LAS u64*)(L+163328+(threadIdx.x>>8)*128+(threadIdx.x&15)*8),__ATOMIC_RELAXED,__HIP_MEMORY_SCOPE_AGENT); } }while(0)
#define STINIT() do{ if((threadIdx.x&255)<16){ *(LAS u64*)(L+163328+(threadIdx.x>>8)*128+(threadIdx.x&15)*8)=0; } }while(0)

DI u32 pk_bf16(float a, float b){ f32x2 v={a,b}; return __builtin_bit_cast(u32,__builtin_convertvector(v,bf16x2)); }
DI u32 pk_f16(f32x2 v){ return __builtin_bit_cast(u32,__builtin_convertvector(v,f16x2)); }
DI float bflo(u32 x){ return __builtin_bit_cast(float,x<<16); }
DI float bfhi(u32 x){ return __builtin_bit_cast(float,x&0xffff0000u); }
DI float rbf(float a){ return bflo(pk_bf16(a,a)); }
template<class T> DI T lds_ld(lptr p){ return *(LAS T*)p; }
template<class T> DI void lds_st(lptr p, T v){ *(LAS T*)p=v; }
DI u32x2 lds_ld64(lptr p){ return *(volatile LAS u32x2*)p; }
DI u32x2 lds_tr(lptr p){ return __builtin_bit_cast(u32x2,__builtin_amdgcn_ds_read_tr16_b64_v4i16((LAS s16x4*)p)); }
DI f32x4 mfma16(u32x4 a,u32x4 b,f32x4 c){ return __builtin_amdgcn_mfma_f32_16x16x32_bf16(__builtin_bit_cast(bf16x8,a),__builtin_bit_cast(bf16x8,b),c,0,0,0); }
DI u32x4 cat(u32x2 a,u32x2 b){ u32x4 r={a.x,a.y,b.x,b.y}; return r; }
DI u32 p_ll(u32 a,u32 b){ return __builtin_amdgcn_perm(b,a,0x05040100u); }
DI u32 p_lh(u32 a,u32 b){ return __builtin_amdgcn_perm(b,a,0x07060100u); }
DI u32 p_hl(u32 a,u32 b){ return __builtin_amdgcn_perm(b,a,0x05040302u); }
DI u32 p_hh(u32 a,u32 b){ return __builtin_amdgcn_perm(b,a,0x07060302u); }
#define SB() __builtin_amdgcn_sched_barrier(0)
DI void rbar(){ asm volatile("":::"memory"); __builtin_amdgcn_s_barrier(); asm volatile("":::"memory"); }
DI void bar(){ asm volatile("s_waitcnt lgkmcnt(0)":::"memory"); __builtin_amdgcn_s_barrier(); asm volatile("":::"memory"); }
template<int N> DI void waitv(){ asm volatile("s_waitcnt vmcnt(%0)"::"i"(N):"memory"); }
constexpr f32x4 Z4={0.f,0.f,0.f,0.f};
DI float wave_max(float m){
  m=fmaxf(m,__builtin_bit_cast(float,__builtin_amdgcn_update_dpp(0,__builtin_bit_cast(int,m),0xB1,0xf,0xf,true)));
  m=fmaxf(m,__builtin_bit_cast(float,__builtin_amdgcn_update_dpp(0,__builtin_bit_cast(int,m),0x4E,0xf,0xf,true)));
  m=fmaxf(m,__builtin_bit_cast(float,__builtin_amdgcn_update_dpp(0,__builtin_bit_cast(int,m),0x141,0xf,0xf,true)));
  m=fmaxf(m,__builtin_bit_cast(float,__builtin_amdgcn_update_dpp(0,__builtin_bit_cast(int,m),0x140,0xf,0xf,true)));
  int x=__builtin_bit_cast(int,m);
  float a=__builtin_bit_cast(float,__builtin_amdgcn_readlane(x,0)), b=__builtin_bit_cast(float,__builtin_amdgcn_readlane(x,16));
  float c=__builtin_bit_cast(float,__builtin_amdgcn_readlane(x,32)), d=__builtin_bit_cast(float,__builtin_amdgcn_readlane(x,48));
  return fmaxf(fmaxf(a,b),fmaxf(c,d)); }

constexpr u32 WS_FLAG=256, WS_P1=32768, WS_Q=WS_P1+32*16*3584, WS_O=WS_Q+32*2048*40, WS_END=WS_O+128*20*1024;
DI int KF(int g,int r){ return 16*(16*g+r); }
DI int QF(int i){ return 16*(128+i); }
DI int HF(int i){ return 16*(192+i); }
constexpr u32 SENT=0xFFFFFFFFu, OOB=0x80000000u;
constexpr int SPIN_MAX=1<<17;
DI void dma16(const void* sb, u32 voff, lptr l){ u32 keep;
  asm volatile("s_mov_b32 %0, m0\n\ts_mov_b32 m0, %3\n\ts_nop 0\n\tglobal_load_lds_dwordx4 %1, %2\n\ts_mov_b32 m0, %0":"=&s"(keep):"v"(voff),"s"(sb),"s"((u32)(uintptr_t)l):"memory"); }
#define DMA4_BODY(SC) asm volatile("s_mov_b32 %0, m0\n\ts_mov_b32 m0, %6\n\ts_nop 0\n\tglobal_load_lds_dwordx4 %1, %5" SC "\n\tglobal_load_lds_dwordx4 %2, %5 offset:1024" SC \
    "\n\tglobal_load_lds_dwordx4 %3, %5 offset:2048" SC "\n\tglobal_load_lds_dwordx4 %4, %5 offset:3072" SC "\n\ts_mov_b32 m0, %0" \
    :"=&s"(keep):"v"(v0),"v"(v1),"v"(v2),"v"(v3),"s"(sb),"s"((u32)(uintptr_t)l):"memory")
DI void dma16x4(const void* sb,u32 v0,u32 v1,u32 v2,u32 v3,lptr l){ u32 keep; DMA4_BODY(""); }
DI void dma16x4_sc1(const void* sb,u32 v0,u32 v1,u32 v2,u32 v3,lptr l){ u32 keep; DMA4_BODY(" sc1"); }
template<int N> DI void dma16xn(const void* sb,u32 v0,u32 v1,u32 v2,lptr l){ u32 keep;
  if(N==2) asm volatile("s_mov_b32 %0, m0\n\ts_mov_b32 m0, %4\n\ts_nop 0\n\tglobal_load_lds_dwordx4 %1, %3\n\tglobal_load_lds_dwordx4 %2, %3 offset:1024\n\ts_mov_b32 m0, %0"
    :"=&s"(keep):"v"(v0),"v"(v1),"s"(sb),"s"((u32)(uintptr_t)l):"memory");
  else asm volatile("s_mov_b32 %0, m0\n\ts_mov_b32 m0, %5\n\ts_nop 0\n\tglobal_load_lds_dwordx4 %1, %4\n\tglobal_load_lds_dwordx4 %2, %4 offset:1024\n\tglobal_load_lds_dwordx4 %3, %4 offset:2048\n\ts_mov_b32 m0, %0"
    :"=&s"(keep):"v"(v0),"v"(v1),"v"(v2),"s"(sb),"s"((u32)(uintptr_t)l):"memory"); }
DI u32x4 ld_sc1(rsrc_t rs,u32 off){ return __builtin_amdgcn_raw_buffer_load_b128(rs,off,0,16); }
DI void st_sc1(rsrc_t rs,u32 off,u32x4 v){ __builtin_amdgcn_raw_buffer_store_b128(v,rs,off,0,16); }
DI void gather16(rsrc_t rs,u32 off,u32x4* v){
  for(int it=0;it<SPIN_MAX;++it){
    u32 todo=0;
    #pragma unroll
    for(int k=0;k<4;++k){ u32 m=0;
      #pragma unroll
      for(int i=0;i<4;++i) m=max(max(m,max(v[4*k+i].x,v[4*k+i].y)),max(v[4*k+i].z,v[4*k+i].w));
      if(__any(m==SENT)) todo|=1u<<k; }
    if(!todo) break;
    asm volatile("":::"memory");
    #pragma unroll
    for(int k=0;k<4;++k) if(todo>>k&1){
      #pragma unroll
      for(int i=0;i<4;++i) v[4*k+i]=ld_sc1(rs,off+(4*k+i)*3584); } } }
DI void flag_set(void* ws,int i,u64 E){ __hip_atomic_store((GAS u64*)((char*)ws+WS_FLAG)+i,E,__ATOMIC_RELAXED,__HIP_MEMORY_SCOPE_AGENT); }
DI u64 flag_get(const void* ws,int i){ return __hip_atomic_load((const GAS u64*)((const char*)ws+WS_FLAG)+i,__ATOMIC_RELAXED,__HIP_MEMORY_SCOPE_AGENT); }
DI void flag_wait(const void* ws,u64 E,int idx,u64 f){
  for(int it=0;it<SPIN_MAX;++it){ if(__all(f==E)) break; __builtin_amdgcn_s_sleep(2); f=flag_get(ws,idx); } }

constexpr int A_UB=4576, A_US=9120, A_KB=4224, A_KS=8704;

DI f32x2 cmul(f32x2 a, f32x2 b){ f32x2 sw={-b.y,b.x}; return __builtin_elementwise_fma(a.yy,sw,a.xx*b); }
DI f32x2 cexp_e(float re2, float rev, float e){
  float m=__builtin_amdgcn_exp2f(re2*e); float a=__builtin_amdgcn_fractf(rev*e);
  f32x2 r={m*__builtin_amdgcn_cosf(a), m*__builtin_amdgcn_sinf(a)}; return r; }
struct SsmP { u32 are,aim,bre,bim,cre,cim,ld; float dt; };
struct SsmIn { const u16 *are,*aim,*bre,*bim,*cre,*cim,*logdt; };
DI SsmP load_ssm(const SsmIn& in, int ch, int lane){
  SsmP p; int o=ch*64+lane;
  p.are=((const u32*)in.are)[o]; p.aim=((const u32*)in.aim)[o]; p.bre=((const u32*)in.bre)[o];
  p.bim=((const u32*)in.bim)[o]; p.cre=((const u32*)in.cre)[o]; p.cim=((const u32*)in.cim)[o];
  p.ld=in.logdt[ch];
  return p; }
DI f32x2 ssm_w(const SsmP& p, int half){
  f32x2 b={half?bfhi(p.bre):bflo(p.bre), half?bfhi(p.bim):bflo(p.bim)};
  f32x2 c={half?bfhi(p.cre):bflo(p.cre), half?bfhi(p.cim):bflo(p.cim)};
  return cmul(c,b*p.dt); }
DI void ssm_prep(SsmP& p, float& s, float& inv){
  asm volatile("":"+v"(p.ld));
  p.dt=rbf(expf(bflo(p.ld)));
  f32x2 w0=ssm_w(p,0), w1=ssm_w(p,1);
  float m=wave_max(fmaxf(fmaxf(fabsf(w0.x),fabsf(w0.y)),fmaxf(fabsf(w1.x),fabsf(w1.y))));
  int e=__builtin_amdgcn_frexp_expf(m); e=max(-100,min(100,e));
  s=__builtin_amdgcn_ldexpf(1.0f,-e); inv=__builtin_amdgcn_ldexpf(1.0f,e); }
DI void kgen_half(const SsmP& p, int half, float s, lptr tab, int lane, f32x16& acc){
  const float are=(half?bfhi(p.are):bflo(p.are))*p.dt, aim=(half?bfhi(p.aim):bflo(p.aim))*p.dt;
  const float re2=are*1.4426950408889634f, rev=aim*0.15915494309189535f;
  lptr wr=tab+lane*4; lptr rd=tab+(lane&31)*272+(lane>>5)*16;
  { f32x2 e8=cexp_e(re2,rev,8.f), e16=cmul(e8,e8);
    f32x2 a={1.f,0.f}, b=e8;
    #pragma unroll
    for(int j=0;j<32;j+=2){ lds_st<u32>(wr+j*272,pk_f16(a)); lds_st<u32>(wr+(j+1)*272,pk_f16(b)); if(j<30){ a=(j==0)?e16:cmul(a,e16); b=cmul(b,e16);} } }
  u32x4 bf[8];
  #pragma unroll
  for(int k=0;k<8;++k) bf[k]=lds_ld<u32x4>(rd+k*32);
  { f32x2 w=ssm_w(p,half)*s; w.y=-w.y;
    f32x2 e1=cexp_e(re2,-rev,1.f), g=cexp_e(re2,-rev,256.f);
    f32x2 g2=cmul(g,g), g3=cmul(g2,g), g4=cmul(g2,g2);
    f32x2 x[8]; x[0]=w;
    #pragma unroll
    for(int i=1;i<8;++i) x[i]=cmul(x[i-1],e1);
    #pragma unroll
    for(int i=0;i<8;++i) lds_st<u32>(wr+i*272,pk_f16(x[i]));
    #pragma unroll
    for(int i=0;i<4;++i) lds_st<u32>(wr+(8+i)*272,pk_f16(cmul(x[2*i],g)));
    lds_st<u32>(wr+12*272,pk_f16(cmul(x[0],g2))); lds_st<u32>(wr+13*272,pk_f16(cmul(x[4],g2)));
    lds_st<u32>(wr+14*272,pk_f16(cmul(x[0],g3))); lds_st<u32>(wr+15*272,pk_f16(cmul(x[4],g3)));
    f32x2 y=cmul(w,g4);
    #pragma unroll
    for(int i=0;i<5;++i){ lds_st<u32>(wr+(16+i)*272,pk_f16(y)); if(i<4) y=cmul(y,g); } }
  #pragma unroll
  for(int k=0;k<8;++k){ u32x4 af=lds_ld<u32x4>(rd+k*32);
    acc=__builtin_amdgcn_mfma_f32_32x32x16_f16(__builtin_bit_cast(f16x8,af),__builtin_bit_cast(f16x8,bf[k]),acc,0,0,0); }
}
DI void k_finish(const float* v, lptr ka, int lane){
  lptr ks=ka; lptr kb=ka+A_KB;
  { const int j=lane&31, h=lane>>5;
    lds_st<u32x2>(ks+(8*j+4*h)*2,u32x2{pk_bf16(v[0],v[1]),pk_bf16(v[2],v[3])});
    u32x2 b={pk_bf16(v[4],v[5]),pk_bf16(v[6],v[7])};
    u32 c0=pk_bf16(v[8],v[9]), c1=pk_bf16(v[10],v[11]);
    if(h==0){ lds_st<u32x2>(ks+(256+4*j)*2,b);
      lds_st<u16>(ks+(512+j)*2,(u16)c0); lds_st<u16>(ks+(544+j)*2,(u16)(c0>>16));
      lds_st<u16>(ks+(576+j)*2,(u16)c1); lds_st<u16>(ks+(608+j)*2,(u16)(c1>>16));
    } else { lds_st<u32>(ks+(384+2*j)*2,b.x); lds_st<u32>(ks+(448+2*j)*2,b.y); if(j==0) lds_st<u16>(ks+640*2,(u16)c0); } }
  const int M=lane; const bool lo=M<32;
  u32x4 v0=lds_ld<u32x4>(ks+(M&31)*16); u32x2 v1=lds_ld<u32x2>(ks+(128+4*(M|32))*2);
  u32 v4=lds_ld<u16>(ks+(132+4*(M|32))*2);
  u32 pv=lds_ld<u16>(ks+(lo?max(8*M-1,0):((M==32)?255:(128+4*M)))*2); if(M==0) pv=0;
  u32 ab=lds_ld<u32>(ks+(384+2*M)*2), c2=lds_ld<u16>(ks+(386+2*M)*2);
  u32 a3[2],b3[2];
  #pragma unroll
  for(int i=0;i<2;++i){ a3[i]=lds_ld<u16>(ks+(512+M+64*i)*2); b3[i]=lds_ld<u16>(ks+(513+M+64*i)*2); }
  asm volatile("":"+v"(v0),"+v"(v1),"+v"(v4),"+v"(pv),"+v"(ab),"+v"(c2),"+v"(a3[0]),"+v"(a3[1]),"+v"(b3[0]),"+v"(b3[1])::"memory");
  { u32x4 A,B;
    if(lo){ B.x=p_hl(v0.w,v0.w); B.y=p_hl(v0.z,v0.z); B.z=p_hl(v0.y,v0.y); B.w=p_hl(v0.x,v0.x);
      A.x=p_lh(v0.w,v0.z); A.y=p_lh(v0.z,v0.y); A.z=p_lh(v0.y,v0.x); A.w=p_ll(v0.x,pv);
    } else { B.x=p_lh(v4,v1.y); B.y=p_ll(v1.y,v1.y); B.z=p_lh(v1.y,v1.x); B.w=p_ll(v1.x,v1.x);
      A.x=p_hl(v1.y,v1.y); A.y=B.y; A.z=p_hl(v1.x,v1.x); A.w=p_ll(v1.x,pv); }
    lds_st<u32x4>(ka+(255-M)*16,A); lds_st<u32x4>(kb+(255-M)*16,B); }
  { u32x4 A,B; B.x=p_ll(c2,c2); B.y=p_hh(ab,ab); B.z=p_hl(ab,ab); B.w=p_ll(ab,ab);
    A.x=p_lh(c2,ab); A.y=B.y; A.z=B.w; A.w=B.w;
    lds_st<u32x4>(ka+(191-M)*16,A); lds_st<u32x4>(kb+(191-M)*16,B); }
  #pragma unroll
  for(int i=0;i<2;++i){ u32 a=a3[i], b=b3[i], c=(M&1)?b:a; u32x4 A,B;
    B.x=p_ll(b,b); B.y=p_ll(b,c); B.z=p_ll(a,a); B.w=B.z;
    A.x=B.x; A.y=p_ll(c,a); A.z=B.z; A.w=B.z;
    lds_st<u32x4>(ka+(127-64*i-M)*16,A); lds_st<u32x4>(kb+(127-64*i-M)*16,B); }
  if(lane<8) lds_st<u32x4>(((lane&4)?kb:ka)+(256+(lane&3))*16,u32x4{0,0,0,0});
}
DI void conv_steps(lptr ua, lptr ka, int lane, f32x4* acc){
  constexpr int NS=8, s0=0, kbo=A_KB;
  const int a=lane&15, g=lane>>4;
  lptr up=ua+((a&1)?0:A_UB)+(32*(a>>1)+8*g+32*s0)*2;
  lptr kp=ka+((a&1)?kbo:0)+(28-4*(a>>1)+8*g+32*s0)*2;
  u32x4 A[2][8],B[2][8];
  auto ldA=[&](int s,int q){ A[s&1][q]=cat(lds_ld64(kp+(1792-256*q+32*s)*2),lds_ld64(kp+(1792-256*q+32*s)*2+8)); };
  auto ldB=[&](int s,int q){ B[s&1][q]=lds_ld<u32x4>(up+(256*q+32*s)*2); };
  #pragma unroll
  for(int q=0;q<8;++q){ ldB(0,q); ldA(0,q); }
  #pragma unroll
  for(int c=0;c<8;++c) acc[c]=Z4;
  SB();
  #pragma unroll
  for(int s=0;s<NS;++s){ int n=0; u32x2 lo;
    #pragma unroll
    for(int q=0;q<8;++q){
      #pragma unroll
      for(int c=q;c<8;++c){
        acc[c]=mfma16(A[s&1][q],B[s&1][c-q],acc[c]);
        SB();
        if(s+1<NS){
          if(n==0) ldB(s+1,0);
          else if(n<3){ if(n==1) lo=lds_ld64(kp+(1792+32*(s+1))*2); else A[(s+1)&1][0]=cat(lo,lds_ld64(kp+(1792+32*(s+1))*2+8)); }
          else if(n<10) ldB(s+1,n-2);
          else if(n<24){ const int j=1+((n-10)>>1); if(!(n&1)) lo=lds_ld64(kp+(1792-256*j+32*(s+1))*2); else A[(s+1)&1][j]=cat(lo,lds_ld64(kp+(1792-256*j+32*(s+1))*2+8)); } }
        SB();
        ++n; } } }
}
DI u32 fin2(float y0,float y1,u32 u,float dsk){
  u32 y=pk_bf16(y0,y1), s=pk_bf16(bflo(u)*dsk,bfhi(u)*dsk);
  return pk_bf16(bflo(y)+bflo(s),bfhi(y)+bfhi(s)); }
DI void u_scatter(const bool BIG, int ur, lptr L, const u32x4* um){
  #pragma unroll
  for(int c=0;c<4;++c){
    lptr d=L+(224+4*ur)*2+(BIG?2*c*A_US:(2*(c&1)*A_US+(c>>1)*2048)); const bool last=BIG?(ur==511):(c>=2&&ur==255);
    const u32x2 e={p_ll(um[0][c],um[1][c]),p_ll(um[2][c],um[3][c])}, o={p_hh(um[0][c],um[1][c]),p_hh(um[2][c],um[3][c])};
    lds_st<u32x2>(d,e); lds_st<u32x2>(d+A_US,o);
    lds_st<u32>(d+A_UB+4,e.x); lds_st<u32>(d+A_US+A_UB+4,o.x);
    if(!last){ lds_st<u32>(d+A_UB+8,e.y); lds_st<u32>(d+A_US+A_UB+8,o.y); } } }
DI void conv_job(const bool BIG, int wi, int ch0, int rot, lptr L, SsmP& p0, u32 dsk, const u16* u, u16* out, void* ws){
  const int lane=threadIdx.x&63;
  lptr us=L+wi*A_US, kw=L+(BIG?8:4)*A_US+wi*A_KS;
  if(lane<56) lds_st<u32x4>(us+(lane<28?lane*16:A_UB+(lane-28)*16),u32x4{0,0,0,0});
  if(lane==56) lds_st<u32>(us+A_UB+448,0u);
  u32x4 um[4];
  const int ur=(64*(wi+rot)+lane)&511;
  ST(1);
  { float s,inv; ssm_prep(p0,s,inv); f32x16 acc={};
    #pragma unroll 1
    for(int h=0;h<2;++h){
      if(BIG&&h==1){ const char* up=(const char*)u+(size_t)ur*4*1280+2*ch0;
        #pragma unroll
        for(int r=0;r<4;++r) um[r]=*(const u32x4*)(up+r*1280); }
      kgen_half(p0,h,s,kw,lane,acc); }
    float v[12];
    #pragma unroll
    for(int r=0;r<12;++r) v[r]=acc[r]*inv;
    k_finish(v,kw,lane); }
  ST(2);
  if(BIG) u_scatter(true,ur,L,um);
  bar();
  ST(3);
  f32x4 acc[8];
  conv_steps(us,kw,lane,acc);
  ST(4);
  { const int tl=32*((lane&15)>>1)+8*(lane>>4)+2*(lane&1); asm volatile("":"+v"(dsk)); const float d=bflo(dsk);
    u32 uu[16];
    #pragma unroll
    for(int c=0;c<8;++c){ uu[2*c]=lds_ld<u32>(us+(224+256*c+tl)*2); uu[2*c+1]=lds_ld<u32>(us+(228+256*c+tl)*2); }
    #pragma unroll
    for(int c=0;c<8;++c){ lds_st<u32>(kw+(256*c+tl)*2,fin2(acc[c][0],acc[c][1],uu[2*c],d)); lds_st<u32>(kw+(256*c+tl+4)*2,fin2(acc[c][2],acc[c][3],uu[2*c+1],d)); } }
  bar();
  ST(5);
  { lptr yp=L+(BIG?8:4)*A_US+((lane&15)>>2)*A_KS+(256*wi+16*(lane>>4)+4*(lane&3))*2;
    char* op=(char*)out+(size_t)(256*wi+lane)*2560+2*ch0;
    #pragma unroll
    for(int i=0;i<4;++i){
      if(BIG) *(u32x4*)(op+(size_t)i*64*2560)=cat(lds_tr(yp+i*128),lds_tr(yp+4*A_KS+i*128));
      else { *(u32x2*)(op+(size_t)i*64*2560)=lds_tr(yp+i*128); *(u32x2*)(op+(size_t)(i*64+1024)*2560)=lds_tr(yp+2048+i*128); } } }
  ST(7);
}

constexpr int B_ST=36864, B_PV=2*B_ST, B_PVS=18432, B_WP=4*B_ST, B_Z=B_WP+6400, B_KT=159744, B_F=163168;
DI float max3(float a,float b,float c){ return __builtin_fmaxf(__builtin_fmaxf(a,b),c); }
template<class H> DI void softmax20x2(const f32x4 (*z)[5], u32 (*o)[10], H hook){
  const float c=1.4426950408889634f; float mc[2];
  { float m0[2],m1[2],m2[2];
    #pragma unroll
    for(int t=0;t<2;++t){ m0[t]=max3(z[t][0][0],z[t][0][1],z[t][0][2]); m1[t]=max3(z[t][0][3],z[t][1][0],z[t][1][1]); m2[t]=max3(z[t][1][2],z[t][1][3],z[t][2][0]); }
    #pragma unroll
    for(int t=0;t<2;++t){ m0[t]=max3(m0[t],z[t][2][1],z[t][2][2]); m1[t]=max3(m1[t],z[t][2][3],z[t][3][0]); m2[t]=max3(m2[t],z[t][3][1],z[t][3][2]); }
    #pragma unroll
    for(int t=0;t<2;++t){ m0[t]=max3(m0[t],z[t][3][3],z[t][4][0]); m1[t]=max3(m1[t],z[t][4][1],z[t][4][2]); m2[t]=__builtin_fmaxf(m2[t],z[t][4][3]); }
    #pragma unroll
    for(int t=0;t<2;++t) mc[t]=max3(m0[t],m1[t],m2[t])*-c; }
  SB();
  f32x2 e[2][10];
  #pragma unroll
  for(int i=0;i<10;++i)
    #pragma unroll
    for(int t=0;t<2;++t) e[t][i]=__builtin_elementwise_fma(f32x2{z[t][i>>1][(i&1)*2],z[t][i>>1][(i&1)*2+1]},f32x2{c,c},f32x2{mc[t],mc[t]});
  SB();
  #pragma unroll
  for(int i=0;i<10;++i)
    #pragma unroll
    for(int t=0;t<2;++t){ hook(2*i+t); e[t][i]=f32x2{__builtin_amdgcn_exp2f(e[t][i].x),__builtin_amdgcn_exp2f(e[t][i].y)}; SB(); }
  float r[2];
  { f32x2 s[2][5];
    #pragma unroll
    for(int i=0;i<5;++i)
      #pragma unroll
      for(int t=0;t<2;++t) s[t][i]=e[t][2*i]+e[t][2*i+1];
    #pragma unroll
    for(int t=0;t<2;++t){ s[t][0]+=s[t][1]; s[t][2]+=s[t][3]; }
    #pragma unroll
    for(int t=0;t<2;++t){ s[t][0]+=s[t][4]; }
    #pragma unroll
    for(int t=0;t<2;++t){ s[t][0]+=s[t][2]; }
    #pragma unroll
    for(int t=0;t<2;++t) r[t]=__builtin_amdgcn_rcpf(s[t][0].x+s[t][0].y); }
  SB();
  #pragma unroll
  for(int i=0;i<10;++i)
    #pragma unroll
    for(int t=0;t<2;++t) e[t][i]*=r[t];
  SB();
  #pragma unroll
  for(int i=0;i<10;++i)
    #pragma unroll
    for(int t=0;t<2;++t) o[t][i]=pk_bf16(e[t][i].x,e[t][i].y);
  SB();
}
DI u32x4 pack2(f32x4 a,f32x4 b){ u32x4 r={pk_bf16(a[0],a[1]),pk_bf16(a[2],a[3]),pk_bf16(b[0],b[1]),pk_bf16(b[2],b[3])}; return r; }
struct Stream { u32 xo[4], wv[5]; int s0, qw; const u16 *x, *w; lptr L;
  DI void init(int q,int li,int rt,int grp,int qw_,int lane,const u16* x_,const u16* w_,lptr L_){
    qw=qw_; x=x_; w=w_; L=L_;
    s0=((0x8530>>((((li>>(2-q))+((li&(3>>q))<<q))&3)*4))&15)+q;
    #pragma unroll
    for(int i=0;i<4;++i){ const u32 r=(4*qw+i)*8+(lane>>3); xo[i]=(rt*128+r)*1280u+(((lane&7)^((r>>1)&7))<<4)-1024u*i; }
    dma16x4((const char*)x+(s0>=10?s0-10:s0)*128,xo[0],xo[1],xo[2],xo[3],L+4*qw*1024);
    const u32 wlo=(160<<q)*grp+1280*(1-q), dh=1120*(1-q);
    #pragma unroll
    for(int i=0;i<5;++i){ const u32 c=(5*qw+i)*64+lane, r=(c*3277u)>>16, pos=c-r*20, sc=pos^(((r>>3)&1)<<1);
      wv[i]=r*3840u+wlo+(sc<10?0u:dh)+16*sc-1024u*(i&3); } }
  DI void issue(int s,bool withx=true) const { int ss=s+s0; if(ss>=10) ss-=10; lptr d=L+(s&3)*B_ST;
    const char* xb=(const char*)x+ss*128; const char* wb=(const char*)w+(size_t)ss*64*3840;
    if(withx) dma16x4(xb,xo[0],xo[1],xo[2],xo[3],d+4*qw*1024);
    dma16x4(wb,wv[0],wv[1],wv[2],wv[3],d+16384+5*qw*1024); dma16(wb,wv[4],d+16384+(5*qw+4)*1024); } };

template<bool GATHER> DI void o_phase(lptr L,rsrc_t rs,int w,int oh,int tq){
  const int lane=threadIdx.x&63, g=lane>>4, r=lane&15;
  if(GATHER){ *(volatile LAS u32*)(L+B_F+128+4*w)=1u; }
  else for(int it=0;it<SPIN_MAX;++it){ if(__builtin_amdgcn_readfirstlane(*(volatile LAS u32*)(L+B_F+128+4*(w&3)))) break; __builtin_amdgcn_s_sleep(2); }
  asm volatile("":::"memory");
  u32x4 l0[2],l1[2]; u32x2 l2[2];
  { const u32 qo=WS_Q+(oh*2048+512*tq+128*(w&3)+64*(w>>2)+16*(g>>1)+r)*40;
    #pragma unroll
    for(int i=0;i<2;++i){ l0[i]=ld_sc1(rs,qo+i*1280); l1[i]=ld_sc1(rs,qo+i*1280+16); l2[i]=__builtin_amdgcn_raw_buffer_load_b64(rs,qo+i*1280+32,0,16); } }
  lptr kt=L+B_KT, q00,q01,q10,q11; u32 om,ox;
  { const int pc=5*(g&1)+2*(g>>1), pl=5*(g&1)+4, d1=min(r,4); lptr b=kt;
    q00=b+(16*pc+r)*8; q01=b+(16*pl+r)*8+4*(g>>1); q10=b+(160+5*pc+d1)*8; q11=b+(160+5*pl+d1)*8+4*(g>>1);
    const u32 fb=WS_O+((32*tq+8*(w&3)+4*(w>>2))*20+5*(oh>>3))*1024+r*16;
    om=fb+((oh&7)>>1)*1024+(2*(oh&1)+(g>>1))*256+8*(g&1); ox=(g==0)?(fb+4096+((oh&7)>>1)*256+8*(oh&1)):OOB; }
  asm volatile("":"+v"(q00),"+v"(q01),"+v"(q10),"+v"(q11),"+v"(om),"+v"(ox));
  ST(11);
  u32x4 v[16]; const u32 go=(lane<53&&53*w+lane<210)?(WS_P1+oh*16*3584+(53*w+lane)*16):OOB;
  u32 pq[2][10];
  { const u32 sg=(g&1)?0x80008000u:0u; f32x4 z[2][5];
    #pragma unroll
    for(int t=0;t<2;++t){ const u32x4 a=l0[t]^sg, b=l1[t]^sg; const u32x2 c=l2[t]^sg;
      z[t][0]=f32x4{bflo(a.x),bfhi(a.x),bflo(a.y),bfhi(a.y)}; z[t][1]=f32x4{bflo(a.z),bfhi(a.z),bflo(a.w),bfhi(a.w)};
      z[t][2]=f32x4{bflo(b.x),bfhi(b.x),bflo(b.y),bfhi(b.y)}; z[t][3]=f32x4{bflo(b.z),bfhi(b.z),bflo(b.w),bfhi(b.w)};
      z[t][4]=f32x4{bflo(c.x),bfhi(c.x),bflo(c.y),bfhi(c.y)}; }
    SB();
    softmax20x2(z,pq,[&](int k){ if(GATHER&&k<16) v[k]=ld_sc1(rs,go+k*3584); });
    #pragma unroll
    for(int i=0;i<2;++i){
      #pragma unroll
      for(int k=0;k<5;++k){ const int a=(k<4)?k:8, b=(k<4)?k+4:9;
        auto s=__builtin_amdgcn_permlane32_swap(pq[i][a],pq[i][b],false,false); pq[i][a]=s[0]; pq[i][b]=s[1]; }
      #pragma unroll
      for(int k=0;k<10;++k) asm volatile("":"+v"(pq[i][k])); } }
  ST(12);
  if(GATHER){
    gather16(rs,go,v);
    ST(13);
    f32x4 s[4];
    #pragma unroll
    for(int i=0;i<4;++i) s[i]=(__builtin_bit_cast(f32x4,v[4*i])+__builtin_bit_cast(f32x4,v[4*i+1]))+(__builtin_bit_cast(f32x4,v[4*i+2])+__builtin_bit_cast(f32x4,v[4*i+3]));
    s[0]=(s[0]+s[1])+(s[2]+s[3]);
    if(lane<53) *(volatile LAS u32x2*)(kt+(53*w+lane)*8)=u32x2{pk_bf16(s[0][0],s[0][1]),pk_bf16(s[0][2],s[0][3])};
    *(volatile LAS u32*)(L+B_F+32+4*w)=1u;
  }
  u32x4 a0[2]; u32 a1[2];
  for(int it=0;it<SPIN_MAX;++it){
    const u32x4 f=*(volatile LAS u32x4*)(L+B_F+32);
    a0[0]=cat(*(volatile LAS u32x2*)q00,*(volatile LAS u32x2*)(q00+128)); a0[1]=cat(*(volatile LAS u32x2*)q10,*(volatile LAS u32x2*)(q10+40));
    a1[0]=*(volatile LAS u32*)q01; a1[1]=*(volatile LAS u32*)q11;
    if(__builtin_amdgcn_readfirstlane(f.x+f.y+f.z+f.w)==4u) break;
    if(!GATHER) __builtin_amdgcn_s_sleep(1); }
  ST(14);
  f32x4 n[4][2];
  #pragma unroll
  for(int i=1;i>=0;--i){
    #pragma unroll
    for(int t=0;t<4;++t){ const u32* f=pq[t>>1]+4*(t&1); n[t][i]=mfma16(a0[i],u32x4{f[0],f[1],f[2],f[3]},Z4); }
    #pragma unroll
    for(int t=0;t<4;++t) n[t][i]=mfma16(u32x4{a1[i],0u,0u,0u},u32x4{pq[t>>1][8+(t&1)],0u,0u,0u},n[t][i]); }
  SB();
  float den[4];
  #pragma unroll
  for(int t=0;t<4;++t) den[t]=__builtin_bit_cast(float,__builtin_amdgcn_ds_bpermute((r+16)*4,__builtin_bit_cast(int,n[t][1][0])));
  #pragma unroll
  for(int t=0;t<4;++t) den[t]=__builtin_amdgcn_rcpf(rbf(rbf(den[t])+1e-6f));
  SB();
  #pragma unroll
  for(int t=0;t<4;++t){ const f32x4 m=n[t][0]*den[t], e=n[t][1]*den[t];
    __builtin_amdgcn_raw_buffer_store_b64(u32x2{pk_bf16(m[0],m[1]),pk_bf16(m[2],m[3])},rs,om+t*20480,0,16);
    __builtin_amdgcn_raw_buffer_store_b64(u32x2{pk_bf16(e[0],e[1]),pk_bf16(e[2],e[3])},rs,ox+t*20480,0,16); }
}

template<class F> DI bool role_b(const bool ISQ,int X,int li,int rt,int grp,int w,const Stream& st,lptr L,F params,const u16* u,const u16* wo,u16* out,void* ws){
  const int tid=threadIdx.x, lane=tid&63, g=lane>>4;
  const int T=ISQ?16+li:li; const bool doout=T<20; const int tt64=4*X+T/5, ct=T%5;
  const rsrc_t rs=__builtin_amdgcn_make_buffer_rsrc(ws,0,WS_END,0x00020000);
  constexpr int OIM=B_PV;
  u64 E;
  auto count=[&](int word){ asm volatile("":::"memory"); u32 n=0; if(lane==0) n=__hip_atomic_fetch_add((LAS u32*)(L+B_F+4*word),1u,__ATOMIC_RELAXED,__HIP_MEMORY_SCOPE_WORKGROUP); return n; };
  auto until=[&](int word,u32 n){ for(int it=0;it<SPIN_MAX;++it){ if(__builtin_amdgcn_readfirstlane(*(volatile LAS u32*)(L+B_F+4*word))>=n) break; __builtin_amdgcn_s_sleep(2); } asm volatile("":::"memory"); };
  auto arrive=[&](){
    if(__builtin_amdgcn_readfirstlane(count(4))==3u&&lane==0){ flag_set(ws,ISQ?QF(4*rt+grp):KF(grp,rt),E); asm volatile("":::"memory"); *(volatile LAS u32*)(L+B_F+84)=4u; } };
  volatile LAS u32* bad=(volatile LAS u32*)(L+B_F+20);
  const int oh=4*grp+(rt&3), tq=rt>>2;
  const int ot=4*tt64+(w&3);
  int fi=(lane<16)?KF(grp,lane):((lane<32)?KF(lane&7,4*tq+(w&3)):((lane<48)?KF(ot&7,ot>>3):QF(4*(4*tq+(w&3))+(oh>>3))));
  if(w<4){
    const int th=w>>1, hh=w&1;
    if(lane<4) lds_st<u32x4>(L+B_Z+lane*16,u32x4{0,0,0,0});
    if(tid<9) lds_st<u32x4>(L+B_F+tid*16,u32x4{0,0,0,0});
    lptr fad[3][2];
    { const int q=(lane&15)>>2, p=lane&3, hd=ISQ?p:(p&1);
      #pragma unroll
      for(int s=0;s<3;++s)
        #pragma unroll
        for(int t=0;t<2;++t){ const int d=16*(2*s+t)+4*g-20*hd; const bool ok=(d>=0)&&(d<20)&&(2*s+t<5);
          fad[s][t]=ok?(L+B_WP+((ISQ?4*hh:2*hh)+hd)*800+(d+q)*40+(ISQ?0:(p>>1)*3200)):(L+B_Z); if(2*s+t<5) asm volatile("":"+v"(fad[s][t])); } }
    lptr pv=L+B_PV+w*B_PVS;
    lptr pp=pv+(lane&15)*168+(g&1)*80+(g>>1)*40, vp2, vp3, vp4, pa, vb;
    { lptr vp=pv+10752+(lane&15)*104; vp2=vp+((g<2)?(48*g+40):(8*g-16)); vp3=vp+((g==3)?48:(16+8*g)); vp4=vp+56+8*g;
      const int tk=16*(g&1)+4*((lane&15)>>2)+2*(g>>1); pa=pv+tk*168+8*(lane&3)+(th^1)*80; vb=pv+10752+tk*104+8*(lane&3)+(th^1)*48; }
    u32 po0, po1;
    { const u32 po=WS_P1+((4*grp+2*hh+th)*16+rt)*3584; po0=po+lane*16; po1=((lane&15)<5)?(po+(160+5*g+(lane&15))*16):OOB; }
    asm volatile("":"+v"(fi),"+v"(pp),"+v"(vp2),"+v"(vp3),"+v"(vp4),"+v"(pa),"+v"(vb),"+v"(po0),"+v"(po1));
    lptr ab[5], bb[2];
    { const int q=(lane&15)>>2,p=lane&3;
      #pragma unroll
      for(int i=0;i<5;++i){ int n=2*i+(p>>1); int cid=ISQ?(10*hh+n):(5*hh+n+(n>=5?5:0)); ab[i]=L+16384+(8*g+q)*320+((cid^((g&1)<<1))<<4)+8*(p&1); }
      const int b0=(64*th+(lane&15))*128+((g^((lane&15)>>1))<<4); bb[0]=L+b0; bb[1]=L+(b0^64); }
    f32x4 acc[5][4];
    #pragma unroll
    for(int i=0;i<5;++i)
      #pragma unroll
      for(int t=0;t<4;++t){ acc[i][t]=Z4; asm volatile("":"+v"(acc[i][t])); }
    ST(1);
    u32x4 FA[2][5], FB[2][4], fa[2][5];
    u32x2 lo2;
    auto half=[&](int hf,bool mm,bool last,int step){
      u32x2 lo;
      #pragma unroll
      for(int n=0;n<20;++n){ const int i=n>>2, t=n&3;
        if(mm) acc[i][t]=mfma16(FA[hf^1][i],FB[hf^1][t],acc[i][t]);
        SB();
        if(n==0) FB[hf][0]=lds_ld<u32x4>(bb[hf]);
        else if(n==1) lo=lds_tr(ab[0]+hf*10240);
        else if(n==2) FA[hf][0]=cat(lo,lds_tr(ab[0]+hf*10240+1280));
        else if(n<6) FB[hf][n-2]=lds_ld<u32x4>(bb[hf]+2048*(n-2));
        else if(n<14){ const int j=1+((n-6)>>1); if(!(n&1)) lo=lds_tr(ab[j]+hf*10240); else FA[hf][j]=cat(lo,lds_tr(ab[j]+hf*10240+1280)); }
        else if(hf==1&&!last){ if(n<19){ ab[n-14]+=step; asm volatile("":"+v"(ab[n-14])); } if(n>=18){ bb[n-18]+=step; asm volatile("":"+v"(bb[n-18])); } }
        if(last){ const int j=n>>2, s=(n>>1)&1; if(!(n&1)) lo2=lds_tr(fad[s][0]+8*j); else fa[s][j]=cat(lo2,lds_tr(fad[s][1]+8*j)); }
        SB(); } };
    bar();
    ST(2);
    __builtin_amdgcn_s_waitcnt(0xC07F);
    SB();
    half(0,false,false,0);
    #pragma unroll 1
    for(u32 s=1;s<10;++s){
      half(1,true,false,(s&3)?B_ST:-3*B_ST);
      rbar();
      SB();
      half(0,true,false,0); }
    const u32x2 ev=*(volatile LAS u32x2*)(L+B_F+88);
    half(1,true,true,0);
    E=(u64)(u32)__builtin_amdgcn_readfirstlane(ev.x)|((u64)(u32)__builtin_amdgcn_readfirstlane(ev.y)<<32);
    asm volatile("s_waitcnt lgkmcnt(0)":::"memory");
    waitv<0>();
    count(7);
    #pragma unroll
    for(int t=0;t<4;++t)
      #pragma unroll
      for(int i=0;i<5;++i) acc[i][t]=mfma16(FA[1][i],FB[1][t],acc[i][t]);
    u32x2 pk[5][4];
    #pragma unroll
    for(int t=0;t<4;++t)
      #pragma unroll
      for(int i=0;i<5;++i) pk[i][t]=u32x2{pk_bf16(acc[i][t][0],acc[i][t][1]),pk_bf16(acc[i][t][2],acc[i][t][3])};
    ST(3);
    if(ISQ){
      u32x4 f2[5];
      #pragma unroll
      for(int i=0;i<5;++i) f2[i]=cat(lds_tr(fad[2][0]+8*i),u32x2{0u,0u});
      lptr sp_=pv+g*2560+(lane&15)*40;
      f32x4 z[4][5];
      #pragma unroll
      for(int t=0;t<4;++t)
        #pragma unroll
        for(int i=0;i<5;++i) z[t][i]=mfma16(fa[0][i],cat(pk[0][t],pk[1][t]),Z4);
      #pragma unroll
      for(int t=0;t<4;++t)
        #pragma unroll
        for(int i=0;i<5;++i) z[t][i]=mfma16(fa[1][i],cat(pk[2][t],pk[3][t]),z[t][i]);
      SB();
      #pragma unroll
      for(int t=0;t<5;++t){
        #pragma unroll
        for(int i=0;i<5;++i){ if(t<4) z[t][i]=mfma16(f2[i],cat(pk[4][t],u32x2{0u,0u}),z[t][i]);
          if(t>0) lds_st<u32x2>(sp_+(t-1)*640+8*i,u32x2{pk_bf16(z[t-1][i][0],z[t-1][i][1]),pk_bf16(z[t-1][i][2],z[t-1][i][3])}); }
        SB(); }
      ST(10);
      #pragma unroll
      for(int i=0;i<10;++i){ const int c=i*64+lane, hd=(c*205)>>15;
        st_sc1(rs,WS_Q+((8*grp+4*hh+hd)*2048+128*rt+64*th)*40+(c-160*hd)*16,lds_ld<u32x4>(pv+c*16)); }
      ST(11);
      waitv<0>(); ST(12); arrive();
      ST(4);
    } else {
      { f32x4 z[4][5]; u32 ph[4][10];
        auto fm=[&](int t){
          const u32x4 b0=cat(pk[0][t],pk[1][t]), b1=cat(pk[2][t],pk[3][t]);
          #pragma unroll
          for(int i=0;i<5;++i) z[t][i]=mfma16(fa[0][i],b0,Z4);
          #pragma unroll
          for(int i=0;i<5;++i) z[t][i]=mfma16(fa[1][i],b1,z[t][i]); };
        fm(0); fm(1);
        SB();
        softmax20x2(z,ph,[&](int k){ const int t=2+k/10, i=k%5;
          z[t][i]=(k%10<5)?mfma16(fa[0][i],cat(pk[0][t],pk[1][t]),Z4):mfma16(fa[1][i],cat(pk[2][t],pk[3][t]),z[t][i]); });
        auto wp=[&](int t){
          #pragma unroll
          for(int i=0;i<5;++i) lds_st<u32x2>(pp+t*2688+8*i,u32x2{ph[t][2*i],ph[t][2*i+1]}); };
        u32x2 v2[4];
        #pragma unroll
        for(int t=0;t<4;++t) v2[t]=(g<2)?u32x2{0x3F80u,0u}:pk[2][t];
        softmax20x2(z+2,ph+2,[&](int k){
          if(k<10) *(volatile LAS u32x2*)(pp+(k/5)*2688+8*(k%5))=u32x2{ph[k/5][2*(k%5)],ph[k/5][2*(k%5)+1]};
          else if(k<14) *(volatile LAS u32x2*)(vp2+(k-10)*1664)=v2[k-10];
          else if(k<18) *(volatile LAS u32x2*)(vp3+(k-14)*1664)=pk[3][k-14];
          else { *(volatile LAS u32x2*)(vp4+(2*k-36)*1664)=pk[4][2*k-36]; *(volatile LAS u32x2*)(vp4+(2*k-35)*1664)=pk[4][2*k-35]; } });
        wp(2); wp(3); }
      ST(4);
      u64 fl;
      { f32x4 c[2][6];
        #pragma unroll
        for(int h2=0;h2<2;++h2){
          if(h2){ pa+=th?80:-80; vb+=th?48:-48; }
          u32x4 a[2][3], b[2][2];
          #pragma unroll
          for(int ks=0;ks<2;++ks){
            #pragma unroll
            for(int i=0;i<3;++i) a[ks][i]=cat(lds_tr(pa+ks*32*168+32*i),lds_tr(pa+(ks*32+1)*168+32*i));
            #pragma unroll
            for(int dt=0;dt<2;++dt) b[ks][dt]=cat(lds_tr(vb+ks*32*104+32*dt),lds_tr(vb+(ks*32+1)*104+32*dt)); }
          #pragma unroll
          for(int i=0;i<6;++i) c[h2][i]=mfma16(a[0][i%3],b[0][i/3],Z4);
          #pragma unroll
          for(int i=0;i<6;++i) c[h2][i]=mfma16(a[1][i%3],b[1][i/3],c[h2][i]); }
        #pragma unroll
        for(int i=0;i<6;++i) *(volatile LAS f32x4*)(pv+i*1024+lane*16)=c[0][i];
        *(volatile LAS u32*)(L+B_F+4*w)=1u;
        #pragma unroll
        for(int i=0;i<6;++i) asm volatile("":"+v"(c[1][i]));
        lptr ox_=L+B_PV+(w^2)*B_PVS; f32x4 pr[6];
        for(int it=0;it<SPIN_MAX;++it){
          const u32 f=*(volatile LAS u32*)(L+B_F+4*(w^2)), n=*(volatile LAS u32*)(L+B_F+16);
          #pragma unroll
          for(int i=0;i<6;++i) pr[i]=*(volatile LAS f32x4*)(ox_+i*1024+lane*16);
          if(__builtin_amdgcn_readfirstlane(f+n)==5u) break; }
        fl=flag_get(ws,fi);
        #pragma unroll
        for(int i=0;i<6;++i){ u32 o=(i<3)?(po0+i*1024):(po1+(i-3)*320); if(i%3==2) o=(g<2)?o:OOB;
          st_sc1(rs,o,__builtin_bit_cast(u32x4,c[1][i]+pr[i])); } }
      ST(5);
      if(!__all(fl==E)) flag_wait(ws,E,fi,fl);
      asm volatile("":::"memory");
      o_phase<true>(L,rs,w,oh,tq);
      count(6);
      if(rt==15&&grp==7&&w==3){
        for(int it=0;it<SPIN_MAX;++it){ if(__all(flag_get(ws,KF(lane>>4,lane&15))==E&&flag_get(ws,KF(4+(lane>>4),lane&15))==E&&flag_get(ws,QF(lane))==E)) break; __builtin_amdgcn_s_sleep(2); }
        if(lane==0) __hip_atomic_store((GAS u64*)ws,E,__ATOMIC_RELAXED,__HIP_MEMORY_SCOPE_AGENT); }
      ST(6);
    }
    if(!doout){
      until(21,4u);
      const char* up=(const char*)u+(size_t)tid*4*1280+2*(80*X+64+4*(li-4)); u32x4 um[4];
      #pragma unroll
      for(int r=0;r<4;++r) um[r]=cat(*(const u32x2*)(up+r*1280),*(const u32x2*)(up+(r+1024)*1280));
      u_scatter(false,tid,L,um);
      asm volatile("s_waitcnt lgkmcnt(0)":::"memory");
      __builtin_amdgcn_s_waitcnt(0x0F70); return false; }
    until(20,4u);
    if(ISQ){ fi=KF(ot&7,ot>>3); flag_wait(ws,E,fi,flag_get(ws,fi)); }
    if(ISQ){
      const int hi=HF(4*X+li); flag_wait(ws,E,hi,flag_get(ws,hi)); }
    { const u32 ob=WS_O+ot*20480;
      const u32 sm=ob+((lane<32)?((5*(lane>>3)+((lane>>1)&3))*1024+(lane&1)*512+508):((5*((lane-32)>>3)+4)*1024+((lane&7)>>1)*256+244+8*(lane&1)));
      for(int it=0;it<SPIN_MAX;++it){ asm volatile("":::"memory");
        if(__all(__builtin_amdgcn_raw_buffer_load_b32(rs,sm,0,16)!=SENT)) break;
        __builtin_amdgcn_s_sleep(1); }
      ST(15);
      const u32 vo=lane*16;
      for(u32 it=1;it<SPIN_MAX;++it){
        #pragma unroll
        for(int i=0;i<5;++i) dma16x4_sc1((const char*)ws+ob+i*4096,vo,vo,vo,vo,L+OIM+w*20480+i*4096);
        waitv<12>();
        rbar();
        waitv<0>();
        ST(7);
        rbar();
        rbar();
        rbar();
        if(__builtin_amdgcn_readfirstlane(*bad)!=it) break; } }
    __builtin_amdgcn_s_waitcnt(0x0F70);
  } else {
    const int hw=w-4;
    asm volatile("s_load_dwordx2 %0, %1, 0x0":"=s"(E):"s"(ws):"memory");
    ST(9);
    const bool lead3=st.s0==0||st.s0==5||st.s0==1||st.s0==6;
    if(lead3){ st.issue(2); waitv<18>(); rbar(); } else { waitv<9>(); rbar(); st.issue(2); }
    ST(8);
    waitv<9>(); rbar();
    st.issue(3);
    asm volatile("s_waitcnt lgkmcnt(0)":"+s"(E)::"memory"); E+=1;
    if(tid==256) *(volatile LAS u32x2*)(L+B_F+88)=u32x2{(u32)E,(u32)(E>>32)};
    if(!ISQ&&lane<50){ lptr p=L+B_WP+hw*800+lane*16; lds_st<u32x4>(p+3200,lds_ld<u32x4>(p)^0x80008000u); }
    waitv<9>(); bar();
    if(!doout) params();
    st.issue(4);
    const int pr=hw>>1, me2=hw&1;
    u32 wq[40];
    { const int r=lane>>3; const u32 sw=((lane&7)^((((r>>1)&1)|(me2<<1))<<1))<<4, vo=r*1280+sw, vo2=((r>>2)*20+(r&3)+16+32*me2)*1280+sw;
      #pragma unroll
      for(int n=0;n<40;++n){ const int G=n/10, m=n%10, i=(n<22)?n:n-22, k=(i<16)?(i&3):((n>=18&&n<21)?n-18:((i<18)?i-16:0));
        wq[n]=((m<8)?vo+(160*G+20*m)*1280:vo2+(160*G+80*(m-8))*1280)-1024*k; asm volatile("":"+v"(wq[n])); } }
    lptr dst=L+hw*18432, rd, rd1; int x9, x10;
    { const int q=(lane&15)>>2, p=lane&3, f=((q>>1)|((g&1)<<1))<<1, ow=pr*2+(g&1);
      lptr rb=L+ow*18432+(g>>1)*1024+q*128+8*(p&1); rd=rb+(((4*me2+(p>>1))^f)<<4); rd1=rb+(((4*me2+2+(p>>1))^f)<<4);
      x9=B_WP+ow*3072-ow*18432; x10=(g>>1)?(B_PV+17408-1024):(x9+2048); }
    lptr ra9=rd+x9, rb9=rd1+x9, ra10=rd+x10, rb10=rd1+x10;
    asm volatile("":"+v"(rd),"+v"(rd1),"+v"(ra9),"+v"(rb9),"+v"(ra10),"+v"(rb10));
    waitv<9>(); rbar();
    #pragma unroll 1
    for(int s=4;s<9;++s){
      st.issue(s+1);
      waitv<9>(); rbar(); }
    waitv<0>(); rbar();
    if(!doout){ until(7,4u); return true; }
    if(!ISQ){
      const u32x4 sv={SENT,SENT,SENT,SENT};
      #pragma unroll
      for(int i=0;i<4;++i) st_sc1(rs,(i<3||lane<18)?(WS_P1+((4*grp+hw)*16+rt)*3584+i*1024+lane*16):OOB,sv);
      #pragma unroll
      for(int i=0;i<5;++i) st_sc1(rs,WS_O+((8*rt+grp)*20+5*hw+i)*1024+lane*16,sv);
      waitv<0>(); arrive(); }
    u32x4 wf[2][11];
    const char* gb=(const char*)wo+ct*256+pr*128+me2*8*1280;
    auto meet=[&](u32 n){ *(volatile LAS u32*)(L+B_F+48+4*hw)=n; until(12+(hw^1),n); };
    auto fetch18=[&](int n0){
      #pragma unroll
      for(int i=0;i<16;i+=4) dma16x4(gb,wq[n0+i],wq[n0+i+1],wq[n0+i+2],wq[n0+i+3],dst+i*1024);
      dma16xn<2>(gb,wq[n0+16],wq[n0+17],0,dst+16384); };
    until(ISQ?21:7,4u);
    ST(3);
    fetch18(0);
    dma16xn<3>(gb,wq[18],wq[19],wq[20],L+B_WP+hw*3072);
    dma16(gb,wq[21],dst+B_PV+17408);
    waitv<0>(); meet(1u);
    #pragma unroll
    for(int s=0;s<9;++s){ wf[0][s]=cat(lds_tr(rd+s*2048),lds_tr(rd+s*2048+512)); wf[1][s]=cat(lds_tr(rd1+s*2048),lds_tr(rd1+s*2048+512)); }
    wf[0][9]=cat(lds_tr(ra9),lds_tr(ra9+512)); wf[1][9]=cat(lds_tr(rb9),lds_tr(rb9+512));
    wf[0][10]=cat(lds_tr(ra10),lds_tr(ra10+512)); wf[1][10]=cat(lds_tr(rb10),lds_tr(rb10+512));
    asm volatile("s_waitcnt lgkmcnt(0)":::"memory");
    meet(2u);
    ST(10);
    #pragma unroll
    for(int i=0;i<8;i+=4) dma16x4(gb,wq[22+i],wq[23+i],wq[24+i],wq[25+i],dst+i*1024);
    if(!ISQ){ o_phase<false>(L,rs,w,oh,tq); count(6); ST(6); until(6,8u);
      if(li<4&&tid==256) flag_set(ws,HF(4*X+li),E); }
    #pragma unroll
    for(int i=8;i<16;i+=4) dma16x4(gb,wq[22+i],wq[23+i],wq[24+i],wq[25+i],dst+i*1024);
    dma16xn<2>(gb,wq[38],wq[39],0,dst+16384);
    count(20);
    ST(4);
    lptr obt[4];
    #pragma unroll
    for(int t=0;t<4;++t){ obt[t]=L+OIM+lane*16+((t+hw)&3)*20480; asm volatile("":"+v"(obt[t])); }
    waitv<0>();
    for(u32 it=1;it<SPIN_MAX;++it){
      rbar();
      ST(5);
      f32x4 c[2][4];
      #pragma unroll
      for(int t=0;t<4;++t){ c[0][t]=Z4; c[1][t]=Z4; }
      u32x4 ob[3][4], wl[3][2]; u32 chk=0;
      auto fetch=[&](int ks){
        #pragma unroll
        for(int t=0;t<4;++t) ob[ks%3][t]=lds_ld<u32x4>(obt[t]+ks*1024);
        if(ks>=11){ wl[ks%3][0]=cat(lds_tr(rd+(ks-11)*2048),lds_tr(rd+(ks-11)*2048+512)); wl[ks%3][1]=cat(lds_tr(rd1+(ks-11)*2048),lds_tr(rd1+(ks-11)*2048+512)); } };
      fetch(0); fetch(1);
      SB();
      #pragma unroll
      for(int ks=0;ks<20;++ks){ u32x2 lo;
        if(ks==6){ rbar(); SB(); }
        #pragma unroll
        for(int n=0;n<8;++n){ const int t=n>>1, cc=n&1, k2=ks+2;
          c[cc][t]=mfma16((ks<11)?wf[cc][ks<11?ks:0]:wl[ks%3][cc],ob[ks%3][t],c[cc][t]);
          SB();
          if(k2<20){
            if(n<4) ob[k2%3][n]=lds_ld<u32x4>(obt[n]+k2*1024);
            else if(k2>=11){ lptr p=((n<6)?rd:rd1)+(k2-11)*2048; if(!(n&1)) lo=lds_tr(p); else wl[k2%3][(n-4)>>1]=cat(lo,lds_tr(p+512)); } }
          if(n==6) chk=max(chk,max(ob[ks%3][0].x,ob[ks%3][0].y));
          if(n==7) chk=max(chk,max(ob[ks%3][0].z,ob[ks%3][0].w));
          SB(); } }
      if(__any(chk==SENT)) *bad=it;
      bar();
      #pragma unroll
      for(int t=0;t<4;++t)
        #pragma unroll
        for(int cc=0;cc<2;++cc) lds_st<u32x2>(L+OIM+(16*((t+hw)&3)+(lane&15))*272+hw*64+cc*32+8*g,u32x2{pk_bf16(c[cc][t][0],c[cc][t][1]),pk_bf16(c[cc][t][2],c[cc][t][3])});
      ST(7);
      bar();
      if(__builtin_amdgcn_readfirstlane(*bad)!=it) break; }
  }
  #pragma unroll
  for(int i=0;i<2;++i){ const int r=32*i+(tid>>4);
    __builtin_amdgcn_raw_buffer_store_b128(lds_ld<u32x4>(L+OIM+r*272+(tid&15)*16),__builtin_amdgcn_make_buffer_rsrc(out,0,2048*2560,0x00020000),(64*tt64+r)*2560+1280+ct*256+(tid&15)*16,0,16); }
  if(w<4) ST(8);
  return false;
}

constexpr int CODE_LINES=320;
__global__ __launch_bounds__(512,2) void fused(const u16* u,const u16* are,const u16* aim,const u16* bre,const u16* bim,const u16* cre,const u16* cim,
    const u16* logdt,const u16* dskip,const u16* x,const u16* wqkv,const u16* wphi,const u16* wo,u16* out,void* ws){
  extern __shared__ __attribute__((aligned(16))) char smem[];
  lptr L=(lptr)smem;
  const int b=blockIdx.x, X=b&7, idx=b>>3;
  u64 pc; asm volatile("s_getpc_b64 %0":"=s"(pc));
  const u64 t00=__builtin_amdgcn_s_memrealtime();
  const int w=__builtin_amdgcn_readfirstlane(threadIdx.x>>6);
  const bool isa=(idx>>3)==2;
  const int q=idx>=24, li=idx-24*q, rt=4*(X>>1)+(li>>(2-q)), grp=((X&1)<<(2-q))+(li&(3>>q));
  if(idx<16&&w<2){
    const u32 n=(w?(X*16+idx)*50:idx*32)+(threadIdx.x&63); u32 keep;
    if(w?((threadIdx.x&63)<50):(threadIdx.x<32&&n<CODE_LINES))
      asm volatile("s_mov_b32 %0, m0\n\ts_mov_b32 m0, %3\n\ts_nop 0\n\tglobal_load_lds_dword %1, %2\n\ts_mov_b32 m0, %0":"=&s"(keep):"v"(n*128),"s"(w?(u64)wo:(pc&~255ull)),"s"(162560+w*256):"memory"); }
  Stream st;
  if(__builtin_expect(!isa&&w>=4,1)){ st.init(q,li,rt,grp,w&3,threadIdx.x&63,x,wqkv,L); st.issue(0,false);
    { const u32 nb=800<<q, o=(threadIdx.x&63)*16; const char* wp=(const char*)wphi+(4*grp+(w&3))*nb;
      if(o<nb) dma16(wp,o,L+B_WP+(w&3)*nb);
      if(o+1024<nb) dma16(wp,o+1024,L+B_WP+(w&3)*nb+1024); }
    st.issue(1); }
  SB();
  STINIT();
  if((threadIdx.x&255)==0) *(LAS u64*)(L+163328+(threadIdx.x>>8)*128)=t00;
  const int ch0=isa?(80*X+8*(idx-16)):(80*X+64+4*(idx-28)), wi=isa?w:w-4;
  SsmP sp; u32 dsk;
  auto params=[&](){ const SsmIn in={are,aim,bre,bim,cre,cim,logdt}; sp=load_ssm(in,ch0+wi,threadIdx.x&63); dsk=dskip[ch0+wi]; };
  bool job=true;
  if(__builtin_expect(!isa,1)) job=role_b(q,X,li,rt,grp,w,st,L,params,u,wo,out,ws);
  else params();
  if(job){
    conv_job(isa,wi,ch0,idx&7,L,sp,dsk,u,out,ws); }
  STDUMP();
  if(b==135&&threadIdx.x<64){ const int lane=threadIdx.x; const u64 t0=t00;
    while(__builtin_amdgcn_s_memrealtime()-t0<6000) __builtin_amdgcn_s_sleep(8);
    const GAS u64* s=(const GAS u64*)((char*)ws+(200u<<20)); u32 mn=~0u, mx=0;
    for(int i=lane;i<512;i+=64){ u64 v=__hip_atomic_load(s+i*16,__ATOMIC_RELAXED,__HIP_MEMORY_SCOPE_AGENT)+1000-t0; if(v<100000) mn=min(mn,(u32)v);
      const int wg=i>>1; if(QSEL){ u64 e=__hip_atomic_load(s+i*16+QCOL,__ATOMIC_RELAXED,__HIP_MEMORY_SCOPE_AGENT)+1000-t0; if(e<100000) mx=max(mx,(u32)e); } }
    for(int o=1;o<64;o<<=1){ mn=min(mn,(u32)__shfl_xor((int)mn,o)); mx=max(mx,(u32)__shfl_xor((int)mx,o)); }
    const u32 span=(MULT)?(mx-mn):0;
    while(__builtin_amdgcn_s_memrealtime()-t0<7000+(u64)MULT*span) __builtin_amdgcn_s_sleep(8); }
}

extern "C" void kernel_launch(void* const* d_in, const int* in_sizes, int n_in, void* d_out, int out_size, void* d_ws, size_t ws_size, hipStream_t stream){
  (void)hipFuncSetAttribute((const void*)fused,hipFuncAttributeMaxDynamicSharedMemorySize,163840);
  fused<<<256,512,163840,stream>>>((const u16*)d_in[0],(const u16*)d_in[1],(const u16*)d_in[2],(const u16*)d_in[3],(const u16*)d_in[4],(const u16*)d_in[5],(const u16*)d_in[6],
    (const u16*)d_in[7],(const u16*)d_in[8],(const u16*)d_in[9],(const u16*)d_in[10],(const u16*)d_in[11],(const u16*)d_in[12],(u16*)d_out,d_ws);
}
```

```cpp
#define WBSEL (!BIG&&rot==7)
#include <hip/hip_runtime.h>
#include <stdint.h>

typedef unsigned short u16; typedef unsigned u32; typedef unsigned long long u64;
typedef __attribute__((ext_vector_type(4))) short s16x4;
typedef __attribute__((ext_vector_type(8))) _Float16 f16x8;
typedef __attribute__((ext_vector_type(8))) __bf16 bf16x8;
typedef __attribute__((ext_vector_type(2))) __bf16 bf16x2;
typedef __attribute__((ext_vector_type(2))) _Float16 f16x2;
typedef __attribute__((ext_vector_type(2))) float f32x2;
typedef __attribute__((ext_vector_type(4))) float f32x4;
typedef __attribute__((ext_vector_type(16))) float f32x16;
typedef __attribute__((ext_vector_type(2))) unsigned u32x2;
typedef __attribute__((ext_vector_type(3))) unsigned u32x3;
typedef __attribute__((ext_vector_type(4))) unsigned u32x4;
#define LAS __attribute__((address_space(3)))
#define GAS __attribute__((address_space(1)))
#define DI __device__ __forceinline__
typedef LAS char* lptr;
typedef __amdgpu_buffer_rsrc_t rsrc_t;

#define ST(i) do{}while(0)
#define STDUMP() do{}while(0)
#define STINIT() do{}while(0)

DI u32 pk_bf16(float a, float b){ f32x2 v={a,b}; return __builtin_bit_cast(u32,__builtin_convertvector(v,bf16x2)); }
DI u32 pk_f16(f32x2 v){ return __builtin_bit_cast(u32,__builtin_convertvector(v,f16x2)); }
DI float bflo(u32 x){ return __builtin_bit_cast(float,x<<16); }
DI float bfhi(u32 x){ return __builtin_bit_cast(float,x&0xffff0000u); }
DI float rbf(float a){ return bflo(pk_bf16(a,a)); }
template<class T> DI T lds_ld(lptr p){ return *(LAS T*)p; }
template<class T> DI void lds_st(lptr p, T v){ *(LAS T*)p=v; }
DI u32x2 lds_ld64(lptr p){ return *(volatile LAS u32x2*)p; }
DI u32x2 lds_tr(lptr p){ return __builtin_bit_cast(u32x2,__builtin_amdgcn_ds_read_tr16_b64_v4i16((LAS s16x4*)p)); }
DI f32x4 mfma16(u32x4 a,u32x4 b,f32x4 c){ return __builtin_amdgcn_mfma_f32_16x16x32_bf16(__builtin_bit_cast(bf16x8,a),__builtin_bit_cast(bf16x8,b),c,0,0,0); }
DI u32x4 cat(u32x2 a,u32x2 b){ u32x4 r={a.x,a.y,b.x,b.y}; return r; }
DI u32 p_ll(u32 a,u32 b){ return __builtin_amdgcn_perm(b,a,0x05040100u); }
DI u32 p_lh(u32 a,u32 b){ return __builtin_amdgcn_perm(b,a,0x07060100u); }
DI u32 p_hl(u32 a,u32 b){ return __builtin_amdgcn_perm(b,a,0x05040302u); }
DI u32 p_hh(u32 a,u32 b){ return __builtin_amdgcn_perm(b,a,0x07060302u); }
#define SB() __builtin_amdgcn_sched_barrier(0)
DI void rbar(){ asm volatile("":::"memory"); __builtin_amdgcn_s_barrier(); asm volatile("":::"memory"); }
DI void bar(){ asm volatile("s_waitcnt lgkmcnt(0)":::"memory"); __builtin_amdgcn_s_barrier(); asm volatile("":::"memory"); }
template<int N> DI void waitv(){ asm volatile("s_waitcnt vmcnt(%0)"::"i"(N):"memory"); }
constexpr f32x4 Z4={0.f,0.f,0.f,0.f};
DI float wave_max(float m){
  m=fmaxf(m,__builtin_bit_cast(float,__builtin_amdgcn_update_dpp(0,__builtin_bit_cast(int,m),0xB1,0xf,0xf,true)));
  m=fmaxf(m,__builtin_bit_cast(float,__builtin_amdgcn_update_dpp(0,__builtin_bit_cast(int,m),0x4E,0xf,0xf,true)));
  m=fmaxf(m,__builtin_bit_cast(float,__builtin_amdgcn_update_dpp(0,__builtin_bit_cast(int,m),0x141,0xf,0xf,true)));
  m=fmaxf(m,__builtin_bit_cast(float,__builtin_amdgcn_update_dpp(0,__builtin_bit_cast(int,m),0x140,0xf,0xf,true)));
  int x=__builtin_bit_cast(int,m);
  float a=__builtin_bit_cast(float,__builtin_amdgcn_readlane(x,0)), b=__builtin_bit_cast(float,__builtin_amdgcn_readlane(x,16));
  float c=__builtin_bit_cast(float,__builtin_amdgcn_readlane(x,32)), d=__builtin_bit_cast(float,__builtin_amdgcn_readlane(x,48));
  return fmaxf(fmaxf(a,b),fmaxf(c,d)); }

constexpr u32 WS_FLAG=256, WS_P1=32768, WS_Q=WS_P1+32*16*3584, WS_O=WS_Q+32*2048*40, WS_END=WS_O+128*20*1024;
DI int KF(int g,int r){ return 16*(16*g+r); }
DI int QF(int i){ return 16*(128+i); }
DI int HF(int i){ return 16*(192+i); }
constexpr u32 SENT=0xFFFFFFFFu, OOB=0x80000000u;
constexpr int SPIN_MAX=1<<17;
DI void dma16(const void* sb, u32 voff, lptr l){ u32 keep;
  asm volatile("s_mov_b32 %0, m0\n\ts_mov_b32 m0, %3\n\ts_nop 0\n\tglobal_load_lds_dwordx4 %1, %2\n\ts_mov_b32 m0, %0":"=&s"(keep):"v"(voff),"s"(sb),"s"((u32)(uintptr_t)l):"memory"); }
#define DMA4_BODY(SC) asm volatile("s_mov_b32 %0, m0\n\ts_mov_b32 m0, %6\n\ts_nop 0\n\tglobal_load_lds_dwordx4 %1, %5" SC "\n\tglobal_load_lds_dwordx4 %2, %5 offset:1024" SC \
    "\n\tglobal_load_lds_dwordx4 %3, %5 offset:2048" SC "\n\tglobal_load_lds_dwordx4 %4, %5 offset:3072" SC "\n\ts_mov_b32 m0, %0" \
    :"=&s"(keep):"v"(v0),"v"(v1),"v"(v2),"v"(v3),"s"(sb),"s"((u32)(uintptr_t)l):"memory")
DI void dma16x4(const void* sb,u32 v0,u32 v1,u32 v2,u32 v3,lptr l){ u32 keep; DMA4_BODY(""); }
DI void dma16x4_sc1(const void* sb,u32 v0,u32 v1,u32 v2,u32 v3,lptr l){ u32 keep; DMA4_BODY(" sc1"); }
template<int N> DI void dma16xn(const void* sb,u32 v0,u32 v1,u32 v2,lptr l){ u32 keep;
  if(N==2) asm volatile("s_mov_b32 %0, m0\n\ts_mov_b32 m0, %4\n\ts_nop 0\n\tglobal_load_lds_dwordx4 %1, %3\n\tglobal_load_lds_dwordx4 %2, %3 offset:1024\n\ts_mov_b32 m0, %0"
    :"=&s"(keep):"v"(v0),"v"(v1),"s"(sb),"s"((u32)(uintptr_t)l):"memory");
  else asm volatile("s_mov_b32 %0, m0\n\ts_mov_b32 m0, %5\n\ts_nop 0\n\tglobal_load_lds_dwordx4 %1, %4\n\tglobal_load_lds_dwordx4 %2, %4 offset:1024\n\tglobal_load_lds_dwordx4 %3, %4 offset:2048\n\ts_mov_b32 m0, %0"
    :"=&s"(keep):"v"(v0),"v"(v1),"v"(v2),"s"(sb),"s"((u32)(uintptr_t)l):"memory"); }
DI u32x4 ld_sc1(rsrc_t rs,u32 off){ return __builtin_amdgcn_raw_buffer_load_b128(rs,off,0,16); }
DI void st_sc1(rsrc_t rs,u32 off,u32x4 v){ __builtin_amdgcn_raw_buffer_store_b128(v,rs,off,0,16); }
DI void gather16(rsrc_t rs,u32 off,u32x4* v){
  for(int it=0;it<SPIN_MAX;++it){
    u32 todo=0;
    #pragma unroll
    for(int k=0;k<4;++k){ u32 m=0;
      #pragma unroll
      for(int i=0;i<4;++i) m=max(max(m,max(v[4*k+i].x,v[4*k+i].y)),max(v[4*k+i].z,v[4*k+i].w));
      if(__any(m==SENT)) todo|=1u<<k; }
    if(!todo) break;
    asm volatile("":::"memory");
    #pragma unroll
    for(int k=0;k<4;++k) if(todo>>k&1){
      #pragma unroll
      for(int i=0;i<4;++i) v[4*k+i]=ld_sc1(rs,off+(4*k+i)*3584); } } }
DI void flag_set(void* ws,int i,u64 E){ __hip_atomic_store((GAS u64*)((char*)ws+WS_FLAG)+i,E,__ATOMIC_RELAXED,__HIP_MEMORY_SCOPE_AGENT); }
DI u64 flag_get(const void* ws,int i){ return __hip_atomic_load((const GAS u64*)((const char*)ws+WS_FLAG)+i,__ATOMIC_RELAXED,__HIP_MEMORY_SCOPE_AGENT); }
DI void flag_wait(const void* ws,u64 E,int idx,u64 f){
  for(int it=0;it<SPIN_MAX;++it){ if(__all(f==E)) break; __builtin_amdgcn_s_sleep(2); f=flag_get(ws,idx); } }

constexpr int A_UB=4576, A_US=9120, A_KB=4224, A_KS=8704;

DI f32x2 cmul(f32x2 a, f32x2 b){ f32x2 sw={-b.y,b.x}; return __builtin_elementwise_fma(a.yy,sw,a.xx*b); }
DI f32x2 cexp_e(float re2, float rev, float e){
  float m=__builtin_amdgcn_exp2f(re2*e); float a=__builtin_amdgcn_fractf(rev*e);
  f32x2 r={m*__builtin_amdgcn_cosf(a), m*__builtin_amdgcn_sinf(a)}; return r; }
struct SsmP { u32 are,aim,bre,bim,cre,cim,ld; float dt; };
struct SsmIn { const u16 *are,*aim,*bre,*bim,*cre,*cim,*logdt; };
DI SsmP load_ssm(const SsmIn& in, int ch, int lane){
  SsmP p; int o=ch*64+lane;
  p.are=((const u32*)in.are)[o]; p.aim=((const u32*)in.aim)[o]; p.bre=((const u32*)in.bre)[o];
  p.bim=((const u32*)in.bim)[o]; p.cre=((const u32*)in.cre)[o]; p.cim=((const u32*)in.cim)[o];
  p.ld=in.logdt[ch];
  return p; }
DI f32x2 ssm_w(const SsmP& p, int half){
  f32x2 b={half?bfhi(p.bre):bflo(p.bre), half?bfhi(p.bim):bflo(p.bim)};
  f32x2 c={half?bfhi(p.cre):bflo(p.cre), half?bfhi(p.cim):bflo(p.cim)};
  return cmul(c,b*p.dt); }
DI void ssm_prep(SsmP& p, float& s, float& inv){
  asm volatile("":"+v"(p.ld));
  p.dt=rbf(expf(bflo(p.ld)));
  f32x2 w0=ssm_w(p,0), w1=ssm_w(p,1);
  float m=wave_max(fmaxf(fmaxf(fabsf(w0.x),fabsf(w0.y)),fmaxf(fabsf(w1.x),fabsf(w1.y))));
  int e=__builtin_amdgcn_frexp_expf(m); e=max(-100,min(100,e));
  s=__builtin_amdgcn_ldexpf(1.0f,-e); inv=__builtin_amdgcn_ldexpf(1.0f,e); }
DI void kgen_half(const SsmP& p, int half, float s, lptr tab, int lane, f32x16& acc){
  const float are=(half?bfhi(p.are):bflo(p.are))*p.dt, aim=(half?bfhi(p.aim):bflo(p.aim))*p.dt;
  const float re2=are*1.4426950408889634f, rev=aim*0.15915494309189535f;
  lptr wr=tab+lane*4; lptr rd=tab+(lane&31)*272+(lane>>5)*16;
  { f32x2 e8=cexp_e(re2,rev,8.f), e16=cmul(e8,e8);
    f32x2 a={1.f,0.f}, b=e8;
    #pragma unroll
    for(int j=0;j<32;j+=2){ lds_st<u32>(wr+j*272,pk_f16(a)); lds_st<u32>(wr+(j+1)*272,pk_f16(b)); if(j<30){ a=(j==0)?e16:cmul(a,e16); b=cmul(b,e16);} } }
  u32x4 bf[8];
  #pragma unroll
  for(int k=0;k<8;++k) bf[k]=lds_ld<u32x4>(rd+k*32);
  { f32x2 w=ssm_w(p,half)*s; w.y=-w.y;
    f32x2 e1=cexp_e(re2,-rev,1.f), g=cexp_e(re2,-rev,256.f);
    f32x2 g2=cmul(g,g), g3=cmul(g2,g), g4=cmul(g2,g2);
    f32x2 x[8]; x[0]=w;
    #pragma unroll
    for(int i=1;i<8;++i) x[i]=cmul(x[i-1],e1);
    #pragma unroll
    for(int i=0;i<8;++i) lds_st<u32>(wr+i*272,pk_f16(x[i]));
    #pragma unroll
    for(int i=0;i<4;++i) lds_st<u32>(wr+(8+i)*272,pk_f16(cmul(x[2*i],g)));
    lds_st<u32>(wr+12*272,pk_f16(cmul(x[0],g2))); lds_st<u32>(wr+13*272,pk_f16(cmul(x[4],g2)));
    lds_st<u32>(wr+14*272,pk_f16(cmul(x[0],g3))); lds_st<u32>(wr+15*272,pk_f16(cmul(x[4],g3)));
    f32x2 y=cmul(w,g4);
    #pragma unroll
    for(int i=0;i<5;++i){ lds_st<u32>(wr+(16+i)*272,pk_f16(y)); if(i<4) y=cmul(y,g); } }
  #pragma unroll
  for(int k=0;k<8;++k){ u32x4 af=lds_ld<u32x4>(rd+k*32);
    acc=__builtin_amdgcn_mfma_f32_32x32x16_f16(__builtin_bit_cast(f16x8,af),__builtin_bit_cast(f16x8,bf[k]),acc,0,0,0); }
}
DI void k_finish(const float* v, lptr ka, int lane){
  lptr ks=ka; lptr kb=ka+A_KB;
  { const int j=lane&31, h=lane>>5;
    lds_st<u32x2>(ks+(8*j+4*h)*2,u32x2{pk_bf16(v[0],v[1]),pk_bf16(v[2],v[3])});
    u32x2 b={pk_bf16(v[4],v[5]),pk_bf16(v[6],v[7])};
    u32 c0=pk_bf16(v[8],v[9]), c1=pk_bf16(v[10],v[11]);
    if(h==0){ lds_st<u32x2>(ks+(256+4*j)*2,b);
      lds_st<u16>(ks+(512+j)*2,(u16)c0); lds_st<u16>(ks+(544+j)*2,(u16)(c0>>16));
      lds_st<u16>(ks+(576+j)*2,(u16)c1); lds_st<u16>(ks+(608+j)*2,(u16)(c1>>16));
    } else { lds_st<u32>(ks+(384+2*j)*2,b.x); lds_st<u32>(ks+(448+2*j)*2,b.y); if(j==0) lds_st<u16>(ks+640*2,(u16)c0); } }
  const int M=lane; const bool lo=M<32;
  u32x4 v0=lds_ld<u32x4>(ks+(M&31)*16); u32x2 v1=lds_ld<u32x2>(ks+(128+4*(M|32))*2);
  u32 v4=lds_ld<u16>(ks+(132+4*(M|32))*2);
  u32 pv=lds_ld<u16>(ks+(lo?max(8*M-1,0):((M==32)?255:(128+4*M)))*2); if(M==0) pv=0;
  u32 ab=lds_ld<u32>(ks+(384+2*M)*2), c2=lds_ld<u16>(ks+(386+2*M)*2);
  u32 a3[2],b3[2];
  #pragma unroll
  for(int i=0;i<2;++i){ a3[i]=lds_ld<u16>(ks+(512+M+64*i)*2); b3[i]=lds_ld<u16>(ks+(513+M+64*i)*2); }
  asm volatile("":"+v"(v0),"+v"(v1),"+v"(v4),"+v"(pv),"+v"(ab),"+v"(c2),"+v"(a3[0]),"+v"(a3[1]),"+v"(b3[0]),"+v"(b3[1])::"memory");
  { u32x4 A,B;
    if(lo){ B.x=p_hl(v0.w,v0.w); B.y=p_hl(v0.z,v0.z); B.z=p_hl(v0.y,v0.y); B.w=p_hl(v0.x,v0.x);
      A.x=p_lh(v0.w,v0.z); A.y=p_lh(v0.z,v0.y); A.z=p_lh(v0.y,v0.x); A.w=p_ll(v0.x,pv);
    } else { B.x=p_lh(v4,v1.y); B.y=p_ll(v1.y,v1.y); B.z=p_lh(v1.y,v1.x); B.w=p_ll(v1.x,v1.x);
      A.x=p_hl(v1.y,v1.y); A.y=B.y; A.z=p_hl(v1.x,v1.x); A.w=p_ll(v1.x,pv); }
    lds_st<u32x4>(ka+(255-M)*16,A); lds_st<u32x4>(kb+(255-M)*16,B); }
  { u32x4 A,B; B.x=p_ll(c2,c2); B.y=p_hh(ab,ab); B.z=p_hl(ab,ab); B.w=p_ll(ab,ab);
    A.x=p_lh(c2,ab); A.y=B.y; A.z=B.w; A.w=B.w;
    lds_st<u32x4>(ka+(191-M)*16,A); lds_st<u32x4>(kb+(191-M)*16,B); }
  #pragma unroll
  for(int i=0;i<2;++i){ u32 a=a3[i], b=b3[i], c=(M&1)?b:a; u32x4 A,B;
    B.x=p_ll(b,b); B.y=p_ll(b,c); B.z=p_ll(a,a); B.w=B.z;
    A.x=B.x; A.y=p_ll(c,a); A.z=B.z; A.w=B.z;
    lds_st<u32x4>(ka+(127-64*i-M)*16,A); lds_st<u32x4>(kb+(127-64*i-M)*16,B); }
  if(lane<8) lds_st<u32x4>(((lane&4)?kb:ka)+(256+(lane&3))*16,u32x4{0,0,0,0});
}
DI void conv_steps(lptr ua, lptr ka, int lane, f32x4* acc){
  constexpr int NS=8, s0=0, kbo=A_KB;
  const int a=lane&15, g=lane>>4;
  lptr up=ua+((a&1)?0:A_UB)+(32*(a>>1)+8*g+32*s0)*2;
  lptr kp=ka+((a&1)?kbo:0)+(28-4*(a>>1)+8*g+32*s0)*2;
  u32x4 A[2][8],B[2][8];
  auto ldA=[&](int s,int q){ A[s&1][q]=cat(lds_ld64(kp+(1792-256*q+32*s)*2),lds_ld64(kp+(1792-256*q+32*s)*2+8)); };
  auto ldB=[&](int s,int q){ B[s&1][q]=lds_ld<u32x4>(up+(256*q+32*s)*2); };
  #pragma unroll
  for(int q=0;q<8;++q){ ldB(0,q); ldA(0,q); }
  #pragma unroll
  for(int c=0;c<8;++c) acc[c]=Z4;
  SB();
  #pragma unroll
  for(int s=0;s<NS;++s){ int n=0; u32x2 lo;
    #pragma unroll
    for(int q=0;q<8;++q){
      #pragma unroll
      for(int c=q;c<8;++c){
        acc[c]=mfma16(A[s&1][q],B[s&1][c-q],acc[c]);
        SB();
        if(s+1<NS){
          if(n==0) ldB(s+1,0);
          else if(n<3){ if(n==1) lo=lds_ld64(kp+(1792+32*(s+1))*2); else A[(s+1)&1][0]=cat(lo,lds_ld64(kp+(1792+32*(s+1))*2+8)); }
          else if(n<10) ldB(s+1,n-2);
          else if(n<24){ const int j=1+((n-10)>>1); if(!(n&1)) lo=lds_ld64(kp+(1792-256*j+32*(s+1))*2); else A[(s+1)&1][j]=cat(lo,lds_ld64(kp+(1792-256*j+32*(s+1))*2+8)); } }
        SB();
        ++n; } } }
}
DI u32 fin2(float y0,float y1,u32 u,float dsk){
  u32 y=pk_bf16(y0,y1), s=pk_bf16(bflo(u)*dsk,bfhi(u)*dsk);
  return pk_bf16(bflo(y)+bflo(s),bfhi(y)+bfhi(s)); }
DI void u_scatter(const bool BIG, int ur, lptr L, const u32x4* um){
  #pragma unroll
  for(int c=0;c<4;++c){
    lptr d=L+(224+4*ur)*2+(BIG?2*c*A_US:(2*(c&1)*A_US+(c>>1)*2048)); const bool last=BIG?(ur==511):(c>=2&&ur==255);
    const u32x2 e={p_ll(um[0][c],um[1][c]),p_ll(um[2][c],um[3][c])}, o={p_hh(um[0][c],um[1][c]),p_hh(um[2][c],um[3][c])};
    lds_st<u32x2>(d,e); lds_st<u32x2>(d+A_US,o);
    lds_st<u32>(d+A_UB+4,e.x); lds_st<u32>(d+A_US+A_UB+4,o.x);
    if(!last){ lds_st<u32>(d+A_UB+8,e.y); lds_st<u32>(d+A_US+A_UB+8,o.y); } } }
DI void conv_job(const bool BIG, int wi, int ch0, int rot, lptr L, SsmP& p0, u32 dsk, const u16* u, u16* out, void* ws){
  const int lane=threadIdx.x&63;
  lptr us=L+wi*A_US, kw=L+(BIG?8:4)*A_US+wi*A_KS;
  if(lane<56) lds_st<u32x4>(us+(lane<28?lane*16:A_UB+(lane-28)*16),u32x4{0,0,0,0});
  if(lane==56) lds_st<u32>(us+A_UB+448,0u);
  u32x4 um[4];
  const int ur=(64*(wi+rot)+lane)&511;
  ST(1);
  { float s,inv; ssm_prep(p0,s,inv); f32x16 acc={};
    #pragma unroll 1
    for(int h=0;h<2;++h){
      if(BIG&&h==1){ const char* up=(const char*)u+(size_t)ur*4*1280+2*ch0;
        #pragma unroll
        for(int r=0;r<4;++r) um[r]=*(const u32x4*)(up+r*1280); }
      kgen_half(p0,h,s,kw,lane,acc); }
    float v[12];
    #pragma unroll
    for(int r=0;r<12;++r) v[r]=acc[r]*inv;
    k_finish(v,kw,lane); }
  ST(2);
  if(BIG) u_scatter(true,ur,L,um);
  bar();
  ST(3);
  f32x4 acc[8];
  conv_steps(us,kw,lane,acc);
  ST(4);
  { const int tl=32*((lane&15)>>1)+8*(lane>>4)+2*(lane&1); asm volatile("":"+v"(dsk)); const float d=bflo(dsk);
    u32 uu[16];
    #pragma unroll
    for(int c=0;c<8;++c){ uu[2*c]=lds_ld<u32>(us+(224+256*c+tl)*2); uu[2*c+1]=lds_ld<u32>(us+(228+256*c+tl)*2); }
    #pragma unroll
    for(int c=0;c<8;++c){ lds_st<u32>(kw+(256*c+tl)*2,fin2(acc[c][0],acc[c][1],uu[2*c],d)); lds_st<u32>(kw+(256*c+tl+4)*2,fin2(acc[c][2],acc[c][3],uu[2*c+1],d)); } }
  bar();
  ST(5);
  { lptr yp=L+(BIG?8:4)*A_US+((lane&15)>>2)*A_KS+(256*wi+16*(lane>>4)+4*(lane&3))*2;
    char* op=(char*)out+(size_t)(256*wi+lane)*2560+2*ch0;
    #pragma unroll
    for(int i=0;i<4;++i){
      if(BIG) *(u32x4*)(op+(size_t)i*64*2560)=cat(lds_tr(yp+i*128),lds_tr(yp+4*A_KS+i*128));
      else { *(u32x2*)(op+(size_t)i*64*2560)=lds_tr(yp+i*128); *(u32x2*)(op+(size_t)(i*64+1024)*2560)=lds_tr(yp+2048+i*128); } } }
  if(WBSEL){
    waitv<0>(); rbar();
    if(wi==0) asm volatile("buffer_wbl2 sc1\n\ts_waitcnt vmcnt(0)":::"memory"); }
  ST(7);
}

constexpr int B_ST=36864, B_PV=2*B_ST, B_PVS=18432, B_WP=4*B_ST, B_Z=B_WP+6400, B_KT=159744, B_F=163168;
DI float max3(float a,float b,float c){ return __builtin_fmaxf(__builtin_fmaxf(a,b),c); }
template<class H> DI void softmax20x2(const f32x4 (*z)[5], u32 (*o)[10], H hook){
  const float c=1.4426950408889634f; float mc[2];
  { float m0[2],m1[2],m2[2];
    #pragma unroll
    for(int t=0;t<2;++t){ m0[t]=max3(z[t][0][0],z[t][0][1],z[t][0][2]); m1[t]=max3(z[t][0][3],z[t][1][0],z[t][1][1]); m2[t]=max3(z[t][1][2],z[t][1][3],z[t][2][0]); }
    #pragma unroll
    for(int t=0;t<2;++t){ m0[t]=max3(m0[t],z[t][2][1],z[t][2][2]); m1[t]=max3(m1[t],z[t][2][3],z[t][3][0]); m2[t]=max3(m2[t],z[t][3][1],z[t][3][2]); }
    #pragma unroll
    for(int t=0;t<2;++t){ m0[t]=max3(m0[t],z[t][3][3],z[t][4][0]); m1[t]=max3(m1[t],z[t][4][1],z[t][4][2]); m2[t]=__builtin_fmaxf(m2[t],z[t][4][3]); }
    #pragma unroll
    for(int t=0;t<2;++t) mc[t]=max3(m0[t],m1[t],m2[t])*-c; }
  SB();
  f32x2 e[2][10];
  #pragma unroll
  for(int i=0;i<10;++i)
    #pragma unroll
    for(int t=0;t<2;++t) e[t][i]=__builtin_elementwise_fma(f32x2{z[t][i>>1][(i&1)*2],z[t][i>>1][(i&1)*2+1]},f32x2{c,c},f32x2{mc[t],mc[t]});
  SB();
  #pragma unroll
  for(int i=0;i<10;++i)
    #pragma unroll
    for(int t=0;t<2;++t){ hook(2*i+t); e[t][i]=f32x2{__builtin_amdgcn_exp2f(e[t][i].x),__builtin_amdgcn_exp2f(e[t][i].y)}; SB(); }
  float r[2];
  { f32x2 s[2][5];
    #pragma unroll
    for(int i=0;i<5;++i)
      #pragma unroll
      for(int t=0;t<2;++t) s[t][i]=e[t][2*i]+e[t][2*i+1];
    #pragma unroll
    for(int t=0;t<2;++t){ s[t][0]+=s[t][1]; s[t][2]+=s[t][3]; }
    #pragma unroll
    for(int t=0;t<2;++t){ s[t][0]+=s[t][4]; }
    #pragma unroll
    for(int t=0;t<2;++t){ s[t][0]+=s[t][2]; }
    #pragma unroll
    for(int t=0;t<2;++t) r[t]=__builtin_amdgcn_rcpf(s[t][0].x+s[t][0].y); }
  SB();
  #pragma unroll
  for(int i=0;i<10;++i)
    #pragma unroll
    for(int t=0;t<2;++t) e[t][i]*=r[t];
  SB();
  #pragma unroll
  for(int i=0;i<10;++i)
    #pragma unroll
    for(int t=0;t<2;++t) o[t][i]=pk_bf16(e[t][i].x,e[t][i].y);
  SB();
}
DI u32x4 pack2(f32x4 a,f32x4 b){ u32x4 r={pk_bf16(a[0],a[1]),pk_bf16(a[2],a[3]),pk_bf16(b[0],b[1]),pk_bf16(b[2],b[3])}; return r; }
struct Stream { u32 xo[4], wv[5]; int s0, qw; const u16 *x, *w; lptr L;
  DI void init(int q,int li,int rt,int grp,int qw_,int lane,const u16* x_,const u16* w_,lptr L_){
    qw=qw_; x=x_; w=w_; L=L_;
    s0=((0x8530>>((((li>>(2-q))+((li&(3>>q))<<q))&3)*4))&15)+q;
    #pragma unroll
    for(int i=0;i<4;++i){ const u32 r=(4*qw+i)*8+(lane>>3); xo[i]=(rt*128+r)*1280u+(((lane&7)^((r>>1)&7))<<4)-1024u*i; }
    dma16x4((const char*)x+(s0>=10?s0-10:s0)*128,xo[0],xo[1],xo[2],xo[3],L+4*qw*1024);
    const u32 wlo=(160<<q)*grp+1280*(1-q), dh=1120*(1-q);
    #pragma unroll
    for(int i=0;i<5;++i){ const u32 c=(5*qw+i)*64+lane, r=(c*3277u)>>16, pos=c-r*20, sc=pos^(((r>>3)&1)<<1);
      wv[i]=r*3840u+wlo+(sc<10?0u:dh)+16*sc-1024u*(i&3); } }
  DI void issue(int s,bool withx=true) const { int ss=s+s0; if(ss>=10) ss-=10; lptr d=L+(s&3)*B_ST;
    const char* xb=(const char*)x+ss*128; const char* wb=(const char*)w+(size_t)ss*64*3840;
    if(withx) dma16x4(xb,xo[0],xo[1],xo[2],xo[3],d+4*qw*1024);
    dma16x4(wb,wv[0],wv[1],wv[2],wv[3],d+16384+5*qw*1024); dma16(wb,wv[4],d+16384+(5*qw+4)*1024); } };

template<bool GATHER> DI void o_phase(lptr L,rsrc_t rs,int w,int oh,int tq){
  const int lane=threadIdx.x&63, g=lane>>4, r=lane&15;
  if(GATHER){ *(volatile LAS u32*)(L+B_F+128+4*w)=1u; }
  else for(int it=0;it<SPIN_MAX;++it){ if(__builtin_amdgcn_readfirstlane(*(volatile LAS u32*)(L+B_F+128+4*(w&3)))) break; __builtin_amdgcn_s_sleep(2); }
  asm volatile("":::"memory");
  u32x4 l0[2],l1[2]; u32x2 l2[2];
  { const u32 qo=WS_Q+(oh*2048+512*tq+128*(w&3)+64*(w>>2)+16*(g>>1)+r)*40;
    #pragma unroll
    for(int i=0;i<2;++i){ l0[i]=ld_sc1(rs,qo+i*1280); l1[i]=ld_sc1(rs,qo+i*1280+16); l2[i]=__builtin_amdgcn_raw_buffer_load_b64(rs,qo+i*1280+32,0,16); } }
  lptr kt=L+B_KT, q00,q01,q10,q11; u32 om,ox;
  { const int pc=5*(g&1)+2*(g>>1), pl=5*(g&1)+4, d1=min(r,4); lptr b=kt;
    q00=b+(16*pc+r)*8; q01=b+(16*pl+r)*8+4*(g>>1); q10=b+(160+5*pc+d1)*8; q11=b+(160+5*pl+d1)*8+4*(g>>1);
    const u32 fb=WS_O+((32*tq+8*(w&3)+4*(w>>2))*20+5*(oh>>3))*1024+r*16;
    om=fb+((oh&7)>>1)*1024+(2*(oh&1)+(g>>1))*256+8*(g&1); ox=(g==0)?(fb+4096+((oh&7)>>1)*256+8*(oh&1)):OOB; }
  asm volatile("":"+v"(q00),"+v"(q01),"+v"(q10),"+v"(q11),"+v"(om),"+v"(ox));
  ST(11);
  u32x4 v[16]; const u32 go=(lane<53&&53*w+lane<210)?(WS_P1+oh*16*3584+(53*w+lane)*16):OOB;
  u32 pq[2][10];
  { const u32 sg=(g&1)?0x80008000u:0u; f32x4 z[2][5];
    #pragma unroll
    for(int t=0;t<2;++t){ const u32x4 a=l0[t]^sg, b=l1[t]^sg; const u32x2 c=l2[t]^sg;
      z[t][0]=f32x4{bflo(a.x),bfhi(a.x),bflo(a.y),bfhi(a.y)}; z[t][1]=f32x4{bflo(a.z),bfhi(a.z),bflo(a.w),bfhi(a.w)};
      z[t][2]=f32x4{bflo(b.x),bfhi(b.x),bflo(b.y),bfhi(b.y)}; z[t][3]=f32x4{bflo(b.z),bfhi(b.z),bflo(b.w),bfhi(b.w)};
      z[t][4]=f32x4{bflo(c.x),bfhi(c.x),bflo(c.y),bfhi(c.y)}; }
    SB();
    softmax20x2(z,pq,[&](int k){ if(GATHER&&k<16) v[k]=ld_sc1(rs,go+k*3584); });
    #pragma unroll
    for(int i=0;i<2;++i){
      #pragma unroll
      for(int k=0;k<5;++k){ const int a=(k<4)?k:8, b=(k<4)?k+4:9;
        auto s=__builtin_amdgcn_permlane32_swap(pq[i][a],pq[i][b],false,false); pq[i][a]=s[0]; pq[i][b]=s[1]; }
      #pragma unroll
      for(int k=0;k<10;++k) asm volatile("":"+v"(pq[i][k])); } }
  ST(12);
  if(GATHER){
    gather16(rs,go,v);
    ST(13);
    f32x4 s[4];
    #pragma unroll
    for(int i=0;i<4;++i) s[i]=(__builtin_bit_cast(f32x4,v[4*i])+__builtin_bit_cast(f32x4,v[4*i+1]))+(__builtin_bit_cast(f32x4,v[4*i+2])+__builtin_bit_cast(f32x4,v[4*i+3]));
    s[0]=(s[0]+s[1])+(s[2]+s[3]);
    if(lane<53) *(volatile LAS u32x2*)(kt+(53*w+lane)*8)=u32x2{pk_bf16(s[0][0],s[0][1]),pk_bf16(s[0][2],s[0][3])};
    *(volatile LAS u32*)(L+B_F+32+4*w)=1u;
  }
  u32x4 a0[2]; u32 a1[2];
  for(int it=0;it<SPIN_MAX;++it){
    const u32x4 f=*(volatile LAS u32x4*)(L+B_F+32);
    a0[0]=cat(*(volatile LAS u32x2*)q00,*(volatile LAS u32x2*)(q00+128)); a0[1]=cat(*(volatile LAS u32x2*)q10,*(volatile LAS u32x2*)(q10+40));
    a1[0]=*(volatile LAS u32*)q01; a1[1]=*(volatile LAS u32*)q11;
    if(__builtin_amdgcn_readfirstlane(f.x+f.y+f.z+f.w)==4u) break;
    if(!GATHER) __builtin_amdgcn_s_sleep(1); }
  ST(14);
  f32x4 n[4][2];
  #pragma unroll
  for(int i=1;i>=0;--i){
    #pragma unroll
    for(int t=0;t<4;++t){ const u32* f=pq[t>>1]+4*(t&1); n[t][i]=mfma16(a0[i],u32x4{f[0],f[1],f[2],f[3]},Z4); }
    #pragma unroll
    for(int t=0;t<4;++t) n[t][i]=mfma16(u32x4{a1[i],0u,0u,0u},u32x4{pq[t>>1][8+(t&1)],0u,0u,0u},n[t][i]); }
  SB();
  float den[4];
  #pragma unroll
  for(int t=0;t<4;++t) den[t]=__builtin_bit_cast(float,__builtin_amdgcn_ds_bpermute((r+16)*4,__builtin_bit_cast(int,n[t][1][0])));
  #pragma unroll
  for(int t=0;t<4;++t) den[t]=__builtin_amdgcn_rcpf(rbf(rbf(den[t])+1e-6f));
  SB();
  #pragma unroll
  for(int t=0;t<4;++t){ const f32x4 m=n[t][0]*den[t], e=n[t][1]*den[t];
    __builtin_amdgcn_raw_buffer_store_b64(u32x2{pk_bf16(m[0],m[1]),pk_bf16(m[2],m[3])},rs,om+t*20480,0,16);
    __builtin_amdgcn_raw_buffer_store_b64(u32x2{pk_bf16(e[0],e[1]),pk_bf16(e[2],e[3])},rs,ox+t*20480,0,16); }
}

template<class F> DI bool role_b(const bool ISQ,int X,int li,int rt,int grp,int w,const Stream& st,lptr L,F params,const u16* u,const u16* wo,u16* out,void* ws){
  const int tid=threadIdx.x, lane=tid&63, g=lane>>4;
  const int T=ISQ?16+li:li; const bool doout=T<20; const int tt64=4*X+T/5, ct=T%5;
  const rsrc_t rs=__builtin_amdgcn_make_buffer_rsrc(ws,0,WS_END,0x00020000);
  constexpr int OIM=B_PV;
  u64 E;
  auto count=[&](int word){ asm volatile("":::"memory"); u32 n=0; if(lane==0) n=__hip_atomic_fetch_add((LAS u32*)(L+B_F+4*word),1u,__ATOMIC_RELAXED,__HIP_MEMORY_SCOPE_WORKGROUP); return n; };
  auto until=[&](int word,u32 n){ for(int it=0;it<SPIN_MAX;++it){ if(__builtin_amdgcn_readfirstlane(*(volatile LAS u32*)(L+B_F+4*word))>=n) break; __builtin_amdgcn_s_sleep(2); } asm volatile("":::"memory"); };
  auto arrive=[&](){
    if(__builtin_amdgcn_readfirstlane(count(4))==3u&&lane==0){ flag_set(ws,ISQ?QF(4*rt+grp):KF(grp,rt),E); asm volatile("":::"memory"); *(volatile LAS u32*)(L+B_F+84)=4u; } };
  volatile LAS u32* bad=(volatile LAS u32*)(L+B_F+20);
  const int oh=4*grp+(rt&3), tq=rt>>2;
  const int ot=4*tt64+(w&3);
  int fi=(lane<16)?KF(grp,lane):((lane<32)?KF(lane&7,4*tq+(w&3)):((lane<48)?KF(ot&7,ot>>3):QF(4*(4*tq+(w&3))+(oh>>3))));
  if(w<4){
    const int th=w>>1, hh=w&1;
    if(lane<4) lds_st<u32x4>(L+B_Z+lane*16,u32x4{0,0,0,0});
    if(tid<9) lds_st<u32x4>(L+B_F+tid*16,u32x4{0,0,0,0});
    lptr fad[3][2];
    { const int q=(lane&15)>>2, p=lane&3, hd=ISQ?p:(p&1);
      #pragma unroll
      for(int s=0;s<3;++s)
        #pragma unroll
        for(int t=0;t<2;++t){ const int d=16*(2*s+t)+4*g-20*hd; const bool ok=(d>=0)&&(d<20)&&(2*s+t<5);
          fad[s][t]=ok?(L+B_WP+((ISQ?4*hh:2*hh)+hd)*800+(d+q)*40+(ISQ?0:(p>>1)*3200)):(L+B_Z); if(2*s+t<5) asm volatile("":"+v"(fad[s][t])); } }
    lptr pv=L+B_PV+w*B_PVS;
    lptr pp=pv+(lane&15)*168+(g&1)*80+(g>>1)*40, vp2, vp3, vp4, pa, vb;
    { lptr vp=pv+10752+(lane&15)*104; vp2=vp+((g<2)?(48*g+40):(8*g-16)); vp3=vp+((g==3)?48:(16+8*g)); vp4=vp+56+8*g;
      const int tk=16*(g&1)+4*((lane&15)>>2)+2*(g>>1); pa=pv+tk*168+8*(lane&3)+(th^1)*80; vb=pv+10752+tk*104+8*(lane&3)+(th^1)*48; }
    u32 po0, po1;
    { const u32 po=WS_P1+((4*grp+2*hh+th)*16+rt)*3584; po0=po+lane*16; po1=((lane&15)<5)?(po+(160+5*g+(lane&15))*16):OOB; }
    asm volatile("":"+v"(fi),"+v"(pp),"+v"(vp2),"+v"(vp3),"+v"(vp4),"+v"(pa),"+v"(vb),"+v"(po0),"+v"(po1));
    lptr ab[5], bb[2];
    { const int q=(lane&15)>>2,p=lane&3;
      #pragma unroll
      for(int i=0;i<5;++i){ int n=2*i+(p>>1); int cid=ISQ?(10*hh+n):(5*hh+n+(n>=5?5:0)); ab[i]=L+16384+(8*g+q)*320+((cid^((g&1)<<1))<<4)+8*(p&1); }
      const int b0=(64*th+(lane&15))*128+((g^((lane&15)>>1))<<4); bb[0]=L+b0; bb[1]=L+(b0^64); }
    f32x4 acc[5][4];
    #pragma unroll
    for(int i=0;i<5;++i)
      #pragma unroll
      for(int t=0;t<4;++t){ acc[i][t]=Z4; asm volatile("":"+v"(acc[i][t])); }
    ST(1);
    u32x4 FA[2][5], FB[2][4], fa[2][5];
    u32x2 lo2;
    auto half=[&](int hf,bool mm,bool last,int step){
      u32x2 lo;
      #pragma unroll
      for(int n=0;n<20;++n){ const int i=n>>2, t=n&3;
        if(mm) acc[i][t]=mfma16(FA[hf^1][i],FB[hf^1][t],acc[i][t]);
        SB();
        if(n==0) FB[hf][0]=lds_ld<u32x4>(bb[hf]);
        else if(n==1) lo=lds_tr(ab[0]+hf*10240);
        else if(n==2) FA[hf][0]=cat(lo,lds_tr(ab[0]+hf*10240+1280));
        else if(n<6) FB[hf][n-2]=lds_ld<u32x4>(bb[hf]+2048*(n-2));
        else if(n<14){ const int j=1+((n-6)>>1); if(!(n&1)) lo=lds_tr(ab[j]+hf*10240); else FA[hf][j]=cat(lo,lds_tr(ab[j]+hf*10240+1280)); }
        else if(hf==1&&!last){ if(n<19){ ab[n-14]+=step; asm volatile("":"+v"(ab[n-14])); } if(n>=18){ bb[n-18]+=step; asm volatile("":"+v"(bb[n-18])); } }
        if(last){ const int j=n>>2, s=(n>>1)&1; if(!(n&1)) lo2=lds_tr(fad[s][0]+8*j); else fa[s][j]=cat(lo2,lds_tr(fad[s][1]+8*j)); }
        SB(); } };
    bar();
    ST(2);
    __builtin_amdgcn_s_waitcnt(0xC07F);
    SB();
    half(0,false,false,0);
    #pragma unroll 1
    for(u32 s=1;s<10;++s){
      half(1,true,false,(s&3)?B_ST:-3*B_ST);
      rbar();
      SB();
      half(0,true,false,0); }
    const u32x2 ev=*(volatile LAS u32x2*)(L+B_F+88);
    half(1,true,true,0);
    E=(u64)(u32)__builtin_amdgcn_readfirstlane(ev.x)|((u64)(u32)__builtin_amdgcn_readfirstlane(ev.y)<<32);
    asm volatile("s_waitcnt lgkmcnt(0)":::"memory");
    waitv<0>();
    count(7);
    #pragma unroll
    for(int t=0;t<4;++t)
      #pragma unroll
      for(int i=0;i<5;++i) acc[i][t]=mfma16(FA[1][i],FB[1][t],acc[i][t]);
    u32x2 pk[5][4];
    #pragma unroll
    for(int t=0;t<4;++t)
      #pragma unroll
      for(int i=0;i<5;++i) pk[i][t]=u32x2{pk_bf16(acc[i][t][0],acc[i][t][1]),pk_bf16(acc[i][t][2],acc[i][t][3])};
    ST(3);
    if(ISQ){
      u32x4 f2[5];
      #pragma unroll
      for(int i=0;i<5;++i) f2[i]=cat(lds_tr(fad[2][0]+8*i),u32x2{0u,0u});
      lptr sp_=pv+g*2560+(lane&15)*40;
      f32x4 z[4][5];
      #pragma unroll
      for(int t=0;t<4;++t)
        #pragma unroll
        for(int i=0;i<5;++i) z[t][i]=mfma16(fa[0][i],cat(pk[0][t],pk[1][t]),Z4);
      #pragma unroll
      for(int t=0;t<4;++t)
        #pragma unroll
        for(int i=0;i<5;++i) z[t][i]=mfma16(fa[1][i],cat(pk[2][t],pk[3][t]),z[t][i]);
      SB();
      #pragma unroll
      for(int t=0;t<5;++t){
        #pragma unroll
        for(int i=0;i<5;++i){ if(t<4) z[t][i]=mfma16(f2[i],cat(pk[4][t],u32x2{0u,0u}),z[t][i]);
          if(t>0) lds_st<u32x2>(sp_+(t-1)*640+8*i,u32x2{pk_bf16(z[t-1][i][0],z[t-1][i][1]),pk_bf16(z[t-1][i][2],z[t-1][i][3])}); }
        SB(); }
      ST(10);
      #pragma unroll
      for(int i=0;i<10;++i){ const int c=i*64+lane, hd=(c*205)>>15;
        st_sc1(rs,WS_Q+((8*grp+4*hh+hd)*2048+128*rt+64*th)*40+(c-160*hd)*16,lds_ld<u32x4>(pv+c*16)); }
      ST(11);
      waitv<0>(); ST(12); arrive();
      ST(4);
    } else {
      { f32x4 z[4][5]; u32 ph[4][10];
        auto fm=[&](int t){
          const u32x4 b0=cat(pk[0][t],pk[1][t]), b1=cat(pk[2][t],pk[3][t]);
          #pragma unroll
          for(int i=0;i<5;++i) z[t][i]=mfma16(fa[0][i],b0,Z4);
          #pragma unroll
          for(int i=0;i<5;++i) z[t][i]=mfma16(fa[1][i],b1,z[t][i]); };
        fm(0); fm(1);
        SB();
        softmax20x2(z,ph,[&](int k){ const int t=2+k/10, i=k%5;
          z[t][i]=(k%10<5)?mfma16(fa[0][i],cat(pk[0][t],pk[1][t]),Z4):mfma16(fa[1][i],cat(pk[2][t],pk[3][t]),z[t][i]); });
        auto wp=[&](int t){
          #pragma unroll
          for(int i=0;i<5;++i) lds_st<u32x2>(pp+t*2688+8*i,u32x2{ph[t][2*i],ph[t][2*i+1]}); };
        u32x2 v2[4];
        #pragma unroll
        for(int t=0;t<4;++t) v2[t]=(g<2)?u32x2{0x3F80u,0u}:pk[2][t];
        softmax20x2(z+2,ph+2,[&](int k){
          if(k<10) *(volatile LAS u32x2*)(pp+(k/5)*2688+8*(k%5))=u32x2{ph[k/5][2*(k%5)],ph[k/5][2*(k%5)+1]};
          else if(k<14) *(volatile LAS u32x2*)(vp2+(k-10)*1664)=v2[k-10];
          else if(k<18) *(volatile LAS u32x2*)(vp3+(k-14)*1664)=pk[3][k-14];
          else { *(volatile LAS u32x2*)(vp4+(2*k-36)*1664)=pk[4][2*k-36]; *(volatile LAS u32x2*)(vp4+(2*k-35)*1664)=pk[4][2*k-35]; } });
        wp(2); wp(3); }
      ST(4);
      u64 fl;
      { f32x4 c[2][6];
        #pragma unroll
        for(int h2=0;h2<2;++h2){
          if(h2){ pa+=th?80:-80; vb+=th?48:-48; }
          u32x4 a[2][3], b[2][2];
          #pragma unroll
          for(int ks=0;ks<2;++ks){
            #pragma unroll
            for(int i=0;i<3;++i) a[ks][i]=cat(lds_tr(pa+ks*32*168+32*i),lds_tr(pa+(ks*32+1)*168+32*i));
            #pragma unroll
            for(int dt=0;dt<2;++dt) b[ks][dt]=cat(lds_tr(vb+ks*32*104+32*dt),lds_tr(vb+(ks*32+1)*104+32*dt)); }
          #pragma unroll
          for(int i=0;i<6;++i) c[h2][i]=mfma16(a[0][i%3],b[0][i/3],Z4);
          #pragma unroll
          for(int i=0;i<6;++i) c[h2][i]=mfma16(a[1][i%3],b[1][i/3],c[h2][i]); }
        #pragma unroll
        for(int i=0;i<6;++i) *(volatile LAS f32x4*)(pv+i*1024+lane*16)=c[0][i];
        *(volatile LAS u32*)(L+B_F+4*w)=1u;
        #pragma unroll
        for(int i=0;i<6;++i) asm volatile("":"+v"(c[1][i]));
        lptr ox_=L+B_PV+(w^2)*B_PVS; f32x4 pr[6];
        for(int it=0;it<SPIN_MAX;++it){
          const u32 f=*(volatile LAS u32*)(L+B_F+4*(w^2)), n=*(volatile LAS u32*)(L+B_F+16);
          #pragma unroll
          for(int i=0;i<6;++i) pr[i]=*(volatile LAS f32x4*)(ox_+i*1024+lane*16);
          if(__builtin_amdgcn_readfirstlane(f+n)==5u) break; }
        fl=flag_get(ws,fi);
        #pragma unroll
        for(int i=0;i<6;++i){ u32 o=(i<3)?(po0+i*1024):(po1+(i-3)*320); if(i%3==2) o=(g<2)?o:OOB;
          st_sc1(rs,o,__builtin_bit_cast(u32x4,c[1][i]+pr[i])); } }
      ST(5);
      if(!__all(fl==E)) flag_wait(ws,E,fi,fl);
      asm volatile("":::"memory");
      o_phase<true>(L,rs,w,oh,tq);
      count(6);
      if(rt==15&&grp==7&&w==3){
        for(int it=0;it<SPIN_MAX;++it){ if(__all(flag_get(ws,KF(lane>>4,lane&15))==E&&flag_get(ws,KF(4+(lane>>4),lane&15))==E&&flag_get(ws,QF(lane))==E)) break; __builtin_amdgcn_s_sleep(2); }
        if(lane==0) __hip_atomic_store((GAS u64*)ws,E,__ATOMIC_RELAXED,__HIP_MEMORY_SCOPE_AGENT); }
      ST(6);
    }
    if(!doout){
      until(21,4u);
      const char* up=(const char*)u+(size_t)tid*4*1280+2*(80*X+64+4*(li-4)); u32x4 um[4];
      #pragma unroll
      for(int r=0;r<4;++r) um[r]=cat(*(const u32x2*)(up+r*1280),*(const u32x2*)(up+(r+1024)*1280));
      u_scatter(false,tid,L,um);
      asm volatile("s_waitcnt lgkmcnt(0)":::"memory");
      __builtin_amdgcn_s_waitcnt(0x0F70); return false; }
    until(20,4u);
    if(ISQ){ fi=KF(ot&7,ot>>3); flag_wait(ws,E,fi,flag_get(ws,fi)); }
    if(ISQ){
      const int hi=HF(4*X+li); flag_wait(ws,E,hi,flag_get(ws,hi)); }
    { const u32 ob=WS_O+ot*20480;
      const u32 sm=ob+((lane<32)?((5*(lane>>3)+((lane>>1)&3))*1024+(lane&1)*512+508):((5*((lane-32)>>3)+4)*1024+((lane&7)>>1)*256+244+8*(lane&1)));
      for(int it=0;it<SPIN_MAX;++it){ asm volatile("":::"memory");
        if(__all(__builtin_amdgcn_raw_buffer_load_b32(rs,sm,0,16)!=SENT)) break;
        __builtin_amdgcn_s_sleep(1); }
      ST(15);
      const u32 vo=lane*16;
      for(u32 it=1;it<SPIN_MAX;++it){
        #pragma unroll
        for(int i=0;i<5;++i) dma16x4_sc1((const char*)ws+ob+i*4096,vo,vo,vo,vo,L+OIM+w*20480+i*4096);
        waitv<12>();
        rbar();
        waitv<0>();
        ST(7);
        rbar();
        rbar();
        rbar();
        if(__builtin_amdgcn_readfirstlane(*bad)!=it) break; } }
    __builtin_amdgcn_s_waitcnt(0x0F70);
  } else {
    const int hw=w-4;
    asm volatile("s_load_dwordx2 %0, %1, 0x0":"=s"(E):"s"(ws):"memory");
    ST(9);
    const bool lead3=st.s0==0||st.s0==5||st.s0==1||st.s0==6;
    if(lead3){ st.issue(2); waitv<18>(); rbar(); } else { waitv<9>(); rbar(); st.issue(2); }
    ST(8);
    waitv<9>(); rbar();
    st.issue(3);
    asm volatile("s_waitcnt lgkmcnt(0)":"+s"(E)::"memory"); E+=1;
    if(tid==256) *(volatile LAS u32x2*)(L+B_F+88)=u32x2{(u32)E,(u32)(E>>32)};
    if(!ISQ&&lane<50){ lptr p=L+B_WP+hw*800+lane*16; lds_st<u32x4>(p+3200,lds_ld<u32x4>(p)^0x80008000u); }
    waitv<9>(); bar();
    if(!doout) params();
    st.issue(4);
    const int pr=hw>>1, me2=hw&1;
    u32 wq[40];
    { const int r=lane>>3; const u32 sw=((lane&7)^((((r>>1)&1)|(me2<<1))<<1))<<4, vo=r*1280+sw, vo2=((r>>2)*20+(r&3)+16+32*me2)*1280+sw;
      #pragma unroll
      for(int n=0;n<40;++n){ const int G=n/10, m=n%10, i=(n<22)?n:n-22, k=(i<16)?(i&3):((n>=18&&n<21)?n-18:((i<18)?i-16:0));
        wq[n]=((m<8)?vo+(160*G+20*m)*1280:vo2+(160*G+80*(m-8))*1280)-1024*k; asm volatile("":"+v"(wq[n])); } }
    lptr dst=L+hw*18432, rd, rd1; int x9, x10;
    { const int q=(lane&15)>>2, p=lane&3, f=((q>>1)|((g&1)<<1))<<1, ow=pr*2+(g&1);
      lptr rb=L+ow*18432+(g>>1)*1024+q*128+8*(p&1); rd=rb+(((4*me2+(p>>1))^f)<<4); rd1=rb+(((4*me2+2+(p>>1))^f)<<4);
      x9=B_WP+ow*3072-ow*18432; x10=(g>>1)?(B_PV+17408-1024):(x9+2048); }
    lptr ra9=rd+x9, rb9=rd1+x9, ra10=rd+x10, rb10=rd1+x10;
    asm volatile("":"+v"(rd),"+v"(rd1),"+v"(ra9),"+v"(rb9),"+v"(ra10),"+v"(rb10));
    waitv<9>(); rbar();
    #pragma unroll 1
    for(int s=4;s<9;++s){
      st.issue(s+1);
      waitv<9>(); rbar(); }
    waitv<0>(); rbar();
    if(!doout){ until(7,4u); return true; }
    if(!ISQ){
      const u32x4 sv={SENT,SENT,SENT,SENT};
      #pragma unroll
      for(int i=0;i<4;++i) st_sc1(rs,(i<3||lane<18)?(WS_P1+((4*grp+hw)*16+rt)*3584+i*1024+lane*16):OOB,sv);
      #pragma unroll
      for(int i=0;i<5;++i) st_sc1(rs,WS_O+((8*rt+grp)*20+5*hw+i)*1024+lane*16,sv);
      waitv<0>(); arrive(); }
    u32x4 wf[2][11];
    const char* gb=(const char*)wo+ct*256+pr*128+me2*8*1280;
    auto meet=[&](u32 n){ *(volatile LAS u32*)(L+B_F+48+4*hw)=n; until(12+(hw^1),n); };
    auto fetch18=[&](int n0){
      #pragma unroll
      for(int i=0;i<16;i+=4) dma16x4(gb,wq[n0+i],wq[n0+i+1],wq[n0+i+2],wq[n0+i+3],dst+i*1024);
      dma16xn<2>(gb,wq[n0+16],wq[n0+17],0,dst+16384); };
    until(ISQ?21:7,4u);
    ST(3);
    fetch18(0);
    dma16xn<3>(gb,wq[18],wq[19],wq[20],L+B_WP+hw*3072);
    dma16(gb,wq[21],dst+B_PV+17408);
    waitv<0>(); meet(1u);
    #pragma unroll
    for(int s=0;s<9;++s){ wf[0][s]=cat(lds_tr(rd+s*2048),lds_tr(rd+s*2048+512)); wf[1][s]=cat(lds_tr(rd1+s*2048),lds_tr(rd1+s*2048+512)); }
    wf[0][9]=cat(lds_tr(ra9),lds_tr(ra9+512)); wf[1][9]=cat(lds_tr(rb9),lds_tr(rb9+512));
    wf[0][10]=cat(lds_tr(ra10),lds_tr(ra10+512)); wf[1][10]=cat(lds_tr(rb10),lds_tr(rb10+512));
    asm volatile("s_waitcnt lgkmcnt(0)":::"memory");
    meet(2u);
    ST(10);
    #pragma unroll
    for(int i=0;i<8;i+=4) dma16x4(gb,wq[22+i],wq[23+i],wq[24+i],wq[25+i],dst+i*1024);
    if(!ISQ){ o_phase<false>(L,rs,w,oh,tq); count(6); ST(6); until(6,8u);
      if(li<4&&tid==256) flag_set(ws,HF(4*X+li),E); }
    #pragma unroll
    for(int i=8;i<16;i+=4) dma16x4(gb,wq[22+i],wq[23+i],wq[24+i],wq[25+i],dst+i*1024);
    dma16xn<2>(gb,wq[38],wq[39],0,dst+16384);
    count(20);
    ST(4);
    lptr obt[4];
    #pragma unroll
    for(int t=0;t<4;++t){ obt[t]=L+OIM+lane*16+((t+hw)&3)*20480; asm volatile("":"+v"(obt[t])); }
    waitv<0>();
    for(u32 it=1;it<SPIN_MAX;++it){
      rbar();
      ST(5);
      f32x4 c[2][4];
      #pragma unroll
      for(int t=0;t<4;++t){ c[0][t]=Z4; c[1][t]=Z4; }
      u32x4 ob[3][4], wl[3][2]; u32 chk=0;
      auto fetch=[&](int ks){
        #pragma unroll
        for(int t=0;t<4;++t) ob[ks%3][t]=lds_ld<u32x4>(obt[t]+ks*1024);
        if(ks>=11){ wl[ks%3][0]=cat(lds_tr(rd+(ks-11)*2048),lds_tr(rd+(ks-11)*2048+512)); wl[ks%3][1]=cat(lds_tr(rd1+(ks-11)*2048),lds_tr(rd1+(ks-11)*2048+512)); } };
      fetch(0); fetch(1);
      SB();
      #pragma unroll
      for(int ks=0;ks<20;++ks){ u32x2 lo;
        if(ks==6){ rbar(); SB(); }
        #pragma unroll
        for(int n=0;n<8;++n){ const int t=n>>1, cc=n&1, k2=ks+2;
          c[cc][t]=mfma16((ks<11)?wf[cc][ks<11?ks:0]:wl[ks%3][cc],ob[ks%3][t],c[cc][t]);
          SB();
          if(k2<20){
            if(n<4) ob[k2%3][n]=lds_ld<u32x4>(obt[n]+k2*1024);
            else if(k2>=11){ lptr p=((n<6)?rd:rd1)+(k2-11)*2048; if(!(n&1)) lo=lds_tr(p); else wl[k2%3][(n-4)>>1]=cat(lo,lds_tr(p+512)); } }
          if(n==6) chk=max(chk,max(ob[ks%3][0].x,ob[ks%3][0].y));
          if(n==7) chk=max(chk,max(ob[ks%3][0].z,ob[ks%3][0].w));
          SB(); } }
      if(__any(chk==SENT)) *bad=it;
      bar();
      #pragma unroll
      for(int t=0;t<4;++t)
        #pragma unroll
        for(int cc=0;cc<2;++cc) lds_st<u32x2>(L+OIM+(16*((t+hw)&3)+(lane&15))*272+hw*64+cc*32+8*g,u32x2{pk_bf16(c[cc][t][0],c[cc][t][1]),pk_bf16(c[cc][t][2],c[cc][t][3])});
      ST(7);
      bar();
      if(__builtin_amdgcn_readfirstlane(*bad)!=it) break; }
  }
  #pragma unroll
  for(int i=0;i<2;++i){ const int r=32*i+(tid>>4);
    __builtin_amdgcn_raw_buffer_store_b128(lds_ld<u32x4>(L+OIM+r*272+(tid&15)*16),__builtin_amdgcn_make_buffer_rsrc(out,0,2048*2560,0x00020000),(64*tt64+r)*2560+1280+ct*256+(tid&15)*16,0,16); }
  if(w<4) ST(8);
  return false;
}

constexpr int CODE_LINES=320;
__global__ __launch_bounds__(512,2) void fused(const u16* u,const u16* are,const u16* aim,const u16* bre,const u16* bim,const u16* cre,const u16* cim,
    const u16* logdt,const u16* dskip,const u16* x,const u16* wqkv,const u16* wphi,const u16* wo,u16* out,void* ws){
  extern __shared__ __attribute__((aligned(16))) char smem[];
  lptr L=(lptr)smem;
  const int b=blockIdx.x, X=b&7, idx=b>>3;
  u64 pc; asm volatile("s_getpc_b64 %0":"=s"(pc));
  const int w=__builtin_amdgcn_readfirstlane(threadIdx.x>>6);
  const bool isa=(idx>>3)==2;
  const int q=idx>=24, li=idx-24*q, rt=4*(X>>1)+(li>>(2-q)), grp=((X&1)<<(2-q))+(li&(3>>q));
  if(idx<16&&w<2){
    const u32 n=(w?(X*16+idx)*50:idx*32)+(threadIdx.x&63); u32 keep;
    if(w?((threadIdx.x&63)<50):(threadIdx.x<32&&n<CODE_LINES))
      asm volatile("s_mov_b32 %0, m0\n\ts_mov_b32 m0, %3\n\ts_nop 0\n\tglobal_load_lds_dword %1, %2\n\ts_mov_b32 m0, %0":"=&s"(keep):"v"(n*128),"s"(w?(u64)wo:(pc&~255ull)),"s"(162560+w*256):"memory"); }
  Stream st;
  if(__builtin_expect(!isa&&w>=4,1)){ st.init(q,li,rt,grp,w&3,threadIdx.x&63,x,wqkv,L); st.issue(0,false);
    { const u32 nb=800<<q, o=(threadIdx.x&63)*16; const char* wp=(const char*)wphi+(4*grp+(w&3))*nb;
      if(o<nb) dma16(wp,o,L+B_WP+(w&3)*nb);
      if(o+1024<nb) dma16(wp,o+1024,L+B_WP+(w&3)*nb+1024); }
    st.issue(1); }
  SB();
  STINIT();
  const int ch0=isa?(80*X+8*(idx-16)):(80*X+64+4*(idx-28)), wi=isa?w:w-4;
  SsmP sp; u32 dsk;
  auto params=[&](){ const SsmIn in={are,aim,bre,bim,cre,cim,logdt}; sp=load_ssm(in,ch0+wi,threadIdx.x&63); dsk=dskip[ch0+wi]; };
  bool job=true;
  if(__builtin_expect(!isa,1)) job=role_b(q,X,li,rt,grp,w,st,L,params,u,wo,out,ws);
  else params();
  if(job){
    conv_job(isa,wi,ch0,idx&7,L,sp,dsk,u,out,ws); }
  STDUMP();
}

extern "C" void kernel_launch(void* const* d_in, const int* in_sizes, int n_in, void* d_out, int out_size, void* d_ws, size_t ws_size, hipStream_t stream){
  (void)hipFuncSetAttribute((const void*)fused,hipFuncAttributeMaxDynamicSharedMemorySize,163840);
  fused<<<256,512,163840,stream>>>((const u16*)d_in[0],(const u16*)d_in[1],(const u16*)d_in[2],(const u16*)d_in[3],(const u16*)d_in[4],(const u16*)d_in[5],(const u16*)d_in[6],
    (const u16*)d_in[7],(const u16*)d_in[8],(const u16*)d_in[9],(const u16*)d_in[10],(const u16*)d_in[11],(const u16*)d_in[12],(u16*)d_out,d_ws);
}
```

```cpp
#include <hip/hip_runtime.h>
#include <stdint.h>

typedef unsigned short u16; typedef unsigned u32; typedef unsigned long long u64;
typedef __attribute__((ext_vector_type(4))) short s16x4;
typedef __attribute__((ext_vector_type(8))) _Float16 f16x8;
typedef __attribute__((ext_vector_type(8))) __bf16 bf16x8;
typedef __attribute__((ext_vector_type(2))) __bf16 bf16x2;
typedef __attribute__((ext_vector_type(2))) _Float16 f16x2;
typedef __attribute__((ext_vector_type(2))) float f32x2;
typedef __attribute__((ext_vector_type(4))) float f32x4;
typedef __attribute__((ext_vector_type(16))) float f32x16;
typedef __attribute__((ext_vector_type(2))) unsigned u32x2;
typedef __attribute__((ext_vector_type(3))) unsigned u32x3;
typedef __attribute__((ext_vector_type(4))) unsigned u32x4;
#define LAS __attribute__((address_space(3)))
#define GAS __attribute__((address_space(1)))
#define DI __device__ __forceinline__
typedef LAS char* lptr;
typedef __amdgpu_buffer_rsrc_t rsrc_t;

DI u32 pk_bf16(float a, float b){ f32x2 v={a,b}; return __builtin_bit_cast(u32,__builtin_convertvector(v,bf16x2)); }
DI u32 pk_f16(f32x2 v){ return __builtin_bit_cast(u32,__builtin_convertvector(v,f16x2)); }
DI float bflo(u32 x){ return __builtin_bit_cast(float,x<<16); }
DI float bfhi(u32 x){ return __builtin_bit_cast(float,x&0xffff0000u); }
DI float rbf(float a){ return bflo(pk_bf16(a,a)); }
template<class T> DI T lds_ld(lptr p){ return *(LAS T*)p; }
template<class T> DI void lds_st(lptr p, T v){ *(LAS T*)p=v; }
DI u32x2 lds_ld64(lptr p){ return *(volatile LAS u32x2*)p; }
DI u32x2 lds_tr(lptr p){ return __builtin_bit_cast(u32x2,__builtin_amdgcn_ds_read_tr16_b64_v4i16((LAS s16x4*)p)); }
DI f32x4 mfma16(u32x4 a,u32x4 b,f32x4 c){ return __builtin_amdgcn_mfma_f32_16x16x32_bf16(__builtin_bit_cast(bf16x8,a),__builtin_bit_cast(bf16x8,b),c,0,0,0); }
DI u32x4 cat(u32x2 a,u32x2 b){ u32x4 r={a.x,a.y,b.x,b.y}; return r; }
DI u32 p_ll(u32 a,u32 b){ return __builtin_amdgcn_perm(b,a,0x05040100u); }
DI u32 p_lh(u32 a,u32 b){ return __builtin_amdgcn_perm(b,a,0x07060100u); }
DI u32 p_hl(u32 a,u32 b){ return __builtin_amdgcn_perm(b,a,0x05040302u); }
DI u32 p_hh(u32 a,u32 b){ return __builtin_amdgcn_perm(b,a,0x07060302u); }
#define SB() __builtin_amdgcn_sched_barrier(0)
DI void rbar(){ asm volatile("":::"memory"); __builtin_amdgcn_s_barrier(); asm volatile("":::"memory"); }
DI void bar(){ asm volatile("s_waitcnt lgkmcnt(0)":::"memory"); __builtin_amdgcn_s_barrier(); asm volatile("":::"memory"); }
template<int N> DI void waitv(){ asm volatile("s_waitcnt vmcnt(%0)"::"i"(N):"memory"); }
constexpr f32x4 Z4={0.f,0.f,0.f,0.f};
DI float wave_max(float m){
  m=fmaxf(m,__builtin_bit_cast(float,__builtin_amdgcn_update_dpp(0,__builtin_bit_cast(int,m),0xB1,0xf,0xf,true)));
  m=fmaxf(m,__builtin_bit_cast(float,__builtin_amdgcn_update_dpp(0,__builtin_bit_cast(int,m),0x4E,0xf,0xf,true)));
  m=fmaxf(m,__builtin_bit_cast(float,__builtin_amdgcn_update_dpp(0,__builtin_bit_cast(int,m),0x141,0xf,0xf,true)));
  m=fmaxf(m,__builtin_bit_cast(float,__builtin_amdgcn_update_dpp(0,__builtin_bit_cast(int,m),0x140,0xf,0xf,true)));
  int x=__builtin_bit_cast(int,m);
  float a=__builtin_bit_cast(float,__builtin_amdgcn_readlane(x,0)), b=__builtin_bit_cast(float,__builtin_amdgcn_readlane(x,16));
  float c=__builtin_bit_cast(float,__builtin_amdgcn_readlane(x,32)), d=__builtin_bit_cast(float,__builtin_amdgcn_readlane(x,48));
  return fmaxf(fmaxf(a,b),fmaxf(c,d)); }

constexpr u32 WS_FLAG=256, WS_P1=32768, WS_Q=WS_P1+32*16*3584, WS_O=WS_Q+32*2048*40, WS_END=WS_O+128*20*1024;
DI int KF(int g,int r){ return 16*(16*g+r); }
DI int QF(int i){ return 16*(128+i); }
DI int HF(int i){ return 16*(192+i); }
constexpr u32 SENT=0xFFFFFFFFu, OOB=0x80000000u;
constexpr int SPIN_MAX=1<<17;
DI void dma16(const void* sb, u32 voff, lptr l){ u32 keep;
  asm volatile("s_mov_b32 %0, m0\n\ts_mov_b32 m0, %3\n\ts_nop 0\n\tglobal_load_lds_dwordx4 %1, %2\n\ts_mov_b32 m0, %0":"=&s"(keep):"v"(voff),"s"(sb),"s"((u32)(uintptr_t)l):"memory"); }
#define DMA4_BODY(SC) asm volatile("s_mov_b32 %0, m0\n\ts_mov_b32 m0, %6\n\ts_nop 0\n\tglobal_load_lds_dwordx4 %1, %5" SC "\n\tglobal_load_lds_dwordx4 %2, %5 offset:1024" SC \
    "\n\tglobal_load_lds_dwordx4 %3, %5 offset:2048" SC "\n\tglobal_load_lds_dwordx4 %4, %5 offset:3072" SC "\n\ts_mov_b32 m0, %0" \
    :"=&s"(keep):"v"(v0),"v"(v1),"v"(v2),"v"(v3),"s"(sb),"s"((u32)(uintptr_t)l):"memory")
DI void dma16x4(const void* sb,u32 v0,u32 v1,u32 v2,u32 v3,lptr l){ u32 keep; DMA4_BODY(""); }
DI void dma16x4_sc1(const void* sb,u32 v0,u32 v1,u32 v2,u32 v3,lptr l){ u32 keep; DMA4_BODY(" sc1"); }
template<int N> DI void dma16xn(const void* sb,u32 v0,u32 v1,u32 v2,lptr l){ u32 keep;
  if(N==2) asm volatile("s_mov_b32 %0, m0\n\ts_mov_b32 m0, %4\n\ts_nop 0\n\tglobal_load_lds_dwordx4 %1, %3\n\tglobal_load_lds_dwordx4 %2, %3 offset:1024\n\ts_mov_b32 m0, %0"
    :"=&s"(keep):"v"(v0),"v"(v1),"s"(sb),"s"((u32)(uintptr_t)l):"memory");
  else asm volatile("s_mov_b32 %0, m0\n\ts_mov_b32 m0, %5\n\ts_nop 0\n\tglobal_load_lds_dwordx4 %1, %4\n\tglobal_load_lds_dwordx4 %2, %4 offset:1024\n\tglobal_load_lds_dwordx4 %3, %4 offset:2048\n\ts_mov_b32 m0, %0"
    :"=&s"(keep):"v"(v0),"v"(v1),"v"(v2),"s"(sb),"s"((u32)(uintptr_t)l):"memory"); }
DI u32x4 ld_sc1(rsrc_t rs,u32 off){ return __builtin_amdgcn_raw_buffer_load_b128(rs,off,0,16); }
DI void st_sc1(rsrc_t rs,u32 off,u32x4 v){ __builtin_amdgcn_raw_buffer_store_b128(v,rs,off,0,16); }
DI void gather16(rsrc_t rs,u32 off,u32x4* v){
  for(int it=0;it<SPIN_MAX;++it){
    u32 todo=0;
    #pragma unroll
    for(int k=0;k<4;++k){ u32 m=0;
      #pragma unroll
      for(int i=0;i<4;++i) m=max(max(m,max(v[4*k+i].x,v[4*k+i].y)),max(v[4*k+i].z,v[4*k+i].w));
      if(__any(m==SENT)) todo|=1u<<k; }
    if(!todo) break;
    asm volatile("":::"memory");
    #pragma unroll
    for(int k=0;k<4;++k) if(todo>>k&1){
      #pragma unroll
      for(int i=0;i<4;++i) v[4*k+i]=ld_sc1(rs,off+(4*k+i)*3584); } } }
DI void flag_set(void* ws,int i,u64 E){ __hip_atomic_store((GAS u64*)((char*)ws+WS_FLAG)+i,E,__ATOMIC_RELAXED,__HIP_MEMORY_SCOPE_AGENT); }
DI u64 flag_get(const void* ws,int i){ return __hip_atomic_load((const GAS u64*)((const char*)ws+WS_FLAG)+i,__ATOMIC_RELAXED,__HIP_MEMORY_SCOPE_AGENT); }
DI void flag_wait(const void* ws,u64 E,int idx,u64 f){
  for(int it=0;it<SPIN_MAX;++it){ if(__all(f==E)) break; __builtin_amdgcn_s_sleep(2); f=flag_get(ws,idx); } }

constexpr int A_UB=4576, A_US=9120, A_KB=4224, A_KS=8704;

DI f32x2 cmul(f32x2 a, f32x2 b){ f32x2 sw={-b.y,b.x}; return __builtin_elementwise_fma(a.yy,sw,a.xx*b); }
DI f32x2 cexp_e(float re2, float rev, float e){
  float m=__builtin_amdgcn_exp2f(re2*e); float a=__builtin_amdgcn_fractf(rev*e);
  f32x2 r={m*__builtin_amdgcn_cosf(a), m*__builtin_amdgcn_sinf(a)}; return r; }
struct SsmP { u32 are,aim,bre,bim,cre,cim,ld; float dt; };
struct SsmIn { const u16 *are,*aim,*bre,*bim,*cre,*cim,*logdt; };
DI SsmP load_ssm(const SsmIn& in, int ch, int lane){
  SsmP p; int o=ch*64+lane;
  p.are=((const u32*)in.are)[o]; p.aim=((const u32*)in.aim)[o]; p.bre=((const u32*)in.bre)[o];
  p.bim=((const u32*)in.bim)[o]; p.cre=((const u32*)in.cre)[o]; p.cim=((const u32*)in.cim)[o];
  p.ld=in.logdt[ch];
  return p; }
DI f32x2 ssm_w(const SsmP& p, int half){
  f32x2 b={half?bfhi(p.bre):bflo(p.bre), half?bfhi(p.bim):bflo(p.bim)};
  f32x2 c={half?bfhi(p.cre):bflo(p.cre), half?bfhi(p.cim):bflo(p.cim)};
  return cmul(c,b*p.dt); }
DI void ssm_prep(SsmP& p, float& s, float& inv){
  asm volatile("":"+v"(p.ld));
  p.dt=rbf(expf(bflo(p.ld)));
  f32x2 w0=ssm_w(p,0), w1=ssm_w(p,1);
  float m=wave_max(fmaxf(fmaxf(fabsf(w0.x),fabsf(w0.y)),fmaxf(fabsf(w1.x),fabsf(w1.y))));
  int e=__builtin_amdgcn_frexp_expf(m); e=max(-100,min(100,e));
  s=__builtin_amdgcn_ldexpf(1.0f,-e); inv=__builtin_amdgcn_ldexpf(1.0f,e); }
DI void kgen_half(const SsmP& p, int half, float s, lptr tab, int lane, f32x16& acc){
  const float are=(half?bfhi(p.are):bflo(p.are))*p.dt, aim=(half?bfhi(p.aim):bflo(p.aim))*p.dt;
  const float re2=are*1.4426950408889634f, rev=aim*0.15915494309189535f;
  lptr wr=tab+lane*4; lptr rd=tab+(lane&31)*272+(lane>>5)*16;
  { f32x2 e8=cexp_e(re2,rev,8.f), e16=cmul(e8,e8);
    f32x2 a={1.f,0.f}, b=e8;
    #pragma unroll
    for(int j=0;j<32;j+=2){ lds_st<u32>(wr+j*272,pk_f16(a)); lds_st<u32>(wr+(j+1)*272,pk_f16(b)); if(j<30){ a=(j==0)?e16:cmul(a,e16); b=cmul(b,e16);} } }
  u32x4 bf[8];
  #pragma unroll
  for(int k=0;k<8;++k) bf[k]=lds_ld<u32x4>(rd+k*32);
  { f32x2 w=ssm_w(p,half)*s; w.y=-w.y;
    f32x2 e1=cexp_e(re2,-rev,1.f), g=cexp_e(re2,-rev,256.f);
    f32x2 g2=cmul(g,g), g3=cmul(g2,g), g4=cmul(g2,g2);
    f32x2 x[8]; x[0]=w;
    #pragma unroll
    for(int i=1;i<8;++i) x[i]=cmul(x[i-1],e1);
    #pragma unroll
    for(int i=0;i<8;++i) lds_st<u32>(wr+i*272,pk_f16(x[i]));
    #pragma unroll
    for(int i=0;i<4;++i) lds_st<u32>(wr+(8+i)*272,pk_f16(cmul(x[2*i],g)));
    lds_st<u32>(wr+12*272,pk_f16(cmul(x[0],g2))); lds_st<u32>(wr+13*272,pk_f16(cmul(x[4],g2)));
    lds_st<u32>(wr+14*272,pk_f16(cmul(x[0],g3))); lds_st<u32>(wr+15*272,pk_f16(cmul(x[4],g3)));
    f32x2 y=cmul(w,g4);
    #pragma unroll
    for(int i=0;i<5;++i){ lds_st<u32>(wr+(16+i)*272,pk_f16(y)); if(i<4) y=cmul(y,g); } }
  #pragma unroll
  for(int k=0;k<8;++k){ u32x4 af=lds_ld<u32x4>(rd+k*32);
    acc=__builtin_amdgcn_mfma_f32_32x32x16_f16(__builtin_bit_cast(f16x8,af),__builtin_bit_cast(f16x8,bf[k]),acc,0,0,0); }
}
DI void k_finish(const float* v, lptr ka, int lane){
  lptr ks=ka; lptr kb=ka+A_KB;
  { const int j=lane&31, h=lane>>5;
    lds_st<u32x2>(ks+(8*j+4*h)*2,u32x2{pk_bf16(v[0],v[1]),pk_bf16(v[2],v[3])});
    u32x2 b={pk_bf16(v[4],v[5]),pk_bf16(v[6],v[7])};
    u32 c0=pk_bf16(v[8],v[9]), c1=pk_bf16(v[10],v[11]);
    if(h==0){ lds_st<u32x2>(ks+(256+4*j)*2,b);
      lds_st<u16>(ks+(512+j)*2,(u16)c0); lds_st<u16>(ks+(544+j)*2,(u16)(c0>>16));
      lds_st<u16>(ks+(576+j)*2,(u16)c1); lds_st<u16>(ks+(608+j)*2,(u16)(c1>>16));
    } else { lds_st<u32>(ks+(384+2*j)*2,b.x); lds_st<u32>(ks+(448+2*j)*2,b.y); if(j==0) lds_st<u16>(ks+640*2,(u16)c0); } }
  const int M=lane; const bool lo=M<32;
  u32x4 v0=lds_ld<u32x4>(ks+(M&31)*16); u32x2 v1=lds_ld<u32x2>(ks+(128+4*(M|32))*2);
  u32 v4=lds_ld<u16>(ks+(132+4*(M|32))*2);
  u32 pv=lds_ld<u16>(ks+(lo?max(8*M-1,0):((M==32)?255:(128+4*M)))*2); if(M==0) pv=0;
  u32 ab=lds_ld<u32>(ks+(384+2*M)*2), c2=lds_ld<u16>(ks+(386+2*M)*2);
  u32 a3[2],b3[2];
  #pragma unroll
  for(int i=0;i<2;++i){ a3[i]=lds_ld<u16>(ks+(512+M+64*i)*2); b3[i]=lds_ld<u16>(ks+(513+M+64*i)*2); }
  asm volatile("":"+v"(v0),"+v"(v1),"+v"(v4),"+v"(pv),"+v"(ab),"+v"(c2),"+v"(a3[0]),"+v"(a3[1]),"+v"(b3[0]),"+v"(b3[1])::"memory");
  { u32x4 A,B;
    if(lo){ B.x=p_hl(v0.w,v0.w); B.y=p_hl(v0.z,v0.z); B.z=p_hl(v0.y,v0.y); B.w=p_hl(v0.x,v0.x);
      A.x=p_lh(v0.w,v0.z); A.y=p_lh(v0.z,v0.y); A.z=p_lh(v0.y,v0.x); A.w=p_ll(v0.x,pv);
    } else { B.x=p_lh(v4,v1.y); B.y=p_ll(v1.y,v1.y); B.z=p_lh(v1.y,v1.x); B.w=p_ll(v1.x,v1.x);
      A.x=p_hl(v1.y,v1.y); A.y=B.y; A.z=p_hl(v1.x,v1.x); A.w=p_ll(v1.x,pv); }
    lds_st<u32x4>(ka+(255-M)*16,A); lds_st<u32x4>(kb+(255-M)*16,B); }
  { u32x4 A,B; B.x=p_ll(c2,c2); B.y=p_hh(ab,ab); B.z=p_hl(ab,ab); B.w=p_ll(ab,ab);
    A.x=p_lh(c2,ab); A.y=B.y; A.z=B.w; A.w=B.w;
    lds_st<u32x4>(ka+(191-M)*16,A); lds_st<u32x4>(kb+(191-M)*16,B); }
  #pragma unroll
  for(int i=0;i<2;++i){ u32 a=a3[i], b=b3[i], c=(M&1)?b:a; u32x4 A,B;
    B.x=p_ll(b,b); B.y=p_ll(b,c); B.z=p_ll(a,a); B.w=B.z;
    A.x=B.x; A.y=p_ll(c,a); A.z=B.z; A.w=B.z;
    lds_st<u32x4>(ka+(127-64*i-M)*16,A); lds_st<u32x4>(kb+(127-64*i-M)*16,B); }
  if(lane<8) lds_st<u32x4>(((lane&4)?kb:ka)+(256+(lane&3))*16,u32x4{0,0,0,0});
}
DI void conv_steps(lptr ua, lptr ka, int lane, f32x4* acc){
  constexpr int NS=8, s0=0, kbo=A_KB;
  const int a=lane&15, g=lane>>4;
  lptr up=ua+((a&1)?0:A_UB)+(32*(a>>1)+8*g+32*s0)*2;
  lptr kp=ka+((a&1)?kbo:0)+(28-4*(a>>1)+8*g+32*s0)*2;
  u32x4 A[2][8],B[2][8];
  auto ldA=[&](int s,int q){ A[s&1][q]=cat(lds_ld64(kp+(1792-256*q+32*s)*2),lds_ld64(kp+(1792-256*q+32*s)*2+8)); };
  auto ldB=[&](int s,int q){ B[s&1][q]=lds_ld<u32x4>(up+(256*q+32*s)*2); };
  #pragma unroll
  for(int q=0;q<8;++q){ ldB(0,q); ldA(0,q); }
  #pragma unroll
  for(int c=0;c<8;++c) acc[c]=Z4;
  SB();
  #pragma unroll
  for(int s=0;s<NS;++s){ int n=0; u32x2 lo;
    #pragma unroll
    for(int q=0;q<8;++q){
      #pragma unroll
      for(int c=q;c<8;++c){
        acc[c]=mfma16(A[s&1][q],B[s&1][c-q],acc[c]);
        SB();
        if(s+1<NS){
          if(n==0) ldB(s+1,0);
          else if(n<3){ if(n==1) lo=lds_ld64(kp+(1792+32*(s+1))*2); else A[(s+1)&1][0]=cat(lo,lds_ld64(kp+(1792+32*(s+1))*2+8)); }
          else if(n<10) ldB(s+1,n-2);
          else if(n<24){ const int j=1+((n-10)>>1); if(!(n&1)) lo=lds_ld64(kp+(1792-256*j+32*(s+1))*2); else A[(s+1)&1][j]=cat(lo,lds_ld64(kp+(1792-256*j+32*(s+1))*2+8)); } }
        SB();
        ++n; } } }
}
DI u32 fin2(float y0,float y1,u32 u,float dsk){
  u32 y=pk_bf16(y0,y1), s=pk_bf16(bflo(u)*dsk,bfhi(u)*dsk);
  return pk_bf16(bflo(y)+bflo(s),bfhi(y)+bfhi(s)); }
DI void u_scatter(const bool BIG, int ur, lptr L, const u32x4* um){
  #pragma unroll
  for(int c=0;c<4;++c){
    lptr d=L+(224+4*ur)*2+(BIG?2*c*A_US:(2*(c&1)*A_US+(c>>1)*2048)); const bool last=BIG?(ur==511):(c>=2&&ur==255);
    const u32x2 e={p_ll(um[0][c],um[1][c]),p_ll(um[2][c],um[3][c])}, o={p_hh(um[0][c],um[1][c]),p_hh(um[2][c],um[3][c])};
    lds_st<u32x2>(d,e); lds_st<u32x2>(d+A_US,o);
    lds_st<u32>(d+A_UB+4,e.x); lds_st<u32>(d+A_US+A_UB+4,o.x);
    if(!last){ lds_st<u32>(d+A_UB+8,e.y); lds_st<u32>(d+A_US+A_UB+8,o.y); } } }
DI void conv_job(const bool BIG, int wi, int ch0, int rot, lptr L, SsmP& p0, u32 dsk, const u16* u, u16* out, void* ws){
  const int lane=threadIdx.x&63;
  lptr us=L+wi*A_US, kw=L+(BIG?8:4)*A_US+wi*A_KS;
  if(lane<56) lds_st<u32x4>(us+(lane<28?lane*16:A_UB+(lane-28)*16),u32x4{0,0,0,0});
  if(lane==56) lds_st<u32>(us+A_UB+448,0u);
  u32x4 um[4];
  const int ur=(64*(wi+rot)+lane)&511;
  { float s,inv; ssm_prep(p0,s,inv); f32x16 acc={};
    #pragma unroll 1
    for(int h=0;h<2;++h){
      if(BIG&&h==1){ const char* up=(const char*)u+(size_t)ur*4*1280+2*ch0;
        #pragma unroll
        for(int r=0;r<4;++r) um[r]=*(const u32x4*)(up+r*1280); }
      kgen_half(p0,h,s,kw,lane,acc); }
    float v[12];
    #pragma unroll
    for(int r=0;r<12;++r) v[r]=acc[r]*inv;
    k_finish(v,kw,lane); }
  if(BIG) u_scatter(true,ur,L,um);
  bar();
  f32x4 acc[8];
  conv_steps(us,kw,lane,acc);
  { const int tl=32*((lane&15)>>1)+8*(lane>>4)+2*(lane&1); asm volatile("":"+v"(dsk)); const float d=bflo(dsk);
    u32 uu[16];
    #pragma unroll
    for(int c=0;c<8;++c){ uu[2*c]=lds_ld<u32>(us+(224+256*c+tl)*2); uu[2*c+1]=lds_ld<u32>(us+(228+256*c+tl)*2); }
    #pragma unroll
    for(int c=0;c<8;++c){ lds_st<u32>(kw+(256*c+tl)*2,fin2(acc[c][0],acc[c][1],uu[2*c],d)); lds_st<u32>(kw+(256*c+tl+4)*2,fin2(acc[c][2],acc[c][3],uu[2*c+1],d)); } }
  bar();
  { lptr yp=L+(BIG?8:4)*A_US+((lane&15)>>2)*A_KS+(256*wi+16*(lane>>4)+4*(lane&3))*2;
    char* op=(char*)out+(size_t)(256*wi+lane)*2560+2*ch0;
    #pragma unroll
    for(int i=0;i<4;++i){
      if(BIG) *(u32x4*)(op+(size_t)i*64*2560)=cat(lds_tr(yp+i*128),lds_tr(yp+4*A_KS+i*128));
      else { *(u32x2*)(op+(size_t)i*64*2560)=lds_tr(yp+i*128); *(u32x2*)(op+(size_t)(i*64+1024)*2560)=lds_tr(yp+2048+i*128); } } }
}

constexpr int B_ST=36864, B_PV=2*B_ST, B_PVS=18432, B_WP=4*B_ST, B_Z=B_WP+6400, B_KT=159744, B_F=163168;
DI float max3(float a,float b,float c){ return __builtin_fmaxf(__builtin_fmaxf(a,b),c); }
template<class H> DI void softmax20x2(const f32x4 (*z)[5], u32 (*o)[10], H hook){
  const float c=1.4426950408889634f; float mc[2];
  { float m0[2],m1[2],m2[2];
    #pragma unroll
    for(int t=0;t<2;++t){ m0[t]=max3(z[t][0][0],z[t][0][1],z[t][0][2]); m1[t]=max3(z[t][0][3],z[t][1][0],z[t][1][1]); m2[t]=max3(z[t][1][2],z[t][1][3],z[t][2][0]); }
    #pragma unroll
    for(int t=0;t<2;++t){ m0[t]=max3(m0[t],z[t][2][1],z[t][2][2]); m1[t]=max3(m1[t],z[t][2][3],z[t][3][0]); m2[t]=max3(m2[t],z[t][3][1],z[t][3][2]); }
    #pragma unroll
    for(int t=0;t<2;++t){ m0[t]=max3(m0[t],z[t][3][3],z[t][4][0]); m1[t]=max3(m1[t],z[t][4][1],z[t][4][2]); m2[t]=__builtin_fmaxf(m2[t],z[t][4][3]); }
    #pragma unroll
    for(int t=0;t<2;++t) mc[t]=max3(m0[t],m1[t],m2[t])*-c; }
  SB();
  f32x2 e[2][10];
  #pragma unroll
  for(int i=0;i<10;++i)
    #pragma unroll
    for(int t=0;t<2;++t) e[t][i]=__builtin_elementwise_fma(f32x2{z[t][i>>1][(i&1)*2],z[t][i>>1][(i&1)*2+1]},f32x2{c,c},f32x2{mc[t],mc[t]});
  SB();
  #pragma unroll
  for(int i=0;i<10;++i)
    #pragma unroll
    for(int t=0;t<2;++t){ hook(2*i+t); e[t][i]=f32x2{__builtin_amdgcn_exp2f(e[t][i].x),__builtin_amdgcn_exp2f(e[t][i].y)}; SB(); }
  float r[2];
  { f32x2 s[2][5];
    #pragma unroll
    for(int i=0;i<5;++i)
      #pragma unroll
      for(int t=0;t<2;++t) s[t][i]=e[t][2*i]+e[t][2*i+1];
    #pragma unroll
    for(int t=0;t<2;++t){ s[t][0]+=s[t][1]; s[t][2]+=s[t][3]; }
    #pragma unroll
    for(int t=0;t<2;++t){ s[t][0]+=s[t][4]; }
    #pragma unroll
    for(int t=0;t<2;++t){ s[t][0]+=s[t][2]; }
    #pragma unroll
    for(int t=0;t<2;++t) r[t]=__builtin_amdgcn_rcpf(s[t][0].x+s[t][0].y); }
  SB();
  #pragma unroll
  for(int i=0;i<10;++i)
    #pragma unroll
    for(int t=0;t<2;++t) e[t][i]*=r[t];
  SB();
  #pragma unroll
  for(int i=0;i<10;++i)
    #pragma unroll
    for(int t=0;t<2;++t) o[t][i]=pk_bf16(e[t][i].x,e[t][i].y);
  SB();
}
DI u32x4 pack2(f32x4 a,f32x4 b){ u32x4 r={pk_bf16(a[0],a[1]),pk_bf16(a[2],a[3]),pk_bf16(b[0],b[1]),pk_bf16(b[2],b[3])}; return r; }
struct Stream { u32 xo[4], wv[5]; int s0, qw; const u16 *x, *w; lptr L;
  DI void init(int q,int li,int rt,int grp,int qw_,int lane,const u16* x_,const u16* w_,lptr L_){
    qw=qw_; x=x_; w=w_; L=L_;
    s0=((0x8530>>((((li>>(2-q))+((li&(3>>q))<<q))&3)*4))&15)+q;
    #pragma unroll
    for(int i=0;i<4;++i){ const u32 r=(4*qw+i)*8+(lane>>3); xo[i]=(rt*128+r)*1280u+(((lane&7)^((r>>1)&7))<<4)-1024u*i; }
    dma16x4((const char*)x+(s0>=10?s0-10:s0)*128,xo[0],xo[1],xo[2],xo[3],L+4*qw*1024);
    const u32 wlo=(160<<q)*grp+1280*(1-q), dh=1120*(1-q);
    #pragma unroll
    for(int i=0;i<5;++i){ const u32 c=(5*qw+i)*64+lane, r=(c*3277u)>>16, pos=c-r*20, sc=pos^(((r>>3)&1)<<1);
      wv[i]=r*3840u+wlo+(sc<10?0u:dh)+16*sc-1024u*(i&3); } }
  DI void issue(int s,bool withx=true) const { int ss=s+s0; if(ss>=10) ss-=10; lptr d=L+(s&3)*B_ST;
    const char* xb=(const char*)x+ss*128; const char* wb=(const char*)w+(size_t)ss*64*3840;
    if(withx) dma16x4(xb,xo[0],xo[1],xo[2],xo[3],d+4*qw*1024);
    dma16x4(wb,wv[0],wv[1],wv[2],wv[3],d+16384+5*qw*1024); dma16(wb,wv[4],d+16384+(5*qw+4)*1024); } };

template<bool GATHER> DI void o_phase(lptr L,rsrc_t rs,int w,int oh,int tq){
  const int lane=threadIdx.x&63, g=lane>>4, r=lane&15;
  if(GATHER){ *(volatile LAS u32*)(L+B_F+128+4*w)=1u; }
  else for(int it=0;it<SPIN_MAX;++it){ if(__builtin_amdgcn_readfirstlane(*(volatile LAS u32*)(L+B_F+128+4*(w&3)))) break; __builtin_amdgcn_s_sleep(2); }
  asm volatile("":::"memory");
  u32x4 l0[2],l1[2]; u32x2 l2[2];
  { const u32 qo=WS_Q+(oh*2048+512*tq+128*(w&3)+64*(w>>2)+16*(g>>1)+r)*40;
    #pragma unroll
    for(int i=0;i<2;++i){ l0[i]=ld_sc1(rs,qo+i*1280); l1[i]=ld_sc1(rs,qo+i*1280+16); l2[i]=__builtin_amdgcn_raw_buffer_load_b64(rs,qo+i*1280+32,0,16); } }
  lptr kt=L+B_KT, q00,q01,q10,q11; u32 om,ox;
  { const int pc=5*(g&1)+2*(g>>1), pl=5*(g&1)+4, d1=min(r,4); lptr b=kt;
    q00=b+(16*pc+r)*8; q01=b+(16*pl+r)*8+4*(g>>1); q10=b+(160+5*pc+d1)*8; q11=b+(160+5*pl+d1)*8+4*(g>>1);
    const u32 fb=WS_O+((32*tq+8*(w&3)+4*(w>>2))*20+5*(oh>>3))*1024+r*16;
    om=fb+((oh&7)>>1)*1024+(2*(oh&1)+(g>>1))*256+8*(g&1); ox=(g==0)?(fb+4096+((oh&7)>>1)*256+8*(oh&1)):OOB; }
  asm volatile("":"+v"(q00),"+v"(q01),"+v"(q10),"+v"(q11),"+v"(om),"+v"(ox));
  u32x4 v[16]; const u32 go=(lane<53&&53*w+lane<210)?(WS_P1+oh*16*3584+(53*w+lane)*16):OOB;
  u32 pq[2][10];
  { const u32 sg=(g&1)?0x80008000u:0u; f32x4 z[2][5];
    #pragma unroll
    for(int t=0;t<2;++t){ const u32x4 a=l0[t]^sg, b=l1[t]^sg; const u32x2 c=l2[t]^sg;
      z[t][0]=f32x4{bflo(a.x),bfhi(a.x),bflo(a.y),bfhi(a.y)}; z[t][1]=f32x4{bflo(a.z),bfhi(a.z),bflo(a.w),bfhi(a.w)};
      z[t][2]=f32x4{bflo(b.x),bfhi(b.x),bflo(b.y),bfhi(b.y)}; z[t][3]=f32x4{bflo(b.z),bfhi(b.z),bflo(b.w),bfhi(b.w)};
      z[t][4]=f32x4{bflo(c.x),bfhi(c.x),bflo(c.y),bfhi(c.y)}; }
    SB();
    softmax20x2(z,pq,[&](int k){ if(GATHER&&k<16) v[k]=ld_sc1(rs,go+k*3584); });
    #pragma unroll
    for(int i=0;i<2;++i){
      #pragma unroll
      for(int k=0;k<5;++k){ const int a=(k<4)?k:8, b=(k<4)?k+4:9;
        auto s=__builtin_amdgcn_permlane32_swap(pq[i][a],pq[i][b],false,false); pq[i][a]=s[0]; pq[i][b]=s[1]; }
      #pragma unroll
      for(int k=0;k<10;++k) asm volatile("":"+v"(pq[i][k])); } }
  if(GATHER){
    gather16(rs,go,v);
    f32x4 s[4];
    #pragma unroll
    for(int i=0;i<4;++i) s[i]=(__builtin_bit_cast(f32x4,v[4*i])+__builtin_bit_cast(f32x4,v[4*i+1]))+(__builtin_bit_cast(f32x4,v[4*i+2])+__builtin_bit_cast(f32x4,v[4*i+3]));
    s[0]=(s[0]+s[1])+(s[2]+s[3]);
    if(lane<53) *(volatile LAS u32x2*)(kt+(53*w+lane)*8)=u32x2{pk_bf16(s[0][0],s[0][1]),pk_bf16(s[0][2],s[0][3])};
    *(volatile LAS u32*)(L+B_F+32+4*w)=1u;
  }
  u32x4 a0[2]; u32 a1[2];
  for(int it=0;it<SPIN_MAX;++it){
    const u32x4 f=*(volatile LAS u32x4*)(L+B_F+32);
    a0[0]=cat(*(volatile LAS u32x2*)q00,*(volatile LAS u32x2*)(q00+128)); a0[1]=cat(*(volatile LAS u32x2*)q10,*(volatile LAS u32x2*)(q10+40));
    a1[0]=*(volatile LAS u32*)q01; a1[1]=*(volatile LAS u32*)q11;
    if(__builtin_amdgcn_readfirstlane(f.x+f.y+f.z+f.w)==4u) break;
    if(!GATHER) __builtin_amdgcn_s_sleep(1); }
  f32x4 n[4][2];
  #pragma unroll
  for(int i=1;i>=0;--i){
    #pragma unroll
    for(int t=0;t<4;++t){ const u32* f=pq[t>>1]+4*(t&1); n[t][i]=mfma16(a0[i],u32x4{f[0],f[1],f[2],f[3]},Z4); }
    #pragma unroll
    for(int t=0;t<4;++t) n[t][i]=mfma16(u32x4{a1[i],0u,0u,0u},u32x4{pq[t>>1][8+(t&1)],0u,0u,0u},n[t][i]); }
  SB();
  float den[4];
  #pragma unroll
  for(int t=0;t<4;++t) den[t]=__builtin_bit_cast(float,__builtin_amdgcn_ds_bpermute((r+16)*4,__builtin_bit_cast(int,n[t][1][0])));
  #pragma unroll
  for(int t=0;t<4;++t) den[t]=__builtin_amdgcn_rcpf(rbf(rbf(den[t])+1e-6f));
  SB();
  #pragma unroll
  for(int t=0;t<4;++t){ const f32x4 m=n[t][0]*den[t], e=n[t][1]*den[t];
    __builtin_amdgcn_raw_buffer_store_b64(u32x2{pk_bf16(m[0],m[1]),pk_bf16(m[2],m[3])},rs,om+t*20480,0,16);
    __builtin_amdgcn_raw_buffer_store_b64(u32x2{pk_bf16(e[0],e[1]),pk_bf16(e[2],e[3])},rs,ox+t*20480,0,16); }
}

template<class F> DI bool role_b(const bool ISQ,int X,int li,int rt,int grp,int w,const Stream& st,lptr L,F params,const u16* u,const u16* wo,u16* out,void* ws){
  const int tid=threadIdx.x, lane=tid&63, g=lane>>4;
  const int T=ISQ?16+li:li; const bool doout=T<20; const int tt64=4*X+T/5, ct=T%5;
  const rsrc_t rs=__builtin_amdgcn_make_buffer_rsrc(ws,0,WS_END,0x00020000);
  constexpr int OIM=B_PV;
  u64 E;
  auto count=[&](int word){ asm volatile("":::"memory"); u32 n=0; if(lane==0) n=__hip_atomic_fetch_add((LAS u32*)(L+B_F+4*word),1u,__ATOMIC_RELAXED,__HIP_MEMORY_SCOPE_WORKGROUP); return n; };
  auto until=[&](int word,u32 n){ for(int it=0;it<SPIN_MAX;++it){ if(__builtin_amdgcn_readfirstlane(*(volatile LAS u32*)(L+B_F+4*word))>=n) break; __builtin_amdgcn_s_sleep(2); } asm volatile("":::"memory"); };
  auto arrive=[&](){
    if(__builtin_amdgcn_readfirstlane(count(4))==3u&&lane==0){ flag_set(ws,ISQ?QF(4*rt+grp):KF(grp,rt),E); asm volatile("":::"memory"); *(volatile LAS u32*)(L+B_F+84)=4u; } };
  volatile LAS u32* bad=(volatile LAS u32*)(L+B_F+20);
  const int oh=4*grp+(rt&3), tq=rt>>2;
  const int ot=4*tt64+(w&3);
  int fi=(lane<16)?KF(grp,lane):((lane<32)?KF(lane&7,4*tq+(w&3)):((lane<48)?KF(ot&7,ot>>3):QF(4*(4*tq+(w&3))+(oh>>3))));
  if(w<4){
    const int th=w>>1, hh=w&1;
    if(lane<4) lds_st<u32x4>(L+B_Z+lane*16,u32x4{0,0,0,0});
    if(tid<9) lds_st<u32x4>(L+B_F+tid*16,u32x4{0,0,0,0});
    lptr fad[3][2];
    { const int q=(lane&15)>>2, p=lane&3, hd=ISQ?p:(p&1);
      #pragma unroll
      for(int s=0;s<3;++s)
        #pragma unroll
        for(int t=0;t<2;++t){ const int d=16*(2*s+t)+4*g-20*hd; const bool ok=(d>=0)&&(d<20)&&(2*s+t<5);
          fad[s][t]=ok?(L+B_WP+((ISQ?4*hh:2*hh)+hd)*800+(d+q)*40+(ISQ?0:(p>>1)*3200)):(L+B_Z); if(2*s+t<5) asm volatile("":"+v"(fad[s][t])); } }
    lptr pv=L+B_PV+w*B_PVS;
    lptr pp=pv+(lane&15)*168+(g&1)*80+(g>>1)*40, vp2, vp3, vp4, pa, vb;
    { lptr vp=pv+10752+(lane&15)*104; vp2=vp+((g<2)?(48*g+40):(8*g-16)); vp3=vp+((g==3)?48:(16+8*g)); vp4=vp+56+8*g;
      const int tk=16*(g&1)+4*((lane&15)>>2)+2*(g>>1); pa=pv+tk*168+8*(lane&3)+(th^1)*80; vb=pv+10752+tk*104+8*(lane&3)+(th^1)*48; }
    u32 po0, po1;
    { const u32 po=WS_P1+((4*grp+2*hh+th)*16+rt)*3584; po0=po+lane*16; po1=((lane&15)<5)?(po+(160+5*g+(lane&15))*16):OOB; }
    asm volatile("":"+v"(fi),"+v"(pp),"+v"(vp2),"+v"(vp3),"+v"(vp4),"+v"(pa),"+v"(vb),"+v"(po0),"+v"(po1));
    lptr ab[5], bb[2];
    { const int q=(lane&15)>>2,p=lane&3;
      #pragma unroll
      for(int i=0;i<5;++i){ int n=2*i+(p>>1); int cid=ISQ?(10*hh+n):(5*hh+n+(n>=5?5:0)); ab[i]=L+16384+(8*g+q)*320+((cid^((g&1)<<1))<<4)+8*(p&1); }
      const int b0=(64*th+(lane&15))*128+((g^((lane&15)>>1))<<4); bb[0]=L+b0; bb[1]=L+(b0^64); }
    f32x4 acc[5][4];
    #pragma unroll
    for(int i=0;i<5;++i)
      #pragma unroll
      for(int t=0;t<4;++t){ acc[i][t]=Z4; asm volatile("":"+v"(acc[i][t])); }
    u32x4 FA[2][5], FB[2][4], fa[2][5];
    u32x2 lo2;
    auto half=[&](int hf,bool mm,bool last,int step){
      u32x2 lo;
      #pragma unroll
      for(int n=0;n<20;++n){ const int i=n>>2, t=n&3;
        if(mm) acc[i][t]=mfma16(FA[hf^1][i],FB[hf^1][t],acc[i][t]);
        SB();
        if(n==0) FB[hf][0]=lds_ld<u32x4>(bb[hf]);
        else if(n==1) lo=lds_tr(ab[0]+hf*10240);
        else if(n==2) FA[hf][0]=cat(lo,lds_tr(ab[0]+hf*10240+1280));
        else if(n<6) FB[hf][n-2]=lds_ld<u32x4>(bb[hf]+2048*(n-2));
        else if(n<14){ const int j=1+((n-6)>>1); if(!(n&1)) lo=lds_tr(ab[j]+hf*10240); else FA[hf][j]=cat(lo,lds_tr(ab[j]+hf*10240+1280)); }
        else if(hf==1&&!last){ if(n<19){ ab[n-14]+=step; asm volatile("":"+v"(ab[n-14])); } if(n>=18){ bb[n-18]+=step; asm volatile("":"+v"(bb[n-18])); } }
        if(last){ const int j=n>>2, s=(n>>1)&1; if(!(n&1)) lo2=lds_tr(fad[s][0]+8*j); else fa[s][j]=cat(lo2,lds_tr(fad[s][1]+8*j)); }
        SB(); } };
    bar();
    __builtin_amdgcn_s_waitcnt(0xC07F);
    SB();
    half(0,false,false,0);
    #pragma unroll 1
    for(u32 s=1;s<10;++s){
      half(1,true,false,(s&3)?B_ST:-3*B_ST);
      rbar();
      SB();
      half(0,true,false,0); }
    const u32x2 ev=*(volatile LAS u32x2*)(L+B_F+88);
    half(1,true,true,0);
    E=(u64)(u32)__builtin_amdgcn_readfirstlane(ev.x)|((u64)(u32)__builtin_amdgcn_readfirstlane(ev.y)<<32);
    asm volatile("s_waitcnt lgkmcnt(0)":::"memory");
    waitv<0>();
    count(7);
    #pragma unroll
    for(int t=0;t<4;++t)
      #pragma unroll
      for(int i=0;i<5;++i) acc[i][t]=mfma16(FA[1][i],FB[1][t],acc[i][t]);
    u32x2 pk[5][4];
    #pragma unroll
    for(int t=0;t<4;++t)
      #pragma unroll
      for(int i=0;i<5;++i) pk[i][t]=u32x2{pk_bf16(acc[i][t][0],acc[i][t][1]),pk_bf16(acc[i][t][2],acc[i][t][3])};
    if(ISQ){
      u32x4 f2[5];
      #pragma unroll
      for(int i=0;i<5;++i) f2[i]=cat(lds_tr(fad[2][0]+8*i),u32x2{0u,0u});
      lptr sp_=pv+g*2560+(lane&15)*40;
      f32x4 z[4][5];
      #pragma unroll
      for(int t=0;t<4;++t)
        #pragma unroll
        for(int i=0;i<5;++i) z[t][i]=mfma16(fa[0][i],cat(pk[0][t],pk[1][t]),Z4);
      #pragma unroll
      for(int t=0;t<4;++t)
        #pragma unroll
        for(int i=0;i<5;++i) z[t][i]=mfma16(fa[1][i],cat(pk[2][t],pk[3][t]),z[t][i]);
      SB();
      #pragma unroll
      for(int t=0;t<5;++t){
        #pragma unroll
        for(int i=0;i<5;++i){ if(t<4) z[t][i]=mfma16(f2[i],cat(pk[4][t],u32x2{0u,0u}),z[t][i]);
          if(t>0) lds_st<u32x2>(sp_+(t-1)*640+8*i,u32x2{pk_bf16(z[t-1][i][0],z[t-1][i][1]),pk_bf16(z[t-1][i][2],z[t-1][i][3])}); }
        SB(); }
      #pragma unroll
      for(int i=0;i<10;++i){ const int c=i*64+lane, hd=(c*205)>>15;
        st_sc1(rs,WS_Q+((8*grp+4*hh+hd)*2048+128*rt+64*th)*40+(c-160*hd)*16,lds_ld<u32x4>(pv+c*16)); }
      waitv<0>(); arrive();
    } else {
      { f32x4 z[4][5]; u32 ph[4][10];
        auto fm=[&](int t){
          const u32x4 b0=cat(pk[0][t],pk[1][t]), b1=cat(pk[2][t],pk[3][t]);
          #pragma unroll
          for(int i=0;i<5;++i) z[t][i]=mfma16(fa[0][i],b0,Z4);
          #pragma unroll
          for(int i=0;i<5;++i) z[t][i]=mfma16(fa[1][i],b1,z[t][i]); };
        fm(0); fm(1);
        SB();
        softmax20x2(z,ph,[&](int k){ const int t=2+k/10, i=k%5;
          z[t][i]=(k%10<5)?mfma16(fa[0][i],cat(pk[0][t],pk[1][t]),Z4):mfma16(fa[1][i],cat(pk[2][t],pk[3][t]),z[t][i]); });
        auto wp=[&](int t){
          #pragma unroll
          for(int i=0;i<5;++i) lds_st<u32x2>(pp+t*2688+8*i,u32x2{ph[t][2*i],ph[t][2*i+1]}); };
        u32x2 v2[4];
        #pragma unroll
        for(int t=0;t<4;++t) v2[t]=(g<2)?u32x2{0x3F80u,0u}:pk[2][t];
        softmax20x2(z+2,ph+2,[&](int k){
          if(k<10) *(volatile LAS u32x2*)(pp+(k/5)*2688+8*(k%5))=u32x2{ph[k/5][2*(k%5)],ph[k/5][2*(k%5)+1]};
          else if(k<14) *(volatile LAS u32x2*)(vp2+(k-10)*1664)=v2[k-10];
          else if(k<18) *(volatile LAS u32x2*)(vp3+(k-14)*1664)=pk[3][k-14];
          else { *(volatile LAS u32x2*)(vp4+(2*k-36)*1664)=pk[4][2*k-36]; *(volatile LAS u32x2*)(vp4+(2*k-35)*1664)=pk[4][2*k-35]; } });
        wp(2); wp(3); }
      u64 fl;
      { f32x4 c[2][6];
        #pragma unroll
        for(int h2=0;h2<2;++h2){
          if(h2){ pa+=th?80:-80; vb+=th?48:-48; }
          u32x4 a[2][3], b[2][2];
          #pragma unroll
          for(int ks=0;ks<2;++ks){
            #pragma unroll
            for(int i=0;i<3;++i) a[ks][i]=cat(lds_tr(pa+ks*32*168+32*i),lds_tr(pa+(ks*32+1)*168+32*i));
            #pragma unroll
            for(int dt=0;dt<2;++dt) b[ks][dt]=cat(lds_tr(vb+ks*32*104+32*dt),lds_tr(vb+(ks*32+1)*104+32*dt)); }
          #pragma unroll
          for(int i=0;i<6;++i) c[h2][i]=mfma16(a[0][i%3],b[0][i/3],Z4);
          #pragma unroll
          for(int i=0;i<6;++i) c[h2][i]=mfma16(a[1][i%3],b[1][i/3],c[h2][i]); }
        #pragma unroll
        for(int i=0;i<6;++i) *(volatile LAS f32x4*)(pv+i*1024+lane*16)=c[0][i];
        *(volatile LAS u32*)(L+B_F+4*w)=1u;
        #pragma unroll
        for(int i=0;i<6;++i) asm volatile("":"+v"(c[1][i]));
        lptr ox_=L+B_PV+(w^2)*B_PVS; f32x4 pr[6];
        for(int it=0;it<SPIN_MAX;++it){
          const u32 f=*(volatile LAS u32*)(L+B_F+4*(w^2)), n=*(volatile LAS u32*)(L+B_F+16);
          #pragma unroll
          for(int i=0;i<6;++i) pr[i]=*(volatile LAS f32x4*)(ox_+i*1024+lane*16);
          if(__builtin_amdgcn_readfirstlane(f+n)==5u) break; }
        fl=flag_get(ws,fi);
        #pragma unroll
        for(int i=0;i<6;++i){ u32 o=(i<3)?(po0+i*1024):(po1+(i-3)*320); if(i%3==2) o=(g<2)?o:OOB;
          st_sc1(rs,o,__builtin_bit_cast(u32x4,c[1][i]+pr[i])); } }
      if(!__all(fl==E)) flag_wait(ws,E,fi,fl);
      asm volatile("":::"memory");
      o_phase<true>(L,rs,w,oh,tq);
      count(6);
      if(rt==15&&grp==7&&w==3){
        for(int it=0;it<SPIN_MAX;++it){ if(__all(flag_get(ws,KF(lane>>4,lane&15))==E&&flag_get(ws,KF(4+(lane>>4),lane&15))==E&&flag_get(ws,QF(lane))==E)) break; __builtin_amdgcn_s_sleep(2); }
        if(lane==0) __hip_atomic_store((GAS u64*)ws,E,__ATOMIC_RELAXED,__HIP_MEMORY_SCOPE_AGENT); }
    }
    if(!doout){
      until(21,4u);
      const char* up=(const char*)u+(size_t)tid*4*1280+2*(80*X+64+4*(li-4)); u32x4 um[4];
      #pragma unroll
      for(int r=0;r<4;++r) um[r]=cat(*(const u32x2*)(up+r*1280),*(const u32x2*)(up+(r+1024)*1280));
      u_scatter(false,tid,L,um);
      asm volatile("s_waitcnt lgkmcnt(0)":::"memory");
      __builtin_amdgcn_s_waitcnt(0x0F70); return false; }
    until(20,4u);
    if(ISQ){ fi=KF(ot&7,ot>>3); flag_wait(ws,E,fi,flag_get(ws,fi)); }
    if(ISQ){
      const int hi=HF(4*X+li); flag_wait(ws,E,hi,flag_get(ws,hi)); }
    { const u32 ob=WS_O+ot*20480;
      const u32 sm=ob+((lane<32)?((5*(lane>>3)+((lane>>1)&3))*1024+(lane&1)*512+508):((5*((lane-32)>>3)+4)*1024+((lane&7)>>1)*256+244+8*(lane&1)));
      for(int it=0;it<SPIN_MAX;++it){ asm volatile("":::"memory");
        if(__all(__builtin_amdgcn_raw_buffer_load_b32(rs,sm,0,16)!=SENT)) break;
        __builtin_amdgcn_s_sleep(1); }
      const u32 vo=lane*16;
      for(u32 it=1;it<SPIN_MAX;++it){
        #pragma unroll
        for(int i=0;i<5;++i) dma16x4_sc1((const char*)ws+ob+i*4096,vo,vo,vo,vo,L+OIM+w*20480+i*4096);
        waitv<12>();
        rbar();
        waitv<0>();
        rbar();
        rbar();
        rbar();
        if(__builtin_amdgcn_readfirstlane(*bad)!=it) break; } }
    __builtin_amdgcn_s_waitcnt(0x0F70);
  } else {
    const int hw=w-4;
    asm volatile("s_load_dwordx2 %0, %1, 0x0":"=s"(E):"s"(ws):"memory");
    const bool lead3=st.s0==0||st.s0==5||st.s0==1||st.s0==6;
    if(lead3){ st.issue(2); waitv<18>(); rbar(); } else { waitv<9>(); rbar(); st.issue(2); }
    waitv<9>(); rbar();
    st.issue(3);
    asm volatile("s_waitcnt lgkmcnt(0)":"+s"(E)::"memory"); E+=1;
    if(tid==256) *(volatile LAS u32x2*)(L+B_F+88)=u32x2{(u32)E,(u32)(E>>32)};
    if(!ISQ&&lane<50){ lptr p=L+B_WP+hw*800+lane*16; lds_st<u32x4>(p+3200,lds_ld<u32x4>(p)^0x80008000u); }
    waitv<9>(); bar();
    if(!doout) params();
    st.issue(4);
    const int pr=hw>>1, me2=hw&1;
    u32 wq[40];
    { const int r=lane>>3; const u32 sw=((lane&7)^((((r>>1)&1)|(me2<<1))<<1))<<4, vo=r*1280+sw, vo2=((r>>2)*20+(r&3)+16+32*me2)*1280+sw;
      #pragma unroll
      for(int n=0;n<40;++n){ const int G=n/10, m=n%10, i=(n<22)?n:n-22, k=(i<16)?(i&3):((n>=18&&n<21)?n-18:((i<18)?i-16:0));
        wq[n]=((m<8)?vo+(160*G+20*m)*1280:vo2+(160*G+80*(m-8))*1280)-1024*k; asm volatile("":"+v"(wq[n])); } }
    lptr dst=L+hw*18432, rd, rd1; int x9, x10;
    { const int q=(lane&15)>>2, p=lane&3, f=((q>>1)|((g&1)<<1))<<1, ow=pr*2+(g&1);
      lptr rb=L+ow*18432+(g>>1)*1024+q*128+8*(p&1); rd=rb+(((4*me2+(p>>1))^f)<<4); rd1=rb+(((4*me2+2+(p>>1))^f)<<4);
      x9=B_WP+ow*3072-ow*18432; x10=(g>>1)?(B_PV+17408-1024):(x9+2048); }
    lptr ra9=rd+x9, rb9=rd1+x9, ra10=rd+x10, rb10=rd1+x10;
    asm volatile("":"+v"(rd),"+v"(rd1),"+v"(ra9),"+v"(rb9),"+v"(ra10),"+v"(rb10));
    waitv<9>(); rbar();
    #pragma unroll 1
    for(int s=4;s<9;++s){
      st.issue(s+1);
      waitv<9>(); rbar(); }
    waitv<0>(); rbar();
    if(!doout){ until(7,4u); return true; }
    if(!ISQ){
      const u32x4 sv={SENT,SENT,SENT,SENT};
      #pragma unroll
      for(int i=0;i<4;++i) st_sc1(rs,(i<3||lane<18)?(WS_P1+((4*grp+hw)*16+rt)*3584+i*1024+lane*16):OOB,sv);
      #pragma unroll
      for(int i=0;i<5;++i) st_sc1(rs,WS_O+((8*rt+grp)*20+5*hw+i)*1024+lane*16,sv);
      waitv<0>(); arrive(); }
    u32x4 wf[2][11];
    const char* gb=(const char*)wo+ct*256+pr*128+me2*8*1280;
    auto meet=[&](u32 n){ *(volatile LAS u32*)(L+B_F+48+4*hw)=n; until(12+(hw^1),n); };
    auto fetch18=[&](int n0){
      #pragma unroll
      for(int i=0;i<16;i+=4) dma16x4(gb,wq[n0+i],wq[n0+i+1],wq[n0+i+2],wq[n0+i+3],dst+i*1024);
      dma16xn<2>(gb,wq[n0+16],wq[n0+17],0,dst+16384); };
    until(ISQ?21:7,4u);
    fetch18(0);
    dma16xn<3>(gb,wq[18],wq[19],wq[20],L+B_WP+hw*3072);
    dma16(gb,wq[21],dst+B_PV+17408);
    waitv<0>(); meet(1u);
    #pragma unroll
    for(int s=0;s<9;++s){ wf[0][s]=cat(lds_tr(rd+s*2048),lds_tr(rd+s*2048+512)); wf[1][s]=cat(lds_tr(rd1+s*2048),lds_tr(rd1+s*2048+512)); }
    wf[0][9]=cat(lds_tr(ra9),lds_tr(ra9+512)); wf[1][9]=cat(lds_tr(rb9),lds_tr(rb9+512));
    wf[0][10]=cat(lds_tr(ra10),lds_tr(ra10+512)); wf[1][10]=cat(lds_tr(rb10),lds_tr(rb10+512));
    asm volatile("s_waitcnt lgkmcnt(0)":::"memory");
    meet(2u);
    #pragma unroll
    for(int i=0;i<8;i+=4) dma16x4(gb,wq[22+i],wq[23+i],wq[24+i],wq[25+i],dst+i*1024);
    if(!ISQ){ o_phase<false>(L,rs,w,oh,tq); count(6); until(6,8u);
      if(li<4&&tid==256) flag_set(ws,HF(4*X+li),E); }
    #pragma unroll
    for(int i=8;i<16;i+=4) dma16x4(gb,wq[22+i],wq[23+i],wq[24+i],wq[25+i],dst+i*1024);
    dma16xn<2>(gb,wq[38],wq[39],0,dst+16384);
    count(20);
    lptr obt[4];
    #pragma unroll
    for(int t=0;t<4;++t){ obt[t]=L+OIM+lane*16+((t+hw)&3)*20480; asm volatile("":"+v"(obt[t])); }
    waitv<0>();
    for(u32 it=1;it<SPIN_MAX;++it){
      rbar();
      f32x4 c[2][4];
      #pragma unroll
      for(int t=0;t<4;++t){ c[0][t]=Z4; c[1][t]=Z4; }
      u32x4 ob[3][4], wl[3][2]; u32 chk=0;
      auto fetch=[&](int ks){
        #pragma unroll
        for(int t=0;t<4;++t) ob[ks%3][t]=lds_ld<u32x4>(obt[t]+ks*1024);
        if(ks>=11){ wl[ks%3][0]=cat(lds_tr(rd+(ks-11)*2048),lds_tr(rd+(ks-11)*2048+512)); wl[ks%3][1]=cat(lds_tr(rd1+(ks-11)*2048),lds_tr(rd1+(ks-11)*2048+512)); } };
      fetch(0); fetch(1);
      SB();
      #pragma unroll
      for(int ks=0;ks<20;++ks){ u32x2 lo;
        if(ks==6){ rbar(); SB(); }
        #pragma unroll
        for(int n=0;n<8;++n){ const int t=n>>1, cc=n&1, k2=ks+2;
          c[cc][t]=mfma16((ks<11)?wf[cc][ks<11?ks:0]:wl[ks%3][cc],ob[ks%3][t],c[cc][t]);
          SB();
          if(k2<20){
            if(n<4) ob[k2%3][n]=lds_ld<u32x4>(obt[n]+k2*1024);
            else if(k2>=11){ lptr p=((n<6)?rd:rd1)+(k2-11)*2048; if(!(n&1)) lo=lds_tr(p); else wl[k2%3][(n-4)>>1]=cat(lo,lds_tr(p+512)); } }
          if(n==6) chk=max(chk,max(ob[ks%3][0].x,ob[ks%3][0].y));
          if(n==7) chk=max(chk,max(ob[ks%3][0].z,ob[ks%3][0].w));
          SB(); } }
      if(__any(chk==SENT)) *bad=it;
      bar();
      #pragma unroll
      for(int t=0;t<4;++t)
        #pragma unroll
        for(int cc=0;cc<2;++cc) lds_st<u32x2>(L+OIM+(16*((t+hw)&3)+(lane&15))*272+hw*64+cc*32+8*g,u32x2{pk_bf16(c[cc][t][0],c[cc][t][1]),pk_bf16(c[cc][t][2],c[cc][t][3])});
      bar();
      if(__builtin_amdgcn_readfirstlane(*bad)!=it) break; }
  }
  #pragma unroll
  for(int i=0;i<2;++i){ const int r=32*i+(tid>>4);
    __builtin_amdgcn_raw_buffer_store_b128(lds_ld<u32x4>(L+OIM+r*272+(tid&15)*16),__builtin_amdgcn_make_buffer_rsrc(out,0,2048*2560,0x00020000),(64*tt64+r)*2560+1280+ct*256+(tid&15)*16,0,16); }
  return false;
}

constexpr int CODE_LINES=320;
__global__ __launch_bounds__(512,2) void fused(const u16* u,const u16* are,const u16* aim,const u16* bre,const u16* bim,const u16* cre,const u16* cim,
    const u16* logdt,const u16* dskip,const u16* x,const u16* wqkv,const u16* wphi,const u16* wo,u16* out,void* ws){
  extern __shared__ __attribute__((aligned(16))) char smem[];
  lptr L=(lptr)smem;
  const int b=blockIdx.x, X=b&7, idx=b>>3;
  u64 pc; asm volatile("s_getpc_b64 %0":"=s"(pc));
  const int w=__builtin_amdgcn_readfirstlane(threadIdx.x>>6);
  const bool isa=(idx>>3)==2;
  const int q=idx>=24, li=idx-24*q, rt=4*(X>>1)+(li>>(2-q)), grp=((X&1)<<(2-q))+(li&(3>>q));
  if(idx<16&&w<2){
    const u32 n=(w?(X*16+idx)*50:idx*32)+(threadIdx.x&63); u32 keep;
    if(w?((threadIdx.x&63)<50):(threadIdx.x<32&&n<CODE_LINES))
      asm volatile("s_mov_b32 %0, m0\n\ts_mov_b32 m0, %3\n\ts_nop 0\n\tglobal_load_lds_dword %1, %2\n\ts_mov_b32 m0, %0":"=&s"(keep):"v"(n*128),"s"(w?(u64)wo:(pc&~255ull)),"s"(162560+w*256):"memory"); }
  Stream st;
  if(__builtin_expect(!isa&&w>=4,1)){ st.init(q,li,rt,grp,w&3,threadIdx.x&63,x,wqkv,L); st.issue(0,false);
    { const u32 nb=800<<q, o=(threadIdx.x&63)*16; const char* wp=(const char*)wphi+(4*grp+(w&3))*nb;
      if(o<nb) dma16(wp,o,L+B_WP+(w&3)*nb);
      if(o+1024<nb) dma16(wp,o+1024,L+B_WP+(w&3)*nb+1024); }
    st.issue(1); }
  SB();
  const int ch0=isa?(80*X+8*(idx-16)):(80*X+64+4*(idx-28)), wi=isa?w:w-4;
  SsmP sp; u32 dsk;
  auto params=[&](){ const SsmIn in={are,aim,bre,bim,cre,cim,logdt}; sp=load_ssm(in,ch0+wi,threadIdx.x&63); dsk=dskip[ch0+wi]; };
  bool job=true;
  if(__builtin_expect(!isa,1)) job=role_b(q,X,li,rt,grp,w,st,L,params,u,wo,out,ws);
  else params();
  if(job){
    conv_job(isa,wi,ch0,idx&7,L,sp,dsk,u,out,ws); }
}

extern "C" void kernel_launch(void* const* d_in, const int* in_sizes, int n_in, void* d_out, int out_size, void* d_ws, size_t ws_size, hipStream_t stream){
  (void)hipFuncSetAttribute((const void*)fused,hipFuncAttributeMaxDynamicSharedMemorySize,163840);
  fused<<<256,512,163840,stream>>>((const u16*)d_in[0],(const u16*)d_in[1],(const u16*)d_in[2],(const u16*)d_in[3],(const u16*)d_in[4],(const u16*)d_in[5],(const u16*)d_in[6],
    (const u16*)d_in[7],(const u16*)d_in[8],(const u16*)d_in[9],(const u16*)d_in[10],(const u16*)d_in[11],(const u16*)d_in[12],(u16*)d_out,d_ws);
}
```
